```python
import torch

def _softmax(x, axis=-1):
    return torch.softmax(x, dim=axis)
def _gelu(x):
    return torch.nn.functional.gelu(x, approximate="tanh")
def _silu(x):
    return torch.nn.functional.silu(x)
def _sigmoid(x):
    return torch.sigmoid(x)
def _layernorm(x, axis=-1, eps=1e-5):
    m = x.mean(dim=axis, keepdim=True)
    v = x.var(dim=axis, keepdim=True, unbiased=False)
    return (x - m) / torch.sqrt(v + eps)
def _scatter_add(tgt, idx, src, axis=0):
    return tgt.clone().index_add_(axis, idx, src)
def _segment_sum(src, seg, n):
    out = torch.zeros((n,) + src.shape[1:], dtype=src.dtype)
    return out.index_add_(0, seg, src)
def _pad(x, pw):
    flat = tuple(v for pair in reversed(pw) for v in pair)
    return torch.nn.functional.pad(x, flat)

kernel_name = 'standalone__seq2seq_rnn_t_joint__speech'

def setup_inputs():

    _w = (lambda a: (lambda t: t.to(torch.bfloat16) if t.is_floating_point() else t)(torch.as_tensor(a)))
    B, T, D, K = 2, 16000, 256, 31
    U = 8
    sc = (1.0 / (K * D)) ** 0.5
    sd = (1.0 / D) ** 0.5
    return {
        "x": _w((lambda *s: torch.randn(*s, dtype=torch.bfloat16) * 0.2)(B, T, D) * 0.5),
        "w1": _w((lambda *s: torch.randn(*s, dtype=torch.bfloat16) * 3.2)(K, D, D) * sc),
        "w2": _w((lambda *s: torch.randn(*s, dtype=torch.bfloat16) * 3.2)(K, D, D) * sc),
        "g": _w((lambda *s: torch.randn(*s, dtype=torch.bfloat16) * 3.2)(B, U, D) * 0.5),
        "w_f": _w((lambda *s: torch.randn(*s, dtype=torch.bfloat16) * 3.2)(D, D) * sd),
        "w_g": _w((lambda *s: torch.randn(*s, dtype=torch.bfloat16) * 3.2)(D, D) * sd),
        "w_o": _w((lambda *s: torch.randn(*s, dtype=torch.bfloat16) * 3.2)(D, D) * sd),
    }

def reference(x, w1, w2, g, w_f, w_g, w_o):
    B, T, D = x.shape
    K = w1.shape[0]
    pad = K // 2
    xp = _pad(x, ((0,0),(pad,pad),(0,0)))
    h = torch.zeros_like(x)
    for i in range(K):
        h = h + torch.einsum("btc,cd->btd", xp[:, i:i+T, :], w1[i])
    h = _gelu(h)[:, ::2, :]
    T1 = h.shape[1]
    hp = _pad(h, ((0,0),(pad,pad),(0,0)))
    f = torch.zeros_like(h)
    for i in range(K):
        f = f + torch.einsum("btc,cd->btd", hp[:, i:i+T1, :], w2[i])
    f = _gelu(f)[:, ::2, :]
    fe = torch.einsum("btd,de->bte", f, w_f)
    ge = torch.einsum("bud,de->bue", g, w_g)
    j = torch.tanh(fe[:, :, None, :] + ge[:, None, :, :])
    return torch.einsum("btud,dv->btuv", j, w_o)
```

```cpp
#include <hip/hip_runtime.h>
#include <stdint.h>

typedef __attribute__((ext_vector_type(8))) short bf16x8;
typedef __attribute__((ext_vector_type(16))) float f32x16;
typedef __attribute__((ext_vector_type(4))) float f32x4;
typedef __attribute__((ext_vector_type(2))) float f32x2;
typedef __attribute__((ext_vector_type(4))) unsigned int u32x4;
typedef __attribute__((ext_vector_type(2))) unsigned int u32x2;

namespace {

constexpr int DCH = 256;

__device__ __forceinline__ float bf2f(uint32_t v) { return __uint_as_float(v << 16); }
__device__ __forceinline__ uint32_t f2bf(float f) {
  uint32_t u = __float_as_uint(f);
  u += 0x7FFF + ((u >> 16) & 1);
  return u >> 16;
}
__device__ __forceinline__ uint32_t pk2bf(float a, float b) {
  typedef __attribute__((ext_vector_type(2))) __bf16 b2;
  f32x2 v = {a, b};
  b2 r = __builtin_convertvector(v, b2);
  return __builtin_bit_cast(uint32_t, r);
}
__device__ __forceinline__ uint32_t gelu2(float a0, float a1) {
  const f32x2 x = {a0, a1};
  const f32x2 c0 = {-2.302208198144325f, -2.302208198144325f}, c1 = {-0.10294323970002349f, -0.10294323970002349f};
  const f32x2 z = x * __builtin_elementwise_fma(x * x, c1, c0);
  const f32x2 e = {__builtin_amdgcn_exp2f(z[0]), __builtin_amdgcn_exp2f(z[1])};
  const f32x2 d = e + f32x2{1.0f, 1.0f};
  const f32x2 y = x * f32x2{__builtin_amdgcn_rcpf(d[0]), __builtin_amdgcn_rcpf(d[1])};
  return pk2bf(y[0], y[1]);
}
__device__ __forceinline__ f32x16 mfma32(const u32x4& a, const u32x4& b, const f32x16& c) {
  return __builtin_amdgcn_mfma_f32_32x32x16_bf16(__builtin_bit_cast(bf16x8, a), __builtin_bit_cast(bf16x8, b), c, 0, 0, 0);
}
__device__ __forceinline__ f32x4 mfma16(const u32x4& a, const u32x4& b, const f32x4& c) {
  return __builtin_amdgcn_mfma_f32_16x16x32_bf16(__builtin_bit_cast(bf16x8, a), __builtin_bit_cast(bf16x8, b), c, 0, 0, 0);
}
__device__ __forceinline__ f32x16 zero16() {
  f32x16 z;
#pragma unroll
  for (int i = 0; i < 16; ++i) z[i] = 0.f;
  return z;
}
__device__ __forceinline__ __amdgpu_buffer_rsrc_t mkbuf(const void* p, int nbytes) {
  return __builtin_amdgcn_make_buffer_rsrc((void*)p, (short)0, nbytes, 0x00020000);
}
template <int V> struct IC { static constexpr int value = V; };
template <int I, int N, class F>
__device__ __forceinline__ void sfor(F&& f) {
  if constexpr (I < N) {
    f(IC<I>{});
    sfor<I + 1, N>(f);
  }
}
#define SB() __builtin_amdgcn_sched_barrier(0)

constexpr double PI_ = 3.14159265358979323846;
constexpr double c128(int m) {
  m = ((m % 128) + 128) % 128;
  if (m > 64) m = 128 - m;
  double sg = 1.0;
  if (m > 32) { m = 64 - m; sg = -1.0; }
  if (m == 32) return 0.0;
  const double a = PI_ * m / 64.0;
  double term = 1.0, sum = 1.0;
  for (int i = 1; i < 16; ++i) { term *= -a * a / ((2 * i - 1) * (2 * i)); sum += term; }
  return sg * sum;
}
constexpr double s128(int m) { return c128(m - 32); }
constexpr uint16_t cbf(double v) {
  uint32_t u = __builtin_bit_cast(uint32_t, (float)v);
  u += 0x7FFF + ((u >> 16) & 1);
  return (uint16_t)(u >> 16);
}
constexpr double cbf2d(uint16_t v) { return (double)__builtin_bit_cast(float, (uint32_t)v << 16); }
struct Tabs {
  uint16_t f1[2][3][64][8];
  uint16_t e1[2][2][64][8];
  uint16_t e2[2][64][8];
  uint16_t g1[64][64];
  uint16_t f2[64][8];
  uint16_t g2[16][32];
};
constexpr Tabs make_tabs() {
  Tabs t{};
  for (int l = 0; l < 64; ++l)
    for (int e = 0; e < 8; ++e) {
      const int h = l >> 5, f = l & 31, ri = f & 1;
      for (int fb = 0; fb < 2; ++fb) {
        const int k = 16 * fb + (f >> 1);
        for (int ks = 0; ks < 3; ++ks) {
          const int a = (2 * k + 1) * (16 * ks + 8 * h + e);
          t.f1[fb][ks][l][e] = cbf(ri ? -s128(a) : c128(a));
        }
        const int a = (2 * k + 1) * (8 * h + e);
        const double v = ri ? s128(a) : c128(a);
        t.e1[fb][0][l][e] = cbf(v);
        t.e1[fb][1][l][e] = cbf(v - cbf2d(cbf(v)));
      }
      const int a = 2 * (2 * (f >> 1) + 1) * (8 * h + e);
      const double v = ri ? s128(a) : c128(a);
      t.e2[0][l][e] = cbf(v);
      t.e2[1][l][e] = cbf(v - cbf2d(cbf(v)));
      t.f2[l][e] = cbf(ri ? -v : v);
    }
  for (int m = -15; m < 48; ++m)
    for (int f = 0; f < 64; ++f) {
      const int a = (2 * (f >> 1) + 1) * m;
      t.g1[m + 16][f] = cbf(((f & 1) ? -s128(a) : c128(a)) / 32.0);
    }
  for (int m = 0; m < 16; ++m)
    for (int f = 0; f < 32; ++f) {
      const int a = 2 * (2 * (f >> 1) + 1) * m;
      t.g2[m][f] = cbf(((f & 1) ? -s128(a) : c128(a)) / 16.0);
    }
  return t;
}
__device__ __attribute__((aligned(64))) const Tabs tabs = make_tabs();

constexpr int OOB = 0x7FFFFF00;
constexpr int WT = 16;
constexpr int NT = 2;
constexpr int R1 = 336;
constexpr int R2 = 504;
constexpr int PL1 = R1 * 128;
constexpr int PL2 = R2 * 128;
constexpr int U1BYTES = 32 * 16 * PL1, U2BYTES = 16 * 16 * PL2;
constexpr int WBIN = 16 * 16 * 2 * 1024;
constexpr int YP1 = R1 * 64;
constexpr int YP2 = 500 * 64;
constexpr int Y1BYTES = 32 * 16 * YP1, Y2BYTES = 16 * 16 * YP2;
__device__ __forceinline__ int dmap(int cb, int n) { return 64 * (cb >> 2) + 32 * (cb & 1) + 2 * n + ((cb >> 1) & 1); }

constexpr int NBX = 336, NBW1 = 128, NBW2 = 128, NBM = 32;

template <int CONV>
__device__ __forceinline__ void wtrans(const uint16_t* __restrict__ w, char* __restrict__ wh, int wid, int lane) {
  const int e = wid & 1, dq = (wid >> 1) & 3, pc = wid >> 3;
  const int ks = pc >> 2, kq = pc & 3;
  const int n32 = lane & 31, h = lane >> 5;
  const int r = CONV == 1 ? ks >> 3 : kq >> 1;
  const int c0 = CONV == 1 ? (ks & 7) * 32 + kq * 8 : dmap(ks, 4 * (kq & 1));
  const __amdgpu_buffer_rsrc_t wr = mkbuf(w, 31 * 131072);
  const int vb = (16 * h + r + 1) * 131072 + c0 * 512 + dq * 128 + n32 * 4;
  const int vb0 = (h + r == 0) ? OOB : vb - 262144;
  uint32_t v[8][8];
#pragma unroll
  for (int el = 0; el < 8; ++el)
#pragma unroll
    for (int jj = 0; jj < 8; ++jj) {
      const int co = CONV == 1 ? el * 512 : (el & 3) * 1024;
      const int so = (CONV == 1 ? 0 : (el >> 2) * 8192) + (jj ? (jj - 1) * 262144 : 0);
      v[el][jj] = __builtin_amdgcn_raw_buffer_load_b32(wr, (jj ? vb : vb0) + co, so, 0);
    }
  constexpr int NFB = CONV == 1 ? 2 : 1;
  const u32x4* et = CONV == 1 ? (const u32x4*)&tabs.e1[0][0][0][0] : (const u32x4*)&tabs.e2[0][0][0];
  u32x4 E[NFB][2];
#pragma unroll
  for (int fb = 0; fb < NFB; ++fb) { E[fb][0] = et[fb * 128 + lane]; E[fb][1] = et[fb * 128 + 64 + lane]; }
  const __amdgpu_buffer_rsrc_t dr = mkbuf(wh, (CONV == 1 ? 32 : 16) * WBIN);
  const int vo = 2 * h * WBIN + (4 * dq + 2 * e + (n32 >> 4)) * 32768 + ks * 2048 + kq * 256 + (n32 & 15) * 16;
  const uint32_t ps = e ? 0x07060302u : 0x05040100u;
  u32x4 b[8];
#pragma unroll
  for (int el = 0; el < 8; ++el)
#pragma unroll
    for (int j = 0; j < 4; ++j) b[el][j] = __builtin_amdgcn_perm(v[el][2 * j + 1], v[el][2 * j], ps);
#pragma unroll
  for (int fb = 0; fb < NFB; ++fb) {
    u32x4 o[16];
#pragma unroll
    for (int ep = 0; ep < 4; ++ep) {
      f32x16 d[2];
#pragma unroll
      for (int z = 0; z < 2; ++z) {
        d[z] = mfma32(E[fb][1], b[2 * ep + z], zero16());
        d[z] = mfma32(E[fb][0], b[2 * ep + z], d[z]);
      }
#pragma unroll
      for (int i = 0; i < 16; ++i) o[i][ep] = pk2bf(d[0][i], d[1][i]);
      SB();
    }
#pragma unroll
    for (int i = 0; i < 16; ++i)
      __builtin_amdgcn_raw_buffer_store_b128(o[i], dr, vo + (i & 1) * 1024, (16 * fb + 4 * (i >> 2) + ((i >> 1) & 1)) * WBIN, WT);
    SB();
  }
}

__global__ __launch_bounds__(256, 3) void pre_kernel(const uint16_t* __restrict__ x, const uint16_t* __restrict__ w1,
                                                  const uint16_t* __restrict__ w2, const uint16_t* __restrict__ g,
                                                  const uint16_t* __restrict__ wg, const uint16_t* __restrict__ wf,
                                                  const uint16_t* __restrict__ wo, char* __restrict__ u1,
                                                  char* __restrict__ w1h, char* __restrict__ w2h, u32x4* __restrict__ wff,
                                                  u32x4* __restrict__ wof, float* __restrict__ ge) {
  __shared__ float part[16][17];
  const int blk = blockIdx.x;
  const int tid = threadIdx.x, lane = tid & 63;
  const int wave = __builtin_amdgcn_readfirstlane(tid >> 6);
  if (blk < NBW1) {
    wtrans<1>(w1, w1h, blk * 4 + wave, lane);
  } else if (blk < NBW1 + NBX) {
    const int wid = (blk - NBW1) * 4 + wave;
    const int sg = wid >> 3, cq = wid & 7;
    const int n32 = lane & 31, h = lane >> 5;
    uint32_t v[2][3][8];
#pragma unroll
    for (int b = 0; b < 2; ++b) {
      const __amdgpu_buffer_rsrc_t xr = mkbuf(x + (size_t)b * 16000 * DCH, 8192000);
#pragma unroll
      for (int ks = 0; ks < 3; ++ks) {
        const int row0 = 48 * sg - 24 + 16 * ks + 8 * h;
        const int vb = row0 >= 0 ? row0 * 1024 + cq * 128 + n32 * 4 : OOB;
#pragma unroll
        for (int e = 0; e < 8; ++e) v[b][ks][e] = __builtin_amdgcn_raw_buffer_load_b32(xr, vb + (e & 3) * 1024, (e >> 2) * 4096, NT);
      }
    }
    u32x4 F[2][3];
#pragma unroll
    for (int fb = 0; fb < 2; ++fb)
#pragma unroll
      for (int ks = 0; ks < 3; ++ks) F[fb][ks] = ((const u32x4*)&tabs.f1[fb][ks][0][0])[lane];
    const __amdgpu_buffer_rsrc_t ur = mkbuf(u1, U1BYTES);
    const int vo = ((n32 >> 1) * 16 + 2 * cq) * PL1 + 2 * sg * 128 + (n32 & 1) * 32 + h * 16;
#pragma unroll
    for (int b = 0; b < 2; ++b) {
      u32x4 a[2][3];
#pragma unroll
      for (int ks = 0; ks < 3; ++ks)
#pragma unroll
        for (int j = 0; j < 4; ++j) {
          a[0][ks][j] = __builtin_amdgcn_perm(v[b][ks][2 * j + 1], v[b][ks][2 * j], 0x05040100u);
          a[1][ks][j] = __builtin_amdgcn_perm(v[b][ks][2 * j + 1], v[b][ks][2 * j], 0x07060302u);
        }
#pragma unroll
      for (int fb = 0; fb < 2; ++fb) {
        f32x16 d[2];
#pragma unroll
        for (int e = 0; e < 2; ++e) {
          d[e] = zero16();
#pragma unroll
          for (int ks = 0; ks < 3; ++ks) d[e] = mfma32(a[e][ks], F[fb][ks], d[e]);
        }
#pragma unroll
        for (int q = 0; q < 4; ++q) {
          u32x4 o;
#pragma unroll
          for (int i = 0; i < 4; ++i) o[i] = pk2bf(d[0][4 * q + i], d[1][4 * q + i]);
          __builtin_amdgcn_raw_buffer_store_b128(o, ur, vo + b * 128 + (q & 1) * 64, (fb * 256 + (q >> 1)) * PL1, WT);
        }
      }
    }
  } else if (blk < NBW1 + NBX + NBW2) {
    wtrans<2>(w2, w2h, (blk - NBW1 - NBX) * 4 + wave, lane);
  } else if (blk < NBW1 + NBX + NBW2 + NBM) {
    const int P = (blk - NBW1 - NBX - NBW2) * 256 + tid;
    const int s = (P >> 6) & 15, eb = P >> 10;
    const int n = lane & 31, h = lane >> 5;
    const uint16_t* src = wf + (size_t)dmap(s, 4 * h) * DCH + 32 * eb + n;
    uint32_t v[8];
#pragma unroll
    for (int j = 0; j < 8; ++j) v[j] = src[2 * (8 * (j >> 2) + (j & 3)) * DCH];
    wff[P] = u32x4{v[0] | (v[1] << 16), v[2] | (v[3] << 16), v[4] | (v[5] << 16), v[6] | (v[7] << 16)};
  } else if (blk < NBW1 + NBX + NBW2 + 2 * NBM) {
    const int P = (blk - NBW1 - NBX - NBW2 - NBM) * 256 + tid;
    const int s2 = (P >> 6) & 15, vb = P >> 10;
    const int n = lane & 31, h = lane >> 5;
    const uint16_t* src = wo + (size_t)(16 * s2 + 4 * h) * DCH + 32 * vb + n;
    uint32_t v[8];
#pragma unroll
    for (int j = 0; j < 8; ++j) v[j] = src[(8 * (j >> 2) + (j & 3)) * DCH];
    wof[P] = u32x4{v[0] | (v[1] << 16), v[2] | (v[3] << 16), v[4] | (v[5] << 16), v[6] | (v[7] << 16)};
  } else {
    const int r = blk - (NBW1 + NBX + NBW2 + 2 * NBM);
    const int bu = r >> 4, e0 = (r & 15) * 16;
    const int el = tid & 15, dq = tid >> 4;
    float acc = 0.f;
    uint32_t wv[16], gv[16];
#pragma unroll
    for (int i = 0; i < 16; ++i) {
      wv[i] = wg[(dq * 16 + i) * 256 + e0 + el];
      gv[i] = g[bu * 256 + dq * 16 + i];
    }
#pragma unroll
    for (int i = 0; i < 16; ++i) acc += bf2f(gv[i]) * bf2f(wv[i]);
    part[dq][el] = acc;
    __syncthreads();
    if (tid < 16) {
      float s = 0.f;
#pragma unroll
      for (int i = 0; i < 16; ++i) s += part[i][tid];
      const float gs = bf2f(f2bf(s)) * 2.885390081777927f;
      const uint32_t hi = f2bf(gs), lo = f2bf(gs - bf2f(hi));
      ((uint32_t*)ge)[bu * 256 + e0 + tid] = hi | (lo << 16);
    }
  }
}

typedef __attribute__((address_space(3))) u32x4 lds_u32x4;
__device__ __forceinline__ u32x4 lds_ld(int a) { return *(const lds_u32x4*)(uintptr_t)(uint32_t)a; }
template <int SZ>
__device__ __forceinline__ void dma(__amdgpu_buffer_rsrc_t rsrc, char* lds, int voff, int soff) {
#if defined(__HIP_DEVICE_COMPILE__)
  typedef __attribute__((address_space(3))) void* ldsp;
  if constexpr (SZ == 16) __builtin_amdgcn_raw_ptr_buffer_load_lds(rsrc, (ldsp)lds, 16, voff, soff, 0, 0);
  else __builtin_amdgcn_raw_ptr_buffer_load_lds(rsrc, (ldsp)lds, 4, voff, soff, 0, 0);
#endif
}

template <int CONV>
__global__ __launch_bounds__(256, 1) void gemm_kernel(const char* __restrict__ U, const char* __restrict__ W,
                                                      char* __restrict__ Y) {
  constexpr int MB = CONV == 1 ? 11 : 8;
  constexpr int STG = MB * 2048;
  constexpr int NST = CONV == 1 ? 7 : 8;
  constexpr int PL = CONV == 1 ? PL1 : PL2;
  constexpr int NDMA = CONV == 1 ? 6 : 4;
  constexpr int OPS = NDMA + 2;
  extern __shared__ __attribute__((aligned(16))) char smem[];
  const int bid = blockIdx.x, xcd = bid & 7, slot = bid >> 3;
  const int bin = CONV == 1 ? xcd * 4 + (slot >> 3) : xcd * 2 + (slot >> 4);
  const int mt = CONV == 1 ? (slot >> 2) & 1 : (slot >> 2) & 3;
  const int tid = threadIdx.x, lane = tid & 63;
  const int wave = __builtin_amdgcn_readfirstlane(tid >> 6);
  const int cb = (slot & 3) * 4 + wave;
  const int row0 = CONV == 1 ? mt * 168 : mt * 126;
  const __amdgpu_buffer_rsrc_t ur = mkbuf(U + (size_t)bin * 16 * PL + row0 * 128, 16 * PL - row0 * 128 + (CONV == 1 ? 0 : 256));
  const __amdgpu_buffer_rsrc_t wr = mkbuf(W + (size_t)bin * WBIN + cb * 32768, 32768);
  int vo[2];
  if constexpr (CONV == 1) {
    vo[0] = wave * 1024 + (lane >> 3) * 128 + (((lane & 7) ^ ((4 * wave + (lane >> 4)) & 7)) * 16);
    vo[1] = 20480 + wave * 256 + (lane >> 5) * 128 + ((((lane >> 2) & 7) ^ wave) * 16) + (lane & 3) * 4;
  } else {
#pragma unroll
    for (int z = 0; z < 2; ++z) vo[z] = wave * 4096 + (lane >> 3) * 128 + (((lane & 7) ^ (2 * wave + z)) * 16);
  }
  u32x4 B[NST][2];
  auto issue = [&](auto is, auto ii) {
    constexpr int s = decltype(is)::value, i = decltype(ii)::value;
    char* buf = smem + (s % NST) * STG;
    if constexpr (i < 2) {
      B[s % NST][i] = __builtin_amdgcn_raw_buffer_load_b128(wr, lane * 16 + i * 1024, s * 2048, 0);
    } else if constexpr (CONV == 1) {
      if constexpr (i < 7) dma<16>(ur, buf + wave * 1024 + (i - 2) * 4096, vo[0], s * PL + (i - 2) * 4096);
      else dma<4>(ur, buf + 20480 + wave * 256, vo[1], s * PL);
    } else {
      dma<16>(ur, buf + wave * 4096 + (i - 2) * 1024, vo[(i - 2) >> 1], s * PL + (i - 2) * 1024);
    }
  };
  sfor<0, NST - 1>([&](auto is) { sfor<0, OPS>([&](auto ii) { issue(is, ii); SB(); }); });
  const int rho = lane & 15, kq = lane >> 4;
  const int lds0 = (int)(uint32_t)(uintptr_t)(__attribute__((address_space(3))) char*)smem;
  constexpr int WIN = CONV == 1 ? 2 : 4;
  constexpr int NWIN = (NST + WIN - 1) / WIN;
  int ab[NWIN][2];
#pragma unroll
  for (int ri = 0; ri < 2; ++ri) {
    const int pos = ((kq >> 1) * 4 + ri * 2 + (kq & 1)) ^ (rho >> 1);
    const int a = lds0 + (CONV == 1 ? rho * 128 : (rho >> 1) * 2048 + (rho & 1) * 128) + pos * 16;
#pragma unroll
    for (int wn = 0; wn < NWIN; ++wn) { ab[wn][ri] = a + wn * WIN * STG; asm volatile("" : "+v"(ab[wn][ri])); }
  }
  auto aread = [&](int s, int mb, int ri) -> u32x4 {
    const int bf = s % NST;
    return lds_ld(ab[bf / WIN][ri] + (bf % WIN) * STG + mb * (CONV == 1 ? 2048 : 256));
  };
  f32x4 yr[MB], yi[MB];
#pragma unroll
  for (int mb = 0; mb < MB; ++mb) { yr[mb] = f32x4{0.f, 0.f, 0.f, 0.f}; yi[mb] = f32x4{0.f, 0.f, 0.f, 0.f}; }
  constexpr int AR = 4, AL = 3;
  u32x4 A[AR][2];
  asm volatile("s_waitcnt vmcnt(%0)" ::"n"((NST - 2) * OPS) : "memory");
  __builtin_amdgcn_s_barrier();
  asm volatile("" ::: "memory");
#pragma unroll
  for (int z = 0; z < AL; ++z) { A[z][0] = aread(0, z, 0); A[z][1] = aread(0, z, 1); }
  SB();
  u32x4 nwi;
#pragma unroll
  for (int j = 0; j < 4; ++j) nwi[j] = B[0][1][j] ^ 0x80008000u;
  sfor<0, 16>([&](auto is) {
    constexpr int s = decltype(is)::value;
    u32x4 nwn;
    sfor<0, MB>([&](auto im) {
      constexpr int mb = decltype(im)::value;
      constexpr int g = s * MB + mb, gn = g + AL;
      constexpr int BAR = 1;
      if constexpr (mb == BAR && s + 1 < 16) {
        constexpr int last = s + NST - 2 < 15 ? s + NST - 2 : 15;
        asm volatile("s_waitcnt vmcnt(%0)" ::"n"((last - (s + 1)) * OPS) : "memory");
        __builtin_amdgcn_s_barrier();
        asm volatile("" ::: "memory");
        SB();
      }
      yr[mb] = mfma16(B[s % NST][0], A[g % AR][0], yr[mb]);
      SB();
      yr[mb] = mfma16(nwi, A[g % AR][1], yr[mb]);
      if constexpr (gn < 16 * MB) A[gn % AR][0] = aread(gn / MB, gn % MB, 0);
      SB();
      yi[mb] = mfma16(B[s % NST][1], A[g % AR][0], yi[mb]);
      if constexpr (mb >= BAR && mb - BAR < OPS && s + NST - 1 < 16 && s > 0) issue(IC<s + NST - 1>{}, IC<mb - BAR>{});
      if constexpr (mb >= BAR && mb - BAR < OPS && s == 0) issue(IC<NST - 1>{}, IC<mb - BAR>{});
      SB();
      yi[mb] = mfma16(B[s % NST][0], A[g % AR][1], yi[mb]);
      if constexpr (gn < 16 * MB) A[gn % AR][1] = aread(gn / MB, gn % MB, 1);
      if constexpr (mb == MB - 2 && s + 1 < 16) {
#pragma unroll
        for (int j = 0; j < 4; ++j) nwn[j] = B[(s + 1) % NST][1][j] ^ 0x80008000u;
      }
      SB();
    });
    if constexpr (s + 1 < 16) nwi = nwn;
  });
  if constexpr (CONV == 1) {
    const __amdgpu_buffer_rsrc_t yb = mkbuf(Y + (size_t)(bin * 16 + cb) * YP1 + row0 * 64, 168 * 64);
#pragma unroll
    for (int mb = 0; mb < MB; ++mb) {
      u32x4 o;
#pragma unroll
      for (int i = 0; i < 4; ++i) o[i] = pk2bf(yr[mb][i], yi[mb][i]);
      __builtin_amdgcn_raw_buffer_store_b128(o, yb, rho * 64 + kq * 16 + (mb & 3) * 1024, (mb >> 2) * 4096, WT);
    }
  } else {
    const __amdgpu_buffer_rsrc_t yb = mkbuf(Y + (size_t)(bin * 16 + cb) * YP2 + row0 * 64, YP2 - row0 * 64);
    const float sg = (bin & 1) ? -1.f : 1.f;
    f32x4 nr, ni;
#pragma unroll
    for (int i = 0; i < 4; ++i) { nr[i] = __shfl_down(yr[0][i], 2, 16); ni[i] = __shfl_down(yi[0][i], 2, 16); }
    const int vy = (rho >> 1) * 1024 + (rho & 1) * 64 + kq * 16;
#pragma unroll
    for (int mb = 0; mb < MB; ++mb) {
      const f32x4 xr = mb < MB - 1 ? yr[mb < MB - 1 ? mb + 1 : 0] : nr, xi = mb < MB - 1 ? yi[mb < MB - 1 ? mb + 1 : 0] : ni;
      u32x4 o;
#pragma unroll
      for (int i = 0; i < 4; ++i) o[i] = pk2bf(__builtin_fmaf(sg, xi[i], yr[mb][i]), __builtin_fmaf(-sg, xr[i], yi[mb][i]));
      __builtin_amdgcn_raw_buffer_store_b128(o, yb, (mb == MB - 1 && rho >= 14) ? OOB : vy + mb * 128, 0, WT);
    }
  }
}

constexpr int GP = 144;
__global__ __launch_bounds__(256) void mid_kernel(const char* __restrict__ Y, char* __restrict__ U) {
  __shared__ __attribute__((aligned(16))) char gt[64 * GP];
  const int blk = blockIdx.x, tid = threadIdx.x, lane = tid & 63;
  const int wave = __builtin_amdgcn_readfirstlane(tid >> 6);
  const int cb = blk & 15, p = (blk >> 4) * 4 + wave;
  const int n32 = lane & 31, h = lane >> 5;
  const __amdgpu_buffer_rsrc_t yb = mkbuf(Y, Y1BYTES);
  const int vy = (4 * h * 16 + cb) * YP1 + p * 256 + n32 * 4;
  const u32x4 t0 = ((const u32x4*)&tabs.g1[0][0])[tid], t1 = ((const u32x4*)&tabs.g1[0][0])[256 + tid];
  const u32x4 F = ((const u32x4*)&tabs.f2[0][0])[lane];
  u32x4 yv[3][4];
#pragma unroll
  for (int sg = 0; sg < 3; ++sg)
#pragma unroll
    for (int ks = 0; ks < 4; ++ks)
#pragma unroll
      for (int j = 0; j < 4; ++j) yv[sg][ks][j] = __builtin_amdgcn_raw_buffer_load_b32(yb, vy + sg * 128, (8 * ks + j) * 16 * YP1, 0);
  *(u32x4*)(gt + (tid >> 3) * GP + (tid & 7) * 16) = t0;
  *(u32x4*)(gt + (32 + (tid >> 3)) * GP + (tid & 7) * 16) = t1;
  __syncthreads();
  const int tau = 16 * ((n32 >> 2) & 1) + 8 * ((n32 >> 3) & 1) + 2 * (n32 & 3) + (n32 >> 4);
  const char* g0 = gt + tau * GP + h * 16;
  const char* gl = gt + (tau < 16 ? tau + 48 : 0) * GP + h * 16;
  const char* gh = gt + (tau >= 16 ? tau - 16 : 0) * GP + h * 16;
  const __amdgpu_buffer_rsrc_t ub = mkbuf(U, U2BYTES);
  const int vu = ((n32 >> 1) * 16 + cb) * PL2 + 6 * p * 128 + (n32 & 1) * 32 + h * 16;
#pragma unroll
  for (int z = 0; z < 3; ++z) {
    f32x16 d = zero16();
#pragma unroll
    for (int ks = 0; ks < 4; ++ks) {
      if (z == 0) d = mfma32(*(const u32x4*)(g0 + 16 * GP + ks * 32), yv[0][ks], d);
      if (z == 1) d = mfma32(*(const u32x4*)(gl + ks * 32), yv[0][ks], d);
      if (z == 2) d = mfma32(*(const u32x4*)(g0 + 32 * GP + ks * 32), yv[1][ks], d);
    }
#pragma unroll
    for (int ks = 0; ks < 4; ++ks) {
      if (z == 1) d = mfma32(*(const u32x4*)(g0 + ks * 32), yv[1][ks], d);
      if (z == 2) d = mfma32(*(const u32x4*)(gh + ks * 32), yv[2][ks], d);
    }
    const int t0r = 32 * (3 * p + z) - 16 + 16 * h;
    const bool ok = t0r >= 0 && t0r < 8000;
#pragma unroll
    for (int r = 0; r < 2; ++r) {
      u32x4 a;
#pragma unroll
      for (int j = 0; j < 4; ++j) {
        const uint32_t gv = gelu2(d[8 * r + 2 * j], d[8 * r + 2 * j + 1]);
        a[j] = ok ? gv : 0u;
      }
      const f32x16 u = mfma32(a, F, zero16());
#pragma unroll
      for (int b = 0; b < 2; ++b) {
        u32x4 o;
#pragma unroll
        for (int j = 0; j < 4; ++j) o[j] = pk2bf(u[8 * b + 2 * j], u[8 * b + 2 * j + 1]);
        __builtin_amdgcn_raw_buffer_store_b128(o, ub, vu + (2 * z + b) * 128 + r * 64, 0, WT);
      }
    }
  }
}

constexpr int J_F = 0;
constexpr int NPRE = 2;
constexpr int NTB = NPRE + 1;
constexpr int J_TB = 16384;
constexpr int OSP = 528;
constexpr int J_OST = J_TB + NTB * 16384;
constexpr int JOINT_LDS = J_OST + 2 * 32 * OSP;

__device__ __forceinline__ float tanh_pre(float s) {
  return __builtin_fmaf(-2.0f, __builtin_amdgcn_rcpf(__builtin_amdgcn_exp2f(s) + 1.0f), 1.0f);
}

__global__ __launch_bounds__(512, 2) void joint_kernel(const char* __restrict__ Y, const u32x4* __restrict__ wff,
                                                       const u32x4* __restrict__ wof, const uint32_t* __restrict__ geb,
                                                       uint16_t* __restrict__ out) {
  extern __shared__ __attribute__((aligned(16))) char smem[];
  constexpr int T2 = 4000;
  const int grp = blockIdx.x, t0 = grp * 16;
  const int tid = threadIdx.x;
  const int lane = tid & 63;
  const int wave = __builtin_amdgcn_readfirstlane(tid >> 6);
  const int n = lane & 31, h = lane >> 5;
  const __amdgpu_buffer_rsrc_t yb = mkbuf(Y, Y2BYTES);
  const int vy = (4 * h * 16 + 2 * wave) * YP2 + grp * 128 + n * 4;
  u32x4 yv[2][2];
#pragma unroll
  for (int c = 0; c < 2; ++c)
#pragma unroll
    for (int ks = 0; ks < 2; ++ks)
#pragma unroll
      for (int j = 0; j < 4; ++j) yv[c][ks][j] = __builtin_amdgcn_raw_buffer_load_b32(yb, vy, ((8 * ks + j) * 16 + c) * YP2, 0);
  u32x4 gi[2];
#pragma unroll
  for (int ks = 0; ks < 2; ++ks) gi[ks] = *(const u32x4*)&tabs.g2[n & 15][16 * ks + 8 * h];
  u32x4 ga[2];
#pragma unroll
  for (int b = 0; b < 2; ++b)
#pragma unroll
    for (int i = 0; i < 4; ++i) ga[b][i] = geb[(b * 8 + 4 * h + i) * 256 + 32 * wave + n];
  const u32x4* wfp = wff + wave * 16 * 64 + lane;
  const u32x4* wop = wof + wave * 16 * 64 + lane;
  u32x4 wa[16], wo[16];
#pragma unroll
  for (int s = 0; s < 8; ++s) wa[s] = wfp[s * 64];
  SB();
  {
    f32x16 d = zero16();
#pragma unroll
    for (int c = 0; c < 2; ++c)
#pragma unroll
      for (int ks = 0; ks < 2; ++ks) d = mfma32(yv[c][ks], (n >> 4) == c ? gi[ks] : u32x4{0u, 0u, 0u, 0u}, d);
#pragma unroll
    for (int b = 0; b < 2; ++b) {
      u32x4 o;
#pragma unroll
      for (int j = 0; j < 4; ++j) o[j] = gelu2(d[8 * b + 2 * j], d[8 * b + 2 * j + 1]);
      *(u32x4*)(smem + J_F + (2 * wave + (n >> 4)) * 1024 + (h * 32 + b * 16 + (n & 15)) * 16) = o;
    }
  }
  __syncthreads();
  f32x16 fe = zero16();
#pragma unroll
  for (int s = 0; s < 16; ++s) {
    const u32x4 fb = *(const u32x4*)(smem + J_F + s * 1024 + lane * 16);
    fe = mfma32(fb, wa[s], fe);
    if (s < 8) wa[s + 8] = wfp[(s + 8) * 64];
    else if ((s & 1) == 0) wo[(s - 8) >> 1] = wop[((s - 8) >> 1) * 64];
    SB();
  }
  u32x4 gsel;
#pragma unroll
  for (int i = 0; i < 4; ++i) gsel[i] = (4 * h + i == (n & 7)) ? 0x3F803F80u : 0u;
  f32x16 gacc = mfma32(ga[0], gsel, zero16());
  const float c2 = 2.885390081777927f;
  u32x4 xa[4];
#pragma unroll
  for (int q = 0; q < 4; ++q) {
#pragma unroll
    for (int p = 0; p < 2; ++p) {
      const float v0 = fe[4 * q + 2 * p] * c2, v1 = fe[4 * q + 2 * p + 1] * c2;
      const uint32_t hi = pk2bf(v0, v1);
      xa[q][p] = hi;
      xa[q][2 + p] = pk2bf(v0 - __uint_as_float(hi << 16), v1 - __uint_as_float(hi & 0xffff0000u));
    }
  }
  u32x4 sel[2];
  {
    const int i = n >> 3;
    const uint32_t one = (i & 1) ? 0x3F800000u : 0x3F80u;
    const uint32_t d0 = i < 2 ? one : 0u, d1 = i < 2 ? 0u : one;
#pragma unroll
    for (int hh = 0; hh < 2; ++hh) sel[hh] = h == hh ? u32x4{d0, d1, d0, d1} : u32x4{0u, 0u, 0u, 0u};
  }
  uint16_t* orow = out + ((size_t)t0 * 8 + 4 * wave + h) * DCH + n * 8;
  auto ooff = [&](int u, int i) -> size_t { return ((size_t)(u >> 2) * (T2 * 8) + (u & 3) * 32 + 2 * i) * DCH; };
  auto add_ge = [&](int g) -> f32x16 {
    if (g == 4) gacc = mfma32(ga[1], gsel, zero16());
    return mfma32(xa[g >> 1], sel[g & 1], gacc);
  };
  char* const tbw = smem + J_TB + (2 * wave * 64 + lane) * 16;
  const char* const tbr = smem + J_TB + lane * 16;

#pragma unroll
  for (int u = 0; u < NPRE; ++u) {
    const f32x16 s = add_ge(u);
    u32x4 p0, p1;
#pragma unroll
    for (int p = 0; p < 4; ++p) {
      p0[p] = pk2bf(tanh_pre(s[2 * p]), tanh_pre(s[2 * p + 1]));
      if (u < 2 && p < 3) wo[4 + u * 6 + p] = wop[(4 + u * 6 + p) * 64];
      SB();
      p1[p] = pk2bf(tanh_pre(s[8 + 2 * p]), tanh_pre(s[8 + 2 * p + 1]));
      if (u < 2 && p < 3) wo[7 + u * 6 + p] = wop[(7 + u * 6 + p) * 64];
      SB();
    }
    *(u32x4*)(tbw + u * 16384) = p0;
    *(u32x4*)(tbw + u * 16384 + 1024) = p1;
  }
  f32x16 sn = add_ge(NPRE);
  __syncthreads();
  constexpr int PRE = 6;
  u32x4 bt[16];
#pragma unroll
  for (int s2 = 0; s2 < PRE; ++s2) bt[s2] = *(const u32x4*)(tbr + s2 * 1024);
#pragma unroll
  for (int u = 0; u < 8; ++u) {
    const char* tb = tbr + (u % NTB) * 16384;
    const char* tbn = tbr + ((u + 1) % NTB) * 16384;
    u32x4 orv[2];
    if (u > 0) {
      const char* otp = smem + J_OST + ((u + 1) & 1) * (32 * OSP);
#pragma unroll
      for (int i = 0; i < 2; ++i) orv[i] = *(const u32x4*)(otp + (4 * wave + h + 2 * i) * OSP + n * 16);
    }
    f32x16 acc = zero16();
    float th[16];
    u32x4 p0, p1;
    u32x4 btn[PRE];
    SB();
#pragma unroll
    for (int s2 = 0; s2 < 16; ++s2) {
      acc = mfma32(wo[s2], bt[s2], acc);
      if (s2 + PRE < 16) bt[s2 + PRE] = *(const u32x4*)(tb + (s2 + PRE) * 1024);
      else if (u < 7) btn[s2 + PRE - 16] = *(const u32x4*)(tbn + (s2 + PRE - 16) * 1024);
      if ((s2 == 3 || s2 == 5) && u > 0) {
        const int i = (s2 - 3) >> 1;
        __builtin_nontemporal_store(orv[i], (u32x4*)(orow + ooff(u - 1, i)));
      }
      if (u + NPRE < 8) {
        constexpr int NPAIR = 8;
        const int g = s2 >> 1;
        if ((s2 & 1) == 0) {
          if (g < NPAIR) { th[2 * g] = __builtin_amdgcn_exp2f(sn[2 * g]); th[2 * g + 1] = __builtin_amdgcn_exp2f(sn[2 * g + 1]); }
          if (g >= 1) { th[2 * g - 2] = __builtin_fmaf(-2.0f, th[2 * g - 2], 1.0f); th[2 * g - 1] = __builtin_fmaf(-2.0f, th[2 * g - 1], 1.0f); }
        } else {
          const f32x2 a01 = f32x2{th[2 * g], th[2 * g + 1]} + f32x2{1.0f, 1.0f};
          const float a0 = a01[0], a1 = a01[1];
          if (g >= 1) {
            const uint32_t pk = pk2bf(th[2 * g - 2], th[2 * g - 1]);
            if (g - 1 < 4) p0[g - 1] = pk; else p1[g - 5] = pk;
          }
          th[2 * g] = __builtin_amdgcn_rcpf(a0); th[2 * g + 1] = __builtin_amdgcn_rcpf(a1);
        }
        if (s2 == 10) *(u32x4*)(tbw + ((u + NPRE) % NTB) * 16384) = p0;
      }
      SB();
    }
    if (u + 1 + NPRE < 8) sn = add_ge(u + 1 + NPRE);
    if (u + NPRE < 8) p1[3] = pk2bf(__builtin_fmaf(-2.0f, th[14], 1.0f), __builtin_fmaf(-2.0f, th[15], 1.0f));
    if (u + NPRE < 8) *(u32x4*)(tbw + ((u + NPRE) % NTB) * 16384 + 1024) = p1;
    char* ot = smem + J_OST + (u & 1) * (32 * OSP) + n * OSP + wave * 64;
#pragma unroll
    for (int g = 0; g < 4; ++g) {
      u32x2 pk = {pk2bf(acc[4 * g], acc[4 * g + 1]), pk2bf(acc[4 * g + 2], acc[4 * g + 3])};
      *(u32x2*)(ot + (8 * g + 4 * h) * 2) = pk;
    }
#pragma unroll
    for (int s2 = 0; s2 < PRE; ++s2) bt[s2] = btn[s2];
    __syncthreads();
  }
  {
    const char* otp = smem + J_OST + 1 * (32 * OSP);
#pragma unroll
    for (int i = 0; i < 2; ++i) {
      u32x4 v = *(const u32x4*)(otp + (4 * wave + h + 2 * i) * OSP + n * 16);
      __builtin_nontemporal_store(v, (u32x4*)(orow + ooff(7, i)));
    }
  }
}

}

extern "C" void kernel_launch(void* const* d_in, const int* in_sizes, int n_in, void* d_out, int out_size, void* d_ws,
                              size_t ws_size, hipStream_t stream) {
  const uint16_t* x = (const uint16_t*)d_in[0];
  const uint16_t* w1 = (const uint16_t*)d_in[1];
  const uint16_t* w2 = (const uint16_t*)d_in[2];
  const uint16_t* g = (const uint16_t*)d_in[3];
  const uint16_t* wf = (const uint16_t*)d_in[4];
  const uint16_t* wg = (const uint16_t*)d_in[5];
  const uint16_t* wo = (const uint16_t*)d_in[6];
  uint16_t* out = (uint16_t*)d_out;
  char* ws = (char*)d_ws;
  char* u1 = ws;
  char* w1h = ws + (24 << 20);
  char* w2h = ws + (40 << 20);
  char* y1 = ws + (48 << 20);
  char* u2 = ws + (60 << 20);
  char* y2 = ws + (77 << 20);
  u32x4* wff = (u32x4*)(ws + (86 << 20));
  u32x4* wof = (u32x4*)(ws + (87 << 20));
  float* ge = (float*)(ws + (88 << 20));
  constexpr int G1LDS = 7 * 11 * 2048, G2LDS = 8 * 8 * 2048;

  (void)hipFuncSetAttribute((const void*)gemm_kernel<1>, hipFuncAttributeMaxDynamicSharedMemorySize, G1LDS);
  (void)hipFuncSetAttribute((const void*)gemm_kernel<2>, hipFuncAttributeMaxDynamicSharedMemorySize, G2LDS);
  (void)hipFuncSetAttribute((const void*)joint_kernel, hipFuncAttributeMaxDynamicSharedMemorySize, JOINT_LDS);

  pre_kernel<<<NBW1 + NBX + NBW2 + 2 * NBM + 256, 256, 0, stream>>>(x, w1, w2, g, wg, wf, wo, u1, w1h, w2h, wff, wof, ge);
  gemm_kernel<1><<<256, 256, G1LDS, stream>>>(u1, w1h, y1);
  mid_kernel<<<336, 256, 0, stream>>>(y1, u2);
  gemm_kernel<2><<<256, 256, G2LDS, stream>>>(u2, w2h, y2);
  joint_kernel<<<250, 512, JOINT_LDS, stream>>>(y2, wff, wof, (const uint32_t*)ge, out);
}
```

```cpp
#include <hip/hip_runtime.h>
#include <stdint.h>

typedef __attribute__((ext_vector_type(8))) short bf16x8;
typedef __attribute__((ext_vector_type(16))) float f32x16;
typedef __attribute__((ext_vector_type(4))) float f32x4;
typedef __attribute__((ext_vector_type(2))) float f32x2;
typedef __attribute__((ext_vector_type(4))) unsigned int u32x4;
typedef __attribute__((ext_vector_type(2))) unsigned int u32x2;

#define GPRO 2
namespace {

constexpr int DCH = 256;

__device__ __forceinline__ float bf2f(uint32_t v) { return __uint_as_float(v << 16); }
__device__ __forceinline__ uint32_t f2bf(float f) {
  uint32_t u = __float_as_uint(f);
  u += 0x7FFF + ((u >> 16) & 1);
  return u >> 16;
}
__device__ __forceinline__ uint32_t pk2bf(float a, float b) {
  typedef __attribute__((ext_vector_type(2))) __bf16 b2;
  f32x2 v = {a, b};
  b2 r = __builtin_convertvector(v, b2);
  return __builtin_bit_cast(uint32_t, r);
}
__device__ __forceinline__ uint32_t gelu2(float a0, float a1) {
  const f32x2 x = {a0, a1};
  const f32x2 c0 = {-2.302208198144325f, -2.302208198144325f}, c1 = {-0.10294323970002349f, -0.10294323970002349f};
  const f32x2 z = x * __builtin_elementwise_fma(x * x, c1, c0);
  const f32x2 e = {__builtin_amdgcn_exp2f(z[0]), __builtin_amdgcn_exp2f(z[1])};
  const f32x2 d = e + f32x2{1.0f, 1.0f};
  const f32x2 y = x * f32x2{__builtin_amdgcn_rcpf(d[0]), __builtin_amdgcn_rcpf(d[1])};
  return pk2bf(y[0], y[1]);
}
__device__ __forceinline__ f32x16 mfma32(const u32x4& a, const u32x4& b, const f32x16& c) {
  return __builtin_amdgcn_mfma_f32_32x32x16_bf16(__builtin_bit_cast(bf16x8, a), __builtin_bit_cast(bf16x8, b), c, 0, 0, 0);
}
__device__ __forceinline__ f32x4 mfma16(const u32x4& a, const u32x4& b, const f32x4& c) {
  return __builtin_amdgcn_mfma_f32_16x16x32_bf16(__builtin_bit_cast(bf16x8, a), __builtin_bit_cast(bf16x8, b), c, 0, 0, 0);
}
__device__ __forceinline__ f32x16 zero16() {
  f32x16 z;
#pragma unroll
  for (int i = 0; i < 16; ++i) z[i] = 0.f;
  return z;
}
__device__ __forceinline__ __amdgpu_buffer_rsrc_t mkbuf(const void* p, int nbytes) {
  return __builtin_amdgcn_make_buffer_rsrc((void*)p, (short)0, nbytes, 0x00020000);
}
template <int V> struct IC { static constexpr int value = V; };
template <int I, int N, class F>
__device__ __forceinline__ void sfor(F&& f) {
  if constexpr (I < N) {
    f(IC<I>{});
    sfor<I + 1, N>(f);
  }
}
#define SB() __builtin_amdgcn_sched_barrier(0)
#define PHASE() do { SB(); asm volatile("s_waitcnt vmcnt(0)\n\ts_barrier" ::: "memory"); SB(); } while (0)

constexpr double PI_ = 3.14159265358979323846;
constexpr double c128(int m) {
  m = ((m % 128) + 128) % 128;
  if (m > 64) m = 128 - m;
  double sg = 1.0;
  if (m > 32) { m = 64 - m; sg = -1.0; }
  if (m == 32) return 0.0;
  const double a = PI_ * m / 64.0;
  double term = 1.0, sum = 1.0;
  for (int i = 1; i < 16; ++i) { term *= -a * a / ((2 * i - 1) * (2 * i)); sum += term; }
  return sg * sum;
}
constexpr double s128(int m) { return c128(m - 32); }
constexpr uint16_t cbf(double v) {
  uint32_t u = __builtin_bit_cast(uint32_t, (float)v);
  u += 0x7FFF + ((u >> 16) & 1);
  return (uint16_t)(u >> 16);
}
constexpr double cbf2d(uint16_t v) { return (double)__builtin_bit_cast(float, (uint32_t)v << 16); }
struct Tabs {
  uint16_t f1[2][3][64][8];
  uint16_t e1[2][2][64][8];
  uint16_t e2[2][64][8];
  uint16_t g1[5][4][64][8];
  uint16_t f2[64][8];
  uint16_t g2[16][32];
};
constexpr Tabs make_tabs() {
  Tabs t{};
  for (int l = 0; l < 64; ++l)
    for (int e = 0; e < 8; ++e) {
      const int h = l >> 5, f = l & 31, ri = f & 1;
      for (int fb = 0; fb < 2; ++fb) {
        const int k = 16 * fb + (f >> 1);
        for (int ks = 0; ks < 3; ++ks) {
          const int a = (2 * k + 1) * (16 * ks + 8 * h + e);
          t.f1[fb][ks][l][e] = cbf(ri ? -s128(a) : c128(a));
        }
        const int a = (2 * k + 1) * (8 * h + e);
        const double v = ri ? s128(a) : c128(a);
        t.e1[fb][0][l][e] = cbf(v);
        t.e1[fb][1][l][e] = cbf(v - cbf2d(cbf(v)));
      }
      const int a = 2 * (2 * (f >> 1) + 1) * (8 * h + e);
      const double v = ri ? s128(a) : c128(a);
      t.e2[0][l][e] = cbf(v);
      t.e2[1][l][e] = cbf(v - cbf2d(cbf(v)));
      t.f2[l][e] = cbf(ri ? -v : v);
    }
  for (int q = 0; q < 5; ++q)
    for (int l = 0; l < 64; ++l) {
      const int tau = 16 * ((l >> 2) & 1) + 8 * ((l >> 3) & 1) + 2 * (l & 3) + ((l >> 4) & 1);
      const int m = tau + (q == 0 ? 0 : q == 1 ? 32 : q == 2 ? -16 : q == 3 ? 16 : -32);
      for (int f = 0; f < 32; ++f) {
        const int ff = 16 * (f >> 3) + 8 * (l >> 5) + (f & 7), a = (2 * (ff >> 1) + 1) * m;
        t.g1[q][f >> 3][l][f & 7] = m >= -15 && m < 48 ? cbf(((ff & 1) ? -s128(a) : c128(a)) / 32.0) : (uint16_t)0;
      }
    }
  for (int m = 0; m < 16; ++m)
    for (int f = 0; f < 32; ++f) {
      const int a = 2 * (2 * (f >> 1) + 1) * m;
      t.g2[m][f] = cbf(((f & 1) ? -s128(a) : c128(a)) / 16.0);
    }
  return t;
}
__device__ __attribute__((aligned(64))) const Tabs tabs = make_tabs();

constexpr int OOB = 0x7FFFFF00;
constexpr int WT = 16;
constexpr int NT = 2;
constexpr int R1 = 336;
constexpr int R2 = 504;
constexpr int PL1 = R1 * 128;
constexpr int PL2 = R2 * 128;
constexpr int U1BYTES = 32 * 16 * PL1, U2BYTES = 16 * 16 * PL2;
constexpr int WBIN = 16 * 16 * 2 * 1024;
constexpr int YP1 = R1 * 64;
constexpr int YP2 = 500 * 64;
constexpr int Y1BYTES = 32 * 16 * YP1, Y2BYTES = 16 * 16 * YP2;
__device__ __forceinline__ int dmap(int cb, int n) { return 64 * (cb >> 2) + 32 * (cb & 1) + 2 * n + ((cb >> 1) & 1); }

__device__ __forceinline__ void swap16(u32x4& a, u32x4& b) {
#pragma unroll
  for (int i = 0; i < 4; ++i) {
    const auto r = __builtin_amdgcn_permlane16_swap(a[i], b[i], false, false);
    a[i] = r[0];
    b[i] = r[1];
  }
}

template <int CONV>
__device__ __forceinline__ void wtrans(const uint16_t* __restrict__ w, char* __restrict__ wh, int wid, int lane) {
  const int dq = wid & 3, pc = wid >> 2;
  const int ks = pc >> 2, kq = pc & 3;
  const int n32 = lane & 31, h = lane >> 5;
  const int r = CONV == 1 ? ks >> 3 : kq >> 1;
  const int c0 = CONV == 1 ? (ks & 7) * 32 + kq * 8 : dmap(ks, 4 * (kq & 1));
  const __amdgpu_buffer_rsrc_t wr = mkbuf(w, 31 * 131072);
  const int vb = (16 * h + r + 1) * 131072 + c0 * 512 + dq * 128 + n32 * 4;
  const int vb0 = (h + r == 0) ? OOB : vb - 262144;
  constexpr int NFB = CONV == 1 ? 2 : 1;
  const u32x4* et = CONV == 1 ? (const u32x4*)&tabs.e1[0][0][0][0] : (const u32x4*)&tabs.e2[0][0][0];
  u32x4 E[NFB][2];
#pragma unroll
  for (int fb = 0; fb < NFB; ++fb) { E[fb][0] = et[fb * 128 + lane]; E[fb][1] = et[fb * 128 + 64 + lane]; }
  uint32_t v[8][8];
#pragma unroll
  for (int el = 0; el < 8; ++el)
#pragma unroll
    for (int jj = 0; jj < 8; ++jj) {
      const int co = CONV == 1 ? el * 512 : (el & 3) * 1024;
      const int so = (CONV == 1 ? 0 : (el >> 2) * 8192) + (jj ? (jj - 1) * 262144 : 0);
      v[el][jj] = __builtin_amdgcn_raw_buffer_load_b32(wr, (jj ? vb : vb0) + co, so, NT);
    }
  PHASE();
  const __amdgpu_buffer_rsrc_t dr = mkbuf(wh, (CONV == 1 ? 32 : 16) * WBIN);
  const int vo = 2 * h * WBIN + (4 * dq + (n32 >> 4)) * 32768 + ks * 2048 + kq * 256 + (n32 & 15) * 16;
#pragma unroll
  for (int e = 0; e < 2; ++e) {
    u32x4 b[8];
#pragma unroll
    for (int el = 0; el < 8; ++el)
#pragma unroll
      for (int j = 0; j < 4; ++j) b[el][j] = __builtin_amdgcn_perm(v[el][2 * j + 1], v[el][2 * j], e ? 0x07060302u : 0x05040100u);
#pragma unroll
    for (int fb = 0; fb < NFB; ++fb) {
      u32x4 o[16];
#pragma unroll
      for (int ep = 0; ep < 4; ++ep) {
        f32x16 d[2];
#pragma unroll
        for (int z = 0; z < 2; ++z) d[z] = mfma32(E[fb][1], b[2 * ep + z], zero16());
#pragma unroll
        for (int z = 0; z < 2; ++z) d[z] = mfma32(E[fb][0], b[2 * ep + z], d[z]);
#pragma unroll
        for (int i = 0; i < 16; ++i) o[i][ep] = pk2bf(d[0][i], d[1][i]);
      }
#pragma unroll
      for (int i = 0; i < 16; ++i)
        __builtin_amdgcn_raw_buffer_store_b128(o[i], dr, vo + (i & 1) * 1024,
                                               (16 * fb + 4 * (i >> 2) + ((i >> 1) & 1)) * WBIN + e * 65536, WT);
      SB();
    }
  }
}

__device__ __forceinline__ void xload(const uint16_t* __restrict__ x, int u, int lane, uint32_t (&v)[3][8]) {
  const int cq = u & 7, b = (u >> 3) & 1, sg = u >> 4;
  const __amdgpu_buffer_rsrc_t xr = mkbuf(x + (size_t)b * 16000 * DCH, 8192000);
#pragma unroll
  for (int ks = 0; ks < 3; ++ks) {
    const int row0 = 48 * sg - 24 + 16 * ks + 8 * (lane >> 5);
    const int vb = row0 >= 0 ? row0 * 1024 + cq * 128 + (lane & 31) * 4 : OOB;
#pragma unroll
    for (int e = 0; e < 8; ++e) v[ks][e] = __builtin_amdgcn_raw_buffer_load_b32(xr, vb + (e & 3) * 1024, (e >> 2) * 4096, NT);
  }
}
__device__ __forceinline__ void xtrans(char* __restrict__ u1, int u, int lane, const uint32_t (&v)[3][8], const u32x4 (&F)[2][3]) {
  const __amdgpu_buffer_rsrc_t ur = mkbuf(u1 + (2 * (u & 7)) * PL1 + (u >> 3) * 128, U1BYTES - (2 * (u & 7)) * PL1 - (u >> 3) * 128);
  const int vo = ((lane & 15) >> 1) * 16 * PL1 + ((lane >> 4) & 1) * 64 + (lane & 1) * 32 + (lane >> 5) * 16;
  u32x4 a[2][3];
#pragma unroll
  for (int ks = 0; ks < 3; ++ks)
#pragma unroll
    for (int j = 0; j < 4; ++j) {
      a[0][ks][j] = __builtin_amdgcn_perm(v[ks][2 * j + 1], v[ks][2 * j], 0x05040100u);
      a[1][ks][j] = __builtin_amdgcn_perm(v[ks][2 * j + 1], v[ks][2 * j], 0x07060302u);
    }
#pragma unroll
  for (int fb = 0; fb < 2; ++fb) {
    f32x16 d[2] = {zero16(), zero16()};
#pragma unroll
    for (int ks = 0; ks < 3; ++ks)
#pragma unroll
      for (int e = 0; e < 2; ++e) d[e] = mfma32(a[e][ks], F[fb][ks], d[e]);
#pragma unroll
    for (int qp = 0; qp < 2; ++qp) {
      u32x4 o[2];
#pragma unroll
      for (int z = 0; z < 2; ++z)
#pragma unroll
        for (int i = 0; i < 4; ++i) o[z][i] = pk2bf(d[0][8 * qp + 4 * z + i], d[1][8 * qp + 4 * z + i]);
      swap16(o[0], o[1]);
#pragma unroll
      for (int z = 0; z < 2; ++z) __builtin_amdgcn_raw_buffer_store_b128(o[z], ur, vo, ((fb * 16 + z * 8) * 16 + qp) * PL1, WT);
    }
  }
}

__global__ __launch_bounds__(512, 2) void pre_kernel(const uint16_t* __restrict__ x, const uint16_t* __restrict__ w1,
                                                     const uint16_t* __restrict__ w2, const uint16_t* __restrict__ g,
                                                     const uint16_t* __restrict__ wg, const uint16_t* __restrict__ wf,
                                                     const uint16_t* __restrict__ wo, char* __restrict__ u1,
                                                     char* __restrict__ w1h, char* __restrict__ w2h, u32x4* __restrict__ wff,
                                                     u32x4* __restrict__ wof, uint32_t* __restrict__ ge) {
  const int blk = blockIdx.x;
  const int tid = threadIdx.x, lane = tid & 63;
  const int wave = __builtin_amdgcn_readfirstlane(tid >> 6);
  const int n = lane & 31, h = lane >> 5;
  if (wave == 0) {
    wtrans<1>(w1, w1h, blk, lane);
  } else if (wave == 1) {
    wtrans<2>(w2, w2h, blk, lane);
  } else {
    const int bq = (blk & 7) * 32 + (blk >> 3);
    const int u0 = ((21 * bq) >> 1) + wave - 2, ue = (21 * (bq + 1)) >> 1;
    u32x4 F[2][3];
#pragma unroll
    for (int fb = 0; fb < 2; ++fb)
#pragma unroll
      for (int ks = 0; ks < 3; ++ks) F[fb][ks] = ((const u32x4*)&tabs.f1[fb][ks][0][0])[lane];
    uint32_t v0[3][8];
    xload(x, u0, lane, v0);
    if (u0 + 6 < ue) {
      uint32_t v1[3][8];
      xload(x, u0 + 6, lane, v1);
      PHASE();
      xtrans(u1, u0, lane, v0, F);
      SB();
      xtrans(u1, u0 + 6, lane, v1, F);
    } else if (wave == 7) {
      const int s = blk & 15, eb = (blk >> 4) & 7, second = blk >> 7;
      const uint16_t* src = (second ? wo + (16 * s + 4 * h) * DCH : wf + dmap(s, 4 * h) * DCH) + 32 * eb + n;
      const int st = second ? DCH : 2 * DCH;
      uint32_t v[8];
#pragma unroll
      for (int j = 0; j < 8; ++j) v[j] = src[(8 * (j >> 2) + (j & 3)) * st];
      PHASE();
      xtrans(u1, u0, lane, v0, F);
      (second ? wof : wff)[(blk & 127) * 64 + lane] =
          u32x4{v[0] | (v[1] << 16), v[2] | (v[3] << 16), v[4] | (v[5] << 16), v[6] | (v[7] << 16)};
    } else if (blk < 8) {
      const int c0 = 32 * blk + 2 * (lane & 15), kq = lane >> 4;
      uint32_t v[8][8];
      u32x4 ga[8];
#pragma unroll
      for (int ks = 0; ks < 8; ++ks) {
        ga[ks] = *(const u32x4*)(g + (lane & 15) * DCH + ks * 32 + 8 * kq);
#pragma unroll
        for (int e = 0; e < 8; ++e) v[ks][e] = *(const uint32_t*)(wg + (ks * 32 + 8 * kq + e) * DCH + c0);
      }
      PHASE();
      xtrans(u1, u0, lane, v0, F);
      SB();
      f32x4 d[2] = {{0.f, 0.f, 0.f, 0.f}, {0.f, 0.f, 0.f, 0.f}};
#pragma unroll
      for (int ks = 0; ks < 8; ++ks)
#pragma unroll
        for (int e = 0; e < 2; ++e) {
          u32x4 bb;
#pragma unroll
          for (int j = 0; j < 4; ++j) bb[j] = __builtin_amdgcn_perm(v[ks][2 * j + 1], v[ks][2 * j], e ? 0x07060302u : 0x05040100u);
          d[e] = mfma16(ga[ks], bb, d[e]);
        }
#pragma unroll
      for (int i = 0; i < 4; ++i) {
        u32x2 o;
#pragma unroll
        for (int e = 0; e < 2; ++e) {
          const float gs = bf2f(f2bf(d[e][i])) * 2.885390081777927f;
          const uint32_t hi = f2bf(gs), lo = f2bf(gs - bf2f(hi));
          o[e] = hi | (lo << 16);
        }
        *(u32x2*)(ge + (4 * kq + i) * DCH + c0) = o;
      }
    } else {
      PHASE();
      xtrans(u1, u0, lane, v0, F);
    }
  }
}

typedef __attribute__((address_space(3))) u32x4 lds_u32x4;
__device__ __forceinline__ u32x4 lds_ld(int a) { return *(const lds_u32x4*)(uintptr_t)(uint32_t)a; }
template <int SZ>
__device__ __forceinline__ void dma(__amdgpu_buffer_rsrc_t rsrc, char* lds, int voff, int soff) {
#if defined(__HIP_DEVICE_COMPILE__)
  typedef __attribute__((address_space(3))) void* ldsp;
  if constexpr (SZ == 16) __builtin_amdgcn_raw_ptr_buffer_load_lds(rsrc, (ldsp)lds, 16, voff, soff, 0, 0);
  else __builtin_amdgcn_raw_ptr_buffer_load_lds(rsrc, (ldsp)lds, 4, voff, soff, 0, 0);
#endif
}

constexpr int issued(int g, int MB, int OPS, int NST, int BAR, int PRO) {
  int o = PRO * OPS;
  for (int t = 0; t < g; ++t)
    if (o < 16 * OPS && (o / OPS < NST || t >= (o / OPS - NST + 1) * MB + BAR)) ++o;
  return o;
}
template <int CONV>
__global__ __launch_bounds__(256, 1) void gemm_kernel(const char* __restrict__ U, const char* __restrict__ W,
                                                      char* __restrict__ Y) {
  constexpr int MB = CONV == 1 ? 11 : 8;
  constexpr int STG = MB * 2048;
  constexpr int NST = CONV == 1 ? 7 : 8;
  constexpr int PL = CONV == 1 ? PL1 : PL2;
  constexpr int NDMA = CONV == 1 ? 6 : 4;
  constexpr int OPS = NDMA + 2;
  extern __shared__ __attribute__((aligned(16))) char smem[];
  const int bid = blockIdx.x, xcd = bid & 7, slot = bid >> 3;
  const int bin = CONV == 1 ? xcd * 4 + (slot >> 3) : xcd * 2 + (slot >> 4);
  const int mt = CONV == 1 ? (slot >> 2) & 1 : (slot >> 2) & 3;
  const int tid = threadIdx.x, lane = tid & 63;
  const int wave = __builtin_amdgcn_readfirstlane(tid >> 6);
  const int cb = (slot & 3) * 4 + wave;
  const int row0 = CONV == 1 ? mt * 168 : mt * 126;
  const __amdgpu_buffer_rsrc_t ur = mkbuf(U + (size_t)bin * 16 * PL + row0 * 128, 16 * PL - row0 * 128 + (CONV == 1 ? 0 : 256));
  const __amdgpu_buffer_rsrc_t wr = mkbuf(W + (size_t)bin * WBIN + cb * 32768, 32768);
  int vo[2];
  if constexpr (CONV == 1) {
    vo[0] = wave * 1024 + (lane >> 3) * 128 + (((lane & 7) ^ ((4 * wave + (lane >> 4)) & 7)) * 16);
    vo[1] = 20480 + wave * 256 + (lane >> 5) * 128 + ((((lane >> 2) & 7) ^ wave) * 16) + (lane & 3) * 4;
  } else {
#pragma unroll
    for (int z = 0; z < 2; ++z) vo[z] = wave * 4096 + (lane >> 3) * 128 + (((lane & 7) ^ (2 * wave + z)) * 16);
  }
  u32x4 B[NST][2];
  auto issue = [&](auto is, auto ii) {
    constexpr int s = decltype(is)::value, i = decltype(ii)::value;
    char* buf = smem + (s % NST) * STG;
    if constexpr (i < 2) {
      B[s % NST][i] = __builtin_amdgcn_raw_buffer_load_b128(wr, lane * 16 + i * 1024, s * 2048, 0);
    } else if constexpr (CONV == 1) {
      if constexpr (i < 7) dma<16>(ur, buf + wave * 1024 + (i - 2) * 4096, vo[0], s * PL + (i - 2) * 4096);
      else dma<4>(ur, buf + 20480 + wave * 256, vo[1], s * PL);
    } else {
      dma<16>(ur, buf + wave * 4096 + (i - 2) * 1024, vo[(i - 2) >> 1], s * PL + (i - 2) * 1024);
    }
  };
  constexpr int AR = 4, AL = 3;
  constexpr int BAR = MB - AL;
  constexpr int PRO = GPRO;
  sfor<0, PRO>([&](auto is) { sfor<0, OPS>([&](auto ii) { issue(is, ii); SB(); }); });
  const int rho = lane & 15, kq = lane >> 4;
  const int lds0 = (int)(uint32_t)(uintptr_t)(__attribute__((address_space(3))) char*)smem;
  constexpr int WIN = CONV == 1 ? 2 : 4;
  constexpr int NWIN = (NST + WIN - 1) / WIN;
  int ab[NWIN][2];
#pragma unroll
  for (int ri = 0; ri < 2; ++ri) {
    const int pos = ((kq >> 1) * 4 + ri * 2 + (kq & 1)) ^ (rho >> 1);
    const int a = lds0 + (CONV == 1 ? rho * 128 : (rho >> 1) * 2048 + (rho & 1) * 128) + pos * 16;
#pragma unroll
    for (int wn = 0; wn < NWIN; ++wn) { ab[wn][ri] = a + wn * WIN * STG; asm volatile("" : "+v"(ab[wn][ri])); }
  }
  auto aread = [&](int s, int mb, int ri) -> u32x4 {
    const int bf = s % NST;
    return lds_ld(ab[bf / WIN][ri] + (bf % WIN) * STG + mb * (CONV == 1 ? 2048 : 256));
  };
  f32x4 yr[MB], yi[MB];
#pragma unroll
  for (int mb = 0; mb < MB; ++mb) { yr[mb] = f32x4{0.f, 0.f, 0.f, 0.f}; yi[mb] = f32x4{0.f, 0.f, 0.f, 0.f}; }
  u32x4 A[AR][2];
  asm volatile("s_waitcnt vmcnt(%0)" ::"n"((PRO - 1) * OPS) : "memory");
  __builtin_amdgcn_s_barrier();
  asm volatile("" ::: "memory");
#pragma unroll
  for (int z = 0; z < AL; ++z) { A[z][0] = aread(0, z, 0); A[z][1] = aread(0, z, 1); }
  SB();
  u32x4 nwi;
#pragma unroll
  for (int j = 0; j < 4; ++j) nwi[j] = B[0][1][j] ^ 0x80008000u;
  sfor<0, 16>([&](auto is) {
    constexpr int s = decltype(is)::value;
    u32x4 nwn;
    sfor<0, MB>([&](auto im) {
      constexpr int mb = decltype(im)::value;
      constexpr int g = s * MB + mb, gn = g + AL;
      constexpr int o0 = issued(g, MB, OPS, NST, BAR, PRO), o1 = issued(g + 1, MB, OPS, NST, BAR, PRO);
      if constexpr (mb == BAR && s + 1 < 16) {
        static_assert(o0 >= (s + 2) * OPS, "next stage not requested yet");
        asm volatile("s_waitcnt vmcnt(%0)" ::"n"(o0 - (s + 2) * OPS) : "memory");
        __builtin_amdgcn_s_barrier();
        asm volatile("" ::: "memory");
        SB();
      }
      yr[mb] = mfma16(B[s % NST][0], A[g % AR][0], yr[mb]);
      SB();
      yr[mb] = mfma16(nwi, A[g % AR][1], yr[mb]);
      if constexpr (gn < 16 * MB) A[gn % AR][0] = aread(gn / MB, gn % MB, 0);
      SB();
      yi[mb] = mfma16(B[s % NST][1], A[g % AR][0], yi[mb]);
      if constexpr (o1 > o0) issue(IC<o0 / OPS>{}, IC<o0 % OPS>{});
      SB();
      yi[mb] = mfma16(B[s % NST][0], A[g % AR][1], yi[mb]);
      if constexpr (gn < 16 * MB) A[gn % AR][1] = aread(gn / MB, gn % MB, 1);
      if constexpr (mb == MB - 2 && s + 1 < 16) {
#pragma unroll
        for (int j = 0; j < 4; ++j) nwn[j] = B[(s + 1) % NST][1][j] ^ 0x80008000u;
      }
      SB();
    });
    if constexpr (s + 1 < 16) nwi = nwn;
  });
  if constexpr (CONV == 1) {
    const __amdgpu_buffer_rsrc_t yb = mkbuf(Y + (size_t)(bin * 16 + cb) * YP1 + row0 * 64, 168 * 64);
#pragma unroll
    for (int mb = 0; mb < MB; ++mb) {
      u32x4 o;
#pragma unroll
      for (int i = 0; i < 4; ++i) o[i] = pk2bf(yr[mb][i], yi[mb][i]);
      __builtin_amdgcn_raw_buffer_store_b128(o, yb, rho * 64 + kq * 16 + (mb & 3) * 1024, (mb >> 2) * 4096, WT);
    }
  } else {
    const __amdgpu_buffer_rsrc_t yb = mkbuf(Y + (size_t)(bin * 16 + cb) * YP2 + row0 * 64, YP2 - row0 * 64);
    const float sg = (bin & 1) ? -1.f : 1.f;
    f32x4 nr, ni;
#pragma unroll
    for (int i = 0; i < 4; ++i) { nr[i] = __shfl_down(yr[0][i], 2, 16); ni[i] = __shfl_down(yi[0][i], 2, 16); }
    const int vy = (rho >> 1) * 1024 + (rho & 1) * 64 + kq * 16;
#pragma unroll
    for (int mb = 0; mb < MB; ++mb) {
      const f32x4 xr = mb < MB - 1 ? yr[mb < MB - 1 ? mb + 1 : 0] : nr, xi = mb < MB - 1 ? yi[mb < MB - 1 ? mb + 1 : 0] : ni;
      u32x4 o;
#pragma unroll
      for (int i = 0; i < 4; ++i) o[i] = pk2bf(__builtin_fmaf(sg, xi[i], yr[mb][i]), __builtin_fmaf(-sg, xr[i], yi[mb][i]));
      __builtin_amdgcn_raw_buffer_store_b128(o, yb, (mb == MB - 1 && rho >= 14) ? OOB : vy + mb * 128, 0, WT);
    }
  }
}

template <int Z>
__device__ __forceinline__ void mid_unit(const char* __restrict__ Y, char* __restrict__ U, int cb, int p, bool live) {
  const int tid = threadIdx.x, lane = tid & 63;
  const int n32 = lane & 31, h = lane >> 5;
  const __amdgpu_buffer_rsrc_t yb = mkbuf(Y, live ? Y1BYTES : 0);
  const int vy = (4 * h * 16 + cb) * YP1 + p * 256 + (Z == 2 ? 128 : 0) + n32 * 4;
  const u32x4 F = ((const u32x4*)&tabs.f2[0][0])[lane];
  constexpr int NSG = Z == 0 ? 1 : 2;
  u32x4 yv[NSG][4], G[NSG][4];
#pragma unroll
  for (int sg = 0; sg < NSG; ++sg)
#pragma unroll
    for (int ks = 0; ks < 4; ++ks)
#pragma unroll
      for (int j = 0; j < 4; ++j) yv[sg][ks][j] = __builtin_amdgcn_raw_buffer_load_b32(yb, vy + sg * 128, (8 * ks + j) * 16 * YP1, 0);
  SB();
#pragma unroll
  for (int sg = 0; sg < NSG; ++sg)
#pragma unroll
    for (int ks = 0; ks < 4; ++ks) G[sg][ks] = ((const u32x4*)&tabs.g1[Z == 0 ? 0 : 2 * Z - 1 + sg][ks][0][0])[lane];
  SB();
  const __amdgpu_buffer_rsrc_t ub = mkbuf(U, live ? U2BYTES : 0);
  const int vu = (((lane & 15) >> 1) * 16 + cb) * PL2 + (6 * p + 2 * Z) * 128 + ((lane >> 4) & 1) * 64 + (lane & 1) * 32 + h * 16;
  f32x16 d = zero16();
#pragma unroll
  for (int sg = 0; sg < NSG; ++sg)
#pragma unroll
    for (int ks = 0; ks < 4; ++ks) d = mfma32(G[sg][ks], yv[sg][ks], d);
  const int t0r = 32 * (3 * p + Z) - 16 + 16 * h;
  const bool ok = t0r >= 0 && t0r < 8000;
  f32x16 u[2];
#pragma unroll
  for (int r = 0; r < 2; ++r) {
    u32x4 a;
#pragma unroll
    for (int j = 0; j < 4; ++j) {
      const uint32_t gv = gelu2(d[8 * r + 2 * j], d[8 * r + 2 * j + 1]);
      a[j] = ok ? gv : 0u;
    }
    u[r] = mfma32(a, F, zero16());
  }
#pragma unroll
  for (int b = 0; b < 2; ++b) {
    u32x4 o[2];
#pragma unroll
    for (int r = 0; r < 2; ++r)
#pragma unroll
      for (int j = 0; j < 4; ++j) o[r][j] = pk2bf(u[r][8 * b + 2 * j], u[r][8 * b + 2 * j + 1]);
    swap16(o[0], o[1]);
#pragma unroll
    for (int z = 0; z < 2; ++z) __builtin_amdgcn_raw_buffer_store_b128(o[z], ub, vu + b * 128, z * 8 * 16 * PL2, WT);
  }
}
__global__ __launch_bounds__(1024) void mid_kernel(const char* __restrict__ Y, char* __restrict__ U) {
  const int blk = blockIdx.x;
  const int wave = __builtin_amdgcn_readfirstlane(threadIdx.x >> 6);
  const int cb = blk & 15, s = (blk >> 4) * 16 + wave;
  const bool live = s < 251;
  const int p = s / 3, z = s - 3 * p;
  if (z == 0) mid_unit<0>(Y, U, cb, p, live);
  else if (z == 1) mid_unit<1>(Y, U, cb, p, live);
  else mid_unit<2>(Y, U, cb, p, live);
}

constexpr int J_F = 0;
constexpr int NPRE = 2;
constexpr int NTB = NPRE + 1;
constexpr int J_TB = 16384;
constexpr int OSP = 528;
constexpr int J_OST = J_TB + NTB * 16384;
constexpr int JOINT_LDS = J_OST + 2 * 32 * OSP;

__device__ __forceinline__ float tanh_pre(float s) {
  return __builtin_fmaf(-2.0f, __builtin_amdgcn_rcpf(__builtin_amdgcn_exp2f(s) + 1.0f), 1.0f);
}

__global__ __launch_bounds__(512, 2) void joint_kernel(const char* __restrict__ Y, const u32x4* __restrict__ wff,
                                                       const u32x4* __restrict__ wof, const uint32_t* __restrict__ geb,
                                                       uint16_t* __restrict__ out) {
  extern __shared__ __attribute__((aligned(16))) char smem[];
  constexpr int T2 = 4000;
  const int grp = blockIdx.x, t0 = grp * 16;
  const int tid = threadIdx.x;
  const int lane = tid & 63;
  const int wave = __builtin_amdgcn_readfirstlane(tid >> 6);
  const int n = lane & 31, h = lane >> 5;
  const __amdgpu_buffer_rsrc_t yb = mkbuf(Y, Y2BYTES);
  const int vy = (4 * h * 16 + 2 * wave) * YP2 + grp * 128 + n * 4;
  u32x4 yv[2][2];
#pragma unroll
  for (int c = 0; c < 2; ++c)
#pragma unroll
    for (int ks = 0; ks < 2; ++ks)
#pragma unroll
      for (int j = 0; j < 4; ++j) yv[c][ks][j] = __builtin_amdgcn_raw_buffer_load_b32(yb, vy, ((8 * ks + j) * 16 + c) * YP2, 0);
  u32x4 gi[2];
#pragma unroll
  for (int ks = 0; ks < 2; ++ks) gi[ks] = *(const u32x4*)&tabs.g2[n & 15][16 * ks + 8 * h];
  u32x4 ga[2];
#pragma unroll
  for (int b = 0; b < 2; ++b)
#pragma unroll
    for (int i = 0; i < 4; ++i) ga[b][i] = geb[(b * 8 + 4 * h + i) * 256 + 32 * wave + n];
  const u32x4* wfp = wff + wave * 16 * 64 + lane;
  const u32x4* wop = wof + wave * 16 * 64 + lane;
  u32x4 wa[16], wo[16];
#pragma unroll
  for (int s = 0; s < 8; ++s) wa[s] = wfp[s * 64];
  SB();
  {
    f32x16 d = zero16();
#pragma unroll
    for (int c = 0; c < 2; ++c)
#pragma unroll
      for (int ks = 0; ks < 2; ++ks) d = mfma32(yv[c][ks], (n >> 4) == c ? gi[ks] : u32x4{0u, 0u, 0u, 0u}, d);
#pragma unroll
    for (int b = 0; b < 2; ++b) {
      u32x4 o;
#pragma unroll
      for (int j = 0; j < 4; ++j) o[j] = gelu2(d[8 * b + 2 * j], d[8 * b + 2 * j + 1]);
      *(u32x4*)(smem + J_F + (2 * wave + (n >> 4)) * 1024 + (h * 32 + b * 16 + (n & 15)) * 16) = o;
    }
  }
  __syncthreads();
  f32x16 fe = zero16();
#pragma unroll
  for (int s = 0; s < 16; ++s) {
    const u32x4 fb = *(const u32x4*)(smem + J_F + s * 1024 + lane * 16);
    fe = mfma32(fb, wa[s], fe);
    if (s < 8) wa[s + 8] = wfp[(s + 8) * 64];
    else if ((s & 1) == 0) wo[(s - 8) >> 1] = wop[((s - 8) >> 1) * 64];
    SB();
  }
  u32x4 gsel;
#pragma unroll
  for (int i = 0; i < 4; ++i) gsel[i] = (4 * h + i == (n & 7)) ? 0x3F803F80u : 0u;
  f32x16 gacc = mfma32(ga[0], gsel, zero16());
  const float c2 = 2.885390081777927f;
  u32x4 xa[4];
#pragma unroll
  for (int q = 0; q < 4; ++q) {
#pragma unroll
    for (int p = 0; p < 2; ++p) {
      const float v0 = fe[4 * q + 2 * p] * c2, v1 = fe[4 * q + 2 * p + 1] * c2;
      const uint32_t hi = pk2bf(v0, v1);
      xa[q][p] = hi;
      xa[q][2 + p] = pk2bf(v0 - __uint_as_float(hi << 16), v1 - __uint_as_float(hi & 0xffff0000u));
    }
  }
  u32x4 sel[2];
  {
    const int i = n >> 3;
    const uint32_t one = (i & 1) ? 0x3F800000u : 0x3F80u;
    const uint32_t d0 = i < 2 ? one : 0u, d1 = i < 2 ? 0u : one;
#pragma unroll
    for (int hh = 0; hh < 2; ++hh) sel[hh] = h == hh ? u32x4{d0, d1, d0, d1} : u32x4{0u, 0u, 0u, 0u};
  }
  uint16_t* orow = out + ((size_t)t0 * 8 + 4 * wave + h) * DCH + n * 8;
  auto ooff = [&](int u, int i) -> size_t { return ((size_t)(u >> 2) * (T2 * 8) + (u & 3) * 32 + 2 * i) * DCH; };
  auto add_ge = [&](int g) -> f32x16 {
    if (g == 4) gacc = mfma32(ga[1], gsel, zero16());
    return mfma32(xa[g >> 1], sel[g & 1], gacc);
  };
  char* const tbw = smem + J_TB + (2 * wave * 64 + lane) * 16;
  const char* const tbr = smem + J_TB + lane * 16;

#pragma unroll
  for (int u = 0; u < NPRE; ++u) {
    const f32x16 s = add_ge(u);
    u32x4 p0, p1;
#pragma unroll
    for (int p = 0; p < 4; ++p) {
      p0[p] = pk2bf(tanh_pre(s[2 * p]), tanh_pre(s[2 * p + 1]));
      if (u < 2 && p < 3) wo[4 + u * 6 + p] = wop[(4 + u * 6 + p) * 64];
      SB();
      p1[p] = pk2bf(tanh_pre(s[8 + 2 * p]), tanh_pre(s[8 + 2 * p + 1]));
      if (u < 2 && p < 3) wo[7 + u * 6 + p] = wop[(7 + u * 6 + p) * 64];
      SB();
    }
    *(u32x4*)(tbw + u * 16384) = p0;
    *(u32x4*)(tbw + u * 16384 + 1024) = p1;
  }
  f32x16 sn = add_ge(NPRE);
  __syncthreads();
  constexpr int PRE = 6;
  u32x4 bt[16];
#pragma unroll
  for (int s2 = 0; s2 < PRE; ++s2) bt[s2] = *(const u32x4*)(tbr + s2 * 1024);
#pragma unroll
  for (int u = 0; u < 8; ++u) {
    const char* tb = tbr + (u % NTB) * 16384;
    const char* tbn = tbr + ((u + 1) % NTB) * 16384;
    u32x4 orv[2];
    if (u > 0) {
      const char* otp = smem + J_OST + ((u + 1) & 1) * (32 * OSP);
#pragma unroll
      for (int i = 0; i < 2; ++i) orv[i] = *(const u32x4*)(otp + (4 * wave + h + 2 * i) * OSP + n * 16);
    }
    f32x16 acc = zero16();
    float th[16];
    u32x4 p0, p1;
    u32x4 btn[PRE];
    SB();
#pragma unroll
    for (int s2 = 0; s2 < 16; ++s2) {
      acc = mfma32(wo[s2], bt[s2], acc);
      if (s2 + PRE < 16) bt[s2 + PRE] = *(const u32x4*)(tb + (s2 + PRE) * 1024);
      else if (u < 7) btn[s2 + PRE - 16] = *(const u32x4*)(tbn + (s2 + PRE - 16) * 1024);
      if ((s2 == 3 || s2 == 5) && u > 0) {
        const int i = (s2 - 3) >> 1;
        __builtin_nontemporal_store(orv[i], (u32x4*)(orow + ooff(u - 1, i)));
      }
      if (u + NPRE < 8) {
        constexpr int NPAIR = 8;
        const int g = s2 >> 1;
        if ((s2 & 1) == 0) {
          if (g < NPAIR) { th[2 * g] = __builtin_amdgcn_exp2f(sn[2 * g]); th[2 * g + 1] = __builtin_amdgcn_exp2f(sn[2 * g + 1]); }
          if (g >= 1) { th[2 * g - 2] = __builtin_fmaf(-2.0f, th[2 * g - 2], 1.0f); th[2 * g - 1] = __builtin_fmaf(-2.0f, th[2 * g - 1], 1.0f); }
        } else {
          const f32x2 a01 = f32x2{th[2 * g], th[2 * g + 1]} + f32x2{1.0f, 1.0f};
          const float a0 = a01[0], a1 = a01[1];
          if (g >= 1) {
            const uint32_t pk = pk2bf(th[2 * g - 2], th[2 * g - 1]);
            if (g - 1 < 4) p0[g - 1] = pk; else p1[g - 5] = pk;
          }
          th[2 * g] = __builtin_amdgcn_rcpf(a0); th[2 * g + 1] = __builtin_amdgcn_rcpf(a1);
        }
        if (s2 == 10) *(u32x4*)(tbw + ((u + NPRE) % NTB) * 16384) = p0;
      }
      SB();
    }
    if (u + 1 + NPRE < 8) sn = add_ge(u + 1 + NPRE);
    if (u + NPRE < 8) p1[3] = pk2bf(__builtin_fmaf(-2.0f, th[14], 1.0f), __builtin_fmaf(-2.0f, th[15], 1.0f));
    if (u + NPRE < 8) *(u32x4*)(tbw + ((u + NPRE) % NTB) * 16384 + 1024) = p1;
    char* ot = smem + J_OST + (u & 1) * (32 * OSP) + n * OSP + wave * 64;
#pragma unroll
    for (int g = 0; g < 4; ++g) {
      u32x2 pk = {pk2bf(acc[4 * g], acc[4 * g + 1]), pk2bf(acc[4 * g + 2], acc[4 * g + 3])};
      *(u32x2*)(ot + (8 * g + 4 * h) * 2) = pk;
    }
#pragma unroll
    for (int s2 = 0; s2 < PRE; ++s2) bt[s2] = btn[s2];
    __syncthreads();
  }
  {
    const char* otp = smem + J_OST + 1 * (32 * OSP);
#pragma unroll
    for (int i = 0; i < 2; ++i) {
      u32x4 v = *(const u32x4*)(otp + (4 * wave + h + 2 * i) * OSP + n * 16);
      __builtin_nontemporal_store(v, (u32x4*)(orow + ooff(7, i)));
    }
  }
}

}

extern "C" void kernel_launch(void* const* d_in, const int* in_sizes, int n_in, void* d_out, int out_size, void* d_ws,
                              size_t ws_size, hipStream_t stream) {
  const uint16_t* x = (const uint16_t*)d_in[0];
  const uint16_t* w1 = (const uint16_t*)d_in[1];
  const uint16_t* w2 = (const uint16_t*)d_in[2];
  const uint16_t* g = (const uint16_t*)d_in[3];
  const uint16_t* wf = (const uint16_t*)d_in[4];
  const uint16_t* wg = (const uint16_t*)d_in[5];
  const uint16_t* wo = (const uint16_t*)d_in[6];
  uint16_t* out = (uint16_t*)d_out;
  char* ws = (char*)d_ws;
  char* u1 = ws;
  char* w1h = ws + (24 << 20);
  char* w2h = ws + (40 << 20);
  char* y1 = ws + (48 << 20);
  char* u2 = ws + (60 << 20);
  char* y2 = ws + (77 << 20);
  u32x4* wff = (u32x4*)(ws + (86 << 20));
  u32x4* wof = (u32x4*)(ws + (87 << 20));
  uint32_t* ge = (uint32_t*)(ws + (88 << 20));
  constexpr int G1LDS = 7 * 11 * 2048, G2LDS = 8 * 8 * 2048;

  (void)hipFuncSetAttribute((const void*)gemm_kernel<1>, hipFuncAttributeMaxDynamicSharedMemorySize, G1LDS);
  (void)hipFuncSetAttribute((const void*)gemm_kernel<2>, hipFuncAttributeMaxDynamicSharedMemorySize, G2LDS);
  (void)hipFuncSetAttribute((const void*)joint_kernel, hipFuncAttributeMaxDynamicSharedMemorySize, JOINT_LDS);

  pre_kernel<<<256, 512, 0, stream>>>(x, w1, w2, g, wg, wf, wo, u1, w1h, w2h, wff, wof, ge);
  gemm_kernel<1><<<256, 256, G1LDS, stream>>>(u1, w1h, y1);
  mid_kernel<<<256, 1024, 0, stream>>>(y1, u2);
  gemm_kernel<2><<<256, 256, G2LDS, stream>>>(u2, w2h, y2);
  joint_kernel<<<250, 512, JOINT_LDS, stream>>>(y2, wff, wof, (const uint32_t*)ge, out);
}
```

```cpp
#include <hip/hip_runtime.h>
#include <stdint.h>

typedef __attribute__((ext_vector_type(8))) short bf16x8;
typedef __attribute__((ext_vector_type(16))) float f32x16;
typedef __attribute__((ext_vector_type(4))) float f32x4;
typedef __attribute__((ext_vector_type(2))) float f32x2;
typedef __attribute__((ext_vector_type(4))) unsigned int u32x4;
typedef __attribute__((ext_vector_type(2))) unsigned int u32x2;

#define GPRO 2
namespace {

constexpr int DCH = 256;

__device__ __forceinline__ float bf2f(uint32_t v) { return __uint_as_float(v << 16); }
__device__ __forceinline__ uint32_t f2bf(float f) {
  uint32_t u = __float_as_uint(f);
  u += 0x7FFF + ((u >> 16) & 1);
  return u >> 16;
}
__device__ __forceinline__ uint32_t pk2bf(float a, float b) {
  typedef __attribute__((ext_vector_type(2))) __bf16 b2;
  f32x2 v = {a, b};
  b2 r = __builtin_convertvector(v, b2);
  return __builtin_bit_cast(uint32_t, r);
}
__device__ __forceinline__ uint32_t gelu2(float a0, float a1) {
  const f32x2 x = {a0, a1};
  const f32x2 c0 = {-2.302208198144325f, -2.302208198144325f}, c1 = {-0.10294323970002349f, -0.10294323970002349f};
  const f32x2 z = x * __builtin_elementwise_fma(x * x, c1, c0);
  const f32x2 e = {__builtin_amdgcn_exp2f(z[0]), __builtin_amdgcn_exp2f(z[1])};
  const f32x2 d = e + f32x2{1.0f, 1.0f};
  const f32x2 y = x * f32x2{__builtin_amdgcn_rcpf(d[0]), __builtin_amdgcn_rcpf(d[1])};
  return pk2bf(y[0], y[1]);
}
__device__ __forceinline__ f32x16 mfma32(const u32x4& a, const u32x4& b, const f32x16& c) {
  return __builtin_amdgcn_mfma_f32_32x32x16_bf16(__builtin_bit_cast(bf16x8, a), __builtin_bit_cast(bf16x8, b), c, 0, 0, 0);
}
__device__ __forceinline__ f32x4 mfma16(const u32x4& a, const u32x4& b, const f32x4& c) {
  return __builtin_amdgcn_mfma_f32_16x16x32_bf16(__builtin_bit_cast(bf16x8, a), __builtin_bit_cast(bf16x8, b), c, 0, 0, 0);
}
__device__ __forceinline__ f32x16 zero16() {
  f32x16 z;
#pragma unroll
  for (int i = 0; i < 16; ++i) z[i] = 0.f;
  return z;
}
__device__ __forceinline__ __amdgpu_buffer_rsrc_t mkbuf(const void* p, int nbytes) {
  return __builtin_amdgcn_make_buffer_rsrc((void*)p, (short)0, nbytes, 0x00020000);
}
template <int V> struct IC { static constexpr int value = V; };
template <int I, int N, class F>
__device__ __forceinline__ void sfor(F&& f) {
  if constexpr (I < N) {
    f(IC<I>{});
    sfor<I + 1, N>(f);
  }
}
#define SB() __builtin_amdgcn_sched_barrier(0)
#define PHASE() do { SB(); asm volatile("s_waitcnt vmcnt(0)\n\ts_barrier" ::: "memory"); SB(); } while (0)

constexpr double PI_ = 3.14159265358979323846;
constexpr double c128(int m) {
  m = ((m % 128) + 128) % 128;
  if (m > 64) m = 128 - m;
  double sg = 1.0;
  if (m > 32) { m = 64 - m; sg = -1.0; }
  if (m == 32) return 0.0;
  const double a = PI_ * m / 64.0;
  double term = 1.0, sum = 1.0;
  for (int i = 1; i < 16; ++i) { term *= -a * a / ((2 * i - 1) * (2 * i)); sum += term; }
  return sg * sum;
}
constexpr double s128(int m) { return c128(m - 32); }
constexpr uint16_t cbf(double v) {
  uint32_t u = __builtin_bit_cast(uint32_t, (float)v);
  u += 0x7FFF + ((u >> 16) & 1);
  return (uint16_t)(u >> 16);
}
constexpr double cbf2d(uint16_t v) { return (double)__builtin_bit_cast(float, (uint32_t)v << 16); }
struct Tabs {
  uint16_t f1[2][3][64][8];
  uint16_t e1[2][2][64][8];
  uint16_t e2[2][64][8];
  uint16_t g1[64][8][8];
  uint16_t f2[64][8];
  uint16_t g2[16][32];
};
constexpr Tabs make_tabs() {
  Tabs t{};
  for (int l = 0; l < 64; ++l)
    for (int e = 0; e < 8; ++e) {
      const int h = l >> 5, f = l & 31, ri = f & 1;
      for (int fb = 0; fb < 2; ++fb) {
        const int k = 16 * fb + (f >> 1);
        for (int ks = 0; ks < 3; ++ks) {
          const int a = (2 * k + 1) * (16 * ks + 8 * h + e);
          t.f1[fb][ks][l][e] = cbf(ri ? -s128(a) : c128(a));
        }
        const int a = (2 * k + 1) * (8 * h + e);
        const double v = ri ? s128(a) : c128(a);
        t.e1[fb][0][l][e] = cbf(v);
        t.e1[fb][1][l][e] = cbf(v - cbf2d(cbf(v)));
      }
      const int a = 2 * (2 * (f >> 1) + 1) * (8 * h + e);
      const double v = ri ? s128(a) : c128(a);
      t.e2[0][l][e] = cbf(v);
      t.e2[1][l][e] = cbf(v - cbf2d(cbf(v)));
      t.f2[l][e] = cbf(ri ? -v : v);
    }
  for (int m = -15; m < 48; ++m)
    for (int f = 0; f < 64; ++f) {
      const int a = (2 * (f >> 1) + 1) * m;
      t.g1[m + 16][(f >> 3) ^ (((m + 16) >> 1) & 7)][f & 7] = cbf(((f & 1) ? -s128(a) : c128(a)) / 32.0);
    }
  for (int m = 0; m < 16; ++m)
    for (int f = 0; f < 32; ++f) {
      const int a = 2 * (2 * (f >> 1) + 1) * m;
      t.g2[m][f] = cbf(((f & 1) ? -s128(a) : c128(a)) / 16.0);
    }
  return t;
}
__device__ __attribute__((aligned(64))) const Tabs tabs = make_tabs();

constexpr int OOB = 0x7FFFFF00;
constexpr int WT = 16;
constexpr int NT = 2;
constexpr int R1 = 336;
constexpr int R2 = 504;
constexpr int PL1 = R1 * 128;
constexpr int PL2 = R2 * 128;
constexpr int U1BYTES = 32 * 16 * PL1, U2BYTES = 16 * 16 * PL2;
constexpr int WBIN = 16 * 16 * 2 * 1024;
constexpr int YP1 = R1 * 64;
constexpr int YP2 = 500 * 64;
constexpr int Y1BYTES = 32 * 16 * YP1, Y2BYTES = 16 * 16 * YP2;
__device__ __forceinline__ int dmap(int cb, int n) { return 64 * (cb >> 2) + 32 * (cb & 1) + 2 * n + ((cb >> 1) & 1); }

__device__ __forceinline__ void swap16(u32x4& a, u32x4& b) {
#pragma unroll
  for (int i = 0; i < 4; ++i) {
    const auto r = __builtin_amdgcn_permlane16_swap(a[i], b[i], false, false);
    a[i] = r[0];
    b[i] = r[1];
  }
}

template <int CONV>
__device__ __forceinline__ void wtrans(const uint16_t* __restrict__ w, char* __restrict__ wh, int wid, int lane) {
  const int dq = wid & 3, pc = wid >> 2;
  const int ks = pc >> 2, kq = pc & 3;
  const int n32 = lane & 31, h = lane >> 5;
  const int r = CONV == 1 ? ks >> 3 : kq >> 1;
  const int c0 = CONV == 1 ? (ks & 7) * 32 + kq * 8 : dmap(ks, 4 * (kq & 1));
  const __amdgpu_buffer_rsrc_t wr = mkbuf(w, 31 * 131072);
  const int vb = (16 * h + r + 1) * 131072 + c0 * 512 + dq * 128 + n32 * 4;
  const int vb0 = (h + r == 0) ? OOB : vb - 262144;
  constexpr int NFB = CONV == 1 ? 2 : 1;
  const u32x4* et = CONV == 1 ? (const u32x4*)&tabs.e1[0][0][0][0] : (const u32x4*)&tabs.e2[0][0][0];
  u32x4 E[NFB][2];
#pragma unroll
  for (int fb = 0; fb < NFB; ++fb) { E[fb][0] = et[fb * 128 + lane]; E[fb][1] = et[fb * 128 + 64 + lane]; }
  uint32_t v[8][8];
#pragma unroll
  for (int el = 0; el < 8; ++el)
#pragma unroll
    for (int jj = 0; jj < 8; ++jj) {
      const int co = CONV == 1 ? el * 512 : (el & 3) * 1024;
      const int so = (CONV == 1 ? 0 : (el >> 2) * 8192) + (jj ? (jj - 1) * 262144 : 0);
      v[el][jj] = __builtin_amdgcn_raw_buffer_load_b32(wr, (jj ? vb : vb0) + co, so, NT);
    }
  PHASE();
  const __amdgpu_buffer_rsrc_t dr = mkbuf(wh, (CONV == 1 ? 32 : 16) * WBIN);
  const int vo = 2 * h * WBIN + (4 * dq + (n32 >> 4)) * 32768 + ks * 2048 + kq * 256 + (n32 & 15) * 16;
#pragma unroll
  for (int e = 0; e < 2; ++e) {
    u32x4 b[8];
#pragma unroll
    for (int el = 0; el < 8; ++el)
#pragma unroll
      for (int j = 0; j < 4; ++j) b[el][j] = __builtin_amdgcn_perm(v[el][2 * j + 1], v[el][2 * j], e ? 0x07060302u : 0x05040100u);
#pragma unroll
    for (int fb = 0; fb < NFB; ++fb) {
      u32x4 o[16];
#pragma unroll
      for (int ep = 0; ep < 4; ++ep) {
        f32x16 d[2];
#pragma unroll
        for (int z = 0; z < 2; ++z) d[z] = mfma32(E[fb][1], b[2 * ep + z], zero16());
#pragma unroll
        for (int z = 0; z < 2; ++z) d[z] = mfma32(E[fb][0], b[2 * ep + z], d[z]);
#pragma unroll
        for (int i = 0; i < 16; ++i) o[i][ep] = pk2bf(d[0][i], d[1][i]);
      }
#pragma unroll
      for (int i = 0; i < 16; ++i)
        __builtin_amdgcn_raw_buffer_store_b128(o[i], dr, vo + (i & 1) * 1024,
                                               (16 * fb + 4 * (i >> 2) + ((i >> 1) & 1)) * WBIN + e * 65536, WT);
      SB();
    }
  }
}

__device__ __forceinline__ void xload(const uint16_t* __restrict__ x, int u, int lane, uint32_t (&v)[3][8]) {
  const int cq = u & 7, b = (u >> 3) & 1, sg = u >> 4;
  const __amdgpu_buffer_rsrc_t xr = mkbuf(x + (size_t)b * 16000 * DCH, 8192000);
#pragma unroll
  for (int ks = 0; ks < 3; ++ks) {
    const int row0 = 48 * sg - 24 + 16 * ks + 8 * (lane >> 5);
    const int vb = row0 >= 0 ? row0 * 1024 + cq * 128 + (lane & 31) * 4 : OOB;
#pragma unroll
    for (int e = 0; e < 8; ++e) v[ks][e] = __builtin_amdgcn_raw_buffer_load_b32(xr, vb + (e & 3) * 1024, (e >> 2) * 4096, NT);
  }
}
__device__ __forceinline__ void xtrans(char* __restrict__ u1, int u, int lane, const uint32_t (&v)[3][8], const u32x4 (&F)[2][3]) {
  const __amdgpu_buffer_rsrc_t ur = mkbuf(u1 + (2 * (u & 7)) * PL1 + (u >> 3) * 128, U1BYTES - (2 * (u & 7)) * PL1 - (u >> 3) * 128);
  const int vo = ((lane & 15) >> 1) * 16 * PL1 + ((lane >> 4) & 1) * 64 + (lane & 1) * 32 + (lane >> 5) * 16;
  u32x4 a[2][3];
#pragma unroll
  for (int ks = 0; ks < 3; ++ks)
#pragma unroll
    for (int j = 0; j < 4; ++j) {
      a[0][ks][j] = __builtin_amdgcn_perm(v[ks][2 * j + 1], v[ks][2 * j], 0x05040100u);
      a[1][ks][j] = __builtin_amdgcn_perm(v[ks][2 * j + 1], v[ks][2 * j], 0x07060302u);
    }
#pragma unroll
  for (int fb = 0; fb < 2; ++fb) {
    f32x16 d[2] = {zero16(), zero16()};
#pragma unroll
    for (int ks = 0; ks < 3; ++ks)
#pragma unroll
      for (int e = 0; e < 2; ++e) d[e] = mfma32(a[e][ks], F[fb][ks], d[e]);
#pragma unroll
    for (int qp = 0; qp < 2; ++qp) {
      u32x4 o[2];
#pragma unroll
      for (int z = 0; z < 2; ++z)
#pragma unroll
        for (int i = 0; i < 4; ++i) o[z][i] = pk2bf(d[0][8 * qp + 4 * z + i], d[1][8 * qp + 4 * z + i]);
      swap16(o[0], o[1]);
#pragma unroll
      for (int z = 0; z < 2; ++z) __builtin_amdgcn_raw_buffer_store_b128(o[z], ur, vo, ((fb * 16 + z * 8) * 16 + qp) * PL1, WT);
    }
  }
}

__global__ __launch_bounds__(512, 2) void pre_kernel(const uint16_t* __restrict__ x, const uint16_t* __restrict__ w1,
                                                     const uint16_t* __restrict__ w2, const uint16_t* __restrict__ wf,
                                                     const uint16_t* __restrict__ wo, char* __restrict__ u1,
                                                     char* __restrict__ w1h, char* __restrict__ w2h, u32x4* __restrict__ wff,
                                                     u32x4* __restrict__ wof) {
  const int blk = blockIdx.x;
  const int tid = threadIdx.x, lane = tid & 63;
  const int wave = __builtin_amdgcn_readfirstlane(tid >> 6);
  const int n = lane & 31, h = lane >> 5;
  if (wave == 0) {
    wtrans<1>(w1, w1h, blk, lane);
  } else if (wave == 1) {
    wtrans<2>(w2, w2h, blk, lane);
  } else {
    const int bq = (blk & 7) * 32 + (blk >> 3);
    const int u0 = ((21 * bq) >> 1) + wave - 2, ue = (21 * (bq + 1)) >> 1;
    u32x4 F[2][3];
#pragma unroll
    for (int fb = 0; fb < 2; ++fb)
#pragma unroll
      for (int ks = 0; ks < 3; ++ks) F[fb][ks] = ((const u32x4*)&tabs.f1[fb][ks][0][0])[lane];
    uint32_t v0[3][8];
    xload(x, u0, lane, v0);
    if (u0 + 6 < ue) {
      uint32_t v1[3][8];
      xload(x, u0 + 6, lane, v1);
      PHASE();
      xtrans(u1, u0, lane, v0, F);
      SB();
      xtrans(u1, u0 + 6, lane, v1, F);
    } else if (wave == 7) {
      const int s = blk & 15, eb = (blk >> 4) & 7, second = blk >> 7;
      const uint16_t* src = (second ? wo + (16 * s + 4 * h) * DCH : wf + dmap(s, 4 * h) * DCH) + 32 * eb + n;
      const int st = second ? DCH : 2 * DCH;
      uint32_t v[8];
#pragma unroll
      for (int j = 0; j < 8; ++j) v[j] = src[(8 * (j >> 2) + (j & 3)) * st];
      PHASE();
      xtrans(u1, u0, lane, v0, F);
      (second ? wof : wff)[(blk & 127) * 64 + lane] =
          u32x4{v[0] | (v[1] << 16), v[2] | (v[3] << 16), v[4] | (v[5] << 16), v[6] | (v[7] << 16)};
    } else {
      PHASE();
      xtrans(u1, u0, lane, v0, F);
    }
  }
}

typedef __attribute__((address_space(3))) u32x4 lds_u32x4;
__device__ __forceinline__ u32x4 lds_ld(int a) { return *(const lds_u32x4*)(uintptr_t)(uint32_t)a; }
__device__ __forceinline__ uint32_t lds_ld32(int a) { return *(const __attribute__((address_space(3))) uint32_t*)(uintptr_t)(uint32_t)a; }
template <int SZ>
__device__ __forceinline__ void dma(__amdgpu_buffer_rsrc_t rsrc, char* lds, int voff, int soff) {
#if defined(__HIP_DEVICE_COMPILE__)
  typedef __attribute__((address_space(3))) void* ldsp;
  if constexpr (SZ == 16) __builtin_amdgcn_raw_ptr_buffer_load_lds(rsrc, (ldsp)lds, 16, voff, soff, 0, 0);
  else __builtin_amdgcn_raw_ptr_buffer_load_lds(rsrc, (ldsp)lds, 4, voff, soff, 0, 0);
#endif
}

constexpr int issued(int g, int MB, int OPS, int NST, int BAR, int PRO) {
  int o = PRO * OPS;
  for (int t = 0; t < g; ++t)
    if (o < 16 * OPS && (o / OPS < NST || t >= (o / OPS - NST + 1) * MB + BAR)) ++o;
  return o;
}
template <int CONV>
__global__ __launch_bounds__(256, 1) void gemm_kernel(const char* __restrict__ U, const char* __restrict__ W,
                                                      char* __restrict__ Y, const char* __restrict__ pf) {
  constexpr int MB = CONV == 1 ? 11 : 8;
  constexpr int STG = MB * 2048;
  constexpr int NST = CONV == 1 ? 7 : 8;
  constexpr int PL = CONV == 1 ? PL1 : PL2;
  constexpr int NDMA = CONV == 1 ? 6 : 4;
  constexpr int OPS = NDMA + 2;
  extern __shared__ __attribute__((aligned(16))) char smem[];
  const int bid = blockIdx.x, xcd = bid & 7, slot = bid >> 3;
  const int bin = CONV == 1 ? xcd * 4 + (slot >> 3) : xcd * 2 + (slot >> 4);
  const int mt = CONV == 1 ? (slot >> 2) & 1 : (slot >> 2) & 3;
  const int tid = threadIdx.x, lane = tid & 63;
  const int wave = __builtin_amdgcn_readfirstlane(tid >> 6);
  const int cb = (slot & 3) * 4 + wave;
  const int row0 = CONV == 1 ? mt * 168 : mt * 126;
  const __amdgpu_buffer_rsrc_t ur = mkbuf(U + (size_t)bin * 16 * PL + row0 * 128, 16 * PL - row0 * 128 + (CONV == 1 ? 0 : 256));
  const __amdgpu_buffer_rsrc_t wr = mkbuf(W + (size_t)bin * WBIN + cb * 32768, 32768);
  int vo[2];
  if constexpr (CONV == 1) {
    vo[0] = wave * 1024 + (lane >> 3) * 128 + (((lane & 7) ^ ((4 * wave + (lane >> 4)) & 7)) * 16);
    vo[1] = 20480 + wave * 256 + (lane >> 5) * 128 + ((((lane >> 2) & 7) ^ wave) * 16) + (lane & 3) * 4;
  } else {
#pragma unroll
    for (int z = 0; z < 2; ++z) vo[z] = wave * 4096 + (lane >> 3) * 128 + (((lane & 7) ^ (2 * wave + z)) * 16);
  }
  u32x4 B[NST][2];
  auto issue = [&](auto is, auto ii) {
    constexpr int s = decltype(is)::value, i = decltype(ii)::value;
    char* buf = smem + (s % NST) * STG;
    if constexpr (i < 2) {
      B[s % NST][i] = __builtin_amdgcn_raw_buffer_load_b128(wr, lane * 16 + i * 1024, s * 2048, 0);
    } else if constexpr (CONV == 1) {
      if constexpr (i < 7) dma<16>(ur, buf + wave * 1024 + (i - 2) * 4096, vo[0], s * PL + (i - 2) * 4096);
      else dma<4>(ur, buf + 20480 + wave * 256, vo[1], s * PL);
    } else {
      dma<16>(ur, buf + wave * 4096 + (i - 2) * 1024, vo[(i - 2) >> 1], s * PL + (i - 2) * 1024);
    }
  };
  constexpr int AR = 4, AL = 3;
  constexpr int BAR = MB - AL;
  constexpr int PRO = GPRO;
  sfor<0, PRO>([&](auto is) { sfor<0, OPS>([&](auto ii) { issue(is, ii); SB(); }); });
  const int rho = lane & 15, kq = lane >> 4;
  const int lds0 = (int)(uint32_t)(uintptr_t)(__attribute__((address_space(3))) char*)smem;
  constexpr int WIN = CONV == 1 ? 2 : 4;
  constexpr int NWIN = (NST + WIN - 1) / WIN;
  int ab[NWIN][2];
#pragma unroll
  for (int ri = 0; ri < 2; ++ri) {
    const int pos = ((kq >> 1) * 4 + ri * 2 + (kq & 1)) ^ (rho >> 1);
    const int a = lds0 + (CONV == 1 ? rho * 128 : (rho >> 1) * 2048 + (rho & 1) * 128) + pos * 16;
#pragma unroll
    for (int wn = 0; wn < NWIN; ++wn) { ab[wn][ri] = a + wn * WIN * STG; asm volatile("" : "+v"(ab[wn][ri])); }
  }
  auto aread = [&](int s, int mb, int ri) -> u32x4 {
    const int bf = s % NST;
    return lds_ld(ab[bf / WIN][ri] + (bf % WIN) * STG + mb * (CONV == 1 ? 2048 : 256));
  };
  f32x4 yr[MB], yi[MB];
#pragma unroll
  for (int mb = 0; mb < MB; ++mb) { yr[mb] = f32x4{0.f, 0.f, 0.f, 0.f}; yi[mb] = f32x4{0.f, 0.f, 0.f, 0.f}; }
  u32x4 A[AR][2];
  asm volatile("s_waitcnt vmcnt(%0)" ::"n"((PRO - 1) * OPS) : "memory");
  __builtin_amdgcn_s_barrier();
  asm volatile("" ::: "memory");
#pragma unroll
  for (int z = 0; z < AL; ++z) { A[z][0] = aread(0, z, 0); A[z][1] = aread(0, z, 1); }
  SB();
  u32x4 nwi;
#pragma unroll
  for (int j = 0; j < 4; ++j) nwi[j] = B[0][1][j] ^ 0x80008000u;
  sfor<0, 16>([&](auto is) {
    constexpr int s = decltype(is)::value;
    u32x4 nwn;
    sfor<0, MB>([&](auto im) {
      constexpr int mb = decltype(im)::value;
      constexpr int g = s * MB + mb, gn = g + AL;
      constexpr int o0 = issued(g, MB, OPS, NST, BAR, PRO), o1 = issued(g + 1, MB, OPS, NST, BAR, PRO);
      if constexpr (mb == BAR && s + 1 < 16) {
        static_assert(o0 >= (s + 2) * OPS, "next stage not requested yet");
        asm volatile("s_waitcnt vmcnt(%0)" ::"n"(o0 - (s + 2) * OPS) : "memory");
        __builtin_amdgcn_s_barrier();
        asm volatile("" ::: "memory");
        SB();
      }
      yr[mb] = mfma16(B[s % NST][0], A[g % AR][0], yr[mb]);
      SB();
      yr[mb] = mfma16(nwi, A[g % AR][1], yr[mb]);
      if constexpr (gn < 16 * MB) A[gn % AR][0] = aread(gn / MB, gn % MB, 0);
      SB();
      yi[mb] = mfma16(B[s % NST][1], A[g % AR][0], yi[mb]);
      if constexpr (o1 > o0) issue(IC<o0 / OPS>{}, IC<o0 % OPS>{});
      SB();
      yi[mb] = mfma16(B[s % NST][0], A[g % AR][1], yi[mb]);
      if constexpr (gn < 16 * MB) A[gn % AR][1] = aread(gn / MB, gn % MB, 1);
      if constexpr (mb == MB - 2 && s + 1 < 16) {
#pragma unroll
        for (int j = 0; j < 4; ++j) nwn[j] = B[(s + 1) % NST][1][j] ^ 0x80008000u;
      }
      SB();
    });
    if constexpr (s + 1 < 16) nwi = nwn;
    if constexpr (CONV == 2 && s == 14) {
      if (wave == 0) dma<4>(mkbuf(pf, 262144 + 32768), smem, slot * 8192 + lane * 128, 0);
      if (wave == 1 && slot < 4) dma<4>(mkbuf(pf, 262144 + 32768), smem, 262144 + slot * 8192 + lane * 128, 0);
      SB();
    }
  });
  if constexpr (CONV == 1) {
    const __amdgpu_buffer_rsrc_t yb = mkbuf(Y + (size_t)(bin * 16 + cb) * YP1 + row0 * 64, 168 * 64);
#pragma unroll
    for (int mb = 0; mb < MB; ++mb) {
      u32x4 o;
#pragma unroll
      for (int i = 0; i < 4; ++i) o[i] = pk2bf(yr[mb][i], yi[mb][i]);
      __builtin_amdgcn_raw_buffer_store_b128(o, yb, rho * 64 + kq * 16 + (mb & 3) * 1024, (mb >> 2) * 4096, WT);
    }
  } else {
    const __amdgpu_buffer_rsrc_t yb = mkbuf(Y + (size_t)(bin * 16 + cb) * YP2 + row0 * 64, YP2 - row0 * 64);
    const float sg = (bin & 1) ? -1.f : 1.f;
    f32x4 nr, ni;
#pragma unroll
    for (int i = 0; i < 4; ++i) { nr[i] = __shfl_down(yr[0][i], 2, 16); ni[i] = __shfl_down(yi[0][i], 2, 16); }
    const int vy = (rho >> 1) * 1024 + (rho & 1) * 64 + kq * 16;
#pragma unroll
    for (int mb = 0; mb < MB; ++mb) {
      const f32x4 xr = mb < MB - 1 ? yr[mb < MB - 1 ? mb + 1 : 0] : nr, xi = mb < MB - 1 ? yi[mb < MB - 1 ? mb + 1 : 0] : ni;
      u32x4 o;
#pragma unroll
      for (int i = 0; i < 4; ++i) o[i] = pk2bf(__builtin_fmaf(sg, xi[i], yr[mb][i]), __builtin_fmaf(-sg, xr[i], yi[mb][i]));
      __builtin_amdgcn_raw_buffer_store_b128(o, yb, (mb == MB - 1 && rho >= 14) ? OOB : vy + mb * 128, 0, WT);
    }
  }
}

template <int Z>
__device__ __forceinline__ void mid_unit(char* __restrict__ U, int ya, int ga, int cb, int s, const u32x4& F) {
  const int lane = threadIdx.x & 63;
  const int n32 = lane & 31, h = lane >> 5;
  constexpr int NSG = Z == 0 ? 1 : 2;
  const int tau = 16 * ((n32 >> 2) & 1) + 8 * ((n32 >> 3) & 1) + 2 * (n32 & 3) + (n32 >> 4);
  const int r[2] = {Z == 0 ? tau + 16 : Z == 1 ? (tau < 16 ? tau + 48 : 0) : tau + 32, Z == 1 ? tau : (tau >= 16 ? tau - 16 : 0)};
  f32x16 d = zero16();
#pragma unroll
  for (int sg = 0; sg < NSG; ++sg)
#pragma unroll
    for (int ks = 0; ks < 4; ++ks) {
      u32x4 yv;
#pragma unroll
      for (int j = 0; j < 4; ++j) yv[j] = lds_ld32(ya + sg * 4096 + ks * 1024 + j * 128);
      d = mfma32(lds_ld(ga + r[sg] * 128 + (((2 * ks + h) ^ ((r[sg] >> 1) & 7)) * 16)), yv, d);
    }
  const __amdgpu_buffer_rsrc_t ub = mkbuf(U, U2BYTES);
  const int vu = (((lane & 15) >> 1) * 16 + cb) * PL2 + s * 256 + ((lane >> 4) & 1) * 64 + (lane & 1) * 32 + h * 16;
  const int t0r = 32 * s - 16 + 16 * h;
  const bool ok = t0r >= 0 && t0r < 8000;
  f32x16 u[2];
#pragma unroll
  for (int q = 0; q < 2; ++q) {
    u32x4 a;
#pragma unroll
    for (int j = 0; j < 4; ++j) {
      const uint32_t gv = gelu2(d[8 * q + 2 * j], d[8 * q + 2 * j + 1]);
      a[j] = ok ? gv : 0u;
    }
    u[q] = mfma32(a, F, zero16());
  }
#pragma unroll
  for (int b = 0; b < 2; ++b) {
    u32x4 o[2];
#pragma unroll
    for (int q = 0; q < 2; ++q)
#pragma unroll
      for (int j = 0; j < 4; ++j) o[q][j] = pk2bf(u[q][8 * b + 2 * j], u[q][8 * b + 2 * j + 1]);
    swap16(o[0], o[1]);
#pragma unroll
    for (int z = 0; z < 2; ++z) __builtin_amdgcn_raw_buffer_store_b128(o[z], ub, vu + b * 128, z * 8 * 16 * PL2, WT);
  }
}
__global__ __launch_bounds__(1024) void mid_kernel(const char* __restrict__ Y, char* __restrict__ U, const uint16_t* __restrict__ g,
                                                   const uint16_t* __restrict__ wg, uint32_t* __restrict__ ge) {
  __shared__ __attribute__((aligned(16))) char sm[8192 + 49152];
  __shared__ uint32_t landed;
  const int blk = blockIdx.x, lane = threadIdx.x & 63;
  const int wave = __builtin_amdgcn_readfirstlane(threadIdx.x >> 6);
  const int cb = blk & 15, s0 = blk & ~15, s = s0 + wave;
  const int sg0 = (2 * s0) / 3;
  if (wave < 4) {
    const __amdgpu_buffer_rsrc_t tb = mkbuf(&tabs.g1[0][0][0], 8192);
#pragma unroll
    for (int i = 0; i < 2; ++i) dma<16>(tb, sm + wave * 2048 + i * 1024, lane * 16, wave * 2048 + i * 1024);
  }
  {
    const __amdgpu_buffer_rsrc_t yb = mkbuf(Y, Y1BYTES);
    const int vy = ((lane >> 3) * 16 + cb) * YP1 + sg0 * 128 + (lane & 7) * 16;
#pragma unroll
    for (int i = 0; i < 3; ++i) {
      const int pc = 3 * wave + i;
      dma<16>(yb, sm + 8192 + pc * 1024, vy, (pc & 3) * 128 * YP1 + (pc >> 2) * 128);
    }
  }
  const u32x4 F = ((const u32x4*)&tabs.f2[0][0])[lane];
  if (threadIdx.x == 0) landed = 0;
  asm volatile("s_waitcnt lgkmcnt(0)\n\ts_barrier" ::: "memory");
  if (s >= 251 && wave == 15 && cb < 8) {
    const int c0 = 32 * cb + 2 * (lane & 15), kq = lane >> 4;
    uint32_t v[8][8];
    u32x4 ga[8];
#pragma unroll
    for (int ks = 0; ks < 8; ++ks) {
      ga[ks] = *(const u32x4*)(g + (lane & 15) * DCH + ks * 32 + 8 * kq);
#pragma unroll
      for (int e = 0; e < 8; ++e) v[ks][e] = *(const uint32_t*)(wg + (ks * 32 + 8 * kq + e) * DCH + c0);
    }
    SB();
    f32x4 d[2] = {{0.f, 0.f, 0.f, 0.f}, {0.f, 0.f, 0.f, 0.f}};
#pragma unroll
    for (int ks = 0; ks < 8; ++ks)
#pragma unroll
      for (int e = 0; e < 2; ++e) {
        u32x4 bb;
#pragma unroll
        for (int j = 0; j < 4; ++j) bb[j] = __builtin_amdgcn_perm(v[ks][2 * j + 1], v[ks][2 * j], e ? 0x07060302u : 0x05040100u);
        d[e] = mfma16(ga[ks], bb, d[e]);
      }
#pragma unroll
    for (int i = 0; i < 4; ++i) {
      u32x2 o;
#pragma unroll
      for (int e = 0; e < 2; ++e) {
        const float gs = bf2f(f2bf(d[e][i])) * 2.885390081777927f;
        const uint32_t hi = f2bf(gs), lo = f2bf(gs - bf2f(hi));
        o[e] = hi | (lo << 16);
      }
      *(u32x2*)(ge + (4 * kq + i) * DCH + c0) = o;
    }
  } else {
    asm volatile("s_waitcnt vmcnt(0)" ::: "memory");
    if (lane == 0) __hip_atomic_fetch_or(&landed, 1u << wave, __ATOMIC_RELAXED, __HIP_MEMORY_SCOPE_WORKGROUP);
    if (s < 251) {
      const int p = s / 3, z = s - 3 * p;
      const int l0 = 2 * p + (z == 2) - sg0, l1 = l0 + (z != 0);
      const uint32_t need = 15u | (((2u << ((4 * l1 + 3) / 3)) - 1u) & ~((1u << ((4 * l0) / 3)) - 1u));
      while ((__builtin_amdgcn_readfirstlane(__hip_atomic_load(&landed, __ATOMIC_RELAXED, __HIP_MEMORY_SCOPE_WORKGROUP)) & need) != need)
        __builtin_amdgcn_s_sleep(1);
      asm volatile("" ::: "memory");
      const int l = (int)(uint32_t)(uintptr_t)(__attribute__((address_space(3))) char*)sm;
      const int ya = l + 8192 + l0 * 4096 + (lane >> 5) * 512 + (lane & 31) * 4;
      if (z == 0) mid_unit<0>(U, ya, l, cb, s, F);
      else if (z == 1) mid_unit<1>(U, ya, l, cb, s, F);
      else mid_unit<2>(U, ya, l, cb, s, F);
    }
  }
}

constexpr int J_F = 0;
constexpr int NPRE = 2;
constexpr int NTB = NPRE + 1;
constexpr int J_TB = 16384;
constexpr int OSP = 528;
constexpr int J_OST = J_TB + NTB * 16384;
constexpr int JOINT_LDS = J_OST + 2 * 32 * OSP;

__device__ __forceinline__ float tanh_pre(float s) {
  return __builtin_fmaf(-2.0f, __builtin_amdgcn_rcpf(__builtin_amdgcn_exp2f(s) + 1.0f), 1.0f);
}

__global__ __launch_bounds__(512, 2) void joint_kernel(const char* __restrict__ Y, const u32x4* __restrict__ wff,
                                                       const u32x4* __restrict__ wof, const uint32_t* __restrict__ geb,
                                                       uint16_t* __restrict__ out) {
  extern __shared__ __attribute__((aligned(16))) char smem[];
  constexpr int T2 = 4000;
  const int grp = blockIdx.x, t0 = grp * 16;
  const int tid = threadIdx.x;
  const int lane = tid & 63;
  const int wave = __builtin_amdgcn_readfirstlane(tid >> 6);
  const int n = lane & 31, h = lane >> 5;
  const __amdgpu_buffer_rsrc_t yb = mkbuf(Y, Y2BYTES);
  const int vy = (4 * h * 16 + 2 * wave) * YP2 + grp * 128 + n * 4;
  u32x4 yv[2][2];
#pragma unroll
  for (int c = 0; c < 2; ++c)
#pragma unroll
    for (int ks = 0; ks < 2; ++ks)
#pragma unroll
      for (int j = 0; j < 4; ++j) yv[c][ks][j] = __builtin_amdgcn_raw_buffer_load_b32(yb, vy, ((8 * ks + j) * 16 + c) * YP2, 0);
  u32x4 gi[2];
#pragma unroll
  for (int ks = 0; ks < 2; ++ks) gi[ks] = *(const u32x4*)&tabs.g2[n & 15][16 * ks + 8 * h];
  u32x4 ga[2];
#pragma unroll
  for (int b = 0; b < 2; ++b)
#pragma unroll
    for (int i = 0; i < 4; ++i) ga[b][i] = geb[(b * 8 + 4 * h + i) * 256 + 32 * wave + n];
  const u32x4* wfp = wff + wave * 16 * 64 + lane;
  const u32x4* wop = wof + wave * 16 * 64 + lane;
  u32x4 wa[16], wo[16];
#pragma unroll
  for (int s = 0; s < 8; ++s) wa[s] = wfp[s * 64];
  SB();
  {
    f32x16 d = zero16();
#pragma unroll
    for (int c = 0; c < 2; ++c)
#pragma unroll
      for (int ks = 0; ks < 2; ++ks) d = mfma32(yv[c][ks], (n >> 4) == c ? gi[ks] : u32x4{0u, 0u, 0u, 0u}, d);
#pragma unroll
    for (int b = 0; b < 2; ++b) {
      u32x4 o;
#pragma unroll
      for (int j = 0; j < 4; ++j) o[j] = gelu2(d[8 * b + 2 * j], d[8 * b + 2 * j + 1]);
      *(u32x4*)(smem + J_F + (2 * wave + (n >> 4)) * 1024 + (h * 32 + b * 16 + (n & 15)) * 16) = o;
    }
  }
  __syncthreads();
  f32x16 fe = zero16();
#pragma unroll
  for (int s = 0; s < 16; ++s) {
    const u32x4 fb = *(const u32x4*)(smem + J_F + s * 1024 + lane * 16);
    fe = mfma32(fb, wa[s], fe);
    if (s < 8) wa[s + 8] = wfp[(s + 8) * 64];
    else if ((s & 1) == 0) wo[(s - 8) >> 1] = wop[((s - 8) >> 1) * 64];
    SB();
  }
  u32x4 gsel;
#pragma unroll
  for (int i = 0; i < 4; ++i) gsel[i] = (4 * h + i == (n & 7)) ? 0x3F803F80u : 0u;
  f32x16 gacc = mfma32(ga[0], gsel, zero16());
  const float c2 = 2.885390081777927f;
  u32x4 xa[4];
#pragma unroll
  for (int q = 0; q < 4; ++q) {
#pragma unroll
    for (int p = 0; p < 2; ++p) {
      const float v0 = fe[4 * q + 2 * p] * c2, v1 = fe[4 * q + 2 * p + 1] * c2;
      const uint32_t hi = pk2bf(v0, v1);
      xa[q][p] = hi;
      xa[q][2 + p] = pk2bf(v0 - __uint_as_float(hi << 16), v1 - __uint_as_float(hi & 0xffff0000u));
    }
  }
  u32x4 sel[2];
  {
    const int i = n >> 3;
    const uint32_t one = (i & 1) ? 0x3F800000u : 0x3F80u;
    const uint32_t d0 = i < 2 ? one : 0u, d1 = i < 2 ? 0u : one;
#pragma unroll
    for (int hh = 0; hh < 2; ++hh) sel[hh] = h == hh ? u32x4{d0, d1, d0, d1} : u32x4{0u, 0u, 0u, 0u};
  }
  uint16_t* orow = out + ((size_t)t0 * 8 + 4 * wave + h) * DCH + n * 8;
  auto ooff = [&](int u, int i) -> size_t { return ((size_t)(u >> 2) * (T2 * 8) + (u & 3) * 32 + 2 * i) * DCH; };
  auto add_ge = [&](int g) -> f32x16 {
    if (g == 4) gacc = mfma32(ga[1], gsel, zero16());
    return mfma32(xa[g >> 1], sel[g & 1], gacc);
  };
  char* const tbw = smem + J_TB + (2 * wave * 64 + lane) * 16;
  const char* const tbr = smem + J_TB + lane * 16;

#pragma unroll
  for (int u = 0; u < NPRE; ++u) {
    const f32x16 s = add_ge(u);
    u32x4 p0, p1;
#pragma unroll
    for (int p = 0; p < 4; ++p) {
      p0[p] = pk2bf(tanh_pre(s[2 * p]), tanh_pre(s[2 * p + 1]));
      if (u < 2 && p < 3) wo[4 + u * 6 + p] = wop[(4 + u * 6 + p) * 64];
      SB();
      p1[p] = pk2bf(tanh_pre(s[8 + 2 * p]), tanh_pre(s[8 + 2 * p + 1]));
      if (u < 2 && p < 3) wo[7 + u * 6 + p] = wop[(7 + u * 6 + p) * 64];
      SB();
    }
    *(u32x4*)(tbw + u * 16384) = p0;
    *(u32x4*)(tbw + u * 16384 + 1024) = p1;
  }
  f32x16 sn = add_ge(NPRE);
  __syncthreads();
  constexpr int PRE = 6;
  u32x4 bt[16];
#pragma unroll
  for (int s2 = 0; s2 < PRE; ++s2) bt[s2] = *(const u32x4*)(tbr + s2 * 1024);
#pragma unroll
  for (int u = 0; u < 8; ++u) {
    const char* tb = tbr + (u % NTB) * 16384;
    const char* tbn = tbr + ((u + 1) % NTB) * 16384;
    u32x4 orv[2];
    if (u > 0) {
      const char* otp = smem + J_OST + ((u + 1) & 1) * (32 * OSP);
#pragma unroll
      for (int i = 0; i < 2; ++i) orv[i] = *(const u32x4*)(otp + (4 * wave + h + 2 * i) * OSP + n * 16);
    }
    f32x16 acc = zero16();
    float th[16];
    u32x4 p0, p1;
    u32x4 btn[PRE];
    SB();
#pragma unroll
    for (int s2 = 0; s2 < 16; ++s2) {
      acc = mfma32(wo[s2], bt[s2], acc);
      if (s2 + PRE < 16) bt[s2 + PRE] = *(const u32x4*)(tb + (s2 + PRE) * 1024);
      else if (u < 7) btn[s2 + PRE - 16] = *(const u32x4*)(tbn + (s2 + PRE - 16) * 1024);
      if ((s2 == 3 || s2 == 5) && u > 0) {
        const int i = (s2 - 3) >> 1;
        __builtin_nontemporal_store(orv[i], (u32x4*)(orow + ooff(u - 1, i)));
      }
      if (u + NPRE < 8) {
        constexpr int NPAIR = 8;
        const int g = s2 >> 1;
        if ((s2 & 1) == 0) {
          if (g < NPAIR) { th[2 * g] = __builtin_amdgcn_exp2f(sn[2 * g]); th[2 * g + 1] = __builtin_amdgcn_exp2f(sn[2 * g + 1]); }
          if (g >= 1) { th[2 * g - 2] = __builtin_fmaf(-2.0f, th[2 * g - 2], 1.0f); th[2 * g - 1] = __builtin_fmaf(-2.0f, th[2 * g - 1], 1.0f); }
        } else {
          const f32x2 a01 = f32x2{th[2 * g], th[2 * g + 1]} + f32x2{1.0f, 1.0f};
          const float a0 = a01[0], a1 = a01[1];
          if (g >= 1) {
            const uint32_t pk = pk2bf(th[2 * g - 2], th[2 * g - 1]);
            if (g - 1 < 4) p0[g - 1] = pk; else p1[g - 5] = pk;
          }
          th[2 * g] = __builtin_amdgcn_rcpf(a0); th[2 * g + 1] = __builtin_amdgcn_rcpf(a1);
        }
        if (s2 == 10) *(u32x4*)(tbw + ((u + NPRE) % NTB) * 16384) = p0;
      }
      SB();
    }
    if (u + 1 + NPRE < 8) sn = add_ge(u + 1 + NPRE);
    if (u + NPRE < 8) p1[3] = pk2bf(__builtin_fmaf(-2.0f, th[14], 1.0f), __builtin_fmaf(-2.0f, th[15], 1.0f));
    if (u + NPRE < 8) *(u32x4*)(tbw + ((u + NPRE) % NTB) * 16384 + 1024) = p1;
    char* ot = smem + J_OST + (u & 1) * (32 * OSP) + n * OSP + wave * 64;
#pragma unroll
    for (int g = 0; g < 4; ++g) {
      u32x2 pk = {pk2bf(acc[4 * g], acc[4 * g + 1]), pk2bf(acc[4 * g + 2], acc[4 * g + 3])};
      *(u32x2*)(ot + (8 * g + 4 * h) * 2) = pk;
    }
#pragma unroll
    for (int s2 = 0; s2 < PRE; ++s2) bt[s2] = btn[s2];
    __syncthreads();
  }
  {
    const char* otp = smem + J_OST + 1 * (32 * OSP);
#pragma unroll
    for (int i = 0; i < 2; ++i) {
      u32x4 v = *(const u32x4*)(otp + (4 * wave + h + 2 * i) * OSP + n * 16);
      __builtin_nontemporal_store(v, (u32x4*)(orow + ooff(7, i)));
    }
  }
}

}

extern "C" void kernel_launch(void* const* d_in, const int* in_sizes, int n_in, void* d_out, int out_size, void* d_ws,
                              size_t ws_size, hipStream_t stream) {
  const uint16_t* x = (const uint16_t*)d_in[0];
  const uint16_t* w1 = (const uint16_t*)d_in[1];
  const uint16_t* w2 = (const uint16_t*)d_in[2];
  const uint16_t* g = (const uint16_t*)d_in[3];
  const uint16_t* wf = (const uint16_t*)d_in[4];
  const uint16_t* wg = (const uint16_t*)d_in[5];
  const uint16_t* wo = (const uint16_t*)d_in[6];
  uint16_t* out = (uint16_t*)d_out;
#define WOFF 0
  char* ws = (char*)d_ws + ((size_t)WOFF << 20);
  char* u1 = ws;
  char* w1h = ws + (24 << 20);
  char* w2h = ws + (40 << 20);
  char* y1 = ws + (48 << 20);
  char* u2 = ws + (60 << 20);
  char* y2 = ws + (77 << 20);
  u32x4* wff = (u32x4*)(ws + (86 << 20));
  u32x4* wof = (u32x4*)(ws + (86 << 20) + 131072);
  uint32_t* ge = (uint32_t*)(ws + (86 << 20) + 262144);
  constexpr int G1LDS = 7 * 11 * 2048, G2LDS = 8 * 8 * 2048;

  (void)hipFuncSetAttribute((const void*)gemm_kernel<1>, hipFuncAttributeMaxDynamicSharedMemorySize, G1LDS);
  (void)hipFuncSetAttribute((const void*)gemm_kernel<2>, hipFuncAttributeMaxDynamicSharedMemorySize, G2LDS);
  (void)hipFuncSetAttribute((const void*)joint_kernel, hipFuncAttributeMaxDynamicSharedMemorySize, JOINT_LDS);

  pre_kernel<<<256, 512, 0, stream>>>(x, w1, w2, wf, wo, u1, w1h, w2h, wff, wof);
  gemm_kernel<1><<<256, 256, G1LDS, stream>>>(u1, w1h, y1, (const char*)wff);
  mid_kernel<<<256, 1024, 0, stream>>>(y1, u2, g, wg, ge);
  gemm_kernel<2><<<256, 256, G2LDS, stream>>>(u2, w2h, y2, (const char*)wff);
  joint_kernel<<<250, 512, JOINT_LDS, stream>>>(y2, wff, wof, (const uint32_t*)ge, out);
}
```

```cpp
#define DBG_DUMP
#include <hip/hip_runtime.h>
#include <stdint.h>

typedef __attribute__((ext_vector_type(8))) short bf16x8;
typedef __attribute__((ext_vector_type(16))) float f32x16;
typedef __attribute__((ext_vector_type(4))) float f32x4;
typedef __attribute__((ext_vector_type(2))) float f32x2;
typedef __attribute__((ext_vector_type(4))) unsigned int u32x4;
typedef __attribute__((ext_vector_type(2))) unsigned int u32x2;

#define GPRO 2
namespace {

constexpr int DCH = 256;

__device__ __forceinline__ float bf2f(uint32_t v) { return __uint_as_float(v << 16); }
__device__ __forceinline__ uint32_t f2bf(float f) {
  uint32_t u = __float_as_uint(f);
  u += 0x7FFF + ((u >> 16) & 1);
  return u >> 16;
}
__device__ __forceinline__ uint32_t pk2bf(float a, float b) {
  typedef __attribute__((ext_vector_type(2))) __bf16 b2;
  f32x2 v = {a, b};
  b2 r = __builtin_convertvector(v, b2);
  return __builtin_bit_cast(uint32_t, r);
}
__device__ __forceinline__ uint32_t gelu2(float a0, float a1) {
  const f32x2 x = {a0, a1};
  const f32x2 c0 = {-2.302208198144325f, -2.302208198144325f}, c1 = {-0.10294323970002349f, -0.10294323970002349f};
  const f32x2 z = x * __builtin_elementwise_fma(x * x, c1, c0);
  const f32x2 e = {__builtin_amdgcn_exp2f(z[0]), __builtin_amdgcn_exp2f(z[1])};
  const f32x2 d = e + f32x2{1.0f, 1.0f};
  const f32x2 y = x * f32x2{__builtin_amdgcn_rcpf(d[0]), __builtin_amdgcn_rcpf(d[1])};
  return pk2bf(y[0], y[1]);
}
__device__ __forceinline__ f32x16 mfma32(const u32x4& a, const u32x4& b, const f32x16& c) {
  return __builtin_amdgcn_mfma_f32_32x32x16_bf16(__builtin_bit_cast(bf16x8, a), __builtin_bit_cast(bf16x8, b), c, 0, 0, 0);
}
__device__ __forceinline__ f32x4 mfma16(const u32x4& a, const u32x4& b, const f32x4& c) {
  return __builtin_amdgcn_mfma_f32_16x16x32_bf16(__builtin_bit_cast(bf16x8, a), __builtin_bit_cast(bf16x8, b), c, 0, 0, 0);
}
__device__ __forceinline__ f32x16 zero16() {
  f32x16 z;
#pragma unroll
  for (int i = 0; i < 16; ++i) z[i] = 0.f;
  return z;
}
__device__ __forceinline__ __amdgpu_buffer_rsrc_t mkbuf(const void* p, int nbytes) {
  return __builtin_amdgcn_make_buffer_rsrc((void*)p, (short)0, nbytes, 0x00020000);
}
template <int V> struct IC { static constexpr int value = V; };
template <int I, int N, class F>
__device__ __forceinline__ void sfor(F&& f) {
  if constexpr (I < N) {
    f(IC<I>{});
    sfor<I + 1, N>(f);
  }
}
#define SB() __builtin_amdgcn_sched_barrier(0)
#define PHASE() do { SB(); asm volatile("s_waitcnt vmcnt(0)\n\ts_barrier" ::: "memory"); SB(); } while (0)
__device__ unsigned int g_st[5][4096][24];
#define ST_DECL unsigned int st_[24] = {}
#define ST_ARG , unsigned int (&st_)[24]
#define ST_PASS , st_
#define STAMP(k) do { SB(); st_[k] = (unsigned int)__builtin_amdgcn_s_memrealtime(); SB(); } while (0)
#define CSTAMP(k) do { SB(); st_[k] = (unsigned int)__builtin_amdgcn_s_memtime(); SB(); } while (0)
#define ST_OUT(kid, wid) do { asm volatile("s_waitcnt vmcnt(0)" ::: "memory"); STAMP(21); CSTAMP(23); if ((threadIdx.x & 63) == 0) { _Pragma("unroll") for (int i_ = 0; i_ < 24; ++i_) g_st[kid][wid][i_] = st_[i_]; } } while (0)

constexpr double PI_ = 3.14159265358979323846;
constexpr double c128(int m) {
  m = ((m % 128) + 128) % 128;
  if (m > 64) m = 128 - m;
  double sg = 1.0;
  if (m > 32) { m = 64 - m; sg = -1.0; }
  if (m == 32) return 0.0;
  const double a = PI_ * m / 64.0;
  double term = 1.0, sum = 1.0;
  for (int i = 1; i < 16; ++i) { term *= -a * a / ((2 * i - 1) * (2 * i)); sum += term; }
  return sg * sum;
}
constexpr double s128(int m) { return c128(m - 32); }
constexpr uint16_t cbf(double v) {
  uint32_t u = __builtin_bit_cast(uint32_t, (float)v);
  u += 0x7FFF + ((u >> 16) & 1);
  return (uint16_t)(u >> 16);
}
constexpr double cbf2d(uint16_t v) { return (double)__builtin_bit_cast(float, (uint32_t)v << 16); }
struct Tabs {
  uint16_t f1[2][3][64][8];
  uint16_t e1[2][2][64][8];
  uint16_t e2[2][64][8];
  uint16_t g1[64][8][8];
  uint16_t f2[64][8];
  uint16_t g2[16][32];
};
constexpr Tabs make_tabs() {
  Tabs t{};
  for (int l = 0; l < 64; ++l)
    for (int e = 0; e < 8; ++e) {
      const int h = l >> 5, f = l & 31, ri = f & 1;
      for (int fb = 0; fb < 2; ++fb) {
        const int k = 16 * fb + (f >> 1);
        for (int ks = 0; ks < 3; ++ks) {
          const int a = (2 * k + 1) * (16 * ks + 8 * h + e);
          t.f1[fb][ks][l][e] = cbf(ri ? -s128(a) : c128(a));
        }
        const int a = (2 * k + 1) * (8 * h + e);
        const double v = ri ? s128(a) : c128(a);
        t.e1[fb][0][l][e] = cbf(v);
        t.e1[fb][1][l][e] = cbf(v - cbf2d(cbf(v)));
      }
      const int a = 2 * (2 * (f >> 1) + 1) * (8 * h + e);
      const double v = ri ? s128(a) : c128(a);
      t.e2[0][l][e] = cbf(v);
      t.e2[1][l][e] = cbf(v - cbf2d(cbf(v)));
      t.f2[l][e] = cbf(ri ? -v : v);
    }
  for (int m = -15; m < 48; ++m)
    for (int f = 0; f < 64; ++f) {
      const int a = (2 * (f >> 1) + 1) * m;
      t.g1[m + 16][(f >> 3) ^ (((m + 16) >> 1) & 7)][f & 7] = cbf(((f & 1) ? -s128(a) : c128(a)) / 32.0);
    }
  for (int m = 0; m < 16; ++m)
    for (int f = 0; f < 32; ++f) {
      const int a = 2 * (2 * (f >> 1) + 1) * m;
      t.g2[m][f] = cbf(((f & 1) ? -s128(a) : c128(a)) / 16.0);
    }
  return t;
}
__device__ __attribute__((aligned(64))) const Tabs tabs = make_tabs();

constexpr int OOB = 0x7FFFFF00;
constexpr int WT = 16;
constexpr int NT = 2;
constexpr int R1 = 336;
constexpr int R2 = 504;
constexpr int PL1 = R1 * 128;
constexpr int PL2 = R2 * 128;
constexpr int U1BYTES = 32 * 16 * PL1, U2BYTES = 16 * 16 * PL2;
constexpr int WBIN = 16 * 16 * 2 * 1024;
constexpr int YP1 = R1 * 64;
constexpr int YP2 = 500 * 64;
constexpr int Y1BYTES = 32 * 16 * YP1, Y2BYTES = 16 * 16 * YP2;
__device__ __forceinline__ int dmap(int cb, int n) { return 64 * (cb >> 2) + 32 * (cb & 1) + 2 * n + ((cb >> 1) & 1); }

__device__ __forceinline__ void swap16(u32x4& a, u32x4& b) {
#pragma unroll
  for (int i = 0; i < 4; ++i) {
    const auto r = __builtin_amdgcn_permlane16_swap(a[i], b[i], false, false);
    a[i] = r[0];
    b[i] = r[1];
  }
}

template <int CONV>
__device__ __forceinline__ void wtrans(const uint16_t* __restrict__ w, char* __restrict__ wh, int wid, int lane) {
  const int dq = wid & 3, pc = wid >> 2;
  const int ks = pc >> 2, kq = pc & 3;
  const int n32 = lane & 31, h = lane >> 5;
  const int r = CONV == 1 ? ks >> 3 : kq >> 1;
  const int c0 = CONV == 1 ? (ks & 7) * 32 + kq * 8 : dmap(ks, 4 * (kq & 1));
  const __amdgpu_buffer_rsrc_t wr = mkbuf(w, 31 * 131072);
  const int vb = (16 * h + r + 1) * 131072 + c0 * 512 + dq * 128 + n32 * 4;
  const int vb0 = (h + r == 0) ? OOB : vb - 262144;
  constexpr int NFB = CONV == 1 ? 2 : 1;
  const u32x4* et = CONV == 1 ? (const u32x4*)&tabs.e1[0][0][0][0] : (const u32x4*)&tabs.e2[0][0][0];
  u32x4 E[NFB][2];
#pragma unroll
  for (int fb = 0; fb < NFB; ++fb) { E[fb][0] = et[fb * 128 + lane]; E[fb][1] = et[fb * 128 + 64 + lane]; }
  uint32_t v[8][8];
#pragma unroll
  for (int el = 0; el < 8; ++el)
#pragma unroll
    for (int jj = 0; jj < 8; ++jj) {
      const int co = CONV == 1 ? el * 512 : (el & 3) * 1024;
      const int so = (CONV == 1 ? 0 : (el >> 2) * 8192) + (jj ? (jj - 1) * 262144 : 0);
      v[el][jj] = __builtin_amdgcn_raw_buffer_load_b32(wr, (jj ? vb : vb0) + co, so, NT);
    }
  PHASE();
  const __amdgpu_buffer_rsrc_t dr = mkbuf(wh, (CONV == 1 ? 32 : 16) * WBIN);
  const int vo = 2 * h * WBIN + (4 * dq + (n32 >> 4)) * 32768 + ks * 2048 + kq * 256 + (n32 & 15) * 16;
#pragma unroll
  for (int e = 0; e < 2; ++e) {
    u32x4 b[8];
#pragma unroll
    for (int el = 0; el < 8; ++el)
#pragma unroll
      for (int j = 0; j < 4; ++j) b[el][j] = __builtin_amdgcn_perm(v[el][2 * j + 1], v[el][2 * j], e ? 0x07060302u : 0x05040100u);
#pragma unroll
    for (int fb = 0; fb < NFB; ++fb) {
      u32x4 o[16];
#pragma unroll
      for (int ep = 0; ep < 4; ++ep) {
        f32x16 d[2];
#pragma unroll
        for (int z = 0; z < 2; ++z) d[z] = mfma32(E[fb][1], b[2 * ep + z], zero16());
#pragma unroll
        for (int z = 0; z < 2; ++z) d[z] = mfma32(E[fb][0], b[2 * ep + z], d[z]);
#pragma unroll
        for (int i = 0; i < 16; ++i) o[i][ep] = pk2bf(d[0][i], d[1][i]);
      }
#pragma unroll
      for (int i = 0; i < 16; ++i)
        __builtin_amdgcn_raw_buffer_store_b128(o[i], dr, vo + (i & 1) * 1024,
                                               (16 * fb + 4 * (i >> 2) + ((i >> 1) & 1)) * WBIN + e * 65536, WT);
      SB();
    }
  }
}

__device__ __forceinline__ void xload(const uint16_t* __restrict__ x, int u, int lane, uint32_t (&v)[3][8]) {
  const int cq = u & 7, b = (u >> 3) & 1, sg = u >> 4;
  const __amdgpu_buffer_rsrc_t xr = mkbuf(x + (size_t)b * 16000 * DCH, 8192000);
#pragma unroll
  for (int ks = 0; ks < 3; ++ks) {
    const int row0 = 48 * sg - 24 + 16 * ks + 8 * (lane >> 5);
    const int vb = row0 >= 0 ? row0 * 1024 + cq * 128 + (lane & 31) * 4 : OOB;
#pragma unroll
    for (int e = 0; e < 8; ++e) v[ks][e] = __builtin_amdgcn_raw_buffer_load_b32(xr, vb + (e & 3) * 1024, (e >> 2) * 4096, NT);
  }
}
__device__ __forceinline__ void xtrans(char* __restrict__ u1, int u, int lane, const uint32_t (&v)[3][8], const u32x4 (&F)[2][3]) {
  const __amdgpu_buffer_rsrc_t ur = mkbuf(u1 + (2 * (u & 7)) * PL1 + (u >> 3) * 128, U1BYTES - (2 * (u & 7)) * PL1 - (u >> 3) * 128);
  const int vo = ((lane & 15) >> 1) * 16 * PL1 + ((lane >> 4) & 1) * 64 + (lane & 1) * 32 + (lane >> 5) * 16;
  u32x4 a[2][3];
#pragma unroll
  for (int ks = 0; ks < 3; ++ks)
#pragma unroll
    for (int j = 0; j < 4; ++j) {
      a[0][ks][j] = __builtin_amdgcn_perm(v[ks][2 * j + 1], v[ks][2 * j], 0x05040100u);
      a[1][ks][j] = __builtin_amdgcn_perm(v[ks][2 * j + 1], v[ks][2 * j], 0x07060302u);
    }
#pragma unroll
  for (int fb = 0; fb < 2; ++fb) {
    f32x16 d[2] = {zero16(), zero16()};
#pragma unroll
    for (int ks = 0; ks < 3; ++ks)
#pragma unroll
      for (int e = 0; e < 2; ++e) d[e] = mfma32(a[e][ks], F[fb][ks], d[e]);
#pragma unroll
    for (int qp = 0; qp < 2; ++qp) {
      u32x4 o[2];
#pragma unroll
      for (int z = 0; z < 2; ++z)
#pragma unroll
        for (int i = 0; i < 4; ++i) o[z][i] = pk2bf(d[0][8 * qp + 4 * z + i], d[1][8 * qp + 4 * z + i]);
      swap16(o[0], o[1]);
#pragma unroll
      for (int z = 0; z < 2; ++z) __builtin_amdgcn_raw_buffer_store_b128(o[z], ur, vo, ((fb * 16 + z * 8) * 16 + qp) * PL1, WT);
    }
  }
}

__global__ __launch_bounds__(512, 2) void pre_kernel(const uint16_t* __restrict__ x, const uint16_t* __restrict__ w1,
                                                     const uint16_t* __restrict__ w2, const uint16_t* __restrict__ wf,
                                                     const uint16_t* __restrict__ wo, char* __restrict__ u1,
                                                     char* __restrict__ w1h, char* __restrict__ w2h, u32x4* __restrict__ wff,
                                                     u32x4* __restrict__ wof) {
  ST_DECL; STAMP(0); CSTAMP(22);
  const int blk = blockIdx.x;
  const int tid = threadIdx.x, lane = tid & 63;
  const int wave = __builtin_amdgcn_readfirstlane(tid >> 6);
  const int n = lane & 31, h = lane >> 5;
  if (wave == 0) {
    wtrans<1>(w1, w1h, blk, lane);
  } else if (wave == 1) {
    wtrans<2>(w2, w2h, blk, lane);
  } else {
    const int bq = (blk & 7) * 32 + (blk >> 3);
    const int u0 = ((21 * bq) >> 1) + wave - 2, ue = (21 * (bq + 1)) >> 1;
    u32x4 F[2][3];
#pragma unroll
    for (int fb = 0; fb < 2; ++fb)
#pragma unroll
      for (int ks = 0; ks < 3; ++ks) F[fb][ks] = ((const u32x4*)&tabs.f1[fb][ks][0][0])[lane];
    uint32_t v0[3][8];
    xload(x, u0, lane, v0);
    if (u0 + 6 < ue) {
      uint32_t v1[3][8];
      xload(x, u0 + 6, lane, v1);
      PHASE();
      STAMP(1);
      xtrans(u1, u0, lane, v0, F);
      SB();
      xtrans(u1, u0 + 6, lane, v1, F);
    } else if (wave == 7) {
      const int s = blk & 15, eb = (blk >> 4) & 7, second = blk >> 7;
      const uint16_t* src = (second ? wo + (16 * s + 4 * h) * DCH : wf + dmap(s, 4 * h) * DCH) + 32 * eb + n;
      const int st = second ? DCH : 2 * DCH;
      uint32_t v[8];
#pragma unroll
      for (int j = 0; j < 8; ++j) v[j] = src[(8 * (j >> 2) + (j & 3)) * st];
      PHASE();
      xtrans(u1, u0, lane, v0, F);
      (second ? wof : wff)[(blk & 127) * 64 + lane] =
          u32x4{v[0] | (v[1] << 16), v[2] | (v[3] << 16), v[4] | (v[5] << 16), v[6] | (v[7] << 16)};
    } else {
      PHASE();
      xtrans(u1, u0, lane, v0, F);
    }
  }
  STAMP(19);
  ST_OUT(0, blk * 8 + wave);
}

typedef __attribute__((address_space(3))) u32x4 lds_u32x4;
__device__ __forceinline__ u32x4 lds_ld(int a) { return *(const lds_u32x4*)(uintptr_t)(uint32_t)a; }
__device__ __forceinline__ uint32_t lds_ld32(int a) { return *(const __attribute__((address_space(3))) uint32_t*)(uintptr_t)(uint32_t)a; }
template <int SZ>
__device__ __forceinline__ void dma(__amdgpu_buffer_rsrc_t rsrc, char* lds, int voff, int soff) {
#if defined(__HIP_DEVICE_COMPILE__)
  typedef __attribute__((address_space(3))) void* ldsp;
  if constexpr (SZ == 16) __builtin_amdgcn_raw_ptr_buffer_load_lds(rsrc, (ldsp)lds, 16, voff, soff, 0, 0);
  else __builtin_amdgcn_raw_ptr_buffer_load_lds(rsrc, (ldsp)lds, 4, voff, soff, 0, 0);
#endif
}

constexpr int issued(int g, int MB, int OPS, int NST, int BAR, int PRO) {
  int o = PRO * OPS;
  for (int t = 0; t < g; ++t)
    if (o < 16 * OPS && (o / OPS < NST || t >= (o / OPS - NST + 1) * MB + BAR)) ++o;
  return o;
}
template <int CONV>
__global__ __launch_bounds__(256, 1) void gemm_kernel(const char* __restrict__ U, const char* __restrict__ W,
                                                      char* __restrict__ Y, const char* __restrict__ pf) {
  constexpr int MB = CONV == 1 ? 11 : 8;
  constexpr int STG = MB * 2048;
  constexpr int NST = CONV == 1 ? 7 : 8;
  constexpr int PL = CONV == 1 ? PL1 : PL2;
  constexpr int NDMA = CONV == 1 ? 6 : 4;
  constexpr int OPS = NDMA + 2;
  extern __shared__ __attribute__((aligned(16))) char smem[];
  ST_DECL; STAMP(0); CSTAMP(22);
  const int bid = blockIdx.x, xcd = bid & 7, slot = bid >> 3;
  const int bin = CONV == 1 ? xcd * 4 + (slot >> 3) : xcd * 2 + (slot >> 4);
  const int mt = CONV == 1 ? (slot >> 2) & 1 : (slot >> 2) & 3;
  const int tid = threadIdx.x, lane = tid & 63;
  const int wave = __builtin_amdgcn_readfirstlane(tid >> 6);
  const int cb = (slot & 3) * 4 + wave;
  const int row0 = CONV == 1 ? mt * 168 : mt * 126;
  const __amdgpu_buffer_rsrc_t ur = mkbuf(U + (size_t)bin * 16 * PL + row0 * 128, 16 * PL - row0 * 128 + (CONV == 1 ? 0 : 256));
  const __amdgpu_buffer_rsrc_t wr = mkbuf(W + (size_t)bin * WBIN + cb * 32768, 32768);
  int vo[2];
  if constexpr (CONV == 1) {
    vo[0] = wave * 1024 + (lane >> 3) * 128 + (((lane & 7) ^ ((4 * wave + (lane >> 4)) & 7)) * 16);
    vo[1] = 20480 + wave * 256 + (lane >> 5) * 128 + ((((lane >> 2) & 7) ^ wave) * 16) + (lane & 3) * 4;
  } else {
#pragma unroll
    for (int z = 0; z < 2; ++z) vo[z] = wave * 4096 + (lane >> 3) * 128 + (((lane & 7) ^ (2 * wave + z)) * 16);
  }
  u32x4 B[NST][2];
  auto issue = [&](auto is, auto ii) {
    constexpr int s = decltype(is)::value, i = decltype(ii)::value;
    char* buf = smem + (s % NST) * STG;
    if constexpr (i < 2) {
      B[s % NST][i] = __builtin_amdgcn_raw_buffer_load_b128(wr, lane * 16 + i * 1024, s * 2048, 0);
    } else if constexpr (CONV == 1) {
      if constexpr (i < 7) dma<16>(ur, buf + wave * 1024 + (i - 2) * 4096, vo[0], s * PL + (i - 2) * 4096);
      else dma<4>(ur, buf + 20480 + wave * 256, vo[1], s * PL);
    } else {
      dma<16>(ur, buf + wave * 4096 + (i - 2) * 1024, vo[(i - 2) >> 1], s * PL + (i - 2) * 1024);
    }
  };
  constexpr int AR = 4, AL = 3;
  constexpr int BAR = MB - AL;
  constexpr int PRO = GPRO;
  sfor<0, PRO>([&](auto is) { sfor<0, OPS>([&](auto ii) { issue(is, ii); SB(); }); });
  const int rho = lane & 15, kq = lane >> 4;
  const int lds0 = (int)(uint32_t)(uintptr_t)(__attribute__((address_space(3))) char*)smem;
  constexpr int WIN = CONV == 1 ? 2 : 4;
  constexpr int NWIN = (NST + WIN - 1) / WIN;
  int ab[NWIN][2];
#pragma unroll
  for (int ri = 0; ri < 2; ++ri) {
    const int pos = ((kq >> 1) * 4 + ri * 2 + (kq & 1)) ^ (rho >> 1);
    const int a = lds0 + (CONV == 1 ? rho * 128 : (rho >> 1) * 2048 + (rho & 1) * 128) + pos * 16;
#pragma unroll
    for (int wn = 0; wn < NWIN; ++wn) { ab[wn][ri] = a + wn * WIN * STG; asm volatile("" : "+v"(ab[wn][ri])); }
  }
  auto aread = [&](int s, int mb, int ri) -> u32x4 {
    const int bf = s % NST;
    return lds_ld(ab[bf / WIN][ri] + (bf % WIN) * STG + mb * (CONV == 1 ? 2048 : 256));
  };
  f32x4 yr[MB], yi[MB];
#pragma unroll
  for (int mb = 0; mb < MB; ++mb) { yr[mb] = f32x4{0.f, 0.f, 0.f, 0.f}; yi[mb] = f32x4{0.f, 0.f, 0.f, 0.f}; }
  u32x4 A[AR][2];
  STAMP(1);
  asm volatile("s_waitcnt vmcnt(%0)" ::"n"((PRO - 1) * OPS) : "memory");
  __builtin_amdgcn_s_barrier();
  STAMP(2);
  asm volatile("" ::: "memory");
#pragma unroll
  for (int z = 0; z < AL; ++z) { A[z][0] = aread(0, z, 0); A[z][1] = aread(0, z, 1); }
  SB();
  u32x4 nwi;
#pragma unroll
  for (int j = 0; j < 4; ++j) nwi[j] = B[0][1][j] ^ 0x80008000u;
  sfor<0, 16>([&](auto is) {
    constexpr int s = decltype(is)::value;
    u32x4 nwn;
    sfor<0, MB>([&](auto im) {
      constexpr int mb = decltype(im)::value;
      constexpr int g = s * MB + mb, gn = g + AL;
      constexpr int o0 = issued(g, MB, OPS, NST, BAR, PRO), o1 = issued(g + 1, MB, OPS, NST, BAR, PRO);
      if constexpr (mb == BAR && s + 1 < 16) {
        static_assert(o0 >= (s + 2) * OPS, "next stage not requested yet");
        asm volatile("s_waitcnt vmcnt(%0)" ::"n"(o0 - (s + 2) * OPS) : "memory");
        __builtin_amdgcn_s_barrier();
        asm volatile("" ::: "memory");
        SB();
      }
      yr[mb] = mfma16(B[s % NST][0], A[g % AR][0], yr[mb]);
      SB();
      yr[mb] = mfma16(nwi, A[g % AR][1], yr[mb]);
      if constexpr (gn < 16 * MB) A[gn % AR][0] = aread(gn / MB, gn % MB, 0);
      SB();
      yi[mb] = mfma16(B[s % NST][1], A[g % AR][0], yi[mb]);
      if constexpr (o1 > o0) issue(IC<o0 / OPS>{}, IC<o0 % OPS>{});
      SB();
      yi[mb] = mfma16(B[s % NST][0], A[g % AR][1], yi[mb]);
      if constexpr (gn < 16 * MB) A[gn % AR][1] = aread(gn / MB, gn % MB, 1);
      if constexpr (mb == MB - 2 && s + 1 < 16) {
#pragma unroll
        for (int j = 0; j < 4; ++j) nwn[j] = B[(s + 1) % NST][1][j] ^ 0x80008000u;
      }
      SB();
    });
    if constexpr (s + 1 < 16) nwi = nwn;
    if constexpr (CONV == 2 && s == 14) {
      if (wave == 0) dma<4>(mkbuf(pf, 262144 + 32768), smem, slot * 8192 + lane * 128, 0);
      if (wave == 1 && slot < 4) dma<4>(mkbuf(pf, 262144 + 32768), smem, 262144 + slot * 8192 + lane * 128, 0);
      SB();
    }
    if (s == 0 || s == 5 || s == 10 || s == 15) STAMP(3 + s);
  });
  if constexpr (CONV == 1) {
    const __amdgpu_buffer_rsrc_t yb = mkbuf(Y + (size_t)(bin * 16 + cb) * YP1 + row0 * 64, 168 * 64);
#pragma unroll
    for (int mb = 0; mb < MB; ++mb) {
      u32x4 o;
#pragma unroll
      for (int i = 0; i < 4; ++i) o[i] = pk2bf(yr[mb][i], yi[mb][i]);
      __builtin_amdgcn_raw_buffer_store_b128(o, yb, rho * 64 + kq * 16 + (mb & 3) * 1024, (mb >> 2) * 4096, WT);
    }
  } else {
    const __amdgpu_buffer_rsrc_t yb = mkbuf(Y + (size_t)(bin * 16 + cb) * YP2 + row0 * 64, YP2 - row0 * 64);
    const float sg = (bin & 1) ? -1.f : 1.f;
    f32x4 nr, ni;
#pragma unroll
    for (int i = 0; i < 4; ++i) { nr[i] = __shfl_down(yr[0][i], 2, 16); ni[i] = __shfl_down(yi[0][i], 2, 16); }
    const int vy = (rho >> 1) * 1024 + (rho & 1) * 64 + kq * 16;
#pragma unroll
    for (int mb = 0; mb < MB; ++mb) {
      const f32x4 xr = mb < MB - 1 ? yr[mb < MB - 1 ? mb + 1 : 0] : nr, xi = mb < MB - 1 ? yi[mb < MB - 1 ? mb + 1 : 0] : ni;
      u32x4 o;
#pragma unroll
      for (int i = 0; i < 4; ++i) o[i] = pk2bf(__builtin_fmaf(sg, xi[i], yr[mb][i]), __builtin_fmaf(-sg, xr[i], yi[mb][i]));
      __builtin_amdgcn_raw_buffer_store_b128(o, yb, (mb == MB - 1 && rho >= 14) ? OOB : vy + mb * 128, 0, WT);
    }
  }
  STAMP(19);
  ST_OUT(CONV == 1 ? 1 : 3, bid * 4 + wave);
}

template <int Z>
__device__ __forceinline__ void mid_unit(char* __restrict__ U, int ya, int ga, int cb, int s, const u32x4& F ST_ARG) {
  const int lane = threadIdx.x & 63;
  const int n32 = lane & 31, h = lane >> 5;
  constexpr int NSG = Z == 0 ? 1 : 2;
  const int tau = 16 * ((n32 >> 2) & 1) + 8 * ((n32 >> 3) & 1) + 2 * (n32 & 3) + (n32 >> 4);
  const int r[2] = {Z == 0 ? tau + 16 : Z == 1 ? (tau < 16 ? tau + 48 : 0) : tau + 32, Z == 1 ? tau : (tau >= 16 ? tau - 16 : 0)};
  f32x16 d = zero16();
#pragma unroll
  for (int sg = 0; sg < NSG; ++sg)
#pragma unroll
    for (int ks = 0; ks < 4; ++ks) {
      u32x4 yv;
#pragma unroll
      for (int j = 0; j < 4; ++j) yv[j] = lds_ld32(ya + sg * 4096 + ks * 1024 + j * 128);
      d = mfma32(lds_ld(ga + r[sg] * 128 + (((2 * ks + h) ^ ((r[sg] >> 1) & 7)) * 16)), yv, d);
    }
  asm volatile("s_nop 0" : "+v"(d[0]));
  STAMP(4);
  const __amdgpu_buffer_rsrc_t ub = mkbuf(U, U2BYTES);
  const int vu = (((lane & 15) >> 1) * 16 + cb) * PL2 + s * 256 + ((lane >> 4) & 1) * 64 + (lane & 1) * 32 + h * 16;
  const int t0r = 32 * s - 16 + 16 * h;
  const bool ok = t0r >= 0 && t0r < 8000;
  f32x16 u[2];
#pragma unroll
  for (int q = 0; q < 2; ++q) {
    u32x4 a;
#pragma unroll
    for (int j = 0; j < 4; ++j) {
      const uint32_t gv = gelu2(d[8 * q + 2 * j], d[8 * q + 2 * j + 1]);
      a[j] = ok ? gv : 0u;
    }
    u[q] = mfma32(a, F, zero16());
  }
#pragma unroll
  for (int b = 0; b < 2; ++b) {
    u32x4 o[2];
#pragma unroll
    for (int q = 0; q < 2; ++q)
#pragma unroll
      for (int j = 0; j < 4; ++j) o[q][j] = pk2bf(u[q][8 * b + 2 * j], u[q][8 * b + 2 * j + 1]);
    swap16(o[0], o[1]);
#pragma unroll
    for (int z = 0; z < 2; ++z) __builtin_amdgcn_raw_buffer_store_b128(o[z], ub, vu + b * 128, z * 8 * 16 * PL2, WT);
  }
}
__global__ __launch_bounds__(1024) void mid_kernel(const char* __restrict__ Y, char* __restrict__ U, const uint16_t* __restrict__ g,
                                                   const uint16_t* __restrict__ wg, uint32_t* __restrict__ ge) {
  __shared__ __attribute__((aligned(16))) char sm[8192 + 49152];
  __shared__ uint32_t landed;
  ST_DECL; STAMP(0); CSTAMP(22);
  const int blk = blockIdx.x, lane = threadIdx.x & 63;
  const int wave = __builtin_amdgcn_readfirstlane(threadIdx.x >> 6);
  const int cb = blk & 15, s0 = blk & ~15, s = s0 + wave;
  const int sg0 = (2 * s0) / 3;
  if (wave < 4) {
    const __amdgpu_buffer_rsrc_t tb = mkbuf(&tabs.g1[0][0][0], 8192);
#pragma unroll
    for (int i = 0; i < 2; ++i) dma<16>(tb, sm + wave * 2048 + i * 1024, lane * 16, wave * 2048 + i * 1024);
  }
  {
    const __amdgpu_buffer_rsrc_t yb = mkbuf(Y, Y1BYTES);
    const int vy = ((lane >> 3) * 16 + cb) * YP1 + sg0 * 128 + (lane & 7) * 16;
#pragma unroll
    for (int i = 0; i < 3; ++i) {
      const int pc = 3 * wave + i;
      dma<16>(yb, sm + 8192 + pc * 1024, vy, (pc & 3) * 128 * YP1 + (pc >> 2) * 128);
    }
  }
  const u32x4 F = ((const u32x4*)&tabs.f2[0][0])[lane];
  if (threadIdx.x == 0) landed = 0;
  asm volatile("s_waitcnt lgkmcnt(0)\n\ts_barrier" ::: "memory");
  STAMP(1);
  if (s >= 251 && wave == 15 && cb < 8) {
    const int c0 = 32 * cb + 2 * (lane & 15), kq = lane >> 4;
    uint32_t v[8][8];
    u32x4 ga[8];
#pragma unroll
    for (int ks = 0; ks < 8; ++ks) {
      ga[ks] = *(const u32x4*)(g + (lane & 15) * DCH + ks * 32 + 8 * kq);
#pragma unroll
      for (int e = 0; e < 8; ++e) v[ks][e] = *(const uint32_t*)(wg + (ks * 32 + 8 * kq + e) * DCH + c0);
    }
    SB();
    f32x4 d[2] = {{0.f, 0.f, 0.f, 0.f}, {0.f, 0.f, 0.f, 0.f}};
#pragma unroll
    for (int ks = 0; ks < 8; ++ks)
#pragma unroll
      for (int e = 0; e < 2; ++e) {
        u32x4 bb;
#pragma unroll
        for (int j = 0; j < 4; ++j) bb[j] = __builtin_amdgcn_perm(v[ks][2 * j + 1], v[ks][2 * j], e ? 0x07060302u : 0x05040100u);
        d[e] = mfma16(ga[ks], bb, d[e]);
      }
#pragma unroll
    for (int i = 0; i < 4; ++i) {
      u32x2 o;
#pragma unroll
      for (int e = 0; e < 2; ++e) {
        const float gs = bf2f(f2bf(d[e][i])) * 2.885390081777927f;
        const uint32_t hi = f2bf(gs), lo = f2bf(gs - bf2f(hi));
        o[e] = hi | (lo << 16);
      }
      *(u32x2*)(ge + (4 * kq + i) * DCH + c0) = o;
    }
  } else {
    asm volatile("s_waitcnt vmcnt(0)" ::: "memory");
    if (lane == 0) __hip_atomic_fetch_or(&landed, 1u << wave, __ATOMIC_RELAXED, __HIP_MEMORY_SCOPE_WORKGROUP);
    STAMP(2);
    if (s < 251) {
      const int p = s / 3, z = s - 3 * p;
      const int l0 = 2 * p + (z == 2) - sg0, l1 = l0 + (z != 0);
      const uint32_t need = 15u | (((2u << ((4 * l1 + 3) / 3)) - 1u) & ~((1u << ((4 * l0) / 3)) - 1u));
      while ((__builtin_amdgcn_readfirstlane(__hip_atomic_load(&landed, __ATOMIC_RELAXED, __HIP_MEMORY_SCOPE_WORKGROUP)) & need) != need)
        __builtin_amdgcn_s_sleep(1);
      asm volatile("" ::: "memory");
      STAMP(3);
      const int l = (int)(uint32_t)(uintptr_t)(__attribute__((address_space(3))) char*)sm;
      const int ya = l + 8192 + l0 * 4096 + (lane >> 5) * 512 + (lane & 31) * 4;
      if (z == 0) mid_unit<0>(U, ya, l, cb, s, F ST_PASS);
      else if (z == 1) mid_unit<1>(U, ya, l, cb, s, F ST_PASS);
      else mid_unit<2>(U, ya, l, cb, s, F ST_PASS);
    }
  }
  STAMP(19);
  ST_OUT(2, blk * 16 + wave);
}

constexpr int J_F = 0;
constexpr int NPRE = 2;
constexpr int NTB = NPRE + 1;
constexpr int J_TB = 16384;
constexpr int OSP = 528;
constexpr int J_OST = J_TB + NTB * 16384;
constexpr int JOINT_LDS = J_OST + 2 * 32 * OSP;

__device__ __forceinline__ float tanh_pre(float s) {
  return __builtin_fmaf(-2.0f, __builtin_amdgcn_rcpf(__builtin_amdgcn_exp2f(s) + 1.0f), 1.0f);
}

__global__ __launch_bounds__(512, 2) void joint_kernel(const char* __restrict__ Y, const u32x4* __restrict__ wff,
                                                       const u32x4* __restrict__ wof, const uint32_t* __restrict__ geb,
                                                       uint16_t* __restrict__ out) {
  extern __shared__ __attribute__((aligned(16))) char smem[];
  constexpr int T2 = 4000;
  ST_DECL; STAMP(0); CSTAMP(22);
  const int grp = blockIdx.x, t0 = grp * 16;
  const int tid = threadIdx.x;
  const int lane = tid & 63;
  const int wave = __builtin_amdgcn_readfirstlane(tid >> 6);
  const int n = lane & 31, h = lane >> 5;
  const __amdgpu_buffer_rsrc_t yb = mkbuf(Y, Y2BYTES);
  const int vy = (4 * h * 16 + 2 * wave) * YP2 + grp * 128 + n * 4;
  u32x4 yv[2][2];
#pragma unroll
  for (int c = 0; c < 2; ++c)
#pragma unroll
    for (int ks = 0; ks < 2; ++ks)
#pragma unroll
      for (int j = 0; j < 4; ++j) yv[c][ks][j] = __builtin_amdgcn_raw_buffer_load_b32(yb, vy, ((8 * ks + j) * 16 + c) * YP2, 0);
  u32x4 gi[2];
#pragma unroll
  for (int ks = 0; ks < 2; ++ks) gi[ks] = *(const u32x4*)&tabs.g2[n & 15][16 * ks + 8 * h];
  u32x4 ga[2];
#pragma unroll
  for (int b = 0; b < 2; ++b)
#pragma unroll
    for (int i = 0; i < 4; ++i) ga[b][i] = geb[(b * 8 + 4 * h + i) * 256 + 32 * wave + n];
  const u32x4* wfp = wff + wave * 16 * 64 + lane;
  const u32x4* wop = wof + wave * 16 * 64 + lane;
  u32x4 wa[16], wo[16];
#pragma unroll
  for (int s = 0; s < 8; ++s) wa[s] = wfp[s * 64];
  SB();
  {
    f32x16 d = zero16();
#pragma unroll
    for (int c = 0; c < 2; ++c)
#pragma unroll
      for (int ks = 0; ks < 2; ++ks) d = mfma32(yv[c][ks], (n >> 4) == c ? gi[ks] : u32x4{0u, 0u, 0u, 0u}, d);
#pragma unroll
    for (int b = 0; b < 2; ++b) {
      u32x4 o;
#pragma unroll
      for (int j = 0; j < 4; ++j) o[j] = gelu2(d[8 * b + 2 * j], d[8 * b + 2 * j + 1]);
      *(u32x4*)(smem + J_F + (2 * wave + (n >> 4)) * 1024 + (h * 32 + b * 16 + (n & 15)) * 16) = o;
    }
  }
  STAMP(1);
  __syncthreads();
  STAMP(2);
  f32x16 fe = zero16();
#pragma unroll
  for (int s = 0; s < 16; ++s) {
    const u32x4 fb = *(const u32x4*)(smem + J_F + s * 1024 + lane * 16);
    fe = mfma32(fb, wa[s], fe);
    if (s < 8) wa[s + 8] = wfp[(s + 8) * 64];
    else if ((s & 1) == 0) wo[(s - 8) >> 1] = wop[((s - 8) >> 1) * 64];
    SB();
  }
  STAMP(3);
  u32x4 gsel;
#pragma unroll
  for (int i = 0; i < 4; ++i) gsel[i] = (4 * h + i == (n & 7)) ? 0x3F803F80u : 0u;
  f32x16 gacc = mfma32(ga[0], gsel, zero16());
  const float c2 = 2.885390081777927f;
  u32x4 xa[4];
#pragma unroll
  for (int q = 0; q < 4; ++q) {
#pragma unroll
    for (int p = 0; p < 2; ++p) {
      const float v0 = fe[4 * q + 2 * p] * c2, v1 = fe[4 * q + 2 * p + 1] * c2;
      const uint32_t hi = pk2bf(v0, v1);
      xa[q][p] = hi;
      xa[q][2 + p] = pk2bf(v0 - __uint_as_float(hi << 16), v1 - __uint_as_float(hi & 0xffff0000u));
    }
  }
  u32x4 sel[2];
  {
    const int i = n >> 3;
    const uint32_t one = (i & 1) ? 0x3F800000u : 0x3F80u;
    const uint32_t d0 = i < 2 ? one : 0u, d1 = i < 2 ? 0u : one;
#pragma unroll
    for (int hh = 0; hh < 2; ++hh) sel[hh] = h == hh ? u32x4{d0, d1, d0, d1} : u32x4{0u, 0u, 0u, 0u};
  }
  uint16_t* orow = out + ((size_t)t0 * 8 + 4 * wave + h) * DCH + n * 8;
  auto ooff = [&](int u, int i) -> size_t { return ((size_t)(u >> 2) * (T2 * 8) + (u & 3) * 32 + 2 * i) * DCH; };
  auto add_ge = [&](int g) -> f32x16 {
    if (g == 4) gacc = mfma32(ga[1], gsel, zero16());
    return mfma32(xa[g >> 1], sel[g & 1], gacc);
  };
  char* const tbw = smem + J_TB + (2 * wave * 64 + lane) * 16;
  const char* const tbr = smem + J_TB + lane * 16;

#pragma unroll
  for (int u = 0; u < NPRE; ++u) {
    const f32x16 s = add_ge(u);
    u32x4 p0, p1;
#pragma unroll
    for (int p = 0; p < 4; ++p) {
      p0[p] = pk2bf(tanh_pre(s[2 * p]), tanh_pre(s[2 * p + 1]));
      if (u < 2 && p < 3) wo[4 + u * 6 + p] = wop[(4 + u * 6 + p) * 64];
      SB();
      p1[p] = pk2bf(tanh_pre(s[8 + 2 * p]), tanh_pre(s[8 + 2 * p + 1]));
      if (u < 2 && p < 3) wo[7 + u * 6 + p] = wop[(7 + u * 6 + p) * 64];
      SB();
    }
    *(u32x4*)(tbw + u * 16384) = p0;
    *(u32x4*)(tbw + u * 16384 + 1024) = p1;
  }
  f32x16 sn = add_ge(NPRE);
  STAMP(4);
  __syncthreads();
  STAMP(5);
  constexpr int PRE = 6;
  u32x4 bt[16];
#pragma unroll
  for (int s2 = 0; s2 < PRE; ++s2) bt[s2] = *(const u32x4*)(tbr + s2 * 1024);
#pragma unroll
  for (int u = 0; u < 8; ++u) {
    const char* tb = tbr + (u % NTB) * 16384;
    const char* tbn = tbr + ((u + 1) % NTB) * 16384;
    u32x4 orv[2];
    if (u > 0) {
      const char* otp = smem + J_OST + ((u + 1) & 1) * (32 * OSP);
#pragma unroll
      for (int i = 0; i < 2; ++i) orv[i] = *(const u32x4*)(otp + (4 * wave + h + 2 * i) * OSP + n * 16);
    }
    f32x16 acc = zero16();
    float th[16];
    u32x4 p0, p1;
    u32x4 btn[PRE];
    SB();
#pragma unroll
    for (int s2 = 0; s2 < 16; ++s2) {
      acc = mfma32(wo[s2], bt[s2], acc);
      if (s2 + PRE < 16) bt[s2 + PRE] = *(const u32x4*)(tb + (s2 + PRE) * 1024);
      else if (u < 7) btn[s2 + PRE - 16] = *(const u32x4*)(tbn + (s2 + PRE - 16) * 1024);
      if ((s2 == 3 || s2 == 5) && u > 0) {
        const int i = (s2 - 3) >> 1;
        __builtin_nontemporal_store(orv[i], (u32x4*)(orow + ooff(u - 1, i)));
      }
      if (u + NPRE < 8) {
        constexpr int NPAIR = 8;
        const int g = s2 >> 1;
        if ((s2 & 1) == 0) {
          if (g < NPAIR) { th[2 * g] = __builtin_amdgcn_exp2f(sn[2 * g]); th[2 * g + 1] = __builtin_amdgcn_exp2f(sn[2 * g + 1]); }
          if (g >= 1) { th[2 * g - 2] = __builtin_fmaf(-2.0f, th[2 * g - 2], 1.0f); th[2 * g - 1] = __builtin_fmaf(-2.0f, th[2 * g - 1], 1.0f); }
        } else {
          const f32x2 a01 = f32x2{th[2 * g], th[2 * g + 1]} + f32x2{1.0f, 1.0f};
          const float a0 = a01[0], a1 = a01[1];
          if (g >= 1) {
            const uint32_t pk = pk2bf(th[2 * g - 2], th[2 * g - 1]);
            if (g - 1 < 4) p0[g - 1] = pk; else p1[g - 5] = pk;
          }
          th[2 * g] = __builtin_amdgcn_rcpf(a0); th[2 * g + 1] = __builtin_amdgcn_rcpf(a1);
        }
        if (s2 == 10) *(u32x4*)(tbw + ((u + NPRE) % NTB) * 16384) = p0;
      }
      SB();
    }
    if (u + 1 + NPRE < 8) sn = add_ge(u + 1 + NPRE);
    if (u + NPRE < 8) p1[3] = pk2bf(__builtin_fmaf(-2.0f, th[14], 1.0f), __builtin_fmaf(-2.0f, th[15], 1.0f));
    if (u + NPRE < 8) *(u32x4*)(tbw + ((u + NPRE) % NTB) * 16384 + 1024) = p1;
    char* ot = smem + J_OST + (u & 1) * (32 * OSP) + n * OSP + wave * 64;
#pragma unroll
    for (int g = 0; g < 4; ++g) {
      u32x2 pk = {pk2bf(acc[4 * g], acc[4 * g + 1]), pk2bf(acc[4 * g + 2], acc[4 * g + 3])};
      *(u32x2*)(ot + (8 * g + 4 * h) * 2) = pk;
    }
#pragma unroll
    for (int s2 = 0; s2 < PRE; ++s2) bt[s2] = btn[s2];
    __syncthreads();
    STAMP(6 + u);
  }
  {
    const char* otp = smem + J_OST + 1 * (32 * OSP);
#pragma unroll
    for (int i = 0; i < 2; ++i) {
      u32x4 v = *(const u32x4*)(otp + (4 * wave + h + 2 * i) * OSP + n * 16);
      __builtin_nontemporal_store(v, (u32x4*)(orow + ooff(7, i)));
    }
  }
  STAMP(19);
  ST_OUT(4, grp * 8 + wave);
}

__global__ __launch_bounds__(1024) void probe_kernel() {
  __shared__ unsigned int ra[1024], rb[1024];
  unsigned int ref = g_st[0][0][0];
  unsigned int a = 0 ? 0u : ~0u, b = 0 ? 0u : ~0u;
  for (int w = threadIdx.x; w < 4096; w += 1024) {
    const unsigned int va = g_st[0][w][0], vb = g_st[0][w][0];
    if (va) { const unsigned int d = va - ref + 100000u; a = 0 ? max(a, d) : min(a, d); }
    if (vb) { const unsigned int d = vb - ref + 100000u; b = 0 ? max(b, d) : min(b, d); }
  }
  ra[threadIdx.x] = a; rb[threadIdx.x] = b;
  __syncthreads();
  if (threadIdx.x == 0) {
    for (int i = 1; i < 1024; ++i) { a = 0 ? max(a, ra[i]) : min(a, ra[i]); b = 0 ? max(b, rb[i]) : min(b, rb[i]); }
    unsigned int span = b - a;
    if (span > 20000u) span = 20000u;
    const unsigned long long t0 = __builtin_amdgcn_s_memrealtime();
    while (__builtin_amdgcn_s_memrealtime() - t0 < 10ull * span) __builtin_amdgcn_s_sleep(2);
  }
}

}

extern "C" void kernel_launch(void* const* d_in, const int* in_sizes, int n_in, void* d_out, int out_size, void* d_ws,
                              size_t ws_size, hipStream_t stream) {
  const uint16_t* x = (const uint16_t*)d_in[0];
  const uint16_t* w1 = (const uint16_t*)d_in[1];
  const uint16_t* w2 = (const uint16_t*)d_in[2];
  const uint16_t* g = (const uint16_t*)d_in[3];
  const uint16_t* wf = (const uint16_t*)d_in[4];
  const uint16_t* wg = (const uint16_t*)d_in[5];
  const uint16_t* wo = (const uint16_t*)d_in[6];
  uint16_t* out = (uint16_t*)d_out;
#define WOFF 0
  char* ws = (char*)d_ws + ((size_t)WOFF << 20);
  char* u1 = ws;
  char* w1h = ws + (24 << 20);
  char* w2h = ws + (40 << 20);
  char* y1 = ws + (48 << 20);
  char* u2 = ws + (60 << 20);
  char* y2 = ws + (77 << 20);
  u32x4* wff = (u32x4*)(ws + (86 << 20));
  u32x4* wof = (u32x4*)(ws + (86 << 20) + 131072);
  uint32_t* ge = (uint32_t*)(ws + (86 << 20) + 262144);
  constexpr int G1LDS = 7 * 11 * 2048, G2LDS = 8 * 8 * 2048;

  (void)hipFuncSetAttribute((const void*)gemm_kernel<1>, hipFuncAttributeMaxDynamicSharedMemorySize, G1LDS);
  (void)hipFuncSetAttribute((const void*)gemm_kernel<2>, hipFuncAttributeMaxDynamicSharedMemorySize, G2LDS);
  (void)hipFuncSetAttribute((const void*)joint_kernel, hipFuncAttributeMaxDynamicSharedMemorySize, JOINT_LDS);

  pre_kernel<<<256, 512, 0, stream>>>(x, w1, w2, wf, wo, u1, w1h, w2h, wff, wof);
  gemm_kernel<1><<<256, 256, G1LDS, stream>>>(u1, w1h, y1, (const char*)wff);
  mid_kernel<<<256, 1024, 0, stream>>>(y1, u2, g, wg, ge);
  gemm_kernel<2><<<256, 256, G2LDS, stream>>>(u2, w2h, y2, (const char*)wff);
  joint_kernel<<<250, 512, JOINT_LDS, stream>>>(y2, wff, wof, (const uint32_t*)ge, out);
  probe_kernel<<<1, 1024, 0, stream>>>();
}
```

```cpp
#define PF1 7
#include <hip/hip_runtime.h>
#include <stdint.h>

typedef __attribute__((ext_vector_type(8))) short bf16x8;
typedef __attribute__((ext_vector_type(16))) float f32x16;
typedef __attribute__((ext_vector_type(4))) float f32x4;
typedef __attribute__((ext_vector_type(2))) float f32x2;
typedef __attribute__((ext_vector_type(4))) unsigned int u32x4;
typedef __attribute__((ext_vector_type(2))) unsigned int u32x2;

#define GPRO 2
namespace {

constexpr int DCH = 256;

__device__ __forceinline__ float bf2f(uint32_t v) { return __uint_as_float(v << 16); }
__device__ __forceinline__ uint32_t f2bf(float f) {
  uint32_t u = __float_as_uint(f);
  u += 0x7FFF + ((u >> 16) & 1);
  return u >> 16;
}
__device__ __forceinline__ uint32_t pk2bf(float a, float b) {
  typedef __attribute__((ext_vector_type(2))) __bf16 b2;
  f32x2 v = {a, b};
  b2 r = __builtin_convertvector(v, b2);
  return __builtin_bit_cast(uint32_t, r);
}
__device__ __forceinline__ uint32_t gelu2(float a0, float a1) {
  const f32x2 x = {a0, a1};
  const f32x2 c0 = {-2.302208198144325f, -2.302208198144325f}, c1 = {-0.10294323970002349f, -0.10294323970002349f};
  const f32x2 z = x * __builtin_elementwise_fma(x * x, c1, c0);
  const f32x2 e = {__builtin_amdgcn_exp2f(z[0]), __builtin_amdgcn_exp2f(z[1])};
  const f32x2 d = e + f32x2{1.0f, 1.0f};
  const f32x2 y = x * f32x2{__builtin_amdgcn_rcpf(d[0]), __builtin_amdgcn_rcpf(d[1])};
  return pk2bf(y[0], y[1]);
}
__device__ __forceinline__ f32x16 mfma32(const u32x4& a, const u32x4& b, const f32x16& c) {
  return __builtin_amdgcn_mfma_f32_32x32x16_bf16(__builtin_bit_cast(bf16x8, a), __builtin_bit_cast(bf16x8, b), c, 0, 0, 0);
}
__device__ __forceinline__ f32x4 mfma16(const u32x4& a, const u32x4& b, const f32x4& c) {
  return __builtin_amdgcn_mfma_f32_16x16x32_bf16(__builtin_bit_cast(bf16x8, a), __builtin_bit_cast(bf16x8, b), c, 0, 0, 0);
}
__device__ __forceinline__ f32x16 zero16() {
  f32x16 z;
#pragma unroll
  for (int i = 0; i < 16; ++i) z[i] = 0.f;
  return z;
}
__device__ __forceinline__ __amdgpu_buffer_rsrc_t mkbuf(const void* p, int nbytes) {
  return __builtin_amdgcn_make_buffer_rsrc((void*)p, (short)0, nbytes, 0x00020000);
}
template <int V> struct IC { static constexpr int value = V; };
template <int I, int N, class F>
__device__ __forceinline__ void sfor(F&& f) {
  if constexpr (I < N) {
    f(IC<I>{});
    sfor<I + 1, N>(f);
  }
}
#define SB() __builtin_amdgcn_sched_barrier(0)
#define PHASE() do { SB(); asm volatile("s_waitcnt vmcnt(0)\n\ts_barrier" ::: "memory"); SB(); } while (0)

constexpr double PI_ = 3.14159265358979323846;
constexpr double c128(int m) {
  m = ((m % 128) + 128) % 128;
  if (m > 64) m = 128 - m;
  double sg = 1.0;
  if (m > 32) { m = 64 - m; sg = -1.0; }
  if (m == 32) return 0.0;
  const double a = PI_ * m / 64.0;
  double term = 1.0, sum = 1.0;
  for (int i = 1; i < 16; ++i) { term *= -a * a / ((2 * i - 1) * (2 * i)); sum += term; }
  return sg * sum;
}
constexpr double s128(int m) { return c128(m - 32); }
constexpr uint16_t cbf(double v) {
  uint32_t u = __builtin_bit_cast(uint32_t, (float)v);
  u += 0x7FFF + ((u >> 16) & 1);
  return (uint16_t)(u >> 16);
}
constexpr double cbf2d(uint16_t v) { return (double)__builtin_bit_cast(float, (uint32_t)v << 16); }
struct Tabs {
  uint16_t f1[2][3][64][8];
  uint16_t e1[2][2][64][8];
  uint16_t e2[2][64][8];
  uint16_t g1[64][8][8];
  uint16_t f2[64][8];
  uint16_t g2[16][32];
};
constexpr Tabs make_tabs() {
  Tabs t{};
  for (int l = 0; l < 64; ++l)
    for (int e = 0; e < 8; ++e) {
      const int h = l >> 5, f = l & 31, ri = f & 1;
      for (int fb = 0; fb < 2; ++fb) {
        const int k = 16 * fb + (f >> 1);
        for (int ks = 0; ks < 3; ++ks) {
          const int a = (2 * k + 1) * (16 * ks + 8 * h + e);
          t.f1[fb][ks][l][e] = cbf(ri ? -s128(a) : c128(a));
        }
        const int a = (2 * k + 1) * (8 * h + e);
        const double v = ri ? s128(a) : c128(a);
        t.e1[fb][0][l][e] = cbf(v);
        t.e1[fb][1][l][e] = cbf(v - cbf2d(cbf(v)));
      }
      const int a = 2 * (2 * (f >> 1) + 1) * (8 * h + e);
      const double v = ri ? s128(a) : c128(a);
      t.e2[0][l][e] = cbf(v);
      t.e2[1][l][e] = cbf(v - cbf2d(cbf(v)));
      t.f2[l][e] = cbf(ri ? -v : v);
    }
  for (int m = -15; m < 48; ++m)
    for (int f = 0; f < 64; ++f) {
      const int a = (2 * (f >> 1) + 1) * m;
      t.g1[m + 16][(f >> 3) ^ (((m + 16) >> 1) & 7)][f & 7] = cbf(((f & 1) ? -s128(a) : c128(a)) / 32.0);
    }
  for (int m = 0; m < 16; ++m)
    for (int f = 0; f < 32; ++f) {
      const int a = 2 * (2 * (f >> 1) + 1) * m;
      t.g2[m][f] = cbf(((f & 1) ? -s128(a) : c128(a)) / 16.0);
    }
  return t;
}
__device__ __attribute__((aligned(64))) const Tabs tabs = make_tabs();

constexpr int OOB = 0x7FFFFF00;
constexpr int WT = 16;
constexpr int NT = 2;
constexpr int R1 = 336;
constexpr int R2 = 504;
constexpr int PL1 = R1 * 128;
constexpr int PL2 = R2 * 128;
constexpr int U1BYTES = 32 * 16 * PL1, U2BYTES = 16 * 16 * PL2;
constexpr int WBIN = 16 * 16 * 2 * 1024;
constexpr int YP1 = R1 * 64;
constexpr int YP2 = 500 * 64;
constexpr int Y1BYTES = 32 * 16 * YP1, Y2BYTES = 16 * 16 * YP2;
__device__ __forceinline__ int dmap(int cb, int n) { return 64 * (cb >> 2) + 32 * (cb & 1) + 2 * n + ((cb >> 1) & 1); }

__device__ __forceinline__ void swap16(u32x4& a, u32x4& b) {
#pragma unroll
  for (int i = 0; i < 4; ++i) {
    const auto r = __builtin_amdgcn_permlane16_swap(a[i], b[i], false, false);
    a[i] = r[0];
    b[i] = r[1];
  }
}

typedef __attribute__((address_space(3))) u32x4 lds_u32x4;
__device__ __forceinline__ u32x4 lds_ld(int a) { return *(const lds_u32x4*)(uintptr_t)(uint32_t)a; }
__device__ __forceinline__ uint32_t lds_ld32(int a) { return *(const __attribute__((address_space(3))) uint32_t*)(uintptr_t)(uint32_t)a; }
template <int SZ>
__device__ __forceinline__ void dma(__amdgpu_buffer_rsrc_t rsrc, char* lds, int voff, int soff) {
#if defined(__HIP_DEVICE_COMPILE__)
  typedef __attribute__((address_space(3))) void* ldsp;
  if constexpr (SZ == 16) __builtin_amdgcn_raw_ptr_buffer_load_lds(rsrc, (ldsp)lds, 16, voff, soff, 0, 0);
  else __builtin_amdgcn_raw_ptr_buffer_load_lds(rsrc, (ldsp)lds, 4, voff, soff, 0, 0);
#endif
}

#define LABEL_ADDR(p, name) asm volatile("s_getpc_b64 s[40:41]\n\ts_add_u32 s40, s40, " name "@rel32@lo+4\n\ts_addc_u32 s41, s41, " name "@rel32@hi+12\n\ts_mov_b64 %0, s[40:41]" : "=s"(p) :: "s40", "s41", "scc")
__device__ __forceinline__ void code_pf(const char* p, int n, int part, char* lds) {
  dma<4>(mkbuf(p, n), lds, part * 8192 + (threadIdx.x & 63) * 128, 0);
}

template <int CONV>
__device__ __forceinline__ void wtrans(const uint16_t* __restrict__ w, char* __restrict__ wh, int wid, int lane) {
  const int dq = wid & 3, pc = wid >> 2;
  const int ks = pc >> 2, kq = pc & 3;
  const int n32 = lane & 31, h = lane >> 5;
  const int r = CONV == 1 ? ks >> 3 : kq >> 1;
  const int c0 = CONV == 1 ? (ks & 7) * 32 + kq * 8 : dmap(ks, 4 * (kq & 1));
  const __amdgpu_buffer_rsrc_t wr = mkbuf(w, 31 * 131072);
  const int vb = (16 * h + r + 1) * 131072 + c0 * 512 + dq * 128 + n32 * 4;
  const int vb0 = (h + r == 0) ? OOB : vb - 262144;
  constexpr int NFB = CONV == 1 ? 2 : 1;
  const u32x4* et = CONV == 1 ? (const u32x4*)&tabs.e1[0][0][0][0] : (const u32x4*)&tabs.e2[0][0][0];
  u32x4 E[NFB][2];
#pragma unroll
  for (int fb = 0; fb < NFB; ++fb) { E[fb][0] = et[fb * 128 + lane]; E[fb][1] = et[fb * 128 + 64 + lane]; }
  uint32_t v[8][8];
#pragma unroll
  for (int el = 0; el < 8; ++el)
#pragma unroll
    for (int jj = 0; jj < 8; ++jj) {
      const int co = CONV == 1 ? el * 512 : (el & 3) * 1024;
      const int so = (CONV == 1 ? 0 : (el >> 2) * 8192) + (jj ? (jj - 1) * 262144 : 0);
      v[el][jj] = __builtin_amdgcn_raw_buffer_load_b32(wr, (jj ? vb : vb0) + co, so, NT);
    }
  PHASE();
  const __amdgpu_buffer_rsrc_t dr = mkbuf(wh, (CONV == 1 ? 32 : 16) * WBIN);
  const int vo = 2 * h * WBIN + (4 * dq + (n32 >> 4)) * 32768 + ks * 2048 + kq * 256 + (n32 & 15) * 16;
#pragma unroll
  for (int e = 0; e < 2; ++e) {
    u32x4 b[8];
#pragma unroll
    for (int el = 0; el < 8; ++el)
#pragma unroll
      for (int j = 0; j < 4; ++j) b[el][j] = __builtin_amdgcn_perm(v[el][2 * j + 1], v[el][2 * j], e ? 0x07060302u : 0x05040100u);
#pragma unroll
    for (int fb = 0; fb < NFB; ++fb) {
      u32x4 o[16];
#pragma unroll
      for (int ep = 0; ep < 4; ++ep) {
        f32x16 d[2];
#pragma unroll
        for (int z = 0; z < 2; ++z) d[z] = mfma32(E[fb][1], b[2 * ep + z], zero16());
#pragma unroll
        for (int z = 0; z < 2; ++z) d[z] = mfma32(E[fb][0], b[2 * ep + z], d[z]);
#pragma unroll
        for (int i = 0; i < 16; ++i) o[i][ep] = pk2bf(d[0][i], d[1][i]);
      }
#pragma unroll
      for (int i = 0; i < 16; ++i)
        __builtin_amdgcn_raw_buffer_store_b128(o[i], dr, vo + (i & 1) * 1024,
                                               (16 * fb + 4 * (i >> 2) + ((i >> 1) & 1)) * WBIN + e * 65536, WT);
      SB();
    }
  }
}

__device__ __forceinline__ void xload(const uint16_t* __restrict__ x, int u, int lane, uint32_t (&v)[3][8]) {
  const int cq = u & 7, b = (u >> 3) & 1, sg = u >> 4;
  const __amdgpu_buffer_rsrc_t xr = mkbuf(x + (size_t)b * 16000 * DCH, 8192000);
#pragma unroll
  for (int ks = 0; ks < 3; ++ks) {
    const int row0 = 48 * sg - 24 + 16 * ks + 8 * (lane >> 5);
    const int vb = row0 >= 0 ? row0 * 1024 + cq * 128 + (lane & 31) * 4 : OOB;
#pragma unroll
    for (int e = 0; e < 8; ++e) v[ks][e] = __builtin_amdgcn_raw_buffer_load_b32(xr, vb + (e & 3) * 1024, (e >> 2) * 4096, NT);
  }
}
__device__ __forceinline__ void xtrans(char* __restrict__ u1, int u, int lane, const uint32_t (&v)[3][8], const u32x4 (&F)[2][3]) {
  const __amdgpu_buffer_rsrc_t ur = mkbuf(u1 + (2 * (u & 7)) * PL1 + (u >> 3) * 128, U1BYTES - (2 * (u & 7)) * PL1 - (u >> 3) * 128);
  const int vo = ((lane & 15) >> 1) * 16 * PL1 + ((lane >> 4) & 1) * 64 + (lane & 1) * 32 + (lane >> 5) * 16;
  u32x4 a[2][3];
#pragma unroll
  for (int ks = 0; ks < 3; ++ks)
#pragma unroll
    for (int j = 0; j < 4; ++j) {
      a[0][ks][j] = __builtin_amdgcn_perm(v[ks][2 * j + 1], v[ks][2 * j], 0x05040100u);
      a[1][ks][j] = __builtin_amdgcn_perm(v[ks][2 * j + 1], v[ks][2 * j], 0x07060302u);
    }
#pragma unroll
  for (int fb = 0; fb < 2; ++fb) {
    f32x16 d[2] = {zero16(), zero16()};
#pragma unroll
    for (int ks = 0; ks < 3; ++ks)
#pragma unroll
      for (int e = 0; e < 2; ++e) d[e] = mfma32(a[e][ks], F[fb][ks], d[e]);
#pragma unroll
    for (int qp = 0; qp < 2; ++qp) {
      u32x4 o[2];
#pragma unroll
      for (int z = 0; z < 2; ++z)
#pragma unroll
        for (int i = 0; i < 4; ++i) o[z][i] = pk2bf(d[0][8 * qp + 4 * z + i], d[1][8 * qp + 4 * z + i]);
      swap16(o[0], o[1]);
#pragma unroll
      for (int z = 0; z < 2; ++z) __builtin_amdgcn_raw_buffer_store_b128(o[z], ur, vo, ((fb * 16 + z * 8) * 16 + qp) * PL1, WT);
    }
  }
}

__global__ __launch_bounds__(512, 2) void pre_kernel(const uint16_t* __restrict__ x, const uint16_t* __restrict__ w1,
                                                     const uint16_t* __restrict__ w2, const uint16_t* __restrict__ wf,
                                                     const uint16_t* __restrict__ wo, char* __restrict__ u1,
                                                     char* __restrict__ w1h, char* __restrict__ w2h, u32x4* __restrict__ wff,
                                                     u32x4* __restrict__ wof) {
  const int blk = blockIdx.x;
  const int tid = threadIdx.x, lane = tid & 63;
  const int wave = __builtin_amdgcn_readfirstlane(tid >> 6);
  const int n = lane & 31, h = lane >> 5;
  __shared__ char pfs[256];
  if (wave == PF1) {
    const char *p0 = (const char*)&tabs - 320, *p1;
    LABEL_ADDR(p1, ".Lce_g2");
    code_pf(p0, (int)(p1 - p0), blk >> 3, pfs);
  }
  if (wave == 0) {
    wtrans<1>(w1, w1h, blk, lane);
  } else if (wave == 1) {
    wtrans<2>(w2, w2h, blk, lane);
  } else {
    const int bq = (blk & 7) * 32 + (blk >> 3);
    const int u0 = ((21 * bq) >> 1) + wave - 2, ue = (21 * (bq + 1)) >> 1;
    u32x4 F[2][3];
#pragma unroll
    for (int fb = 0; fb < 2; ++fb)
#pragma unroll
      for (int ks = 0; ks < 3; ++ks) F[fb][ks] = ((const u32x4*)&tabs.f1[fb][ks][0][0])[lane];
    uint32_t v0[3][8];
    xload(x, u0, lane, v0);
    if (u0 + 6 < ue) {
      uint32_t v1[3][8];
      xload(x, u0 + 6, lane, v1);
      PHASE();
      xtrans(u1, u0, lane, v0, F);
      SB();
      xtrans(u1, u0 + 6, lane, v1, F);
    } else if (wave == 7) {
      const int s = blk & 15, eb = (blk >> 4) & 7, second = blk >> 7;
      const uint16_t* src = (second ? wo + (16 * s + 4 * h) * DCH : wf + dmap(s, 4 * h) * DCH) + 32 * eb + n;
      const int st = second ? DCH : 2 * DCH;
      uint32_t v[8];
#pragma unroll
      for (int j = 0; j < 8; ++j) v[j] = src[(8 * (j >> 2) + (j & 3)) * st];
      PHASE();
      xtrans(u1, u0, lane, v0, F);
      (second ? wof : wff)[(blk & 127) * 64 + lane] =
          u32x4{v[0] | (v[1] << 16), v[2] | (v[3] << 16), v[4] | (v[5] << 16), v[6] | (v[7] << 16)};
    } else {
      PHASE();
      xtrans(u1, u0, lane, v0, F);
    }
  }
}

constexpr int issued(int g, int MB, int OPS, int NST, int BAR, int PRO) {
  int o = PRO * OPS;
  for (int t = 0; t < g; ++t)
    if (o < 16 * OPS && (o / OPS < NST || t >= (o / OPS - NST + 1) * MB + BAR)) ++o;
  return o;
}
template <int CONV>
__global__ __launch_bounds__(256, 1) void gemm_kernel(const char* __restrict__ U, const char* __restrict__ W,
                                                      char* __restrict__ Y, const char* __restrict__ pf) {
  constexpr int MB = CONV == 1 ? 11 : 8;
  constexpr int STG = MB * 2048;
  constexpr int NST = CONV == 1 ? 7 : 8;
  constexpr int PL = CONV == 1 ? PL1 : PL2;
  constexpr int NDMA = CONV == 1 ? 6 : 4;
  constexpr int OPS = NDMA + 2;
  extern __shared__ __attribute__((aligned(16))) char smem[];
  const int bid = blockIdx.x, xcd = bid & 7, slot = bid >> 3;
  const int bin = CONV == 1 ? xcd * 4 + (slot >> 3) : xcd * 2 + (slot >> 4);
  const int mt = CONV == 1 ? (slot >> 2) & 1 : (slot >> 2) & 3;
  const int tid = threadIdx.x, lane = tid & 63;
  const int wave = __builtin_amdgcn_readfirstlane(tid >> 6);
  const int cb = (slot & 3) * 4 + wave;
  const int row0 = CONV == 1 ? mt * 168 : mt * 126;
  const __amdgpu_buffer_rsrc_t ur = mkbuf(U + (size_t)bin * 16 * PL + row0 * 128, 16 * PL - row0 * 128 + (CONV == 1 ? 0 : 256));
  const __amdgpu_buffer_rsrc_t wr = mkbuf(W + (size_t)bin * WBIN + cb * 32768, 32768);
  int vo[2];
  if constexpr (CONV == 1) {
    vo[0] = wave * 1024 + (lane >> 3) * 128 + (((lane & 7) ^ ((4 * wave + (lane >> 4)) & 7)) * 16);
    vo[1] = 20480 + wave * 256 + (lane >> 5) * 128 + ((((lane >> 2) & 7) ^ wave) * 16) + (lane & 3) * 4;
  } else {
#pragma unroll
    for (int z = 0; z < 2; ++z) vo[z] = wave * 4096 + (lane >> 3) * 128 + (((lane & 7) ^ (2 * wave + z)) * 16);
  }
  u32x4 B[NST][2];
  auto issue = [&](auto is, auto ii) {
    constexpr int s = decltype(is)::value, i = decltype(ii)::value;
    char* buf = smem + (s % NST) * STG;
    if constexpr (i < 2) {
      B[s % NST][i] = __builtin_amdgcn_raw_buffer_load_b128(wr, lane * 16 + i * 1024, s * 2048, 0);
    } else if constexpr (CONV == 1) {
      if constexpr (i < 7) dma<16>(ur, buf + wave * 1024 + (i - 2) * 4096, vo[0], s * PL + (i - 2) * 4096);
      else dma<4>(ur, buf + 20480 + wave * 256, vo[1], s * PL);
    } else {
      dma<16>(ur, buf + wave * 4096 + (i - 2) * 1024, vo[(i - 2) >> 1], s * PL + (i - 2) * 1024);
    }
  };
  constexpr int AR = 4, AL = 3;
  constexpr int BAR = MB - AL;
  constexpr int PRO = GPRO;
  sfor<0, PRO>([&](auto is) { sfor<0, OPS>([&](auto ii) { issue(is, ii); SB(); }); });
  const int rho = lane & 15, kq = lane >> 4;
  const int lds0 = (int)(uint32_t)(uintptr_t)(__attribute__((address_space(3))) char*)smem;
  constexpr int WIN = CONV == 1 ? 2 : 4;
  constexpr int NWIN = (NST + WIN - 1) / WIN;
  int ab[NWIN][2];
#pragma unroll
  for (int ri = 0; ri < 2; ++ri) {
    const int pos = ((kq >> 1) * 4 + ri * 2 + (kq & 1)) ^ (rho >> 1);
    const int a = lds0 + (CONV == 1 ? rho * 128 : (rho >> 1) * 2048 + (rho & 1) * 128) + pos * 16;
#pragma unroll
    for (int wn = 0; wn < NWIN; ++wn) { ab[wn][ri] = a + wn * WIN * STG; asm volatile("" : "+v"(ab[wn][ri])); }
  }
  auto aread = [&](int s, int mb, int ri) -> u32x4 {
    const int bf = s % NST;
    return lds_ld(ab[bf / WIN][ri] + (bf % WIN) * STG + mb * (CONV == 1 ? 2048 : 256));
  };
  f32x4 yr[MB], yi[MB];
#pragma unroll
  for (int mb = 0; mb < MB; ++mb) { yr[mb] = f32x4{0.f, 0.f, 0.f, 0.f}; yi[mb] = f32x4{0.f, 0.f, 0.f, 0.f}; }
  u32x4 A[AR][2];
  asm volatile("s_waitcnt vmcnt(%0)" ::"n"((PRO - 1) * OPS) : "memory");
  __builtin_amdgcn_s_barrier();
  asm volatile("" ::: "memory");
#pragma unroll
  for (int z = 0; z < AL; ++z) { A[z][0] = aread(0, z, 0); A[z][1] = aread(0, z, 1); }
  SB();
  u32x4 nwi;
#pragma unroll
  for (int j = 0; j < 4; ++j) nwi[j] = B[0][1][j] ^ 0x80008000u;
  sfor<0, 16>([&](auto is) {
    constexpr int s = decltype(is)::value;
    u32x4 nwn;
    sfor<0, MB>([&](auto im) {
      constexpr int mb = decltype(im)::value;
      constexpr int g = s * MB + mb, gn = g + AL;
      constexpr int o0 = issued(g, MB, OPS, NST, BAR, PRO), o1 = issued(g + 1, MB, OPS, NST, BAR, PRO);
      if constexpr (mb == BAR && s + 1 < 16) {
        static_assert(o0 >= (s + 2) * OPS, "next stage not requested yet");
        asm volatile("s_waitcnt vmcnt(%0)" ::"n"(o0 - (s + 2) * OPS) : "memory");
        __builtin_amdgcn_s_barrier();
        asm volatile("" ::: "memory");
        SB();
      }
      yr[mb] = mfma16(B[s % NST][0], A[g % AR][0], yr[mb]);
      SB();
      yr[mb] = mfma16(nwi, A[g % AR][1], yr[mb]);
      if constexpr (gn < 16 * MB) A[gn % AR][0] = aread(gn / MB, gn % MB, 0);
      SB();
      yi[mb] = mfma16(B[s % NST][1], A[g % AR][0], yi[mb]);
      if constexpr (o1 > o0) issue(IC<o0 / OPS>{}, IC<o0 % OPS>{});
      SB();
      yi[mb] = mfma16(B[s % NST][0], A[g % AR][1], yi[mb]);
      if constexpr (gn < 16 * MB) A[gn % AR][1] = aread(gn / MB, gn % MB, 1);
      if constexpr (mb == MB - 2 && s + 1 < 16) {
#pragma unroll
        for (int j = 0; j < 4; ++j) nwn[j] = B[(s + 1) % NST][1][j] ^ 0x80008000u;
      }
      SB();
    });
    if constexpr (s + 1 < 16) nwi = nwn;
  });
  if constexpr (CONV == 1) {
    const __amdgpu_buffer_rsrc_t yb = mkbuf(Y + (size_t)(bin * 16 + cb) * YP1 + row0 * 64, 168 * 64);
#pragma unroll
    for (int mb = 0; mb < MB; ++mb) {
      u32x4 o;
#pragma unroll
      for (int i = 0; i < 4; ++i) o[i] = pk2bf(yr[mb][i], yi[mb][i]);
      __builtin_amdgcn_raw_buffer_store_b128(o, yb, rho * 64 + kq * 16 + (mb & 3) * 1024, (mb >> 2) * 4096, WT);
    }
  } else {
    const __amdgpu_buffer_rsrc_t yb = mkbuf(Y + (size_t)(bin * 16 + cb) * YP2 + row0 * 64, YP2 - row0 * 64);
    const float sg = (bin & 1) ? -1.f : 1.f;
    f32x4 nr, ni;
#pragma unroll
    for (int i = 0; i < 4; ++i) { nr[i] = __shfl_down(yr[0][i], 2, 16); ni[i] = __shfl_down(yi[0][i], 2, 16); }
    const int vy = (rho >> 1) * 1024 + (rho & 1) * 64 + kq * 16;
#pragma unroll
    for (int mb = 0; mb < MB; ++mb) {
      const f32x4 xr = mb < MB - 1 ? yr[mb < MB - 1 ? mb + 1 : 0] : nr, xi = mb < MB - 1 ? yi[mb < MB - 1 ? mb + 1 : 0] : ni;
      u32x4 o;
#pragma unroll
      for (int i = 0; i < 4; ++i) o[i] = pk2bf(__builtin_fmaf(sg, xi[i], yr[mb][i]), __builtin_fmaf(-sg, xr[i], yi[mb][i]));
      __builtin_amdgcn_raw_buffer_store_b128(o, yb, (mb == MB - 1 && rho >= 14) ? OOB : vy + mb * 128, 0, WT);
    }
  }
  if constexpr (CONV == 1) asm volatile(".Lce_g1:"); else asm volatile(".Lce_g2:");
}

template <int Z>
__device__ __forceinline__ void mid_unit(char* __restrict__ U, int ya, int ga, int cb, int s, const u32x4& F) {
  const int lane = threadIdx.x & 63;
  const int n32 = lane & 31, h = lane >> 5;
  constexpr int NSG = Z == 0 ? 1 : 2;
  const int tau = 16 * ((n32 >> 2) & 1) + 8 * ((n32 >> 3) & 1) + 2 * (n32 & 3) + (n32 >> 4);
  const int r[2] = {Z == 0 ? tau + 16 : Z == 1 ? (tau < 16 ? tau + 48 : 0) : tau + 32, Z == 1 ? tau : (tau >= 16 ? tau - 16 : 0)};
  f32x16 d = zero16();
#pragma unroll
  for (int sg = 0; sg < NSG; ++sg)
#pragma unroll
    for (int ks = 0; ks < 4; ++ks) {
      u32x4 yv;
#pragma unroll
      for (int j = 0; j < 4; ++j) yv[j] = lds_ld32(ya + sg * 4096 + ks * 1024 + j * 128);
      d = mfma32(lds_ld(ga + r[sg] * 128 + (((2 * ks + h) ^ ((r[sg] >> 1) & 7)) * 16)), yv, d);
    }
  const __amdgpu_buffer_rsrc_t ub = mkbuf(U, U2BYTES);
  const int vu = (((lane & 15) >> 1) * 16 + cb) * PL2 + s * 256 + ((lane >> 4) & 1) * 64 + (lane & 1) * 32 + h * 16;
  const int t0r = 32 * s - 16 + 16 * h;
  const bool ok = t0r >= 0 && t0r < 8000;
  f32x16 u[2];
#pragma unroll
  for (int q = 0; q < 2; ++q) {
    u32x4 a;
#pragma unroll
    for (int j = 0; j < 4; ++j) {
      const uint32_t gv = gelu2(d[8 * q + 2 * j], d[8 * q + 2 * j + 1]);
      a[j] = ok ? gv : 0u;
    }
    u[q] = mfma32(a, F, zero16());
  }
#pragma unroll
  for (int b = 0; b < 2; ++b) {
    u32x4 o[2];
#pragma unroll
    for (int q = 0; q < 2; ++q)
#pragma unroll
      for (int j = 0; j < 4; ++j) o[q][j] = pk2bf(u[q][8 * b + 2 * j], u[q][8 * b + 2 * j + 1]);
    swap16(o[0], o[1]);
#pragma unroll
    for (int z = 0; z < 2; ++z) __builtin_amdgcn_raw_buffer_store_b128(o[z], ub, vu + b * 128, z * 8 * 16 * PL2, WT);
  }
}
__global__ __launch_bounds__(1024) void mid_kernel(const char* __restrict__ Y, char* __restrict__ U, const uint16_t* __restrict__ g,
                                                   const uint16_t* __restrict__ wg, uint32_t* __restrict__ ge) {
  __shared__ __attribute__((aligned(16))) char sm[8192 + 49152];
  __shared__ uint32_t landed;
  const int blk = blockIdx.x, lane = threadIdx.x & 63;
  const int wave = __builtin_amdgcn_readfirstlane(threadIdx.x >> 6);
  const int cb = blk & 15, s0 = blk & ~15, s = s0 + wave;
  const int sg0 = (2 * s0) / 3;
  if (wave < 4) {
    const __amdgpu_buffer_rsrc_t tb = mkbuf(&tabs.g1[0][0][0], 8192);
#pragma unroll
    for (int i = 0; i < 2; ++i) dma<16>(tb, sm + wave * 2048 + i * 1024, lane * 16, wave * 2048 + i * 1024);
  }
  {
    const __amdgpu_buffer_rsrc_t yb = mkbuf(Y, Y1BYTES);
    const int vy = ((lane >> 3) * 16 + cb) * YP1 + sg0 * 128 + (lane & 7) * 16;
#pragma unroll
    for (int i = 0; i < 3; ++i) {
      const int pc = 3 * wave + i;
      dma<16>(yb, sm + 8192 + pc * 1024, vy, (pc & 3) * 128 * YP1 + (pc >> 2) * 128);
    }
  }
  const u32x4 F = ((const u32x4*)&tabs.f2[0][0])[lane];
  if (threadIdx.x == 0) landed = 0;
  asm volatile("s_waitcnt lgkmcnt(0)\n\ts_barrier" ::: "memory");
  if (s >= 251 && wave == 15 && cb < 8) {
    const int c0 = 32 * cb + 2 * (lane & 15), kq = lane >> 4;
    uint32_t v[8][8];
    u32x4 ga[8];
#pragma unroll
    for (int ks = 0; ks < 8; ++ks) {
      ga[ks] = *(const u32x4*)(g + (lane & 15) * DCH + ks * 32 + 8 * kq);
#pragma unroll
      for (int e = 0; e < 8; ++e) v[ks][e] = *(const uint32_t*)(wg + (ks * 32 + 8 * kq + e) * DCH + c0);
    }
    SB();
    f32x4 d[2] = {{0.f, 0.f, 0.f, 0.f}, {0.f, 0.f, 0.f, 0.f}};
#pragma unroll
    for (int ks = 0; ks < 8; ++ks)
#pragma unroll
      for (int e = 0; e < 2; ++e) {
        u32x4 bb;
#pragma unroll
        for (int j = 0; j < 4; ++j) bb[j] = __builtin_amdgcn_perm(v[ks][2 * j + 1], v[ks][2 * j], e ? 0x07060302u : 0x05040100u);
        d[e] = mfma16(ga[ks], bb, d[e]);
      }
#pragma unroll
    for (int i = 0; i < 4; ++i) {
      u32x2 o;
#pragma unroll
      for (int e = 0; e < 2; ++e) {
        const float gs = bf2f(f2bf(d[e][i])) * 2.885390081777927f;
        const uint32_t hi = f2bf(gs), lo = f2bf(gs - bf2f(hi));
        o[e] = hi | (lo << 16);
      }
      *(u32x2*)(ge + (4 * kq + i) * DCH + c0) = o;
    }
  } else {
    asm volatile("s_waitcnt vmcnt(0)" ::: "memory");
    if (lane == 0) __hip_atomic_fetch_or(&landed, 1u << wave, __ATOMIC_RELAXED, __HIP_MEMORY_SCOPE_WORKGROUP);
    if (s < 251) {
      const int p = s / 3, z = s - 3 * p;
      const int l0 = 2 * p + (z == 2) - sg0, l1 = l0 + (z != 0);
      const uint32_t need = 15u | (((2u << ((4 * l1 + 3) / 3)) - 1u) & ~((1u << ((4 * l0) / 3)) - 1u));
      while ((__builtin_amdgcn_readfirstlane(__hip_atomic_load(&landed, __ATOMIC_RELAXED, __HIP_MEMORY_SCOPE_WORKGROUP)) & need) != need)
        __builtin_amdgcn_s_sleep(1);
      asm volatile("" ::: "memory");
      const int l = (int)(uint32_t)(uintptr_t)(__attribute__((address_space(3))) char*)sm;
      const int ya = l + 8192 + l0 * 4096 + (lane >> 5) * 512 + (lane & 31) * 4;
      if (z == 0) mid_unit<0>(U, ya, l, cb, s, F);
      else if (z == 1) mid_unit<1>(U, ya, l, cb, s, F);
      else mid_unit<2>(U, ya, l, cb, s, F);
    }
  }
}

constexpr int J_F = 0;
constexpr int NPRE = 2;
constexpr int NTB = NPRE + 1;
constexpr int J_TB = 16384;
constexpr int OSP = 528;
constexpr int J_OST = J_TB + NTB * 16384;
constexpr int JOINT_LDS = J_OST + 2 * 32 * OSP;

__device__ __forceinline__ float tanh_pre(float s) {
  return __builtin_fmaf(-2.0f, __builtin_amdgcn_rcpf(__builtin_amdgcn_exp2f(s) + 1.0f), 1.0f);
}

__global__ __launch_bounds__(512, 2) void joint_kernel(const char* __restrict__ Y, const u32x4* __restrict__ wff,
                                                       const u32x4* __restrict__ wof, const uint32_t* __restrict__ geb,
                                                       uint16_t* __restrict__ out) {
  extern __shared__ __attribute__((aligned(16))) char smem[];
  constexpr int T2 = 4000;
  const int grp = blockIdx.x, t0 = grp * 16;
  const int tid = threadIdx.x;
  const int lane = tid & 63;
  const int wave = __builtin_amdgcn_readfirstlane(tid >> 6);
  const int n = lane & 31, h = lane >> 5;
  const __amdgpu_buffer_rsrc_t yb = mkbuf(Y, Y2BYTES);
  const int vy = (4 * h * 16 + 2 * wave) * YP2 + grp * 128 + n * 4;
  u32x4 yv[2][2];
#pragma unroll
  for (int c = 0; c < 2; ++c)
#pragma unroll
    for (int ks = 0; ks < 2; ++ks)
#pragma unroll
      for (int j = 0; j < 4; ++j) yv[c][ks][j] = __builtin_amdgcn_raw_buffer_load_b32(yb, vy, ((8 * ks + j) * 16 + c) * YP2, 0);
  u32x4 gi[2];
#pragma unroll
  for (int ks = 0; ks < 2; ++ks) gi[ks] = *(const u32x4*)&tabs.g2[n & 15][16 * ks + 8 * h];
  u32x4 ga[2];
#pragma unroll
  for (int b = 0; b < 2; ++b)
#pragma unroll
    for (int i = 0; i < 4; ++i) ga[b][i] = geb[(b * 8 + 4 * h + i) * 256 + 32 * wave + n];
  const u32x4* wfp = wff + wave * 16 * 64 + lane;
  const u32x4* wop = wof + wave * 16 * 64 + lane;
  u32x4 wa[16], wo[16];
#pragma unroll
  for (int s = 0; s < 8; ++s) wa[s] = wfp[s * 64];
  SB();
  {
    f32x16 d = zero16();
#pragma unroll
    for (int c = 0; c < 2; ++c)
#pragma unroll
      for (int ks = 0; ks < 2; ++ks) d = mfma32(yv[c][ks], (n >> 4) == c ? gi[ks] : u32x4{0u, 0u, 0u, 0u}, d);
#pragma unroll
    for (int b = 0; b < 2; ++b) {
      u32x4 o;
#pragma unroll
      for (int j = 0; j < 4; ++j) o[j] = gelu2(d[8 * b + 2 * j], d[8 * b + 2 * j + 1]);
      *(u32x4*)(smem + J_F + (2 * wave + (n >> 4)) * 1024 + (h * 32 + b * 16 + (n & 15)) * 16) = o;
    }
  }
  __syncthreads();
  f32x16 fe = zero16();
#pragma unroll
  for (int s = 0; s < 16; ++s) {
    const u32x4 fb = *(const u32x4*)(smem + J_F + s * 1024 + lane * 16);
    fe = mfma32(fb, wa[s], fe);
    if (s < 8) wa[s + 8] = wfp[(s + 8) * 64];
    else if ((s & 1) == 0) wo[(s - 8) >> 1] = wop[((s - 8) >> 1) * 64];
    SB();
  }
  u32x4 gsel;
#pragma unroll
  for (int i = 0; i < 4; ++i) gsel[i] = (4 * h + i == (n & 7)) ? 0x3F803F80u : 0u;
  f32x16 gacc = mfma32(ga[0], gsel, zero16());
  const float c2 = 2.885390081777927f;
  u32x4 xa[4];
#pragma unroll
  for (int q = 0; q < 4; ++q) {
#pragma unroll
    for (int p = 0; p < 2; ++p) {
      const float v0 = fe[4 * q + 2 * p] * c2, v1 = fe[4 * q + 2 * p + 1] * c2;
      const uint32_t hi = pk2bf(v0, v1);
      xa[q][p] = hi;
      xa[q][2 + p] = pk2bf(v0 - __uint_as_float(hi << 16), v1 - __uint_as_float(hi & 0xffff0000u));
    }
  }
  u32x4 sel[2];
  {
    const int i = n >> 3;
    const uint32_t one = (i & 1) ? 0x3F800000u : 0x3F80u;
    const uint32_t d0 = i < 2 ? one : 0u, d1 = i < 2 ? 0u : one;
#pragma unroll
    for (int hh = 0; hh < 2; ++hh) sel[hh] = h == hh ? u32x4{d0, d1, d0, d1} : u32x4{0u, 0u, 0u, 0u};
  }
  uint16_t* orow = out + ((size_t)t0 * 8 + 4 * wave + h) * DCH + n * 8;
  auto ooff = [&](int u, int i) -> size_t { return ((size_t)(u >> 2) * (T2 * 8) + (u & 3) * 32 + 2 * i) * DCH; };
  auto add_ge = [&](int g) -> f32x16 {
    if (g == 4) gacc = mfma32(ga[1], gsel, zero16());
    return mfma32(xa[g >> 1], sel[g & 1], gacc);
  };
  char* const tbw = smem + J_TB + (2 * wave * 64 + lane) * 16;
  const char* const tbr = smem + J_TB + lane * 16;

#pragma unroll
  for (int u = 0; u < NPRE; ++u) {
    const f32x16 s = add_ge(u);
    u32x4 p0, p1;
#pragma unroll
    for (int p = 0; p < 4; ++p) {
      p0[p] = pk2bf(tanh_pre(s[2 * p]), tanh_pre(s[2 * p + 1]));
      if (u < 2 && p < 3) wo[4 + u * 6 + p] = wop[(4 + u * 6 + p) * 64];
      SB();
      p1[p] = pk2bf(tanh_pre(s[8 + 2 * p]), tanh_pre(s[8 + 2 * p + 1]));
      if (u < 2 && p < 3) wo[7 + u * 6 + p] = wop[(7 + u * 6 + p) * 64];
      SB();
    }
    *(u32x4*)(tbw + u * 16384) = p0;
    *(u32x4*)(tbw + u * 16384 + 1024) = p1;
  }
  f32x16 sn = add_ge(NPRE);
  __syncthreads();
  constexpr int PRE = 6;
  u32x4 bt[16];
#pragma unroll
  for (int s2 = 0; s2 < PRE; ++s2) bt[s2] = *(const u32x4*)(tbr + s2 * 1024);
#pragma unroll
  for (int u = 0; u < 8; ++u) {
    const char* tb = tbr + (u % NTB) * 16384;
    const char* tbn = tbr + ((u + 1) % NTB) * 16384;
    u32x4 orv[2];
    if (u > 0) {
      const char* otp = smem + J_OST + ((u + 1) & 1) * (32 * OSP);
#pragma unroll
      for (int i = 0; i < 2; ++i) orv[i] = *(const u32x4*)(otp + (4 * wave + h + 2 * i) * OSP + n * 16);
    }
    f32x16 acc = zero16();
    float th[16];
    u32x4 p0, p1;
    u32x4 btn[PRE];
    SB();
#pragma unroll
    for (int s2 = 0; s2 < 16; ++s2) {
      acc = mfma32(wo[s2], bt[s2], acc);
      if (s2 + PRE < 16) bt[s2 + PRE] = *(const u32x4*)(tb + (s2 + PRE) * 1024);
      else if (u < 7) btn[s2 + PRE - 16] = *(const u32x4*)(tbn + (s2 + PRE - 16) * 1024);
      if ((s2 == 3 || s2 == 5) && u > 0) {
        const int i = (s2 - 3) >> 1;
        __builtin_nontemporal_store(orv[i], (u32x4*)(orow + ooff(u - 1, i)));
      }
      if (u + NPRE < 8) {
        constexpr int NPAIR = 8;
        const int g = s2 >> 1;
        if ((s2 & 1) == 0) {
          if (g < NPAIR) { th[2 * g] = __builtin_amdgcn_exp2f(sn[2 * g]); th[2 * g + 1] = __builtin_amdgcn_exp2f(sn[2 * g + 1]); }
          if (g >= 1) { th[2 * g - 2] = __builtin_fmaf(-2.0f, th[2 * g - 2], 1.0f); th[2 * g - 1] = __builtin_fmaf(-2.0f, th[2 * g - 1], 1.0f); }
        } else {
          const f32x2 a01 = f32x2{th[2 * g], th[2 * g + 1]} + f32x2{1.0f, 1.0f};
          const float a0 = a01[0], a1 = a01[1];
          if (g >= 1) {
            const uint32_t pk = pk2bf(th[2 * g - 2], th[2 * g - 1]);
            if (g - 1 < 4) p0[g - 1] = pk; else p1[g - 5] = pk;
          }
          th[2 * g] = __builtin_amdgcn_rcpf(a0); th[2 * g + 1] = __builtin_amdgcn_rcpf(a1);
        }
        if (s2 == 10) *(u32x4*)(tbw + ((u + NPRE) % NTB) * 16384) = p0;
      }
      SB();
    }
    if (u + 1 + NPRE < 8) sn = add_ge(u + 1 + NPRE);
    if (u + NPRE < 8) p1[3] = pk2bf(__builtin_fmaf(-2.0f, th[14], 1.0f), __builtin_fmaf(-2.0f, th[15], 1.0f));
    if (u + NPRE < 8) *(u32x4*)(tbw + ((u + NPRE) % NTB) * 16384 + 1024) = p1;
    char* ot = smem + J_OST + (u & 1) * (32 * OSP) + n * OSP + wave * 64;
#pragma unroll
    for (int g = 0; g < 4; ++g) {
      u32x2 pk = {pk2bf(acc[4 * g], acc[4 * g + 1]), pk2bf(acc[4 * g + 2], acc[4 * g + 3])};
      *(u32x2*)(ot + (8 * g + 4 * h) * 2) = pk;
    }
#pragma unroll
    for (int s2 = 0; s2 < PRE; ++s2) bt[s2] = btn[s2];
    __syncthreads();
  }
  {
    const char* otp = smem + J_OST + 1 * (32 * OSP);
#pragma unroll
    for (int i = 0; i < 2; ++i) {
      u32x4 v = *(const u32x4*)(otp + (4 * wave + h + 2 * i) * OSP + n * 16);
      __builtin_nontemporal_store(v, (u32x4*)(orow + ooff(7, i)));
    }
  }
}

}

extern "C" void kernel_launch(void* const* d_in, const int* in_sizes, int n_in, void* d_out, int out_size, void* d_ws,
                              size_t ws_size, hipStream_t stream) {
  const uint16_t* x = (const uint16_t*)d_in[0];
  const uint16_t* w1 = (const uint16_t*)d_in[1];
  const uint16_t* w2 = (const uint16_t*)d_in[2];
  const uint16_t* g = (const uint16_t*)d_in[3];
  const uint16_t* wf = (const uint16_t*)d_in[4];
  const uint16_t* wg = (const uint16_t*)d_in[5];
  const uint16_t* wo = (const uint16_t*)d_in[6];
  uint16_t* out = (uint16_t*)d_out;
#define WOFF 0
  char* ws = (char*)d_ws + ((size_t)WOFF << 20);
  char* u1 = ws;
  char* w1h = ws + (24 << 20);
  char* w2h = ws + (40 << 20);
  char* y1 = ws + (48 << 20);
  char* u2 = ws + (60 << 20);
  char* y2 = ws + (77 << 20);
  u32x4* wff = (u32x4*)(ws + (86 << 20));
  u32x4* wof = (u32x4*)(ws + (86 << 20) + 131072);
  uint32_t* ge = (uint32_t*)(ws + (86 << 20) + 262144);
  constexpr int G1LDS = 7 * 11 * 2048 + 256, G2LDS = 8 * 8 * 2048 + 256;

  (void)hipFuncSetAttribute((const void*)gemm_kernel<1>, hipFuncAttributeMaxDynamicSharedMemorySize, G1LDS);
  (void)hipFuncSetAttribute((const void*)gemm_kernel<2>, hipFuncAttributeMaxDynamicSharedMemorySize, G2LDS);
  (void)hipFuncSetAttribute((const void*)joint_kernel, hipFuncAttributeMaxDynamicSharedMemorySize, JOINT_LDS);

  pre_kernel<<<256, 512, 0, stream>>>(x, w1, w2, wf, wo, u1, w1h, w2h, wff, wof);
  gemm_kernel<1><<<256, 256, G1LDS, stream>>>(u1, w1h, y1, (const char*)wff);
  mid_kernel<<<256, 1024, 0, stream>>>(y1, u2, g, wg, ge);
  gemm_kernel<2><<<256, 256, G2LDS, stream>>>(u2, w2h, y2, (const char*)wff);
  joint_kernel<<<250, 512, JOINT_LDS, stream>>>(y2, wff, wof, (const uint32_t*)ge, out);
}
```

```cpp
#define PF1 7
#define DBG_DUMP
#include <hip/hip_runtime.h>
#include <stdint.h>

typedef __attribute__((ext_vector_type(8))) short bf16x8;
typedef __attribute__((ext_vector_type(16))) float f32x16;
typedef __attribute__((ext_vector_type(4))) float f32x4;
typedef __attribute__((ext_vector_type(2))) float f32x2;
typedef __attribute__((ext_vector_type(4))) unsigned int u32x4;
typedef __attribute__((ext_vector_type(2))) unsigned int u32x2;

#define GPRO 2
namespace {

constexpr int DCH = 256;

__device__ __forceinline__ float bf2f(uint32_t v) { return __uint_as_float(v << 16); }
__device__ __forceinline__ uint32_t f2bf(float f) {
  uint32_t u = __float_as_uint(f);
  u += 0x7FFF + ((u >> 16) & 1);
  return u >> 16;
}
__device__ __forceinline__ uint32_t pk2bf(float a, float b) {
  typedef __attribute__((ext_vector_type(2))) __bf16 b2;
  f32x2 v = {a, b};
  b2 r = __builtin_convertvector(v, b2);
  return __builtin_bit_cast(uint32_t, r);
}
__device__ __forceinline__ uint32_t gelu2(float a0, float a1) {
  const f32x2 x = {a0, a1};
  const f32x2 c0 = {-2.302208198144325f, -2.302208198144325f}, c1 = {-0.10294323970002349f, -0.10294323970002349f};
  const f32x2 z = x * __builtin_elementwise_fma(x * x, c1, c0);
  const f32x2 e = {__builtin_amdgcn_exp2f(z[0]), __builtin_amdgcn_exp2f(z[1])};
  const f32x2 d = e + f32x2{1.0f, 1.0f};
  const f32x2 y = x * f32x2{__builtin_amdgcn_rcpf(d[0]), __builtin_amdgcn_rcpf(d[1])};
  return pk2bf(y[0], y[1]);
}
__device__ __forceinline__ f32x16 mfma32(const u32x4& a, const u32x4& b, const f32x16& c) {
  return __builtin_amdgcn_mfma_f32_32x32x16_bf16(__builtin_bit_cast(bf16x8, a), __builtin_bit_cast(bf16x8, b), c, 0, 0, 0);
}
__device__ __forceinline__ f32x4 mfma16(const u32x4& a, const u32x4& b, const f32x4& c) {
  return __builtin_amdgcn_mfma_f32_16x16x32_bf16(__builtin_bit_cast(bf16x8, a), __builtin_bit_cast(bf16x8, b), c, 0, 0, 0);
}
__device__ __forceinline__ f32x16 zero16() {
  f32x16 z;
#pragma unroll
  for (int i = 0; i < 16; ++i) z[i] = 0.f;
  return z;
}
__device__ __forceinline__ __amdgpu_buffer_rsrc_t mkbuf(const void* p, int nbytes) {
  return __builtin_amdgcn_make_buffer_rsrc((void*)p, (short)0, nbytes, 0x00020000);
}
template <int V> struct IC { static constexpr int value = V; };
template <int I, int N, class F>
__device__ __forceinline__ void sfor(F&& f) {
  if constexpr (I < N) {
    f(IC<I>{});
    sfor<I + 1, N>(f);
  }
}
#define SB() __builtin_amdgcn_sched_barrier(0)
#define PHASE() do { SB(); asm volatile("s_waitcnt vmcnt(0)\n\ts_barrier" ::: "memory"); SB(); } while (0)
__device__ unsigned int g_st[5][4096][24];
#define ST_DECL unsigned int st_[24] = {}
#define ST_ARG , unsigned int (&st_)[24]
#define ST_PASS , st_
#define STAMP(k) do { SB(); st_[k] = (unsigned int)__builtin_amdgcn_s_memrealtime(); SB(); } while (0)
#define CSTAMP(k) do { SB(); st_[k] = (unsigned int)__builtin_amdgcn_s_memtime(); SB(); } while (0)
#define ST_OUT(kid, wid) do { asm volatile("s_waitcnt vmcnt(0)" ::: "memory"); STAMP(21); CSTAMP(23); if ((threadIdx.x & 63) == 0) { _Pragma("unroll") for (int i_ = 0; i_ < 24; ++i_) g_st[kid][wid][i_] = st_[i_]; } } while (0)

constexpr double PI_ = 3.14159265358979323846;
constexpr double c128(int m) {
  m = ((m % 128) + 128) % 128;
  if (m > 64) m = 128 - m;
  double sg = 1.0;
  if (m > 32) { m = 64 - m; sg = -1.0; }
  if (m == 32) return 0.0;
  const double a = PI_ * m / 64.0;
  double term = 1.0, sum = 1.0;
  for (int i = 1; i < 16; ++i) { term *= -a * a / ((2 * i - 1) * (2 * i)); sum += term; }
  return sg * sum;
}
constexpr double s128(int m) { return c128(m - 32); }
constexpr uint16_t cbf(double v) {
  uint32_t u = __builtin_bit_cast(uint32_t, (float)v);
  u += 0x7FFF + ((u >> 16) & 1);
  return (uint16_t)(u >> 16);
}
constexpr double cbf2d(uint16_t v) { return (double)__builtin_bit_cast(float, (uint32_t)v << 16); }
struct Tabs {
  uint16_t f1[2][3][64][8];
  uint16_t e1[2][2][64][8];
  uint16_t e2[2][64][8];
  uint16_t g1[64][8][8];
  uint16_t f2[64][8];
  uint16_t g2[16][32];
};
constexpr Tabs make_tabs() {
  Tabs t{};
  for (int l = 0; l < 64; ++l)
    for (int e = 0; e < 8; ++e) {
      const int h = l >> 5, f = l & 31, ri = f & 1;
      for (int fb = 0; fb < 2; ++fb) {
        const int k = 16 * fb + (f >> 1);
        for (int ks = 0; ks < 3; ++ks) {
          const int a = (2 * k + 1) * (16 * ks + 8 * h + e);
          t.f1[fb][ks][l][e] = cbf(ri ? -s128(a) : c128(a));
        }
        const int a = (2 * k + 1) * (8 * h + e);
        const double v = ri ? s128(a) : c128(a);
        t.e1[fb][0][l][e] = cbf(v);
        t.e1[fb][1][l][e] = cbf(v - cbf2d(cbf(v)));
      }
      const int a = 2 * (2 * (f >> 1) + 1) * (8 * h + e);
      const double v = ri ? s128(a) : c128(a);
      t.e2[0][l][e] = cbf(v);
      t.e2[1][l][e] = cbf(v - cbf2d(cbf(v)));
      t.f2[l][e] = cbf(ri ? -v : v);
    }
  for (int m = -15; m < 48; ++m)
    for (int f = 0; f < 64; ++f) {
      const int a = (2 * (f >> 1) + 1) * m;
      t.g1[m + 16][(f >> 3) ^ (((m + 16) >> 1) & 7)][f & 7] = cbf(((f & 1) ? -s128(a) : c128(a)) / 32.0);
    }
  for (int m = 0; m < 16; ++m)
    for (int f = 0; f < 32; ++f) {
      const int a = 2 * (2 * (f >> 1) + 1) * m;
      t.g2[m][f] = cbf(((f & 1) ? -s128(a) : c128(a)) / 16.0);
    }
  return t;
}
__device__ __attribute__((aligned(64))) const Tabs tabs = make_tabs();

constexpr int OOB = 0x7FFFFF00;
constexpr int WT = 16;
constexpr int NT = 2;
constexpr int R1 = 336;
constexpr int R2 = 504;
constexpr int PL1 = R1 * 128;
constexpr int PL2 = R2 * 128;
constexpr int U1BYTES = 32 * 16 * PL1, U2BYTES = 16 * 16 * PL2;
constexpr int WBIN = 16 * 16 * 2 * 1024;
constexpr int YP1 = R1 * 64;
constexpr int YP2 = 500 * 64;
constexpr int Y1BYTES = 32 * 16 * YP1, Y2BYTES = 16 * 16 * YP2;
__device__ __forceinline__ int dmap(int cb, int n) { return 64 * (cb >> 2) + 32 * (cb & 1) + 2 * n + ((cb >> 1) & 1); }

__device__ __forceinline__ void swap16(u32x4& a, u32x4& b) {
#pragma unroll
  for (int i = 0; i < 4; ++i) {
    const auto r = __builtin_amdgcn_permlane16_swap(a[i], b[i], false, false);
    a[i] = r[0];
    b[i] = r[1];
  }
}

typedef __attribute__((address_space(3))) u32x4 lds_u32x4;
__device__ __forceinline__ u32x4 lds_ld(int a) { return *(const lds_u32x4*)(uintptr_t)(uint32_t)a; }
__device__ __forceinline__ uint32_t lds_ld32(int a) { return *(const __attribute__((address_space(3))) uint32_t*)(uintptr_t)(uint32_t)a; }
template <int SZ>
__device__ __forceinline__ void dma(__amdgpu_buffer_rsrc_t rsrc, char* lds, int voff, int soff) {
#if defined(__HIP_DEVICE_COMPILE__)
  typedef __attribute__((address_space(3))) void* ldsp;
  if constexpr (SZ == 16) __builtin_amdgcn_raw_ptr_buffer_load_lds(rsrc, (ldsp)lds, 16, voff, soff, 0, 0);
  else __builtin_amdgcn_raw_ptr_buffer_load_lds(rsrc, (ldsp)lds, 4, voff, soff, 0, 0);
#endif
}

#define LABEL_ADDR(p, name) asm volatile("s_getpc_b64 s[40:41]\n\ts_add_u32 s40, s40, " name "@rel32@lo+4\n\ts_addc_u32 s41, s41, " name "@rel32@hi+12\n\ts_mov_b64 %0, s[40:41]" : "=s"(p) :: "s40", "s41", "scc")
__device__ __forceinline__ void code_pf(const char* p, int n, int part, char* lds) {
  dma<4>(mkbuf(p, n), lds, part * 8192 + (threadIdx.x & 63) * 128, 0);
}

template <int CONV>
__device__ __forceinline__ void wtrans(const uint16_t* __restrict__ w, char* __restrict__ wh, int wid, int lane) {
  const int dq = wid & 3, pc = wid >> 2;
  const int ks = pc >> 2, kq = pc & 3;
  const int n32 = lane & 31, h = lane >> 5;
  const int r = CONV == 1 ? ks >> 3 : kq >> 1;
  const int c0 = CONV == 1 ? (ks & 7) * 32 + kq * 8 : dmap(ks, 4 * (kq & 1));
  const __amdgpu_buffer_rsrc_t wr = mkbuf(w, 31 * 131072);
  const int vb = (16 * h + r + 1) * 131072 + c0 * 512 + dq * 128 + n32 * 4;
  const int vb0 = (h + r == 0) ? OOB : vb - 262144;
  constexpr int NFB = CONV == 1 ? 2 : 1;
  const u32x4* et = CONV == 1 ? (const u32x4*)&tabs.e1[0][0][0][0] : (const u32x4*)&tabs.e2[0][0][0];
  u32x4 E[NFB][2];
#pragma unroll
  for (int fb = 0; fb < NFB; ++fb) { E[fb][0] = et[fb * 128 + lane]; E[fb][1] = et[fb * 128 + 64 + lane]; }
  uint32_t v[8][8];
#pragma unroll
  for (int el = 0; el < 8; ++el)
#pragma unroll
    for (int jj = 0; jj < 8; ++jj) {
      const int co = CONV == 1 ? el * 512 : (el & 3) * 1024;
      const int so = (CONV == 1 ? 0 : (el >> 2) * 8192) + (jj ? (jj - 1) * 262144 : 0);
      v[el][jj] = __builtin_amdgcn_raw_buffer_load_b32(wr, (jj ? vb : vb0) + co, so, NT);
    }
  PHASE();
  const __amdgpu_buffer_rsrc_t dr = mkbuf(wh, (CONV == 1 ? 32 : 16) * WBIN);
  const int vo = 2 * h * WBIN + (4 * dq + (n32 >> 4)) * 32768 + ks * 2048 + kq * 256 + (n32 & 15) * 16;
#pragma unroll
  for (int e = 0; e < 2; ++e) {
    u32x4 b[8];
#pragma unroll
    for (int el = 0; el < 8; ++el)
#pragma unroll
      for (int j = 0; j < 4; ++j) b[el][j] = __builtin_amdgcn_perm(v[el][2 * j + 1], v[el][2 * j], e ? 0x07060302u : 0x05040100u);
#pragma unroll
    for (int fb = 0; fb < NFB; ++fb) {
      u32x4 o[16];
#pragma unroll
      for (int ep = 0; ep < 4; ++ep) {
        f32x16 d[2];
#pragma unroll
        for (int z = 0; z < 2; ++z) d[z] = mfma32(E[fb][1], b[2 * ep + z], zero16());
#pragma unroll
        for (int z = 0; z < 2; ++z) d[z] = mfma32(E[fb][0], b[2 * ep + z], d[z]);
#pragma unroll
        for (int i = 0; i < 16; ++i) o[i][ep] = pk2bf(d[0][i], d[1][i]);
      }
#pragma unroll
      for (int i = 0; i < 16; ++i)
        __builtin_amdgcn_raw_buffer_store_b128(o[i], dr, vo + (i & 1) * 1024,
                                               (16 * fb + 4 * (i >> 2) + ((i >> 1) & 1)) * WBIN + e * 65536, WT);
      SB();
    }
  }
}

__device__ __forceinline__ void xload(const uint16_t* __restrict__ x, int u, int lane, uint32_t (&v)[3][8]) {
  const int cq = u & 7, b = (u >> 3) & 1, sg = u >> 4;
  const __amdgpu_buffer_rsrc_t xr = mkbuf(x + (size_t)b * 16000 * DCH, 8192000);
#pragma unroll
  for (int ks = 0; ks < 3; ++ks) {
    const int row0 = 48 * sg - 24 + 16 * ks + 8 * (lane >> 5);
    const int vb = row0 >= 0 ? row0 * 1024 + cq * 128 + (lane & 31) * 4 : OOB;
#pragma unroll
    for (int e = 0; e < 8; ++e) v[ks][e] = __builtin_amdgcn_raw_buffer_load_b32(xr, vb + (e & 3) * 1024, (e >> 2) * 4096, NT);
  }
}
__device__ __forceinline__ void xtrans(char* __restrict__ u1, int u, int lane, const uint32_t (&v)[3][8], const u32x4 (&F)[2][3]) {
  const __amdgpu_buffer_rsrc_t ur = mkbuf(u1 + (2 * (u & 7)) * PL1 + (u >> 3) * 128, U1BYTES - (2 * (u & 7)) * PL1 - (u >> 3) * 128);
  const int vo = ((lane & 15) >> 1) * 16 * PL1 + ((lane >> 4) & 1) * 64 + (lane & 1) * 32 + (lane >> 5) * 16;
  u32x4 a[2][3];
#pragma unroll
  for (int ks = 0; ks < 3; ++ks)
#pragma unroll
    for (int j = 0; j < 4; ++j) {
      a[0][ks][j] = __builtin_amdgcn_perm(v[ks][2 * j + 1], v[ks][2 * j], 0x05040100u);
      a[1][ks][j] = __builtin_amdgcn_perm(v[ks][2 * j + 1], v[ks][2 * j], 0x07060302u);
    }
#pragma unroll
  for (int fb = 0; fb < 2; ++fb) {
    f32x16 d[2] = {zero16(), zero16()};
#pragma unroll
    for (int ks = 0; ks < 3; ++ks)
#pragma unroll
      for (int e = 0; e < 2; ++e) d[e] = mfma32(a[e][ks], F[fb][ks], d[e]);
#pragma unroll
    for (int qp = 0; qp < 2; ++qp) {
      u32x4 o[2];
#pragma unroll
      for (int z = 0; z < 2; ++z)
#pragma unroll
        for (int i = 0; i < 4; ++i) o[z][i] = pk2bf(d[0][8 * qp + 4 * z + i], d[1][8 * qp + 4 * z + i]);
      swap16(o[0], o[1]);
#pragma unroll
      for (int z = 0; z < 2; ++z) __builtin_amdgcn_raw_buffer_store_b128(o[z], ur, vo, ((fb * 16 + z * 8) * 16 + qp) * PL1, WT);
    }
  }
}

__global__ __launch_bounds__(512, 2) void pre_kernel(const uint16_t* __restrict__ x, const uint16_t* __restrict__ w1,
                                                     const uint16_t* __restrict__ w2, const uint16_t* __restrict__ wf,
                                                     const uint16_t* __restrict__ wo, char* __restrict__ u1,
                                                     char* __restrict__ w1h, char* __restrict__ w2h, u32x4* __restrict__ wff,
                                                     u32x4* __restrict__ wof) {
  ST_DECL; STAMP(0); CSTAMP(22);
  const int blk = blockIdx.x;
  const int tid = threadIdx.x, lane = tid & 63;
  const int wave = __builtin_amdgcn_readfirstlane(tid >> 6);
  const int n = lane & 31, h = lane >> 5;
  __shared__ char pfs[256];
  if (wave == PF1) {
    const char *p0 = (const char*)&tabs - 320, *p1;
    LABEL_ADDR(p1, ".Lce_g2");
    code_pf(p0, (int)(p1 - p0), blk >> 3, pfs);
  }
  if (wave == 0) {
    wtrans<1>(w1, w1h, blk, lane);
  } else if (wave == 1) {
    wtrans<2>(w2, w2h, blk, lane);
  } else {
    const int bq = (blk & 7) * 32 + (blk >> 3);
    const int u0 = ((21 * bq) >> 1) + wave - 2, ue = (21 * (bq + 1)) >> 1;
    u32x4 F[2][3];
#pragma unroll
    for (int fb = 0; fb < 2; ++fb)
#pragma unroll
      for (int ks = 0; ks < 3; ++ks) F[fb][ks] = ((const u32x4*)&tabs.f1[fb][ks][0][0])[lane];
    uint32_t v0[3][8];
    xload(x, u0, lane, v0);
    if (u0 + 6 < ue) {
      uint32_t v1[3][8];
      xload(x, u0 + 6, lane, v1);
      PHASE();
      STAMP(1);
      xtrans(u1, u0, lane, v0, F);
      SB();
      xtrans(u1, u0 + 6, lane, v1, F);
    } else if (wave == 7) {
      const int s = blk & 15, eb = (blk >> 4) & 7, second = blk >> 7;
      const uint16_t* src = (second ? wo + (16 * s + 4 * h) * DCH : wf + dmap(s, 4 * h) * DCH) + 32 * eb + n;
      const int st = second ? DCH : 2 * DCH;
      uint32_t v[8];
#pragma unroll
      for (int j = 0; j < 8; ++j) v[j] = src[(8 * (j >> 2) + (j & 3)) * st];
      PHASE();
      xtrans(u1, u0, lane, v0, F);
      (second ? wof : wff)[(blk & 127) * 64 + lane] =
          u32x4{v[0] | (v[1] << 16), v[2] | (v[3] << 16), v[4] | (v[5] << 16), v[6] | (v[7] << 16)};
    } else {
      PHASE();
      xtrans(u1, u0, lane, v0, F);
    }
  }
  STAMP(19);
  ST_OUT(0, blk * 8 + wave);
}

constexpr int issued(int g, int MB, int OPS, int NST, int BAR, int PRO) {
  int o = PRO * OPS;
  for (int t = 0; t < g; ++t)
    if (o < 16 * OPS && (o / OPS < NST || t >= (o / OPS - NST + 1) * MB + BAR)) ++o;
  return o;
}
template <int CONV>
__global__ __launch_bounds__(256, 1) void gemm_kernel(const char* __restrict__ U, const char* __restrict__ W,
                                                      char* __restrict__ Y, const char* __restrict__ pf) {
  constexpr int MB = CONV == 1 ? 11 : 8;
  constexpr int STG = MB * 2048;
  constexpr int NST = CONV == 1 ? 7 : 8;
  constexpr int PL = CONV == 1 ? PL1 : PL2;
  constexpr int NDMA = CONV == 1 ? 6 : 4;
  constexpr int OPS = NDMA + 2;
  extern __shared__ __attribute__((aligned(16))) char smem[];
  ST_DECL; STAMP(0); CSTAMP(22);
  const int bid = blockIdx.x, xcd = bid & 7, slot = bid >> 3;
  const int bin = CONV == 1 ? xcd * 4 + (slot >> 3) : xcd * 2 + (slot >> 4);
  const int mt = CONV == 1 ? (slot >> 2) & 1 : (slot >> 2) & 3;
  const int tid = threadIdx.x, lane = tid & 63;
  const int wave = __builtin_amdgcn_readfirstlane(tid >> 6);
  const int cb = (slot & 3) * 4 + wave;
  const int row0 = CONV == 1 ? mt * 168 : mt * 126;
  const __amdgpu_buffer_rsrc_t ur = mkbuf(U + (size_t)bin * 16 * PL + row0 * 128, 16 * PL - row0 * 128 + (CONV == 1 ? 0 : 256));
  const __amdgpu_buffer_rsrc_t wr = mkbuf(W + (size_t)bin * WBIN + cb * 32768, 32768);
  int vo[2];
  if constexpr (CONV == 1) {
    vo[0] = wave * 1024 + (lane >> 3) * 128 + (((lane & 7) ^ ((4 * wave + (lane >> 4)) & 7)) * 16);
    vo[1] = 20480 + wave * 256 + (lane >> 5) * 128 + ((((lane >> 2) & 7) ^ wave) * 16) + (lane & 3) * 4;
  } else {
#pragma unroll
    for (int z = 0; z < 2; ++z) vo[z] = wave * 4096 + (lane >> 3) * 128 + (((lane & 7) ^ (2 * wave + z)) * 16);
  }
  u32x4 B[NST][2];
  auto issue = [&](auto is, auto ii) {
    constexpr int s = decltype(is)::value, i = decltype(ii)::value;
    char* buf = smem + (s % NST) * STG;
    if constexpr (i < 2) {
      B[s % NST][i] = __builtin_amdgcn_raw_buffer_load_b128(wr, lane * 16 + i * 1024, s * 2048, 0);
    } else if constexpr (CONV == 1) {
      if constexpr (i < 7) dma<16>(ur, buf + wave * 1024 + (i - 2) * 4096, vo[0], s * PL + (i - 2) * 4096);
      else dma<4>(ur, buf + 20480 + wave * 256, vo[1], s * PL);
    } else {
      dma<16>(ur, buf + wave * 4096 + (i - 2) * 1024, vo[(i - 2) >> 1], s * PL + (i - 2) * 1024);
    }
  };
  constexpr int AR = 4, AL = 3;
  constexpr int BAR = MB - AL;
  constexpr int PRO = GPRO;
  sfor<0, PRO>([&](auto is) { sfor<0, OPS>([&](auto ii) { issue(is, ii); SB(); }); });
  const int rho = lane & 15, kq = lane >> 4;
  const int lds0 = (int)(uint32_t)(uintptr_t)(__attribute__((address_space(3))) char*)smem;
  constexpr int WIN = CONV == 1 ? 2 : 4;
  constexpr int NWIN = (NST + WIN - 1) / WIN;
  int ab[NWIN][2];
#pragma unroll
  for (int ri = 0; ri < 2; ++ri) {
    const int pos = ((kq >> 1) * 4 + ri * 2 + (kq & 1)) ^ (rho >> 1);
    const int a = lds0 + (CONV == 1 ? rho * 128 : (rho >> 1) * 2048 + (rho & 1) * 128) + pos * 16;
#pragma unroll
    for (int wn = 0; wn < NWIN; ++wn) { ab[wn][ri] = a + wn * WIN * STG; asm volatile("" : "+v"(ab[wn][ri])); }
  }
  auto aread = [&](int s, int mb, int ri) -> u32x4 {
    const int bf = s % NST;
    return lds_ld(ab[bf / WIN][ri] + (bf % WIN) * STG + mb * (CONV == 1 ? 2048 : 256));
  };
  f32x4 yr[MB], yi[MB];
#pragma unroll
  for (int mb = 0; mb < MB; ++mb) { yr[mb] = f32x4{0.f, 0.f, 0.f, 0.f}; yi[mb] = f32x4{0.f, 0.f, 0.f, 0.f}; }
  u32x4 A[AR][2];
  STAMP(1);
  asm volatile("s_waitcnt vmcnt(%0)" ::"n"((PRO - 1) * OPS) : "memory");
  __builtin_amdgcn_s_barrier();
  STAMP(2);
  asm volatile("" ::: "memory");
#pragma unroll
  for (int z = 0; z < AL; ++z) { A[z][0] = aread(0, z, 0); A[z][1] = aread(0, z, 1); }
  SB();
  u32x4 nwi;
#pragma unroll
  for (int j = 0; j < 4; ++j) nwi[j] = B[0][1][j] ^ 0x80008000u;
  sfor<0, 16>([&](auto is) {
    constexpr int s = decltype(is)::value;
    u32x4 nwn;
    sfor<0, MB>([&](auto im) {
      constexpr int mb = decltype(im)::value;
      constexpr int g = s * MB + mb, gn = g + AL;
      constexpr int o0 = issued(g, MB, OPS, NST, BAR, PRO), o1 = issued(g + 1, MB, OPS, NST, BAR, PRO);
      if constexpr (mb == BAR && s + 1 < 16) {
        static_assert(o0 >= (s + 2) * OPS, "next stage not requested yet");
        asm volatile("s_waitcnt vmcnt(%0)" ::"n"(o0 - (s + 2) * OPS) : "memory");
        __builtin_amdgcn_s_barrier();
        asm volatile("" ::: "memory");
        SB();
      }
      yr[mb] = mfma16(B[s % NST][0], A[g % AR][0], yr[mb]);
      SB();
      yr[mb] = mfma16(nwi, A[g % AR][1], yr[mb]);
      if constexpr (gn < 16 * MB) A[gn % AR][0] = aread(gn / MB, gn % MB, 0);
      SB();
      yi[mb] = mfma16(B[s % NST][1], A[g % AR][0], yi[mb]);
      if constexpr (o1 > o0) issue(IC<o0 / OPS>{}, IC<o0 % OPS>{});
      SB();
      yi[mb] = mfma16(B[s % NST][0], A[g % AR][1], yi[mb]);
      if constexpr (gn < 16 * MB) A[gn % AR][1] = aread(gn / MB, gn % MB, 1);
      if constexpr (mb == MB - 2 && s + 1 < 16) {
#pragma unroll
        for (int j = 0; j < 4; ++j) nwn[j] = B[(s + 1) % NST][1][j] ^ 0x80008000u;
      }
      SB();
    });
    if constexpr (s + 1 < 16) nwi = nwn;
    if (s == 0 || s == 5 || s == 10 || s == 15) STAMP(3 + s);
  });
  if constexpr (CONV == 1) {
    const __amdgpu_buffer_rsrc_t yb = mkbuf(Y + (size_t)(bin * 16 + cb) * YP1 + row0 * 64, 168 * 64);
#pragma unroll
    for (int mb = 0; mb < MB; ++mb) {
      u32x4 o;
#pragma unroll
      for (int i = 0; i < 4; ++i) o[i] = pk2bf(yr[mb][i], yi[mb][i]);
      __builtin_amdgcn_raw_buffer_store_b128(o, yb, rho * 64 + kq * 16 + (mb & 3) * 1024, (mb >> 2) * 4096, WT);
    }
  } else {
    const __amdgpu_buffer_rsrc_t yb = mkbuf(Y + (size_t)(bin * 16 + cb) * YP2 + row0 * 64, YP2 - row0 * 64);
    const float sg = (bin & 1) ? -1.f : 1.f;
    f32x4 nr, ni;
#pragma unroll
    for (int i = 0; i < 4; ++i) { nr[i] = __shfl_down(yr[0][i], 2, 16); ni[i] = __shfl_down(yi[0][i], 2, 16); }
    const int vy = (rho >> 1) * 1024 + (rho & 1) * 64 + kq * 16;
#pragma unroll
    for (int mb = 0; mb < MB; ++mb) {
      const f32x4 xr = mb < MB - 1 ? yr[mb < MB - 1 ? mb + 1 : 0] : nr, xi = mb < MB - 1 ? yi[mb < MB - 1 ? mb + 1 : 0] : ni;
      u32x4 o;
#pragma unroll
      for (int i = 0; i < 4; ++i) o[i] = pk2bf(__builtin_fmaf(sg, xi[i], yr[mb][i]), __builtin_fmaf(-sg, xr[i], yi[mb][i]));
      __builtin_amdgcn_raw_buffer_store_b128(o, yb, (mb == MB - 1 && rho >= 14) ? OOB : vy + mb * 128, 0, WT);
    }
  }
  STAMP(19);
  ST_OUT(CONV == 1 ? 1 : 3, bid * 4 + wave);
  if constexpr (CONV == 1) asm volatile(".Lce_g1:"); else asm volatile(".Lce_g2:");
}

template <int Z>
__device__ __forceinline__ void mid_unit(char* __restrict__ U, int ya, int ga, int cb, int s, const u32x4& F ST_ARG) {
  const int lane = threadIdx.x & 63;
  const int n32 = lane & 31, h = lane >> 5;
  constexpr int NSG = Z == 0 ? 1 : 2;
  const int tau = 16 * ((n32 >> 2) & 1) + 8 * ((n32 >> 3) & 1) + 2 * (n32 & 3) + (n32 >> 4);
  const int r[2] = {Z == 0 ? tau + 16 : Z == 1 ? (tau < 16 ? tau + 48 : 0) : tau + 32, Z == 1 ? tau : (tau >= 16 ? tau - 16 : 0)};
  f32x16 d = zero16();
#pragma unroll
  for (int sg = 0; sg < NSG; ++sg)
#pragma unroll
    for (int ks = 0; ks < 4; ++ks) {
      u32x4 yv;
#pragma unroll
      for (int j = 0; j < 4; ++j) yv[j] = lds_ld32(ya + sg * 4096 + ks * 1024 + j * 128);
      d = mfma32(lds_ld(ga + r[sg] * 128 + (((2 * ks + h) ^ ((r[sg] >> 1) & 7)) * 16)), yv, d);
    }
  asm volatile("s_nop 0" : "+v"(d[0]));
  STAMP(4);
  const __amdgpu_buffer_rsrc_t ub = mkbuf(U, U2BYTES);
  const int vu = (((lane & 15) >> 1) * 16 + cb) * PL2 + s * 256 + ((lane >> 4) & 1) * 64 + (lane & 1) * 32 + h * 16;
  const int t0r = 32 * s - 16 + 16 * h;
  const bool ok = t0r >= 0 && t0r < 8000;
  f32x16 u[2];
#pragma unroll
  for (int q = 0; q < 2; ++q) {
    u32x4 a;
#pragma unroll
    for (int j = 0; j < 4; ++j) {
      const uint32_t gv = gelu2(d[8 * q + 2 * j], d[8 * q + 2 * j + 1]);
      a[j] = ok ? gv : 0u;
    }
    u[q] = mfma32(a, F, zero16());
  }
#pragma unroll
  for (int b = 0; b < 2; ++b) {
    u32x4 o[2];
#pragma unroll
    for (int q = 0; q < 2; ++q)
#pragma unroll
      for (int j = 0; j < 4; ++j) o[q][j] = pk2bf(u[q][8 * b + 2 * j], u[q][8 * b + 2 * j + 1]);
    swap16(o[0], o[1]);
#pragma unroll
    for (int z = 0; z < 2; ++z) __builtin_amdgcn_raw_buffer_store_b128(o[z], ub, vu + b * 128, z * 8 * 16 * PL2, WT);
  }
}
__global__ __launch_bounds__(1024) void mid_kernel(const char* __restrict__ Y, char* __restrict__ U, const uint16_t* __restrict__ g,
                                                   const uint16_t* __restrict__ wg, uint32_t* __restrict__ ge) {
  __shared__ __attribute__((aligned(16))) char sm[8192 + 49152];
  __shared__ uint32_t landed;
  ST_DECL; STAMP(0); CSTAMP(22);
  const int blk = blockIdx.x, lane = threadIdx.x & 63;
  const int wave = __builtin_amdgcn_readfirstlane(threadIdx.x >> 6);
  const int cb = blk & 15, s0 = blk & ~15, s = s0 + wave;
  const int sg0 = (2 * s0) / 3;
  if (wave < 4) {
    const __amdgpu_buffer_rsrc_t tb = mkbuf(&tabs.g1[0][0][0], 8192);
#pragma unroll
    for (int i = 0; i < 2; ++i) dma<16>(tb, sm + wave * 2048 + i * 1024, lane * 16, wave * 2048 + i * 1024);
  }
  {
    const __amdgpu_buffer_rsrc_t yb = mkbuf(Y, Y1BYTES);
    const int vy = ((lane >> 3) * 16 + cb) * YP1 + sg0 * 128 + (lane & 7) * 16;
#pragma unroll
    for (int i = 0; i < 3; ++i) {
      const int pc = 3 * wave + i;
      dma<16>(yb, sm + 8192 + pc * 1024, vy, (pc & 3) * 128 * YP1 + (pc >> 2) * 128);
    }
  }
  const u32x4 F = ((const u32x4*)&tabs.f2[0][0])[lane];
  if (threadIdx.x == 0) landed = 0;
  asm volatile("s_waitcnt lgkmcnt(0)\n\ts_barrier" ::: "memory");
  STAMP(1);
  if (s >= 251 && wave == 15 && cb < 8) {
    const int c0 = 32 * cb + 2 * (lane & 15), kq = lane >> 4;
    uint32_t v[8][8];
    u32x4 ga[8];
#pragma unroll
    for (int ks = 0; ks < 8; ++ks) {
      ga[ks] = *(const u32x4*)(g + (lane & 15) * DCH + ks * 32 + 8 * kq);
#pragma unroll
      for (int e = 0; e < 8; ++e) v[ks][e] = *(const uint32_t*)(wg + (ks * 32 + 8 * kq + e) * DCH + c0);
    }
    SB();
    f32x4 d[2] = {{0.f, 0.f, 0.f, 0.f}, {0.f, 0.f, 0.f, 0.f}};
#pragma unroll
    for (int ks = 0; ks < 8; ++ks)
#pragma unroll
      for (int e = 0; e < 2; ++e) {
        u32x4 bb;
#pragma unroll
        for (int j = 0; j < 4; ++j) bb[j] = __builtin_amdgcn_perm(v[ks][2 * j + 1], v[ks][2 * j], e ? 0x07060302u : 0x05040100u);
        d[e] = mfma16(ga[ks], bb, d[e]);
      }
#pragma unroll
    for (int i = 0; i < 4; ++i) {
      u32x2 o;
#pragma unroll
      for (int e = 0; e < 2; ++e) {
        const float gs = bf2f(f2bf(d[e][i])) * 2.885390081777927f;
        const uint32_t hi = f2bf(gs), lo = f2bf(gs - bf2f(hi));
        o[e] = hi | (lo << 16);
      }
      *(u32x2*)(ge + (4 * kq + i) * DCH + c0) = o;
    }
  } else {
    asm volatile("s_waitcnt vmcnt(0)" ::: "memory");
    if (lane == 0) __hip_atomic_fetch_or(&landed, 1u << wave, __ATOMIC_RELAXED, __HIP_MEMORY_SCOPE_WORKGROUP);
    STAMP(2);
    if (s < 251) {
      const int p = s / 3, z = s - 3 * p;
      const int l0 = 2 * p + (z == 2) - sg0, l1 = l0 + (z != 0);
      const uint32_t need = 15u | (((2u << ((4 * l1 + 3) / 3)) - 1u) & ~((1u << ((4 * l0) / 3)) - 1u));
      while ((__builtin_amdgcn_readfirstlane(__hip_atomic_load(&landed, __ATOMIC_RELAXED, __HIP_MEMORY_SCOPE_WORKGROUP)) & need) != need)
        __builtin_amdgcn_s_sleep(1);
      asm volatile("" ::: "memory");
      STAMP(3);
      const int l = (int)(uint32_t)(uintptr_t)(__attribute__((address_space(3))) char*)sm;
      const int ya = l + 8192 + l0 * 4096 + (lane >> 5) * 512 + (lane & 31) * 4;
      if (z == 0) mid_unit<0>(U, ya, l, cb, s, F ST_PASS);
      else if (z == 1) mid_unit<1>(U, ya, l, cb, s, F ST_PASS);
      else mid_unit<2>(U, ya, l, cb, s, F ST_PASS);
    }
  }
  STAMP(19);
  ST_OUT(2, blk * 16 + wave);
}

constexpr int J_F = 0;
constexpr int NPRE = 2;
constexpr int NTB = NPRE + 1;
constexpr int J_TB = 16384;
constexpr int OSP = 528;
constexpr int J_OST = J_TB + NTB * 16384;
constexpr int JOINT_LDS = J_OST + 2 * 32 * OSP;

__device__ __forceinline__ float tanh_pre(float s) {
  return __builtin_fmaf(-2.0f, __builtin_amdgcn_rcpf(__builtin_amdgcn_exp2f(s) + 1.0f), 1.0f);
}

__global__ __launch_bounds__(512, 2) void joint_kernel(const char* __restrict__ Y, const u32x4* __restrict__ wff,
                                                       const u32x4* __restrict__ wof, const uint32_t* __restrict__ geb,
                                                       uint16_t* __restrict__ out) {
  extern __shared__ __attribute__((aligned(16))) char smem[];
  constexpr int T2 = 4000;
  ST_DECL; STAMP(0); CSTAMP(22);
  const int grp = blockIdx.x, t0 = grp * 16;
  const int tid = threadIdx.x;
  const int lane = tid & 63;
  const int wave = __builtin_amdgcn_readfirstlane(tid >> 6);
  const int n = lane & 31, h = lane >> 5;
  const __amdgpu_buffer_rsrc_t yb = mkbuf(Y, Y2BYTES);
  const int vy = (4 * h * 16 + 2 * wave) * YP2 + grp * 128 + n * 4;
  u32x4 yv[2][2];
#pragma unroll
  for (int c = 0; c < 2; ++c)
#pragma unroll
    for (int ks = 0; ks < 2; ++ks)
#pragma unroll
      for (int j = 0; j < 4; ++j) yv[c][ks][j] = __builtin_amdgcn_raw_buffer_load_b32(yb, vy, ((8 * ks + j) * 16 + c) * YP2, 0);
  u32x4 gi[2];
#pragma unroll
  for (int ks = 0; ks < 2; ++ks) gi[ks] = *(const u32x4*)&tabs.g2[n & 15][16 * ks + 8 * h];
  u32x4 ga[2];
#pragma unroll
  for (int b = 0; b < 2; ++b)
#pragma unroll
    for (int i = 0; i < 4; ++i) ga[b][i] = geb[(b * 8 + 4 * h + i) * 256 + 32 * wave + n];
  const u32x4* wfp = wff + wave * 16 * 64 + lane;
  const u32x4* wop = wof + wave * 16 * 64 + lane;
  u32x4 wa[16], wo[16];
#pragma unroll
  for (int s = 0; s < 8; ++s) wa[s] = wfp[s * 64];
  SB();
  {
    f32x16 d = zero16();
#pragma unroll
    for (int c = 0; c < 2; ++c)
#pragma unroll
      for (int ks = 0; ks < 2; ++ks) d = mfma32(yv[c][ks], (n >> 4) == c ? gi[ks] : u32x4{0u, 0u, 0u, 0u}, d);
#pragma unroll
    for (int b = 0; b < 2; ++b) {
      u32x4 o;
#pragma unroll
      for (int j = 0; j < 4; ++j) o[j] = gelu2(d[8 * b + 2 * j], d[8 * b + 2 * j + 1]);
      *(u32x4*)(smem + J_F + (2 * wave + (n >> 4)) * 1024 + (h * 32 + b * 16 + (n & 15)) * 16) = o;
    }
  }
  STAMP(1);
  __syncthreads();
  STAMP(2);
  f32x16 fe = zero16();
#pragma unroll
  for (int s = 0; s < 16; ++s) {
    const u32x4 fb = *(const u32x4*)(smem + J_F + s * 1024 + lane * 16);
    fe = mfma32(fb, wa[s], fe);
    if (s < 8) wa[s + 8] = wfp[(s + 8) * 64];
    else if ((s & 1) == 0) wo[(s - 8) >> 1] = wop[((s - 8) >> 1) * 64];
    SB();
  }
  STAMP(3);
  u32x4 gsel;
#pragma unroll
  for (int i = 0; i < 4; ++i) gsel[i] = (4 * h + i == (n & 7)) ? 0x3F803F80u : 0u;
  f32x16 gacc = mfma32(ga[0], gsel, zero16());
  const float c2 = 2.885390081777927f;
  u32x4 xa[4];
#pragma unroll
  for (int q = 0; q < 4; ++q) {
#pragma unroll
    for (int p = 0; p < 2; ++p) {
      const float v0 = fe[4 * q + 2 * p] * c2, v1 = fe[4 * q + 2 * p + 1] * c2;
      const uint32_t hi = pk2bf(v0, v1);
      xa[q][p] = hi;
      xa[q][2 + p] = pk2bf(v0 - __uint_as_float(hi << 16), v1 - __uint_as_float(hi & 0xffff0000u));
    }
  }
  u32x4 sel[2];
  {
    const int i = n >> 3;
    const uint32_t one = (i & 1) ? 0x3F800000u : 0x3F80u;
    const uint32_t d0 = i < 2 ? one : 0u, d1 = i < 2 ? 0u : one;
#pragma unroll
    for (int hh = 0; hh < 2; ++hh) sel[hh] = h == hh ? u32x4{d0, d1, d0, d1} : u32x4{0u, 0u, 0u, 0u};
  }
  uint16_t* orow = out + ((size_t)t0 * 8 + 4 * wave + h) * DCH + n * 8;
  auto ooff = [&](int u, int i) -> size_t { return ((size_t)(u >> 2) * (T2 * 8) + (u & 3) * 32 + 2 * i) * DCH; };
  auto add_ge = [&](int g) -> f32x16 {
    if (g == 4) gacc = mfma32(ga[1], gsel, zero16());
    return mfma32(xa[g >> 1], sel[g & 1], gacc);
  };
  char* const tbw = smem + J_TB + (2 * wave * 64 + lane) * 16;
  const char* const tbr = smem + J_TB + lane * 16;

#pragma unroll
  for (int u = 0; u < NPRE; ++u) {
    const f32x16 s = add_ge(u);
    u32x4 p0, p1;
#pragma unroll
    for (int p = 0; p < 4; ++p) {
      p0[p] = pk2bf(tanh_pre(s[2 * p]), tanh_pre(s[2 * p + 1]));
      if (u < 2 && p < 3) wo[4 + u * 6 + p] = wop[(4 + u * 6 + p) * 64];
      SB();
      p1[p] = pk2bf(tanh_pre(s[8 + 2 * p]), tanh_pre(s[8 + 2 * p + 1]));
      if (u < 2 && p < 3) wo[7 + u * 6 + p] = wop[(7 + u * 6 + p) * 64];
      SB();
    }
    *(u32x4*)(tbw + u * 16384) = p0;
    *(u32x4*)(tbw + u * 16384 + 1024) = p1;
  }
  f32x16 sn = add_ge(NPRE);
  STAMP(4);
  __syncthreads();
  STAMP(5);
  constexpr int PRE = 6;
  u32x4 bt[16];
#pragma unroll
  for (int s2 = 0; s2 < PRE; ++s2) bt[s2] = *(const u32x4*)(tbr + s2 * 1024);
#pragma unroll
  for (int u = 0; u < 8; ++u) {
    const char* tb = tbr + (u % NTB) * 16384;
    const char* tbn = tbr + ((u + 1) % NTB) * 16384;
    u32x4 orv[2];
    if (u > 0) {
      const char* otp = smem + J_OST + ((u + 1) & 1) * (32 * OSP);
#pragma unroll
      for (int i = 0; i < 2; ++i) orv[i] = *(const u32x4*)(otp + (4 * wave + h + 2 * i) * OSP + n * 16);
    }
    f32x16 acc = zero16();
    float th[16];
    u32x4 p0, p1;
    u32x4 btn[PRE];
    SB();
#pragma unroll
    for (int s2 = 0; s2 < 16; ++s2) {
      acc = mfma32(wo[s2], bt[s2], acc);
      if (s2 + PRE < 16) bt[s2 + PRE] = *(const u32x4*)(tb + (s2 + PRE) * 1024);
      else if (u < 7) btn[s2 + PRE - 16] = *(const u32x4*)(tbn + (s2 + PRE - 16) * 1024);
      if ((s2 == 3 || s2 == 5) && u > 0) {
        const int i = (s2 - 3) >> 1;
        __builtin_nontemporal_store(orv[i], (u32x4*)(orow + ooff(u - 1, i)));
      }
      if (u + NPRE < 8) {
        constexpr int NPAIR = 8;
        const int g = s2 >> 1;
        if ((s2 & 1) == 0) {
          if (g < NPAIR) { th[2 * g] = __builtin_amdgcn_exp2f(sn[2 * g]); th[2 * g + 1] = __builtin_amdgcn_exp2f(sn[2 * g + 1]); }
          if (g >= 1) { th[2 * g - 2] = __builtin_fmaf(-2.0f, th[2 * g - 2], 1.0f); th[2 * g - 1] = __builtin_fmaf(-2.0f, th[2 * g - 1], 1.0f); }
        } else {
          const f32x2 a01 = f32x2{th[2 * g], th[2 * g + 1]} + f32x2{1.0f, 1.0f};
          const float a0 = a01[0], a1 = a01[1];
          if (g >= 1) {
            const uint32_t pk = pk2bf(th[2 * g - 2], th[2 * g - 1]);
            if (g - 1 < 4) p0[g - 1] = pk; else p1[g - 5] = pk;
          }
          th[2 * g] = __builtin_amdgcn_rcpf(a0); th[2 * g + 1] = __builtin_amdgcn_rcpf(a1);
        }
        if (s2 == 10) *(u32x4*)(tbw + ((u + NPRE) % NTB) * 16384) = p0;
      }
      SB();
    }
    if (u + 1 + NPRE < 8) sn = add_ge(u + 1 + NPRE);
    if (u + NPRE < 8) p1[3] = pk2bf(__builtin_fmaf(-2.0f, th[14], 1.0f), __builtin_fmaf(-2.0f, th[15], 1.0f));
    if (u + NPRE < 8) *(u32x4*)(tbw + ((u + NPRE) % NTB) * 16384 + 1024) = p1;
    char* ot = smem + J_OST + (u & 1) * (32 * OSP) + n * OSP + wave * 64;
#pragma unroll
    for (int g = 0; g < 4; ++g) {
      u32x2 pk = {pk2bf(acc[4 * g], acc[4 * g + 1]), pk2bf(acc[4 * g + 2], acc[4 * g + 3])};
      *(u32x2*)(ot + (8 * g + 4 * h) * 2) = pk;
    }
#pragma unroll
    for (int s2 = 0; s2 < PRE; ++s2) bt[s2] = btn[s2];
    __syncthreads();
    STAMP(6 + u);
  }
  {
    const char* otp = smem + J_OST + 1 * (32 * OSP);
#pragma unroll
    for (int i = 0; i < 2; ++i) {
      u32x4 v = *(const u32x4*)(otp + (4 * wave + h + 2 * i) * OSP + n * 16);
      __builtin_nontemporal_store(v, (u32x4*)(orow + ooff(7, i)));
    }
  }
  STAMP(19);
  ST_OUT(4, grp * 8 + wave);
}

__global__ __launch_bounds__(1024) void probe_kernel() {
  __shared__ unsigned int ra[1024], rb[1024];
  unsigned int ref = g_st[0][0][0];
  unsigned int a = 0 ? 0u : ~0u, b = 0 ? 0u : ~0u;
  for (int w = threadIdx.x; w < 4096; w += 1024) {
    const unsigned int va = g_st[0][w][0], vb = g_st[0][w][0];
    if (va) { const unsigned int d = va - ref + 100000u; a = 0 ? max(a, d) : min(a, d); }
    if (vb) { const unsigned int d = vb - ref + 100000u; b = 0 ? max(b, d) : min(b, d); }
  }
  ra[threadIdx.x] = a; rb[threadIdx.x] = b;
  __syncthreads();
  if (threadIdx.x == 0) {
    for (int i = 1; i < 1024; ++i) { a = 0 ? max(a, ra[i]) : min(a, ra[i]); b = 0 ? max(b, rb[i]) : min(b, rb[i]); }
    unsigned int span = b - a;
    if (span > 20000u) span = 20000u;
    const unsigned long long t0 = __builtin_amdgcn_s_memrealtime();
    while (__builtin_amdgcn_s_memrealtime() - t0 < 10ull * span) __builtin_amdgcn_s_sleep(2);
  }
}

}

extern "C" void kernel_launch(void* const* d_in, const int* in_sizes, int n_in, void* d_out, int out_size, void* d_ws,
                              size_t ws_size, hipStream_t stream) {
  const uint16_t* x = (const uint16_t*)d_in[0];
  const uint16_t* w1 = (const uint16_t*)d_in[1];
  const uint16_t* w2 = (const uint16_t*)d_in[2];
  const uint16_t* g = (const uint16_t*)d_in[3];
  const uint16_t* wf = (const uint16_t*)d_in[4];
  const uint16_t* wg = (const uint16_t*)d_in[5];
  const uint16_t* wo = (const uint16_t*)d_in[6];
  uint16_t* out = (uint16_t*)d_out;
#define WOFF 0
  char* ws = (char*)d_ws + ((size_t)WOFF << 20);
  char* u1 = ws;
  char* w1h = ws + (24 << 20);
  char* w2h = ws + (40 << 20);
  char* y1 = ws + (48 << 20);
  char* u2 = ws + (60 << 20);
  char* y2 = ws + (77 << 20);
  u32x4* wff = (u32x4*)(ws + (86 << 20));
  u32x4* wof = (u32x4*)(ws + (86 << 20) + 131072);
  uint32_t* ge = (uint32_t*)(ws + (86 << 20) + 262144);
  constexpr int G1LDS = 7 * 11 * 2048 + 256, G2LDS = 8 * 8 * 2048 + 256;

  (void)hipFuncSetAttribute((const void*)gemm_kernel<1>, hipFuncAttributeMaxDynamicSharedMemorySize, G1LDS);
  (void)hipFuncSetAttribute((const void*)gemm_kernel<2>, hipFuncAttributeMaxDynamicSharedMemorySize, G2LDS);
  (void)hipFuncSetAttribute((const void*)joint_kernel, hipFuncAttributeMaxDynamicSharedMemorySize, JOINT_LDS);

  pre_kernel<<<256, 512, 0, stream>>>(x, w1, w2, wf, wo, u1, w1h, w2h, wff, wof);
  gemm_kernel<1><<<256, 256, G1LDS, stream>>>(u1, w1h, y1, (const char*)wff);
  mid_kernel<<<256, 1024, 0, stream>>>(y1, u2, g, wg, ge);
  gemm_kernel<2><<<256, 256, G2LDS, stream>>>(u2, w2h, y2, (const char*)wff);
  joint_kernel<<<250, 512, JOINT_LDS, stream>>>(y2, wff, wof, (const uint32_t*)ge, out);

}
```

```cpp
#define PF1 7
#include <hip/hip_runtime.h>
#include <stdint.h>

typedef __attribute__((ext_vector_type(8))) short bf16x8;
typedef __attribute__((ext_vector_type(16))) float f32x16;
typedef __attribute__((ext_vector_type(4))) float f32x4;
typedef __attribute__((ext_vector_type(2))) float f32x2;
typedef __attribute__((ext_vector_type(4))) unsigned int u32x4;
typedef __attribute__((ext_vector_type(2))) unsigned int u32x2;

#define GPRO 2
namespace {

constexpr int DCH = 256;

__device__ __forceinline__ float bf2f(uint32_t v) { return __uint_as_float(v << 16); }
__device__ __forceinline__ uint32_t f2bf(float f) {
  uint32_t u = __float_as_uint(f);
  u += 0x7FFF + ((u >> 16) & 1);
  return u >> 16;
}
__device__ __forceinline__ uint32_t pk2bf(float a, float b) {
  typedef __attribute__((ext_vector_type(2))) __bf16 b2;
  f32x2 v = {a, b};
  b2 r = __builtin_convertvector(v, b2);
  return __builtin_bit_cast(uint32_t, r);
}
__device__ __forceinline__ uint32_t gelu2(float a0, float a1) {
  const f32x2 x = {a0, a1};
  const f32x2 c0 = {-2.302208198144325f, -2.302208198144325f}, c1 = {-0.10294323970002349f, -0.10294323970002349f};
  const f32x2 z = x * __builtin_elementwise_fma(x * x, c1, c0);
  const f32x2 e = {__builtin_amdgcn_exp2f(z[0]), __builtin_amdgcn_exp2f(z[1])};
  const f32x2 d = e + f32x2{1.0f, 1.0f};
  const f32x2 y = x * f32x2{__builtin_amdgcn_rcpf(d[0]), __builtin_amdgcn_rcpf(d[1])};
  return pk2bf(y[0], y[1]);
}
__device__ __forceinline__ f32x16 mfma32(const u32x4& a, const u32x4& b, const f32x16& c) {
  return __builtin_amdgcn_mfma_f32_32x32x16_bf16(__builtin_bit_cast(bf16x8, a), __builtin_bit_cast(bf16x8, b), c, 0, 0, 0);
}
__device__ __forceinline__ f32x4 mfma16(const u32x4& a, const u32x4& b, const f32x4& c) {
  return __builtin_amdgcn_mfma_f32_16x16x32_bf16(__builtin_bit_cast(bf16x8, a), __builtin_bit_cast(bf16x8, b), c, 0, 0, 0);
}
__device__ __forceinline__ f32x16 zero16() {
  f32x16 z;
#pragma unroll
  for (int i = 0; i < 16; ++i) z[i] = 0.f;
  return z;
}
__device__ __forceinline__ __amdgpu_buffer_rsrc_t mkbuf(const void* p, int nbytes) {
  return __builtin_amdgcn_make_buffer_rsrc((void*)p, (short)0, nbytes, 0x00020000);
}
template <int V> struct IC { static constexpr int value = V; };
template <int I, int N, class F>
__device__ __forceinline__ void sfor(F&& f) {
  if constexpr (I < N) {
    f(IC<I>{});
    sfor<I + 1, N>(f);
  }
}
#define SB() __builtin_amdgcn_sched_barrier(0)
#define PHASE() do { SB(); asm volatile("s_waitcnt vmcnt(0)\n\ts_barrier" ::: "memory"); SB(); } while (0)

constexpr double PI_ = 3.14159265358979323846;
constexpr double c128(int m) {
  m = ((m % 128) + 128) % 128;
  if (m > 64) m = 128 - m;
  double sg = 1.0;
  if (m > 32) { m = 64 - m; sg = -1.0; }
  if (m == 32) return 0.0;
  const double a = PI_ * m / 64.0;
  double term = 1.0, sum = 1.0;
  for (int i = 1; i < 16; ++i) { term *= -a * a / ((2 * i - 1) * (2 * i)); sum += term; }
  return sg * sum;
}
constexpr double s128(int m) { return c128(m - 32); }
constexpr uint16_t cbf(double v) {
  uint32_t u = __builtin_bit_cast(uint32_t, (float)v);
  u += 0x7FFF + ((u >> 16) & 1);
  return (uint16_t)(u >> 16);
}
constexpr double cbf2d(uint16_t v) { return (double)__builtin_bit_cast(float, (uint32_t)v << 16); }
struct Tabs {
  uint16_t f1[2][3][64][8];
  uint16_t e1[2][2][64][8];
  uint16_t e2[2][64][8];
  uint16_t g1[64][8][8];
  uint16_t f2[64][8];
  uint16_t g2[16][32];
};
constexpr Tabs make_tabs() {
  Tabs t{};
  for (int l = 0; l < 64; ++l)
    for (int e = 0; e < 8; ++e) {
      const int h = l >> 5, f = l & 31, ri = f & 1;
      for (int fb = 0; fb < 2; ++fb) {
        const int k = 16 * fb + (f >> 1);
        for (int ks = 0; ks < 3; ++ks) {
          const int a = (2 * k + 1) * (16 * ks + 8 * h + e);
          t.f1[fb][ks][l][e] = cbf(ri ? -s128(a) : c128(a));
        }
        const int a = (2 * k + 1) * (8 * h + e);
        const double v = ri ? s128(a) : c128(a);
        t.e1[fb][0][l][e] = cbf(v);
        t.e1[fb][1][l][e] = cbf(v - cbf2d(cbf(v)));
      }
      const int a = 2 * (2 * (f >> 1) + 1) * (8 * h + e);
      const double v = ri ? s128(a) : c128(a);
      t.e2[0][l][e] = cbf(v);
      t.e2[1][l][e] = cbf(v - cbf2d(cbf(v)));
      t.f2[l][e] = cbf(ri ? -v : v);
    }
  for (int m = -15; m < 48; ++m)
    for (int f = 0; f < 64; ++f) {
      const int a = (2 * (f >> 1) + 1) * m;
      t.g1[m + 16][(f >> 3) ^ (((m + 16) >> 1) & 7)][f & 7] = cbf(((f & 1) ? -s128(a) : c128(a)) / 32.0);
    }
  for (int m = 0; m < 16; ++m)
    for (int f = 0; f < 32; ++f) {
      const int a = 2 * (2 * (f >> 1) + 1) * m;
      t.g2[m][f] = cbf(((f & 1) ? -s128(a) : c128(a)) / 16.0);
    }
  return t;
}
__device__ __attribute__((aligned(64))) const Tabs tabs = make_tabs();

constexpr int OOB = 0x7FFFFF00;
constexpr float C2 = 2.885390081777927f;
#define EMAXV 120.f
constexpr float EMAX = EMAXV;
constexpr int WT = 16;
constexpr int NT = 2;
constexpr int R1 = 336;
constexpr int R2 = 504;
constexpr int PL1 = R1 * 128;
constexpr int PL2 = R2 * 128;
constexpr int U1BYTES = 32 * 16 * PL1, U2BYTES = 16 * 16 * PL2;
constexpr int WBIN = 16 * 16 * 2 * 1024;
constexpr int YP1 = R1 * 64;
constexpr int YP2 = 500 * 64;
constexpr int Y1BYTES = 32 * 16 * YP1, Y2BYTES = 16 * 16 * YP2;
__device__ __forceinline__ int dmap(int cb, int n) { return 64 * (cb >> 2) + 32 * (cb & 1) + 2 * n + ((cb >> 1) & 1); }

__device__ __forceinline__ void swap16(u32x4& a, u32x4& b) {
#pragma unroll
  for (int i = 0; i < 4; ++i) {
    const auto r = __builtin_amdgcn_permlane16_swap(a[i], b[i], false, false);
    a[i] = r[0];
    b[i] = r[1];
  }
}

typedef __attribute__((address_space(3))) u32x4 lds_u32x4;
__device__ __forceinline__ u32x4 lds_ld(int a) { return *(const lds_u32x4*)(uintptr_t)(uint32_t)a; }
__device__ __forceinline__ uint32_t lds_ld32(int a) { return *(const __attribute__((address_space(3))) uint32_t*)(uintptr_t)(uint32_t)a; }
template <int SZ>
__device__ __forceinline__ void dma(__amdgpu_buffer_rsrc_t rsrc, char* lds, int voff, int soff) {
#if defined(__HIP_DEVICE_COMPILE__)
  typedef __attribute__((address_space(3))) void* ldsp;
  if constexpr (SZ == 16) __builtin_amdgcn_raw_ptr_buffer_load_lds(rsrc, (ldsp)lds, 16, voff, soff, 0, 0);
  else __builtin_amdgcn_raw_ptr_buffer_load_lds(rsrc, (ldsp)lds, 4, voff, soff, 0, 0);
#endif
}

#define LABEL_ADDR(p, name) asm volatile("s_getpc_b64 s[40:41]\n\ts_add_u32 s40, s40, " name "@rel32@lo+4\n\ts_addc_u32 s41, s41, " name "@rel32@hi+12\n\ts_mov_b64 %0, s[40:41]" : "=s"(p) :: "s40", "s41", "scc")
__device__ __forceinline__ void code_pf(const char* p, int n, int part, char* lds) {
  dma<4>(mkbuf(p, n), lds, part * 8192 + (threadIdx.x & 63) * 128, 0);
}

template <int CONV>
__device__ __forceinline__ void wtrans(const uint16_t* __restrict__ w, char* __restrict__ wh, int wid, int lane) {
  const int dq = wid & 3, pc = wid >> 2;
  const int ks = pc >> 2, kq = pc & 3;
  const int n32 = lane & 31, h = lane >> 5;
  const int r = CONV == 1 ? ks >> 3 : kq >> 1;
  const int c0 = CONV == 1 ? (ks & 7) * 32 + kq * 8 : dmap(ks, 4 * (kq & 1));
  const __amdgpu_buffer_rsrc_t wr = mkbuf(w, 31 * 131072);
  const int vb = (16 * h + r + 1) * 131072 + c0 * 512 + dq * 128 + n32 * 4;
  const int vb0 = (h + r == 0) ? OOB : vb - 262144;
  constexpr int NFB = CONV == 1 ? 2 : 1;
  const u32x4* et = CONV == 1 ? (const u32x4*)&tabs.e1[0][0][0][0] : (const u32x4*)&tabs.e2[0][0][0];
  u32x4 E[NFB][2];
#pragma unroll
  for (int fb = 0; fb < NFB; ++fb) { E[fb][0] = et[fb * 128 + lane]; E[fb][1] = et[fb * 128 + 64 + lane]; }
  uint32_t v[8][8];
#pragma unroll
  for (int el = 0; el < 8; ++el)
#pragma unroll
    for (int jj = 0; jj < 8; ++jj) {
      const int co = CONV == 1 ? el * 512 : (el & 3) * 1024;
      const int so = (CONV == 1 ? 0 : (el >> 2) * 8192) + (jj ? (jj - 1) * 262144 : 0);
      v[el][jj] = __builtin_amdgcn_raw_buffer_load_b32(wr, (jj ? vb : vb0) + co, so, NT);
    }
  PHASE();
  const __amdgpu_buffer_rsrc_t dr = mkbuf(wh, (CONV == 1 ? 32 : 16) * WBIN);
  const int vo = 2 * h * WBIN + (4 * dq + (n32 >> 4)) * 32768 + ks * 2048 + kq * 256 + (n32 & 15) * 16;
#pragma unroll
  for (int e = 0; e < 2; ++e) {
    u32x4 b[8];
#pragma unroll
    for (int el = 0; el < 8; ++el)
#pragma unroll
      for (int j = 0; j < 4; ++j) b[el][j] = __builtin_amdgcn_perm(v[el][2 * j + 1], v[el][2 * j], e ? 0x07060302u : 0x05040100u);
#pragma unroll
    for (int fb = 0; fb < NFB; ++fb) {
      u32x4 o[16];
#pragma unroll
      for (int ep = 0; ep < 4; ++ep) {
        f32x16 d[2];
#pragma unroll
        for (int z = 0; z < 2; ++z) d[z] = mfma32(E[fb][1], b[2 * ep + z], zero16());
#pragma unroll
        for (int z = 0; z < 2; ++z) d[z] = mfma32(E[fb][0], b[2 * ep + z], d[z]);
#pragma unroll
        for (int i = 0; i < 16; ++i) o[i][ep] = pk2bf(d[0][i], d[1][i]);
      }
#pragma unroll
      for (int i = 0; i < 16; ++i)
        __builtin_amdgcn_raw_buffer_store_b128(o[i], dr, vo + (i & 1) * 1024,
                                               (16 * fb + 4 * (i >> 2) + ((i >> 1) & 1)) * WBIN + e * 65536, WT);
      SB();
    }
  }
}

__device__ __forceinline__ void xload(const uint16_t* __restrict__ x, int u, int lane, uint32_t (&v)[3][8]) {
  const int cq = u & 7, b = (u >> 3) & 1, sg = u >> 4;
  const __amdgpu_buffer_rsrc_t xr = mkbuf(x + (size_t)b * 16000 * DCH, 8192000);
#pragma unroll
  for (int ks = 0; ks < 3; ++ks) {
    const int row0 = 48 * sg - 24 + 16 * ks + 8 * (lane >> 5);
    const int vb = row0 >= 0 ? row0 * 1024 + cq * 128 + (lane & 31) * 4 : OOB;
#pragma unroll
    for (int e = 0; e < 8; ++e) v[ks][e] = __builtin_amdgcn_raw_buffer_load_b32(xr, vb + (e & 3) * 1024, (e >> 2) * 4096, NT);
  }
}
__device__ __forceinline__ void xtrans(char* __restrict__ u1, int u, int lane, const uint32_t (&v)[3][8], const u32x4 (&F)[2][3]) {
  const __amdgpu_buffer_rsrc_t ur = mkbuf(u1 + (2 * (u & 7)) * PL1 + (u >> 3) * 128, U1BYTES - (2 * (u & 7)) * PL1 - (u >> 3) * 128);
  const int vo = ((lane & 15) >> 1) * 16 * PL1 + ((lane >> 4) & 1) * 64 + (lane & 1) * 32 + (lane >> 5) * 16;
  u32x4 a[2][3];
#pragma unroll
  for (int ks = 0; ks < 3; ++ks)
#pragma unroll
    for (int j = 0; j < 4; ++j) {
      a[0][ks][j] = __builtin_amdgcn_perm(v[ks][2 * j + 1], v[ks][2 * j], 0x05040100u);
      a[1][ks][j] = __builtin_amdgcn_perm(v[ks][2 * j + 1], v[ks][2 * j], 0x07060302u);
    }
#pragma unroll
  for (int fb = 0; fb < 2; ++fb) {
    f32x16 d[2] = {zero16(), zero16()};
#pragma unroll
    for (int ks = 0; ks < 3; ++ks)
#pragma unroll
      for (int e = 0; e < 2; ++e) d[e] = mfma32(a[e][ks], F[fb][ks], d[e]);
#pragma unroll
    for (int qp = 0; qp < 2; ++qp) {
      u32x4 o[2];
#pragma unroll
      for (int z = 0; z < 2; ++z)
#pragma unroll
        for (int i = 0; i < 4; ++i) o[z][i] = pk2bf(d[0][8 * qp + 4 * z + i], d[1][8 * qp + 4 * z + i]);
      swap16(o[0], o[1]);
#pragma unroll
      for (int z = 0; z < 2; ++z) __builtin_amdgcn_raw_buffer_store_b128(o[z], ur, vo, ((fb * 16 + z * 8) * 16 + qp) * PL1, WT);
    }
  }
}

__global__ __launch_bounds__(512, 2) void pre_kernel(const uint16_t* __restrict__ x, const uint16_t* __restrict__ w1,
                                                     const uint16_t* __restrict__ w2, const uint16_t* __restrict__ wf,
                                                     const uint16_t* __restrict__ wo, const uint16_t* __restrict__ g,
                                                     const uint16_t* __restrict__ wg, char* __restrict__ u1,
                                                     char* __restrict__ w1h, char* __restrict__ w2h, u32x4* __restrict__ wff,
                                                     u32x4* __restrict__ wof, float* __restrict__ ge) {
  const int blk = blockIdx.x;
  const int tid = threadIdx.x, lane = tid & 63;
  const int wave = __builtin_amdgcn_readfirstlane(tid >> 6);
  const int n = lane & 31, h = lane >> 5;
  __shared__ char pfs[256];
  if (wave == PF1) {
    const char *p0 = (const char*)&tabs - 320, *p1;
    LABEL_ADDR(p1, ".Lce_g2");
    code_pf(p0, (int)(p1 - p0), blk >> 3, pfs);
  }
  if (wave == 0) {
    wtrans<1>(w1, w1h, blk, lane);
  } else if (wave == 1) {
    wtrans<2>(w2, w2h, blk, lane);
  } else {
    const int bq = (blk & 7) * 32 + (blk >> 3);
    const int u0 = ((21 * bq) >> 1) + wave - 2, ue = (21 * (bq + 1)) >> 1;
    u32x4 F[2][3];
#pragma unroll
    for (int fb = 0; fb < 2; ++fb)
#pragma unroll
      for (int ks = 0; ks < 3; ++ks) F[fb][ks] = ((const u32x4*)&tabs.f1[fb][ks][0][0])[lane];
    uint32_t v0[3][8];
    xload(x, u0, lane, v0);
    if (u0 + 6 < ue) {
      uint32_t v1[3][8];
      xload(x, u0 + 6, lane, v1);
      PHASE();
      xtrans(u1, u0, lane, v0, F);
      SB();
      xtrans(u1, u0 + 6, lane, v1, F);
    } else if (wave == 7) {
      const int s = blk & 15, eb = (blk >> 4) & 7, second = blk >> 7;
      const int r = (s - 4 * (eb >> 1)) & 15;
      const uint16_t* src = second ? wo + (16 * s + 4 * h) * DCH + 64 * (eb >> 1) + 2 * n + (eb & 1) : wf + dmap(s, 4 * h) * DCH + 32 * eb + n;
      const int st = second ? DCH : 2 * DCH;
      uint32_t v[8];
#pragma unroll
      for (int j = 0; j < 8; ++j) v[j] = src[(8 * (j >> 2) + (j & 3)) * st];
      PHASE();
      xtrans(u1, u0, lane, v0, F);
      const float sc = second ? 2.f : 1.f;
      u32x4 o;
#pragma unroll
      for (int j = 0; j < 4; ++j) o[j] = pk2bf(bf2f(v[2 * j]) * sc, bf2f(v[2 * j + 1]) * sc);
      (second ? wof + (((eb >> 1) * 16 + r) * 2 + (eb & 1)) * 64 : wff + (eb * 16 + r) * 64)[lane] = o;
    } else if (blk < 8) {
      const int c0 = 32 * blk + 2 * (lane & 15), kq = lane >> 4;
      uint32_t v[8][8];
      u32x4 ga[8];
#pragma unroll
      for (int ks = 0; ks < 8; ++ks) {
        ga[ks] = *(const u32x4*)(g + (lane & 15) * DCH + ks * 32 + 8 * kq);
#pragma unroll
        for (int e = 0; e < 8; ++e) v[ks][e] = *(const uint32_t*)(wg + (ks * 32 + 8 * kq + e) * DCH + c0);
      }
      PHASE();
      xtrans(u1, u0, lane, v0, F);
      f32x4 d[2] = {{0.f, 0.f, 0.f, 0.f}, {0.f, 0.f, 0.f, 0.f}};
#pragma unroll
      for (int ks = 0; ks < 8; ++ks)
#pragma unroll
        for (int e = 0; e < 2; ++e) {
          u32x4 bb;
#pragma unroll
          for (int j = 0; j < 4; ++j) bb[j] = __builtin_amdgcn_perm(v[ks][2 * j + 1], v[ks][2 * j], e ? 0x07060302u : 0x05040100u);
          d[e] = mfma16(ga[ks], bb, d[e]);
        }
      float* dst = ge + ((((blk >> 1) * 2 + (kq & 1)) * 2 + (blk & 1)) * 4 + ((lane >> 2) & 3)) * 64 + (((lane >> 1) & 1) * 8 + (kq >> 1) * 4) * 4 + 2 * (lane & 1);
      f32x2 gs[4];
      bool far = false;
#pragma unroll
      for (int i = 0; i < 4; ++i) {
        gs[i] = f32x2{bf2f(f2bf(d[0][i])) * C2, bf2f(f2bf(d[1][i])) * C2};
        far = far || __builtin_fabsf(gs[i][0]) > EMAX || __builtin_fabsf(gs[i][1]) > EMAX;
      }
      far = __builtin_amdgcn_ballot_w64(far) != 0;
#pragma unroll
      for (int i = 0; i < 4; ++i) {
        *(f32x2*)(dst + 4 * i) = far ? f32x2{-1.f, -1.f} : f32x2{__builtin_amdgcn_exp2f(-gs[i][0]), __builtin_amdgcn_exp2f(-gs[i][1])};
        *(f32x2*)(dst + 4096 + 4 * i) = gs[i];
      }
    } else {
      PHASE();
      xtrans(u1, u0, lane, v0, F);
    }
  }
}

constexpr int issued(int g, int MB, int OPS, int NST, int BAR, int PRO) {
  int o = PRO * OPS;
  for (int t = 0; t < g; ++t)
    if (o < 16 * OPS && (o / OPS < NST || t >= (o / OPS - NST + 1) * MB + BAR)) ++o;
  return o;
}
template <int CONV>
__global__ __launch_bounds__(256, 1) void gemm_kernel(const char* __restrict__ U, const char* __restrict__ W,
                                                      char* __restrict__ Y, const char* __restrict__ pf) {
  constexpr int MB = CONV == 1 ? 11 : 8;
  constexpr int STG = MB * 2048;
  constexpr int NST = CONV == 1 ? 7 : 8;
  constexpr int PL = CONV == 1 ? PL1 : PL2;
  constexpr int NDMA = CONV == 1 ? 6 : 4;
  constexpr int OPS = NDMA + 2;
  extern __shared__ __attribute__((aligned(16))) char smem[];
  const int bid = blockIdx.x, xcd = bid & 7, slot = bid >> 3;
  const int bin = CONV == 1 ? xcd * 4 + (slot >> 3) : xcd * 2 + (slot >> 4);
  const int mt = CONV == 1 ? (slot >> 2) & 1 : (slot >> 2) & 3;
  const int tid = threadIdx.x, lane = tid & 63;
  const int wave = __builtin_amdgcn_readfirstlane(tid >> 6);
  const int cb = (slot & 3) * 4 + wave;
  const int row0 = CONV == 1 ? mt * 168 : mt * 126;
  const __amdgpu_buffer_rsrc_t ur = mkbuf(U + (size_t)bin * 16 * PL + row0 * 128, 16 * PL - row0 * 128 + (CONV == 1 ? 0 : 256));
  const __amdgpu_buffer_rsrc_t wr = mkbuf(W + (size_t)bin * WBIN + cb * 32768, 32768);
  int vo[2];
  if constexpr (CONV == 1) {
    vo[0] = wave * 1024 + (lane >> 3) * 128 + (((lane & 7) ^ ((4 * wave + (lane >> 4)) & 7)) * 16);
    vo[1] = 20480 + wave * 256 + (lane >> 5) * 128 + ((((lane >> 2) & 7) ^ wave) * 16) + (lane & 3) * 4;
  } else {
#pragma unroll
    for (int z = 0; z < 2; ++z) vo[z] = wave * 4096 + (lane >> 3) * 128 + (((lane & 7) ^ (2 * wave + z)) * 16);
  }
  u32x4 B[NST][2];
  auto issue = [&](auto is, auto ii) {
    constexpr int s = decltype(is)::value, i = decltype(ii)::value;
    char* buf = smem + (s % NST) * STG;
    if constexpr (i < 2) {
      B[s % NST][i] = __builtin_amdgcn_raw_buffer_load_b128(wr, lane * 16 + i * 1024, s * 2048, 0);
    } else if constexpr (CONV == 1) {
      if constexpr (i < 7) dma<16>(ur, buf + wave * 1024 + (i - 2) * 4096, vo[0], s * PL + (i - 2) * 4096);
      else dma<4>(ur, buf + 20480 + wave * 256, vo[1], s * PL);
    } else {
      dma<16>(ur, buf + wave * 4096 + (i - 2) * 1024, vo[(i - 2) >> 1], s * PL + (i - 2) * 1024);
    }
  };
  constexpr int AR = 4, AL = 3;
  constexpr int BAR = MB - AL;
  constexpr int PRO = GPRO;
  sfor<0, PRO>([&](auto is) { sfor<0, OPS>([&](auto ii) { issue(is, ii); SB(); }); });
  const int rho = lane & 15, kq = lane >> 4;
  const int lds0 = (int)(uint32_t)(uintptr_t)(__attribute__((address_space(3))) char*)smem;
  constexpr int WIN = CONV == 1 ? 2 : 4;
  constexpr int NWIN = (NST + WIN - 1) / WIN;
  int ab[NWIN][2];
#pragma unroll
  for (int ri = 0; ri < 2; ++ri) {
    const int pos = ((kq >> 1) * 4 + ri * 2 + (kq & 1)) ^ (rho >> 1);
    const int a = lds0 + (CONV == 1 ? rho * 128 : (rho >> 1) * 2048 + (rho & 1) * 128) + pos * 16;
#pragma unroll
    for (int wn = 0; wn < NWIN; ++wn) { ab[wn][ri] = a + wn * WIN * STG; asm volatile("" : "+v"(ab[wn][ri])); }
  }
  auto aread = [&](int s, int mb, int ri) -> u32x4 {
    const int bf = s % NST;
    return lds_ld(ab[bf / WIN][ri] + (bf % WIN) * STG + mb * (CONV == 1 ? 2048 : 256));
  };
  f32x4 yr[MB], yi[MB];
#pragma unroll
  for (int mb = 0; mb < MB; ++mb) { yr[mb] = f32x4{0.f, 0.f, 0.f, 0.f}; yi[mb] = f32x4{0.f, 0.f, 0.f, 0.f}; }
  u32x4 A[AR][2];
  asm volatile("s_waitcnt vmcnt(%0)" ::"n"((PRO - 1) * OPS) : "memory");
  __builtin_amdgcn_s_barrier();
  asm volatile("" ::: "memory");
#pragma unroll
  for (int z = 0; z < AL; ++z) { A[z][0] = aread(0, z, 0); A[z][1] = aread(0, z, 1); }
  SB();
  u32x4 nwi;
#pragma unroll
  for (int j = 0; j < 4; ++j) nwi[j] = B[0][1][j] ^ 0x80008000u;
  sfor<0, 16>([&](auto is) {
    constexpr int s = decltype(is)::value;
    u32x4 nwn;
    sfor<0, MB>([&](auto im) {
      constexpr int mb = decltype(im)::value;
      constexpr int g = s * MB + mb, gn = g + AL;
      constexpr int o0 = issued(g, MB, OPS, NST, BAR, PRO), o1 = issued(g + 1, MB, OPS, NST, BAR, PRO);
      if constexpr (mb == BAR && s + 1 < 16) {
        static_assert(o0 >= (s + 2) * OPS, "next stage not requested yet");
        asm volatile("s_waitcnt vmcnt(%0)" ::"n"(o0 - (s + 2) * OPS) : "memory");
        __builtin_amdgcn_s_barrier();
        asm volatile("" ::: "memory");
        SB();
      }
      yr[mb] = mfma16(B[s % NST][0], A[g % AR][0], yr[mb]);
      SB();
      yr[mb] = mfma16(nwi, A[g % AR][1], yr[mb]);
      if constexpr (gn < 16 * MB) A[gn % AR][0] = aread(gn / MB, gn % MB, 0);
      SB();
      yi[mb] = mfma16(B[s % NST][1], A[g % AR][0], yi[mb]);
      if constexpr (o1 > o0) issue(IC<o0 / OPS>{}, IC<o0 % OPS>{});
      SB();
      yi[mb] = mfma16(B[s % NST][0], A[g % AR][1], yi[mb]);
      if constexpr (gn < 16 * MB) A[gn % AR][1] = aread(gn / MB, gn % MB, 1);
      if constexpr (mb == MB - 2 && s + 1 < 16) {
#pragma unroll
        for (int j = 0; j < 4; ++j) nwn[j] = B[(s + 1) % NST][1][j] ^ 0x80008000u;
      }
      SB();
    });
    if constexpr (s + 1 < 16) nwi = nwn;
  });
  if constexpr (CONV == 1) {
    const __amdgpu_buffer_rsrc_t yb = mkbuf(Y + (size_t)(bin * 16 + cb) * YP1 + row0 * 64, 168 * 64);
#pragma unroll
    for (int mb = 0; mb < MB; ++mb) {
      u32x4 o;
#pragma unroll
      for (int i = 0; i < 4; ++i) o[i] = pk2bf(yr[mb][i], yi[mb][i]);
      __builtin_amdgcn_raw_buffer_store_b128(o, yb, rho * 64 + kq * 16 + (mb & 3) * 1024, (mb >> 2) * 4096, WT);
    }
  } else {
    const __amdgpu_buffer_rsrc_t yb = mkbuf(Y + (size_t)(bin * 16 + cb) * YP2 + row0 * 64, YP2 - row0 * 64);
    const float sg = (bin & 1) ? -1.f : 1.f;
    f32x4 nr, ni;
#pragma unroll
    for (int i = 0; i < 4; ++i) { nr[i] = __shfl_down(yr[0][i], 2, 16); ni[i] = __shfl_down(yi[0][i], 2, 16); }
    const int vy = (rho >> 1) * 1024 + (rho & 1) * 64 + kq * 16;
#pragma unroll
    for (int mb = 0; mb < MB; ++mb) {
      const f32x4 xr = mb < MB - 1 ? yr[mb < MB - 1 ? mb + 1 : 0] : nr, xi = mb < MB - 1 ? yi[mb < MB - 1 ? mb + 1 : 0] : ni;
      u32x4 o;
#pragma unroll
      for (int i = 0; i < 4; ++i) o[i] = pk2bf(__builtin_fmaf(sg, xi[i], yr[mb][i]), __builtin_fmaf(-sg, xr[i], yi[mb][i]));
      __builtin_amdgcn_raw_buffer_store_b128(o, yb, (mb == MB - 1 && rho >= 14) ? OOB : vy + mb * 128, 0, WT);
    }
  }
  if constexpr (CONV == 1) asm volatile(".Lce_g1:"); else asm volatile(".Lce_g2:");
}

template <int Z>
__device__ __forceinline__ void mid_unit(char* __restrict__ U, int ya, int ga, int cb, int s, const u32x4& F) {
  const int lane = threadIdx.x & 63;
  const int n32 = lane & 31, h = lane >> 5;
  constexpr int NSG = Z == 0 ? 1 : 2;
  const int tau = 16 * ((n32 >> 2) & 1) + 8 * ((n32 >> 3) & 1) + 2 * (n32 & 3) + (n32 >> 4);
  const int r[2] = {Z == 0 ? tau + 16 : Z == 1 ? (tau < 16 ? tau + 48 : 0) : tau + 32, Z == 1 ? tau : (tau >= 16 ? tau - 16 : 0)};
  f32x16 d = zero16();
#pragma unroll
  for (int sg = 0; sg < NSG; ++sg)
#pragma unroll
    for (int ks = 0; ks < 4; ++ks) {
      u32x4 yv;
#pragma unroll
      for (int j = 0; j < 4; ++j) yv[j] = lds_ld32(ya + sg * 4096 + ks * 1024 + j * 128);
      d = mfma32(lds_ld(ga + r[sg] * 128 + (((2 * ks + h) ^ ((r[sg] >> 1) & 7)) * 16)), yv, d);
    }
  const __amdgpu_buffer_rsrc_t ub = mkbuf(U, U2BYTES);
  const int vu = (((lane & 15) >> 1) * 16 + cb) * PL2 + s * 256 + ((lane >> 4) & 1) * 64 + (lane & 1) * 32 + h * 16;
  const int t0r = 32 * s - 16 + 16 * h;
  const bool ok = t0r >= 0 && t0r < 8000;
  f32x16 u[2];
#pragma unroll
  for (int q = 0; q < 2; ++q) {
    u32x4 a;
#pragma unroll
    for (int j = 0; j < 4; ++j) {
      const uint32_t gv = gelu2(d[8 * q + 2 * j], d[8 * q + 2 * j + 1]);
      a[j] = ok ? gv : 0u;
    }
    u[q] = mfma32(a, F, zero16());
  }
#pragma unroll
  for (int b = 0; b < 2; ++b) {
    u32x4 o[2];
#pragma unroll
    for (int q = 0; q < 2; ++q)
#pragma unroll
      for (int j = 0; j < 4; ++j) o[q][j] = pk2bf(u[q][8 * b + 2 * j], u[q][8 * b + 2 * j + 1]);
    swap16(o[0], o[1]);
#pragma unroll
    for (int z = 0; z < 2; ++z) __builtin_amdgcn_raw_buffer_store_b128(o[z], ub, vu + b * 128, z * 8 * 16 * PL2, WT);
  }
}
__global__ __launch_bounds__(1024) void mid_kernel(const char* __restrict__ Y, char* __restrict__ U) {
  __shared__ __attribute__((aligned(16))) char sm[8192 + 49152];
  __shared__ uint32_t landed;
  const int blk = blockIdx.x, lane = threadIdx.x & 63;
  const int wave = __builtin_amdgcn_readfirstlane(threadIdx.x >> 6);
  const int cb = blk & 15, s0 = blk & ~15, s = s0 + wave;
  const int sg0 = (2 * s0) / 3;
  if (wave < 4) {
    const __amdgpu_buffer_rsrc_t tb = mkbuf(&tabs.g1[0][0][0], 8192);
#pragma unroll
    for (int i = 0; i < 2; ++i) dma<16>(tb, sm + wave * 2048 + i * 1024, lane * 16, wave * 2048 + i * 1024);
  }
  {
    const __amdgpu_buffer_rsrc_t yb = mkbuf(Y, Y1BYTES);
    const int vy = ((lane >> 3) * 16 + cb) * YP1 + sg0 * 128 + (lane & 7) * 16;
#pragma unroll
    for (int i = 0; i < 3; ++i) {
      const int pc = 3 * wave + i;
      dma<16>(yb, sm + 8192 + pc * 1024, vy, (pc & 3) * 128 * YP1 + (pc >> 2) * 128);
    }
  }
  const u32x4 F = ((const u32x4*)&tabs.f2[0][0])[lane];
  if (threadIdx.x == 0) landed = 0;
  asm volatile("s_waitcnt lgkmcnt(0)\n\ts_barrier" ::: "memory");
  {
    asm volatile("s_waitcnt vmcnt(0)" ::: "memory");
    if (lane == 0) __hip_atomic_fetch_or(&landed, 1u << wave, __ATOMIC_RELAXED, __HIP_MEMORY_SCOPE_WORKGROUP);
    if (s < 251) {
      const int p = s / 3, z = s - 3 * p;
      const int l0 = 2 * p + (z == 2) - sg0, l1 = l0 + (z != 0);
      const uint32_t need = 15u | (((2u << ((4 * l1 + 3) / 3)) - 1u) & ~((1u << ((4 * l0) / 3)) - 1u));
      while ((__builtin_amdgcn_readfirstlane(__hip_atomic_load(&landed, __ATOMIC_RELAXED, __HIP_MEMORY_SCOPE_WORKGROUP)) & need) != need)
        __builtin_amdgcn_s_sleep(1);
      asm volatile("" ::: "memory");
      const int l = (int)(uint32_t)(uintptr_t)(__attribute__((address_space(3))) char*)sm;
      const int ya = l + 8192 + l0 * 4096 + (lane >> 5) * 512 + (lane & 31) * 4;
      if (z == 0) mid_unit<0>(U, ya, l, cb, s, F);
      else if (z == 1) mid_unit<1>(U, ya, l, cb, s, F);
      else mid_unit<2>(U, ya, l, cb, s, F);
    }
  }
}

constexpr int J_F = 0;
constexpr int J_T = 16384;
constexpr int J_G = 49152;
constexpr int JOINT_LDS = 65536;
__device__ __forceinline__ void lds_st(int a, const u32x4& v) { *(lds_u32x4*)(uintptr_t)(uint32_t)a = v; }
#define BAR() do { SB(); asm volatile("s_waitcnt lgkmcnt(0)\n\ts_barrier" ::: "memory"); SB(); } while (0)
template <int M, bool Z>
__device__ __forceinline__ void mm(f32x16& c, const u32x4& a, const u32x4& b) {
  if constexpr (Z) {
    if constexpr (M == 0) asm volatile("v_mfma_f32_32x32x16_bf16 %0, %1, %2, 0" : "=&v"(c) : "v"(a), "v"(b));
    else if constexpr (M == 1) asm volatile("v_mfma_f32_32x32x16_bf16 %0, %1, %2, 0" : "=&v"(c) : "a"(a), "v"(b));
    else asm volatile("v_mfma_f32_32x32x16_bf16 %0, %1, %2, 0" : "=&v"(c) : "v"(a), "a"(b));
  } else {
    if constexpr (M == 0) asm volatile("v_mfma_f32_32x32x16_bf16 %0, %1, %2, %0" : "+v"(c) : "v"(a), "v"(b));
    else if constexpr (M == 1) asm volatile("v_mfma_f32_32x32x16_bf16 %0, %1, %2, %0" : "+v"(c) : "a"(a), "v"(b));
    else asm volatile("v_mfma_f32_32x32x16_bf16 %0, %1, %2, %0" : "+v"(c) : "v"(a), "a"(b));
  }
}
#define MWAIT(c0, c1) asm volatile("s_nop 7\n\ts_nop 3" : "+v"(c0), "+v"(c1))
template <int K>
__device__ __forceinline__ float add_quad(float a, float b) {
  float r;
  asm volatile("v_add_f32_dpp %0, %1, %2 quad_perm:[%3,%3,%3,%3] row_mask:0xf bank_mask:0xf" : "=v"(r) : "v"(a), "v"(b), "n"(K));
  return r;
}

#define WI 2
#define WA 6
#define WB 4
#define WZ 3
constexpr int wcount(int t) {
  int c = 0;
  for (int i = 0; i < t; ++i) c += i < 4 ? WI : i == 4 ? WA : i < 8 ? WB : i < 24 ? 0 : i < 56 ? (i % WZ != 0) : i < 76 ? (i & 1) : 1;
  return c < 64 ? c : 64;
}
__global__ __launch_bounds__(256, 1) void joint_kernel(const char* __restrict__ Y, const char* __restrict__ wff,
                                                       const float* __restrict__ ge, uint16_t* __restrict__ out) {
  extern __shared__ __attribute__((aligned(16))) char smem[];
  const int blk = blockIdx.x;
  const int tid = threadIdx.x, lane = tid & 63;
  const int w = __builtin_amdgcn_readfirstlane(tid >> 6);
  const int n = lane & 31, h = lane >> 5;
  const int l0 = (int)(uint32_t)(uintptr_t)(__attribute__((address_space(3))) char*)smem;
  u32x4 yv[4][2], gi[2][2];
#pragma unroll
  for (int ks = 0; ks < 2; ++ks) gi[0][ks] = *(const u32x4*)&tabs.g2[4 * (n & 3) + ((n >> 2) & 3)][16 * ks + 8 * h];
  SB();
  const __amdgpu_buffer_rsrc_t yb = mkbuf(Y, Y2BYTES);
  const int vy = (4 * h * 16 + 4 * w) * YP2 + blk * 128 + n * 4;
  const __amdgpu_buffer_rsrc_t wb = mkbuf(wff + w * 32768, 131072 + 32768);
  u32x4 wt[64];
  auto wload = [&](int i0, int i1) {
#pragma unroll
    for (int i = i0; i < i1; ++i) wt[i] = __builtin_amdgcn_raw_buffer_load_b128(wb, lane * 16 + (i & 3) * 1024, (i >> 5) * 131072 + ((i >> 2) & 7) * 4096, 0);
    SB();
  };
  auto wstep = [&](auto it) { constexpr int i0 = wcount(decltype(it)::value), i1 = wcount(decltype(it)::value + 1); wload(i0, i1); };
  sfor<0, 4>([&](auto ic) {
    constexpr int c = decltype(ic)::value;
#pragma unroll
    for (int ks = 0; ks < 2; ++ks)
#pragma unroll
      for (int j = 0; j < 4; ++j) yv[c][ks][j] = __builtin_amdgcn_raw_buffer_load_b32(yb, vy, ((8 * ks + j) * 16 + c) * YP2, 0);
    SB();
    wstep(ic);
  });
  dma<4>(mkbuf(wff, 262144), smem + J_T + w * 256, (lane * 124 + (blk >> 3) * 4 + w) * 128, 0);
  SB();
#pragma unroll
  for (int ks = 0; ks < 2; ++ks) {
    gi[1][ks] = n >= 16 ? gi[0][ks] : u32x4{0u, 0u, 0u, 0u};
    gi[0][ks] = n < 16 ? gi[0][ks] : u32x4{0u, 0u, 0u, 0u};
  }
  int ra[3];
#pragma unroll
  for (int i = 0; i < 3; ++i) ra[i] = l0 + ((w + 1 + i) & 3) * 4096 + lane * 16;
  const int wa = l0 + w * 4096 + lane * 16;
  const int qa = l0 + J_G + w * 4096 + (h * 8 + (n >> 4) * 4 + (n & 3)) * 16;
  asm volatile("s_nop 1" : "+v"(gi[0][0]), "+v"(gi[0][1]), "+v"(gi[1][0]), "+v"(gi[1][1]));
  u32x4 fb[16];
  sfor<0, 2>([&](auto icp) {
    constexpr int cp = decltype(icp)::value;
    f32x16 d;
    sfor<0, 4>([&](auto iI) {
      constexpr int c = decltype(iI)::value >> 1, ks = decltype(iI)::value & 1;
      mm<0, c + ks == 0>(d, yv[2 * cp + c][ks], gi[c][ks]);
    });
    SB();
    if constexpr (cp) wstep(IC<5>{});
    asm volatile("s_nop 7\n\ts_nop 3" : "+v"(d));
#pragma unroll
    for (int j = 0; j < 4; ++j) fb[2 * cp][j] = gelu2(d[2 * j], d[2 * j + 1]);
    SB();
    if constexpr (cp) wstep(IC<6>{});
#pragma unroll
    for (int j = 0; j < 4; ++j) fb[2 * cp + 1][j] = gelu2(d[8 + 2 * j], d[8 + 2 * j + 1]);
    swap16(fb[2 * cp], fb[2 * cp + 1]);
#pragma unroll
    for (int c = 0; c < 2; ++c) lds_st(wa + J_F + (2 * cp + c) * 1024, fb[2 * cp + c]);
    SB();
    if constexpr (cp) {
      const __amdgpu_buffer_rsrc_t gb = mkbuf(ge + w * 1024, 4096);
#pragma unroll
      for (int i = 0; i < 4; ++i) dma<16>(gb, smem + J_G + w * 4096 + i * 1024, lane * 16, i * 1024);
      SB();
    }
    wstep(IC<cp ? 7 : 4>{});
  });
  f32x16 fe[2];
  f32x4 q[16];
  sfor<0, 16>([&](auto ir) {
    constexpr int r = decltype(ir)::value;
    mm<1, r == 0>(fe[0], wt[r], fb[r]);
    if constexpr (r == 0) { BAR(); }
    SB();
    asm volatile("" :: "a"(wt[r + 1]));
    if constexpr (r >= 3 && r < 15) asm volatile("" :: "v"(fb[r + 1]));
    if constexpr (r < 6) {
      fb[2 * r + 4] = lds_ld(ra[r >> 1] + J_F + ((2 * r) & 3) * 1024);
      fb[2 * r + 5] = lds_ld(ra[r >> 1] + J_F + ((2 * r + 1) & 3) * 1024);
    } else if constexpr (r >= 12) {
      if constexpr (r == 12) asm volatile("" :: "a"(wt[wcount(7)]) : "memory");
      q[2 * r - 24] = __builtin_bit_cast(f32x4, lds_ld(qa + (2 * r - 24) * 256));
      q[2 * r - 23] = __builtin_bit_cast(f32x4, lds_ld(qa + (2 * r - 23) * 256));
    }
    SB();
    wstep(IC<24 + r>{});
  });
  float p[32], x[8], mx = 0.f;
  u32x4 tp[2][4];
  auto stepE = [&](int i) {
    fe[i >> 4][i & 15] *= C2;
    mx = __builtin_fmaxf(mx, __builtin_fabsf(fe[i >> 4][i & 15]));
    p[i] = __builtin_amdgcn_exp2f(fe[i >> 4][i & 15]);
  };
  auto twrite = [&](auto iG) {
    constexpr int W = decltype(iG)::value + 31;
    if constexpr (W >= 0 && W < 256 && (W & 7) == 7) lds_st(wa + J_T + ((W >> 5) & 1) * 16384 + ((W >> 3) & 3) * 1024, tp[(W >> 5) & 1][(W >> 3) & 3]);
    SB();
  };
  auto tslot = [&](auto iG) {
    constexpr int G = decltype(iG)::value;
    constexpr int A = G + 36, B = G + 35, C = G + 34, D = G + 33;
    if constexpr (C >= 0 && C < 256) asm volatile("v_fma_f32 %0, -%1, %0, 0.5" : "+v"(x[C & 7]) : "v"(q[((C >> 5) & 1) * 8 + ((C >> 2) & 7)][C & 3]));
    if constexpr (B >= 0 && B < 256) asm volatile("v_rcp_f32 %0, %0" : "+v"(x[B & 7]));
    if constexpr (A >= 0 && A < 256) x[A & 7] = add_quad<(A >> 6)>(p[A & 31], q[((A >> 5) & 1) * 8 + ((A >> 2) & 7)][A & 3]);
    SB();
    if constexpr (D >= 0 && D < 256 && (D & 1)) tp[(D >> 5) & 1][(D >> 3) & 3][(D >> 1) & 3] = pk2bf(x[(D - 1) & 7], x[D & 7]);
    SB();
  };
  sfor<0, 16>([&](auto ir) {
    constexpr int r = decltype(ir)::value;
    mm<1, r == 0>(fe[1], wt[16 + r], fb[r]);
    SB();
    if constexpr (r < 15) asm volatile("" :: "a"(wt[17 + r]));
    if constexpr (r == 3) asm volatile("" :: "v"(q[7]));
    if constexpr (r == 12) asm volatile("" :: "v"(q[15]));
    if constexpr (r < 8) q[8 + r] = __builtin_bit_cast(f32x4, lds_ld(qa + (8 + r) * 256));
    wstep(IC<40 + r>{});
    if constexpr (r >= 4) twrite(IC<r - 40>{});
    if constexpr (r >= 2 && r < 10) { stepE(2 * r - 4); stepE(2 * r - 3); SB(); }
    if constexpr (r >= 4) tslot(IC<r - 40>{});
  });
  sfor<12, 16>([&](auto ir) { twrite(IC<decltype(ir)::value - 36>{}); tslot(IC<decltype(ir)::value - 36>{}); });
  asm volatile("" : "+v"(fe[1]));
#pragma unroll
  for (int i = 16; i < 32; ++i) stepE(i);
  const __amdgpu_buffer_rsrc_t ob = mkbuf(out + (size_t)blk * 16 * 8 * DCH, 4000 * 4096 + 65536);
  const int vo = h * 4096 + w * 128 + n * 4;
  if (__builtin_expect(__builtin_amdgcn_ballot_w64(mx > EMAX || q[0][0] < 0.f || q[4][0] < 0.f) != 0, 0)) {
    wload(IC<wcount(56)>::value, 64);
#pragma unroll 1
    for (int st = 0; st < 8; ++st) {
      const float* gp = ge + 4096 + (w * 2 + (st & 1)) * 512 + (h * 8 + (n >> 4) * 4 + (n & 3)) * 4;
#pragma unroll
      for (int f = 0; f < 4; ++f) {
        u32x4 t;
#pragma unroll
        for (int c = 0; c < 2; ++c) {
          const f32x4 gv = *(const f32x4*)(gp + (2 * f + c) * 64);
          float y[4];
#pragma unroll
          for (int i = 0; i < 4; ++i)
            y[i] = 0.5f - __builtin_amdgcn_rcpf(__builtin_amdgcn_exp2f(__shfl(fe[f >> 1][8 * (f & 1) + 4 * c + i], (lane & ~3) + (st >> 1)) + gv[i]) + 1.f);
          t[2 * c] = pk2bf(y[0], y[1]);
          t[2 * c + 1] = pk2bf(y[2], y[3]);
        }
        lds_st(wa + J_T + (st & 1) * 16384 + f * 1024, t);
      }
      BAR();
      f32x16 o[2];
      sfor<0, 16>([&](auto ir) {
        constexpr int r = decltype(ir)::value;
        const u32x4 a = lds_ld((r < 4 ? wa : ra[r < 4 ? 0 : (r >> 2) - 1]) + J_T + (st & 1) * 16384 + (r & 3) * 1024);
#pragma unroll
        for (int z = 0; z < 2; ++z) mm<2, r == 0>(o[z], a, wt[32 + 2 * r + z]);
      });
      MWAIT(o[0], o[1]);
#pragma unroll
      for (int i = 0; i < 16; ++i)
        __builtin_amdgcn_raw_buffer_store_b32(pk2bf(o[0][i], o[1][i]), ob, vo + (st & 1) * 2048 + (st >> 1) * 16384 + (i & 3) * 512,
                                              (i >> 3) * (4000 * 4096) + 2 * ((i >> 2) & 1) * 4096, NT);
    }
    return;
  }
  asm volatile("s_nop 1");
  SB();
  sfor<16, 36>([&](auto ir) { constexpr int i = decltype(ir)::value; wstep(IC<40 + i>{}); twrite(IC<i - 36>{}); tslot(IC<i - 36>{}); });
  f32x16 acc[2][2];
  u32x4 a[16];
  uint32_t ov[16];
  auto store = [&](int st, int i) {
    __builtin_amdgcn_raw_buffer_store_b32(ov[i], ob, vo + ((st & 1) * 4 + (i & 3)) * 512,
                                          (i >> 3) * (4000 * 4096) + ((st >> 1) * 4 + 2 * ((i >> 2) & 1)) * 4096, NT);
  };
  sfor<0, 256>([&](auto iG) {
    constexpr int G = decltype(iG)::value, st = G >> 5, g = G & 31, r = g >> 1, z = g & 1;
    mm<2, r == 0>(acc[st & 1][z], r < 4 ? tp[st & 1][r] : a[r], wt[32 + g]);
    SB();
    if constexpr (g == 2) BAR();
    if constexpr (st == 0 && g < 31) asm volatile("" :: "a"(wt[33 + g]));
    if constexpr (z && r >= 3 && r < 15) asm volatile("" :: "v"(a[r + 1]));
    constexpr int rd = g >= 3 && g < 6 ? g + 1 : (g >= 7 && g <= 23 && z) ? (g + 7) / 2 : 0;
    constexpr int sn = g == 1 ? 15 : g == 2 ? 0 : g == 6 ? 1 : g >= 25 ? g - 17 : (g > 8 && !z && (g & 7)) ? (g - 6) / 2 - (g > 16) : -1;
    if constexpr (rd) a[rd] = lds_ld(ra[(rd >> 2) - 1] + J_T + (st & 1) * 16384 + (rd & 3) * 1024);
    if constexpr (sn >= 0 && st > (g == 1)) store(st - 1 - (g == 1), sn);
    if constexpr (st == 0) wstep(IC<76 + g>{});
    twrite(iG);
    tslot(iG);
    if constexpr (st > 0 && z) ov[r] = pk2bf(acc[(st - 1) & 1][0][r], acc[(st - 1) & 1][1][r]);
    SB();
  });
  store(6, 15);
  MWAIT(acc[1][0], acc[1][1]);
#pragma unroll
  for (int i = 0; i < 16; ++i) { ov[i] = pk2bf(acc[1][0][i], acc[1][1][i]); store(7, i); }
}

}

extern "C" void kernel_launch(void* const* d_in, const int* in_sizes, int n_in, void* d_out, int out_size, void* d_ws,
                              size_t ws_size, hipStream_t stream) {
  const uint16_t* x = (const uint16_t*)d_in[0];
  const uint16_t* w1 = (const uint16_t*)d_in[1];
  const uint16_t* w2 = (const uint16_t*)d_in[2];
  const uint16_t* g = (const uint16_t*)d_in[3];
  const uint16_t* wf = (const uint16_t*)d_in[4];
  const uint16_t* wg = (const uint16_t*)d_in[5];
  const uint16_t* wo = (const uint16_t*)d_in[6];
  uint16_t* out = (uint16_t*)d_out;
#define WOFF 0
  char* ws = (char*)d_ws + ((size_t)WOFF << 20);
  char* u1 = ws;
  char* w1h = ws + (24 << 20);
  char* w2h = ws + (40 << 20);
  char* y1 = ws + (48 << 20);
  char* u2 = ws + (60 << 20);
  char* y2 = ws + (77 << 20);
  u32x4* wff = (u32x4*)(ws + (86 << 20));
  u32x4* wof = (u32x4*)(ws + (86 << 20) + 131072);
  float* ge = (float*)(ws + (86 << 20) + 262144);
  constexpr int G1LDS = 7 * 11 * 2048 + 256, G2LDS = 8 * 8 * 2048 + 256;

  (void)hipFuncSetAttribute((const void*)gemm_kernel<1>, hipFuncAttributeMaxDynamicSharedMemorySize, G1LDS);
  (void)hipFuncSetAttribute((const void*)gemm_kernel<2>, hipFuncAttributeMaxDynamicSharedMemorySize, G2LDS);
  (void)hipFuncSetAttribute((const void*)joint_kernel, hipFuncAttributeMaxDynamicSharedMemorySize, JOINT_LDS);

  pre_kernel<<<256, 512, 0, stream>>>(x, w1, w2, wf, wo, g, wg, u1, w1h, w2h, wff, wof, ge);
  gemm_kernel<1><<<256, 256, G1LDS, stream>>>(u1, w1h, y1, (const char*)wff);
  mid_kernel<<<256, 1024, 0, stream>>>(y1, u2);
  gemm_kernel<2><<<256, 256, G2LDS, stream>>>(u2, w2h, y2, (const char*)wff);
  joint_kernel<<<250, 256, JOINT_LDS, stream>>>(y2, (const char*)wff, ge, out);
}
```

```cpp
#define PF1 7
#include <hip/hip_runtime.h>
#include <stdint.h>

typedef __attribute__((ext_vector_type(8))) short bf16x8;
typedef __attribute__((ext_vector_type(16))) float f32x16;
typedef __attribute__((ext_vector_type(4))) float f32x4;
typedef __attribute__((ext_vector_type(2))) float f32x2;
typedef __attribute__((ext_vector_type(4))) unsigned int u32x4;
typedef __attribute__((ext_vector_type(2))) unsigned int u32x2;

#define ORD0 0x0132
#define ORD1 0x2310
#define GLA 6
#define TS0 22
#define TS1 55
#define TU0 22
#define TU1 55
#define THR 7
#define THR2 7
#define GPRO 2
namespace {

constexpr int DCH = 256;

__device__ __forceinline__ float bf2f(uint32_t v) { return __uint_as_float(v << 16); }
__device__ __forceinline__ uint32_t f2bf(float f) {
  uint32_t u = __float_as_uint(f);
  u += 0x7FFF + ((u >> 16) & 1);
  return u >> 16;
}
__device__ __forceinline__ uint32_t pk2bf(float a, float b) {
  typedef __attribute__((ext_vector_type(2))) __bf16 b2;
  f32x2 v = {a, b};
  b2 r = __builtin_convertvector(v, b2);
  return __builtin_bit_cast(uint32_t, r);
}
__device__ __forceinline__ uint32_t gelu2(float a0, float a1) {
  const f32x2 x = {a0, a1};
  const f32x2 c0 = {-2.302208198144325f, -2.302208198144325f}, c1 = {-0.10294323970002349f, -0.10294323970002349f};
  const f32x2 z = x * __builtin_elementwise_fma(x * x, c1, c0);
  const f32x2 e = {__builtin_amdgcn_exp2f(z[0]), __builtin_amdgcn_exp2f(z[1])};
  const f32x2 d = e + f32x2{1.0f, 1.0f};
  const f32x2 y = x * f32x2{__builtin_amdgcn_rcpf(d[0]), __builtin_amdgcn_rcpf(d[1])};
  return pk2bf(y[0], y[1]);
}
__device__ __forceinline__ f32x16 mfma32(const u32x4& a, const u32x4& b, const f32x16& c) {
  return __builtin_amdgcn_mfma_f32_32x32x16_bf16(__builtin_bit_cast(bf16x8, a), __builtin_bit_cast(bf16x8, b), c, 0, 0, 0);
}
__device__ __forceinline__ f32x4 mfma16(const u32x4& a, const u32x4& b, const f32x4& c) {
  return __builtin_amdgcn_mfma_f32_16x16x32_bf16(__builtin_bit_cast(bf16x8, a), __builtin_bit_cast(bf16x8, b), c, 0, 0, 0);
}
__device__ __forceinline__ f32x16 zero16() {
  f32x16 z;
#pragma unroll
  for (int i = 0; i < 16; ++i) z[i] = 0.f;
  return z;
}
__device__ __forceinline__ __amdgpu_buffer_rsrc_t mkbuf(const void* p, int nbytes) {
  return __builtin_amdgcn_make_buffer_rsrc((void*)p, (short)0, nbytes, 0x00020000);
}
template <int V> struct IC { static constexpr int value = V; };
template <int I, int N, class F>
__device__ __forceinline__ void sfor(F&& f) {
  if constexpr (I < N) {
    f(IC<I>{});
    sfor<I + 1, N>(f);
  }
}
#define SB() __builtin_amdgcn_sched_barrier(0)
#define PHASE() do { SB(); asm volatile("s_waitcnt vmcnt(0)\n\ts_barrier" ::: "memory"); SB(); } while (0)

constexpr double PI_ = 3.14159265358979323846;
constexpr double c128(int m) {
  m = ((m % 128) + 128) % 128;
  if (m > 64) m = 128 - m;
  double sg = 1.0;
  if (m > 32) { m = 64 - m; sg = -1.0; }
  if (m == 32) return 0.0;
  const double a = PI_ * m / 64.0;
  double term = 1.0, sum = 1.0;
  for (int i = 1; i < 16; ++i) { term *= -a * a / ((2 * i - 1) * (2 * i)); sum += term; }
  return sg * sum;
}
constexpr double s128(int m) { return c128(m - 32); }
constexpr uint16_t cbf(double v) {
  uint32_t u = __builtin_bit_cast(uint32_t, (float)v);
  u += 0x7FFF + ((u >> 16) & 1);
  return (uint16_t)(u >> 16);
}
constexpr double cbf2d(uint16_t v) { return (double)__builtin_bit_cast(float, (uint32_t)v << 16); }
struct Tabs {
  uint16_t f1[2][3][64][8];
  uint16_t e1[2][2][64][8];
  uint16_t e2[2][64][8];
  uint16_t g1[64][8][8];
  uint16_t f2[64][8];
  uint16_t g2[16][32];
};
constexpr Tabs make_tabs() {
  Tabs t{};
  for (int l = 0; l < 64; ++l)
    for (int e = 0; e < 8; ++e) {
      const int h = l >> 5, f = l & 31, ri = f & 1;
      for (int fb = 0; fb < 2; ++fb) {
        const int k = 16 * fb + (f >> 1);
        for (int ks = 0; ks < 3; ++ks) {
          const int a = (2 * k + 1) * (16 * ks + 8 * h + e);
          t.f1[fb][ks][l][e] = cbf(ri ? -s128(a) : c128(a));
        }
        const int a = (2 * k + 1) * (8 * h + e);
        const double v = ri ? s128(a) : c128(a);
        t.e1[fb][0][l][e] = cbf(v);
        t.e1[fb][1][l][e] = cbf(v - cbf2d(cbf(v)));
      }
      const int a = 2 * (2 * (f >> 1) + 1) * (8 * h + e);
      const double v = ri ? s128(a) : c128(a);
      t.e2[0][l][e] = cbf(v);
      t.e2[1][l][e] = cbf(v - cbf2d(cbf(v)));
      t.f2[l][e] = cbf(ri ? -v : v);
    }
  for (int m = -15; m < 48; ++m)
    for (int f = 0; f < 64; ++f) {
      const int a = (2 * (f >> 1) + 1) * m;
      t.g1[m + 16][(f >> 3) ^ (((m + 16) >> 1) & 7)][f & 7] = cbf(((f & 1) ? -s128(a) : c128(a)) / 32.0);
    }
  for (int m = 0; m < 16; ++m)
    for (int f = 0; f < 32; ++f) {
      const int a = 2 * (2 * (f >> 1) + 1) * m;
      t.g2[m][f] = cbf(((f & 1) ? -s128(a) : c128(a)) / 16.0);
    }
  return t;
}
__device__ __attribute__((aligned(64))) const Tabs tabs = make_tabs();

constexpr int OOB = 0x7FFFFF00;
constexpr float C2 = 2.885390081777927f;
#define EMAXV 120.f
constexpr float EMAX = EMAXV;
constexpr int WT = 16;
constexpr int NT = 2;
constexpr int R1 = 336;
constexpr int R2 = 504;
constexpr int PL1 = R1 * 128;
constexpr int PL2 = R2 * 128;
constexpr int U1BYTES = 32 * 16 * PL1, U2BYTES = 16 * 16 * PL2;
constexpr int WBIN = 16 * 16 * 2 * 1024;
constexpr int YP1 = R1 * 64;
constexpr int YP2 = 500 * 64;
constexpr int Y1BYTES = 32 * 16 * YP1, Y2BYTES = 16 * 16 * YP2;
__device__ __forceinline__ int dmap(int cb, int n) { return 64 * (cb >> 2) + 32 * (cb & 1) + 2 * n + ((cb >> 1) & 1); }

__device__ __forceinline__ void swap16(u32x4& a, u32x4& b) {
#pragma unroll
  for (int i = 0; i < 4; ++i) {
    const auto r = __builtin_amdgcn_permlane16_swap(a[i], b[i], false, false);
    a[i] = r[0];
    b[i] = r[1];
  }
}

typedef __attribute__((address_space(3))) u32x4 lds_u32x4;
__device__ __forceinline__ u32x4 lds_ld(int a) { return *(const lds_u32x4*)(uintptr_t)(uint32_t)a; }
__device__ __forceinline__ uint32_t lds_ld32(int a) { return *(const __attribute__((address_space(3))) uint32_t*)(uintptr_t)(uint32_t)a; }
template <int SZ>
__device__ __forceinline__ void dma(__amdgpu_buffer_rsrc_t rsrc, char* lds, int voff, int soff) {
#if defined(__HIP_DEVICE_COMPILE__)
  typedef __attribute__((address_space(3))) void* ldsp;
  if constexpr (SZ == 16) __builtin_amdgcn_raw_ptr_buffer_load_lds(rsrc, (ldsp)lds, 16, voff, soff, 0, 0);
  else __builtin_amdgcn_raw_ptr_buffer_load_lds(rsrc, (ldsp)lds, 4, voff, soff, 0, 0);
#endif
}

#define LABEL_ADDR(p, name) asm volatile("s_getpc_b64 s[40:41]\n\ts_add_u32 s40, s40, " name "@rel32@lo+4\n\ts_addc_u32 s41, s41, " name "@rel32@hi+12\n\ts_mov_b64 %0, s[40:41]" : "=s"(p) :: "s40", "s41", "scc")
__device__ __forceinline__ void code_pf(const char* p, int n, int part, char* lds) {
  dma<4>(mkbuf(p, n), lds, part * 8192 + (threadIdx.x & 63) * 128, 0);
}

template <int CONV>
__device__ __forceinline__ void wtrans(const uint16_t* __restrict__ w, char* __restrict__ wh, int wid, int lane) {
  const int dq = wid & 3, pc = wid >> 2;
  const int ks = pc >> 2, kq = pc & 3;
  const int n32 = lane & 31, h = lane >> 5;
  const int r = CONV == 1 ? ks >> 3 : kq >> 1;
  const int c0 = CONV == 1 ? (ks & 7) * 32 + kq * 8 : dmap(ks, 4 * (kq & 1));
  const __amdgpu_buffer_rsrc_t wr = mkbuf(w, 31 * 131072);
  const int vb = (16 * h + r + 1) * 131072 + c0 * 512 + dq * 128 + n32 * 4;
  const int vb0 = (h + r == 0) ? OOB : vb - 262144;
  constexpr int NFB = CONV == 1 ? 2 : 1;
  const u32x4* et = CONV == 1 ? (const u32x4*)&tabs.e1[0][0][0][0] : (const u32x4*)&tabs.e2[0][0][0];
  u32x4 E[NFB][2];
#pragma unroll
  for (int fb = 0; fb < NFB; ++fb) { E[fb][0] = et[fb * 128 + lane]; E[fb][1] = et[fb * 128 + 64 + lane]; }
  uint32_t v[8][8];
#pragma unroll
  for (int el = 0; el < 8; ++el)
#pragma unroll
    for (int jj = 0; jj < 8; ++jj) {
      const int co = CONV == 1 ? el * 512 : (el & 3) * 1024;
      const int so = (CONV == 1 ? 0 : (el >> 2) * 8192) + (jj ? (jj - 1) * 262144 : 0);
      v[el][jj] = __builtin_amdgcn_raw_buffer_load_b32(wr, (jj ? vb : vb0) + co, so, NT);
    }
  PHASE();
  const __amdgpu_buffer_rsrc_t dr = mkbuf(wh, (CONV == 1 ? 32 : 16) * WBIN);
  const int vo = 2 * h * WBIN + (4 * dq + (n32 >> 4)) * 32768 + ks * 2048 + kq * 256 + (n32 & 15) * 16;
#pragma unroll
  for (int e = 0; e < 2; ++e) {
    u32x4 b[8];
#pragma unroll
    for (int el = 0; el < 8; ++el)
#pragma unroll
      for (int j = 0; j < 4; ++j) b[el][j] = __builtin_amdgcn_perm(v[el][2 * j + 1], v[el][2 * j], e ? 0x07060302u : 0x05040100u);
#pragma unroll
    for (int fb = 0; fb < NFB; ++fb) {
      u32x4 o[16];
#pragma unroll
      for (int ep = 0; ep < 4; ++ep) {
        f32x16 d[2];
#pragma unroll
        for (int z = 0; z < 2; ++z) d[z] = mfma32(E[fb][1], b[2 * ep + z], zero16());
#pragma unroll
        for (int z = 0; z < 2; ++z) d[z] = mfma32(E[fb][0], b[2 * ep + z], d[z]);
#pragma unroll
        for (int i = 0; i < 16; ++i) o[i][ep] = pk2bf(d[0][i], d[1][i]);
      }
#pragma unroll
      for (int i = 0; i < 16; ++i)
        __builtin_amdgcn_raw_buffer_store_b128(o[i], dr, vo + (i & 1) * 1024,
                                               (16 * fb + 4 * (i >> 2) + ((i >> 1) & 1)) * WBIN + e * 65536, WT);
      SB();
    }
  }
}

__device__ __forceinline__ void xload(const uint16_t* __restrict__ x, int u, int lane, uint32_t (&v)[3][8]) {
  const int cq = u & 7, b = (u >> 3) & 1, sg = u >> 4;
  const __amdgpu_buffer_rsrc_t xr = mkbuf(x + (size_t)b * 16000 * DCH, 8192000);
#pragma unroll
  for (int ks = 0; ks < 3; ++ks) {
    const int row0 = 48 * sg - 24 + 16 * ks + 8 * (lane >> 5);
    const int vb = row0 >= 0 ? row0 * 1024 + cq * 128 + (lane & 31) * 4 : OOB;
#pragma unroll
    for (int e = 0; e < 8; ++e) v[ks][e] = __builtin_amdgcn_raw_buffer_load_b32(xr, vb + (e & 3) * 1024, (e >> 2) * 4096, NT);
  }
}
__device__ __forceinline__ void xtrans(char* __restrict__ u1, int u, int lane, const uint32_t (&v)[3][8], const u32x4 (&F)[2][3]) {
  const __amdgpu_buffer_rsrc_t ur = mkbuf(u1 + (2 * (u & 7)) * PL1 + (u >> 3) * 128, U1BYTES - (2 * (u & 7)) * PL1 - (u >> 3) * 128);
  const int vo = ((lane & 15) >> 1) * 16 * PL1 + ((lane >> 4) & 1) * 64 + (lane & 1) * 32 + (lane >> 5) * 16;
  u32x4 a[2][3];
#pragma unroll
  for (int ks = 0; ks < 3; ++ks)
#pragma unroll
    for (int j = 0; j < 4; ++j) {
      a[0][ks][j] = __builtin_amdgcn_perm(v[ks][2 * j + 1], v[ks][2 * j], 0x05040100u);
      a[1][ks][j] = __builtin_amdgcn_perm(v[ks][2 * j + 1], v[ks][2 * j], 0x07060302u);
    }
#pragma unroll
  for (int fb = 0; fb < 2; ++fb) {
    f32x16 d[2] = {zero16(), zero16()};
#pragma unroll
    for (int ks = 0; ks < 3; ++ks)
#pragma unroll
      for (int e = 0; e < 2; ++e) d[e] = mfma32(a[e][ks], F[fb][ks], d[e]);
#pragma unroll
    for (int qp = 0; qp < 2; ++qp) {
      u32x4 o[2];
#pragma unroll
      for (int z = 0; z < 2; ++z)
#pragma unroll
        for (int i = 0; i < 4; ++i) o[z][i] = pk2bf(d[0][8 * qp + 4 * z + i], d[1][8 * qp + 4 * z + i]);
      swap16(o[0], o[1]);
#pragma unroll
      for (int z = 0; z < 2; ++z) __builtin_amdgcn_raw_buffer_store_b128(o[z], ur, vo, ((fb * 16 + z * 8) * 16 + qp) * PL1, WT);
    }
  }
}

__global__ __launch_bounds__(512, 2) void pre_kernel(const uint16_t* __restrict__ x, const uint16_t* __restrict__ w1,
                                                     const uint16_t* __restrict__ w2, const uint16_t* __restrict__ wf,
                                                     const uint16_t* __restrict__ wo, const uint16_t* __restrict__ g,
                                                     const uint16_t* __restrict__ wg, char* __restrict__ u1,
                                                     char* __restrict__ w1h, char* __restrict__ w2h, u32x4* __restrict__ wff,
                                                     u32x4* __restrict__ wof, float* __restrict__ ge) {
  const int blk = blockIdx.x;
  const int tid = threadIdx.x, lane = tid & 63;
  const int wave = __builtin_amdgcn_readfirstlane(tid >> 6);
  const int n = lane & 31, h = lane >> 5;
  __shared__ char pfs[256];
  if (wave == PF1) {
    const char *p0 = (const char*)&tabs - 320, *p1;
    LABEL_ADDR(p1, ".Lce_g2");
    code_pf(p0, (int)(p1 - p0), blk >> 3, pfs);
  }
  if (wave == 0) {
    wtrans<1>(w1, w1h, blk, lane);
  } else if (wave == 1) {
    wtrans<2>(w2, w2h, blk, lane);
  } else {
    const int bq = (blk & 7) * 32 + (blk >> 3);
    const int u0 = ((21 * bq) >> 1) + wave - 2, ue = (21 * (bq + 1)) >> 1;
    u32x4 F[2][3];
#pragma unroll
    for (int fb = 0; fb < 2; ++fb)
#pragma unroll
      for (int ks = 0; ks < 3; ++ks) F[fb][ks] = ((const u32x4*)&tabs.f1[fb][ks][0][0])[lane];
    uint32_t v0[3][8];
    xload(x, u0, lane, v0);
    if (u0 + 6 < ue) {
      uint32_t v1[3][8];
      xload(x, u0 + 6, lane, v1);
      PHASE();
      xtrans(u1, u0, lane, v0, F);
      SB();
      xtrans(u1, u0 + 6, lane, v1, F);
    } else if (wave == 7) {
      const int s = blk & 15, eb = (blk >> 4) & 7, second = blk >> 7;
      const int r = (s - 4 * (eb >> 1)) & 15;
      const uint16_t* src = second ? wo + (16 * s + 4 * h) * DCH + 64 * (eb >> 1) + 2 * n + (eb & 1) : wf + dmap(s, 4 * h) * DCH + 32 * eb + n;
      const int st = second ? DCH : 2 * DCH;
      uint32_t v[8];
#pragma unroll
      for (int j = 0; j < 8; ++j) v[j] = src[(8 * (j >> 2) + (j & 3)) * st];
      PHASE();
      xtrans(u1, u0, lane, v0, F);
      const float sc = second ? 2.f : 1.f;
      u32x4 o;
#pragma unroll
      for (int j = 0; j < 4; ++j) o[j] = pk2bf(bf2f(v[2 * j]) * sc, bf2f(v[2 * j + 1]) * sc);
      (second ? wof + (((eb >> 1) * 16 + r) * 2 + (eb & 1)) * 64 : wff + (eb * 16 + r) * 64)[lane] = o;
    } else if (blk < 8) {
      const int c0 = 32 * blk + 2 * (lane & 15), kq = lane >> 4;
      uint32_t v[8][8];
      u32x4 ga[8];
#pragma unroll
      for (int ks = 0; ks < 8; ++ks) {
        ga[ks] = *(const u32x4*)(g + (lane & 15) * DCH + ks * 32 + 8 * kq);
#pragma unroll
        for (int e = 0; e < 8; ++e) v[ks][e] = *(const uint32_t*)(wg + (ks * 32 + 8 * kq + e) * DCH + c0);
      }
      PHASE();
      xtrans(u1, u0, lane, v0, F);
      f32x4 d[2] = {{0.f, 0.f, 0.f, 0.f}, {0.f, 0.f, 0.f, 0.f}};
#pragma unroll
      for (int ks = 0; ks < 8; ++ks)
#pragma unroll
        for (int e = 0; e < 2; ++e) {
          u32x4 bb;
#pragma unroll
          for (int j = 0; j < 4; ++j) bb[j] = __builtin_amdgcn_perm(v[ks][2 * j + 1], v[ks][2 * j], e ? 0x07060302u : 0x05040100u);
          d[e] = mfma16(ga[ks], bb, d[e]);
        }
      float* dst = ge + ((((blk >> 1) * 2 + (kq & 1)) * 2 + (blk & 1)) * 4 + ((lane >> 2) & 3)) * 64 + (((lane >> 1) & 1) * 8 + (kq >> 1) * 4) * 4 + 2 * (lane & 1);
      f32x2 gs[4];
      bool far = false;
#pragma unroll
      for (int i = 0; i < 4; ++i) {
        gs[i] = f32x2{bf2f(f2bf(d[0][i])) * C2, bf2f(f2bf(d[1][i])) * C2};
        far = far || __builtin_fabsf(gs[i][0]) > EMAX || __builtin_fabsf(gs[i][1]) > EMAX;
      }
      far = __builtin_amdgcn_ballot_w64(far) != 0;
#pragma unroll
      for (int i = 0; i < 4; ++i) {
        *(f32x2*)(dst + 4 * i) = far ? f32x2{-1.f, -1.f} : f32x2{__builtin_amdgcn_exp2f(-gs[i][0]), __builtin_amdgcn_exp2f(-gs[i][1])};
        *(f32x2*)(dst + 4096 + 4 * i) = gs[i];
      }
    } else {
      PHASE();
      xtrans(u1, u0, lane, v0, F);
    }
  }
}

constexpr int issued(int g, int MB, int OPS, int NST, int BAR, int PRO) {
  int o = PRO * OPS;
  for (int t = 0; t < g; ++t)
    if (o < 16 * OPS && (o / OPS < NST || t >= (o / OPS - NST + 1) * MB + BAR) && (MB != 11 || 2 * o * MB <= (2 * t + GLA * MB) * OPS)) ++o;
  return o;
}
template <int CONV>
__global__ __launch_bounds__(256, 1) void gemm_kernel(const char* __restrict__ U, const char* __restrict__ W,
                                                      char* __restrict__ Y, const char* __restrict__ pf) {
  constexpr int MB = CONV == 1 ? 11 : 8;
  constexpr int STG = MB * 2048;
  constexpr int NST = CONV == 1 ? 7 : 8;
  constexpr int PL = CONV == 1 ? PL1 : PL2;
  constexpr int NDMA = CONV == 1 ? 6 : 4;
  constexpr int OPS = NDMA + 2;
  extern __shared__ __attribute__((aligned(16))) char smem[];
  const int bid = blockIdx.x, xcd = bid & 7, slot = bid >> 3;
  const int bin = CONV == 1 ? xcd * 4 + (slot >> 3) : xcd * 2 + (slot >> 4);
  const int mt = CONV == 1 ? (slot >> 2) & 1 : (slot >> 2) & 3;
  const int tid = threadIdx.x, lane = tid & 63;
  const int wave = __builtin_amdgcn_readfirstlane(tid >> 6);
  const int cb = (slot & 3) * 4 + wave;
  const int row0 = CONV == 1 ? mt * 168 : mt * 126;
  const __amdgpu_buffer_rsrc_t ur = mkbuf(U + (size_t)bin * 16 * PL + row0 * 128, 16 * PL - row0 * 128 + (CONV == 1 ? 0 : 256));
  const __amdgpu_buffer_rsrc_t wr = mkbuf(W + (size_t)bin * WBIN + cb * 32768, 32768);
  int vo[2];
  if constexpr (CONV == 1) {
    vo[0] = wave * 1024 + (lane >> 3) * 128 + (((lane & 7) ^ ((4 * wave + (lane >> 4)) & 7)) * 16);
    vo[1] = 20480 + wave * 256 + (lane >> 5) * 128 + ((((lane >> 2) & 7) ^ wave) * 16) + (lane & 3) * 4;
  } else {
#pragma unroll
    for (int z = 0; z < 2; ++z) vo[z] = wave * 4096 + (lane >> 3) * 128 + (((lane & 7) ^ (2 * wave + z)) * 16);
  }
  u32x4 B[NST][2];
  auto issue = [&](auto is, auto ii) {
    constexpr int s = decltype(is)::value, i = decltype(ii)::value;
    char* buf = smem + (s % NST) * STG;
    if constexpr (i < 2) {
      B[s % NST][i] = __builtin_amdgcn_raw_buffer_load_b128(wr, lane * 16 + i * 1024, s * 2048, 0);
    } else if constexpr (CONV == 1) {
      if constexpr (i < 7) dma<16>(ur, buf + wave * 1024 + (i - 2) * 4096, vo[0], s * PL + (i - 2) * 4096);
      else dma<4>(ur, buf + 20480 + wave * 256, vo[1], s * PL);
    } else {
      dma<16>(ur, buf + wave * 4096 + (i - 2) * 1024, vo[(i - 2) >> 1], s * PL + (i - 2) * 1024);
    }
  };
  constexpr int AR = 4, AL = 3;
  constexpr int BAR = MB - AL;
  constexpr int PRO = GPRO;
  sfor<0, PRO>([&](auto is) { sfor<0, OPS>([&](auto ii) { issue(is, ii); SB(); }); });
  const int rho = lane & 15, kq = lane >> 4;
  const int lds0 = (int)(uint32_t)(uintptr_t)(__attribute__((address_space(3))) char*)smem;
  constexpr int WIN = CONV == 1 ? 2 : 4;
  constexpr int NWIN = (NST + WIN - 1) / WIN;
  int ab[NWIN][2];
#pragma unroll
  for (int ri = 0; ri < 2; ++ri) {
    const int pos = ((kq >> 1) * 4 + ri * 2 + (kq & 1)) ^ (rho >> 1);
    const int a = lds0 + (CONV == 1 ? rho * 128 : (rho >> 1) * 2048 + (rho & 1) * 128) + pos * 16;
#pragma unroll
    for (int wn = 0; wn < NWIN; ++wn) { ab[wn][ri] = a + wn * WIN * STG; asm volatile("" : "+v"(ab[wn][ri])); }
  }
  auto aread = [&](int s, int mb, int ri) -> u32x4 {
    const int bf = s % NST;
    return lds_ld(ab[bf / WIN][ri] + (bf % WIN) * STG + mb * (CONV == 1 ? 2048 : 256));
  };
  f32x4 yr[MB], yi[MB];
#pragma unroll
  for (int mb = 0; mb < MB; ++mb) { yr[mb] = f32x4{0.f, 0.f, 0.f, 0.f}; yi[mb] = f32x4{0.f, 0.f, 0.f, 0.f}; }
  u32x4 A[AR][2];
  asm volatile("s_waitcnt vmcnt(%0)" ::"n"((PRO - 1) * OPS) : "memory");
  __builtin_amdgcn_s_barrier();
  asm volatile("" ::: "memory");
#pragma unroll
  for (int z = 0; z < AL; ++z) { A[z][0] = aread(0, z, 0); A[z][1] = aread(0, z, 1); }
  SB();
  u32x4 nwi;
#pragma unroll
  for (int j = 0; j < 4; ++j) nwi[j] = B[0][1][j] ^ 0x80008000u;
  sfor<0, 16>([&](auto is) {
    constexpr int s = decltype(is)::value;
    u32x4 nwn;
    sfor<0, MB>([&](auto im) {
      constexpr int mb = decltype(im)::value;
      constexpr int g = s * MB + mb, gn = g + AL;
      constexpr int o0 = issued(g, MB, OPS, NST, BAR, PRO), o1 = issued(g + 1, MB, OPS, NST, BAR, PRO);
      if constexpr (mb == BAR && s + 1 < 16) {
        static_assert(o0 >= (s + 2) * OPS, "next stage not requested yet");
        asm volatile("s_waitcnt vmcnt(%0)" ::"n"(o0 - (s + 2) * OPS) : "memory");
        __builtin_amdgcn_s_barrier();
        asm volatile("" ::: "memory");
        SB();
      }
      auto mf = [&](int i) {
        if (i < 2) yr[mb] = mfma16(i ? nwi : B[s % NST][0], A[g % AR][i], yr[mb]);
        else yi[mb] = mfma16(B[s % NST][3 - i], A[g % AR][i - 2], yi[mb]);
      };
      constexpr int ord = (mb & 1) ? ORD1 : ORD0;
      mf((ord >> 12) & 3);
      SB();
      mf((ord >> 8) & 3);
      if constexpr (gn < 16 * MB) A[gn % AR][0] = aread(gn / MB, gn % MB, 0);
      SB();
      mf((ord >> 4) & 3);
      if constexpr (o1 > o0) issue(IC<o0 / OPS>{}, IC<o0 % OPS>{});
      SB();
      mf(ord & 3);
      if constexpr (CONV == 1 ? (g >= TS0 && g < TS1) : (g >= TU0 && g < TU1)) asm volatile("s_nop %0" ::"n"(CONV == 1 ? THR : THR2));
      if constexpr (gn < 16 * MB) A[gn % AR][1] = aread(gn / MB, gn % MB, 1);
      if constexpr (mb == MB - 2 && s + 1 < 16) {
#pragma unroll
        for (int j = 0; j < 4; ++j) nwn[j] = B[(s + 1) % NST][1][j] ^ 0x80008000u;
      }
      SB();
    });
    if constexpr (s + 1 < 16) nwi = nwn;
  });
  if constexpr (CONV == 1) {
    const __amdgpu_buffer_rsrc_t yb = mkbuf(Y + (size_t)(bin * 16 + cb) * YP1 + row0 * 64, 168 * 64);
#pragma unroll
    for (int mb = 0; mb < MB; ++mb) {
      u32x4 o;
#pragma unroll
      for (int i = 0; i < 4; ++i) o[i] = pk2bf(yr[mb][i], yi[mb][i]);
      __builtin_amdgcn_raw_buffer_store_b128(o, yb, rho * 64 + kq * 16 + (mb & 3) * 1024, (mb >> 2) * 4096, WT);
    }
  } else {
    const __amdgpu_buffer_rsrc_t yb = mkbuf(Y + (size_t)(bin * 16 + cb) * YP2 + row0 * 64, YP2 - row0 * 64);
    const float sg = (bin & 1) ? -1.f : 1.f;
    f32x4 nr, ni;
#pragma unroll
    for (int i = 0; i < 4; ++i) { nr[i] = __shfl_down(yr[0][i], 2, 16); ni[i] = __shfl_down(yi[0][i], 2, 16); }
    const int vy = (rho >> 1) * 1024 + (rho & 1) * 64 + kq * 16;
#pragma unroll
    for (int mb = 0; mb < MB; ++mb) {
      const f32x4 xr = mb < MB - 1 ? yr[mb < MB - 1 ? mb + 1 : 0] : nr, xi = mb < MB - 1 ? yi[mb < MB - 1 ? mb + 1 : 0] : ni;
      u32x4 o;
#pragma unroll
      for (int i = 0; i < 4; ++i) o[i] = pk2bf(__builtin_fmaf(sg, xi[i], yr[mb][i]), __builtin_fmaf(-sg, xr[i], yi[mb][i]));
      __builtin_amdgcn_raw_buffer_store_b128(o, yb, (mb == MB - 1 && rho >= 14) ? OOB : vy + mb * 128, 0, WT);
    }
  }
  if constexpr (CONV == 1) asm volatile(".Lce_g1:"); else asm volatile(".Lce_g2:");
}

template <int Z>
__device__ __forceinline__ void mid_unit(char* __restrict__ U, int ya, int ga, int cb, int s, const u32x4& F) {
  const int lane = threadIdx.x & 63;
  const int n32 = lane & 31, h = lane >> 5;
  constexpr int NSG = Z == 0 ? 1 : 2;
  const int tau = 16 * ((n32 >> 2) & 1) + 8 * ((n32 >> 3) & 1) + 2 * (n32 & 3) + (n32 >> 4);
  const int r[2] = {Z == 0 ? tau + 16 : Z == 1 ? (tau < 16 ? tau + 48 : 0) : tau + 32, Z == 1 ? tau : (tau >= 16 ? tau - 16 : 0)};
  f32x16 d = zero16();
#pragma unroll
  for (int sg = 0; sg < NSG; ++sg)
#pragma unroll
    for (int ks = 0; ks < 4; ++ks) {
      u32x4 yv;
#pragma unroll
      for (int j = 0; j < 4; ++j) yv[j] = lds_ld32(ya + sg * 4096 + ks * 1024 + j * 128);
      d = mfma32(lds_ld(ga + r[sg] * 128 + (((2 * ks + h) ^ ((r[sg] >> 1) & 7)) * 16)), yv, d);
    }
  const __amdgpu_buffer_rsrc_t ub = mkbuf(U, U2BYTES);
  const int vu = (((lane & 15) >> 1) * 16 + cb) * PL2 + s * 256 + ((lane >> 4) & 1) * 64 + (lane & 1) * 32 + h * 16;
  const int t0r = 32 * s - 16 + 16 * h;
  const bool ok = t0r >= 0 && t0r < 8000;
  f32x16 u[2];
#pragma unroll
  for (int q = 0; q < 2; ++q) {
    u32x4 a;
#pragma unroll
    for (int j = 0; j < 4; ++j) {
      const uint32_t gv = gelu2(d[8 * q + 2 * j], d[8 * q + 2 * j + 1]);
      a[j] = ok ? gv : 0u;
    }
    u[q] = mfma32(a, F, zero16());
  }
#pragma unroll
  for (int b = 0; b < 2; ++b) {
    u32x4 o[2];
#pragma unroll
    for (int q = 0; q < 2; ++q)
#pragma unroll
      for (int j = 0; j < 4; ++j) o[q][j] = pk2bf(u[q][8 * b + 2 * j], u[q][8 * b + 2 * j + 1]);
    swap16(o[0], o[1]);
#pragma unroll
    for (int z = 0; z < 2; ++z) __builtin_amdgcn_raw_buffer_store_b128(o[z], ub, vu + b * 128, z * 8 * 16 * PL2, WT);
  }
}
__global__ __launch_bounds__(1024) void mid_kernel(const char* __restrict__ Y, char* __restrict__ U) {
  __shared__ __attribute__((aligned(16))) char sm[8192 + 49152];
  __shared__ uint32_t landed;
  const int blk = blockIdx.x, lane = threadIdx.x & 63;
  const int wave = __builtin_amdgcn_readfirstlane(threadIdx.x >> 6);
  const int cb = blk & 15, s0 = blk & ~15, s = s0 + wave;
  const int sg0 = (2 * s0) / 3;
  if (wave < 4) {
    const __amdgpu_buffer_rsrc_t tb = mkbuf(&tabs.g1[0][0][0], 8192);
#pragma unroll
    for (int i = 0; i < 2; ++i) dma<16>(tb, sm + wave * 2048 + i * 1024, lane * 16, wave * 2048 + i * 1024);
  }
  {
    const __amdgpu_buffer_rsrc_t yb = mkbuf(Y, Y1BYTES);
    const int vy = ((lane >> 3) * 16 + cb) * YP1 + sg0 * 128 + (lane & 7) * 16;
#pragma unroll
    for (int i = 0; i < 3; ++i) {
      const int pc = 3 * wave + i;
      dma<16>(yb, sm + 8192 + pc * 1024, vy, (pc & 3) * 128 * YP1 + (pc >> 2) * 128);
    }
  }
  const u32x4 F = ((const u32x4*)&tabs.f2[0][0])[lane];
  if (threadIdx.x == 0) landed = 0;
  asm volatile("s_waitcnt lgkmcnt(0)\n\ts_barrier" ::: "memory");
  {
    asm volatile("s_waitcnt vmcnt(0)" ::: "memory");
    if (lane == 0) __hip_atomic_fetch_or(&landed, 1u << wave, __ATOMIC_RELAXED, __HIP_MEMORY_SCOPE_WORKGROUP);
    if (s < 251) {
      const int p = s / 3, z = s - 3 * p;
      const int l0 = 2 * p + (z == 2) - sg0, l1 = l0 + (z != 0);
      const uint32_t need = 15u | (((2u << ((4 * l1 + 3) / 3)) - 1u) & ~((1u << ((4 * l0) / 3)) - 1u));
      while ((__builtin_amdgcn_readfirstlane(__hip_atomic_load(&landed, __ATOMIC_RELAXED, __HIP_MEMORY_SCOPE_WORKGROUP)) & need) != need)
        __builtin_amdgcn_s_sleep(1);
      asm volatile("" ::: "memory");
      const int l = (int)(uint32_t)(uintptr_t)(__attribute__((address_space(3))) char*)sm;
      const int ya = l + 8192 + l0 * 4096 + (lane >> 5) * 512 + (lane & 31) * 4;
      if (z == 0) mid_unit<0>(U, ya, l, cb, s, F);
      else if (z == 1) mid_unit<1>(U, ya, l, cb, s, F);
      else mid_unit<2>(U, ya, l, cb, s, F);
    }
  }
}

constexpr int J_F = 0;
constexpr int J_T = 16384;
constexpr int J_G = 49152;
constexpr int JOINT_LDS = 65536;
__device__ __forceinline__ void lds_st(int a, const u32x4& v) { *(lds_u32x4*)(uintptr_t)(uint32_t)a = v; }
#define BAR() do { SB(); asm volatile("s_waitcnt lgkmcnt(0)\n\ts_barrier" ::: "memory"); SB(); } while (0)
template <int M, bool Z>
__device__ __forceinline__ void mm(f32x16& c, const u32x4& a, const u32x4& b) {
  if constexpr (Z) {
    if constexpr (M == 0) asm volatile("v_mfma_f32_32x32x16_bf16 %0, %1, %2, 0" : "=&v"(c) : "v"(a), "v"(b));
    else if constexpr (M == 1) asm volatile("v_mfma_f32_32x32x16_bf16 %0, %1, %2, 0" : "=&v"(c) : "a"(a), "v"(b));
    else asm volatile("v_mfma_f32_32x32x16_bf16 %0, %1, %2, 0" : "=&v"(c) : "v"(a), "a"(b));
  } else {
    if constexpr (M == 0) asm volatile("v_mfma_f32_32x32x16_bf16 %0, %1, %2, %0" : "+v"(c) : "v"(a), "v"(b));
    else if constexpr (M == 1) asm volatile("v_mfma_f32_32x32x16_bf16 %0, %1, %2, %0" : "+v"(c) : "a"(a), "v"(b));
    else asm volatile("v_mfma_f32_32x32x16_bf16 %0, %1, %2, %0" : "+v"(c) : "v"(a), "a"(b));
  }
}
#define MWAIT(c0, c1) asm volatile("s_nop 7\n\ts_nop 3" : "+v"(c0), "+v"(c1))
template <int K>
__device__ __forceinline__ float add_quad(float a, float b) {
  float r;
  asm volatile("v_add_f32_dpp %0, %1, %2 quad_perm:[%3,%3,%3,%3] row_mask:0xf bank_mask:0xf" : "=v"(r) : "v"(a), "v"(b), "n"(K));
  return r;
}

#define WI 2
#define WA 6
#define WB 4
#define WZ 3
constexpr int wcount(int t) {
  int c = 0;
  for (int i = 0; i < t; ++i) c += i < 4 ? WI : i == 4 ? WA : i < 8 ? WB : i < 24 ? 0 : i < 56 ? (i % WZ != 0) : i < 76 ? (i & 1) : 1;
  return c < 64 ? c : 64;
}
__global__ __launch_bounds__(256, 1) void joint_kernel(const char* __restrict__ Y, const char* __restrict__ wff,
                                                       const float* __restrict__ ge, uint16_t* __restrict__ out) {
  extern __shared__ __attribute__((aligned(16))) char smem[];
  const int blk = blockIdx.x;
  const int tid = threadIdx.x, lane = tid & 63;
  const int w = __builtin_amdgcn_readfirstlane(tid >> 6);
  const int n = lane & 31, h = lane >> 5;
  const int l0 = (int)(uint32_t)(uintptr_t)(__attribute__((address_space(3))) char*)smem;
  u32x4 yv[4][2], gi[2][2];
#pragma unroll
  for (int ks = 0; ks < 2; ++ks) gi[0][ks] = *(const u32x4*)&tabs.g2[4 * (n & 3) + ((n >> 2) & 3)][16 * ks + 8 * h];
  SB();
  const __amdgpu_buffer_rsrc_t yb = mkbuf(Y, Y2BYTES);
  const int vy = (4 * h * 16 + 4 * w) * YP2 + blk * 128 + n * 4;
  const __amdgpu_buffer_rsrc_t wb = mkbuf(wff + w * 32768, 131072 + 32768);
  u32x4 wt[64];
  auto wload = [&](int i0, int i1) {
#pragma unroll
    for (int i = i0; i < i1; ++i) wt[i] = __builtin_amdgcn_raw_buffer_load_b128(wb, lane * 16 + (i & 3) * 1024, (i >> 5) * 131072 + ((i >> 2) & 7) * 4096, 0);
    SB();
  };
  auto wstep = [&](auto it) { constexpr int i0 = wcount(decltype(it)::value), i1 = wcount(decltype(it)::value + 1); wload(i0, i1); };
  sfor<0, 4>([&](auto ic) {
    constexpr int c = decltype(ic)::value;
#pragma unroll
    for (int ks = 0; ks < 2; ++ks)
#pragma unroll
      for (int j = 0; j < 4; ++j) yv[c][ks][j] = __builtin_amdgcn_raw_buffer_load_b32(yb, vy, ((8 * ks + j) * 16 + c) * YP2, 0);
    SB();
    wstep(ic);
  });
  dma<4>(mkbuf(wff, 262144), smem + J_T + w * 256, (lane * 124 + (blk >> 3) * 4 + w) * 128, 0);
  SB();
#pragma unroll
  for (int ks = 0; ks < 2; ++ks) {
    gi[1][ks] = n >= 16 ? gi[0][ks] : u32x4{0u, 0u, 0u, 0u};
    gi[0][ks] = n < 16 ? gi[0][ks] : u32x4{0u, 0u, 0u, 0u};
  }
  int ra[3];
#pragma unroll
  for (int i = 0; i < 3; ++i) ra[i] = l0 + ((w + 1 + i) & 3) * 4096 + lane * 16;
  const int wa = l0 + w * 4096 + lane * 16;
  const int qa = l0 + J_G + w * 4096 + (h * 8 + (n >> 4) * 4 + (n & 3)) * 16;
  asm volatile("s_nop 1" : "+v"(gi[0][0]), "+v"(gi[0][1]), "+v"(gi[1][0]), "+v"(gi[1][1]));
  u32x4 fb[16];
  sfor<0, 2>([&](auto icp) {
    constexpr int cp = decltype(icp)::value;
    f32x16 d;
    sfor<0, 4>([&](auto iI) {
      constexpr int c = decltype(iI)::value >> 1, ks = decltype(iI)::value & 1;
      mm<0, c + ks == 0>(d, yv[2 * cp + c][ks], gi[c][ks]);
    });
    SB();
    if constexpr (cp) wstep(IC<5>{});
    asm volatile("s_nop 7\n\ts_nop 3" : "+v"(d));
#pragma unroll
    for (int j = 0; j < 4; ++j) fb[2 * cp][j] = gelu2(d[2 * j], d[2 * j + 1]);
    SB();
    if constexpr (cp) wstep(IC<6>{});
#pragma unroll
    for (int j = 0; j < 4; ++j) fb[2 * cp + 1][j] = gelu2(d[8 + 2 * j], d[8 + 2 * j + 1]);
    swap16(fb[2 * cp], fb[2 * cp + 1]);
#pragma unroll
    for (int c = 0; c < 2; ++c) lds_st(wa + J_F + (2 * cp + c) * 1024, fb[2 * cp + c]);
    SB();
    if constexpr (cp) {
      const __amdgpu_buffer_rsrc_t gb = mkbuf(ge + w * 1024, 4096);
#pragma unroll
      for (int i = 0; i < 4; ++i) dma<16>(gb, smem + J_G + w * 4096 + i * 1024, lane * 16, i * 1024);
      SB();
    }
    wstep(IC<cp ? 7 : 4>{});
  });
  f32x16 fe[2];
  f32x4 q[16];
  sfor<0, 16>([&](auto ir) {
    constexpr int r = decltype(ir)::value;
    mm<1, r == 0>(fe[0], wt[r], fb[r]);
    if constexpr (r == 0) { BAR(); }
    SB();
    asm volatile("" :: "a"(wt[r + 1]));
    if constexpr (r >= 3 && r < 15) asm volatile("" :: "v"(fb[r + 1]));
    if constexpr (r < 6) {
      fb[2 * r + 4] = lds_ld(ra[r >> 1] + J_F + ((2 * r) & 3) * 1024);
      fb[2 * r + 5] = lds_ld(ra[r >> 1] + J_F + ((2 * r + 1) & 3) * 1024);
    } else if constexpr (r >= 12) {
      if constexpr (r == 12) asm volatile("" :: "a"(wt[wcount(7)]) : "memory");
      q[2 * r - 24] = __builtin_bit_cast(f32x4, lds_ld(qa + (2 * r - 24) * 256));
      q[2 * r - 23] = __builtin_bit_cast(f32x4, lds_ld(qa + (2 * r - 23) * 256));
    }
    SB();
    wstep(IC<24 + r>{});
  });
  float p[32], x[8], mx = 0.f;
  u32x4 tp[2][4];
  auto stepE = [&](int i) {
    fe[i >> 4][i & 15] *= C2;
    mx = __builtin_fmaxf(mx, __builtin_fabsf(fe[i >> 4][i & 15]));
    p[i] = __builtin_amdgcn_exp2f(fe[i >> 4][i & 15]);
  };
  auto twrite = [&](auto iG) {
    constexpr int W = decltype(iG)::value + 31;
    if constexpr (W >= 0 && W < 256 && (W & 7) == 7) lds_st(wa + J_T + ((W >> 5) & 1) * 16384 + ((W >> 3) & 3) * 1024, tp[(W >> 5) & 1][(W >> 3) & 3]);
    SB();
  };
  auto tslot = [&](auto iG) {
    constexpr int G = decltype(iG)::value;
    constexpr int A = G + 36, B = G + 35, C = G + 34, D = G + 33;
    if constexpr (C >= 0 && C < 256) asm volatile("v_fma_f32 %0, -%1, %0, 0.5" : "+v"(x[C & 7]) : "v"(q[((C >> 5) & 1) * 8 + ((C >> 2) & 7)][C & 3]));
    if constexpr (B >= 0 && B < 256) asm volatile("v_rcp_f32 %0, %0" : "+v"(x[B & 7]));
    if constexpr (A >= 0 && A < 256) x[A & 7] = add_quad<(A >> 6)>(p[A & 31], q[((A >> 5) & 1) * 8 + ((A >> 2) & 7)][A & 3]);
    SB();
    if constexpr (D >= 0 && D < 256 && (D & 1)) tp[(D >> 5) & 1][(D >> 3) & 3][(D >> 1) & 3] = pk2bf(x[(D - 1) & 7], x[D & 7]);
    SB();
  };
  sfor<0, 16>([&](auto ir) {
    constexpr int r = decltype(ir)::value;
    mm<1, r == 0>(fe[1], wt[16 + r], fb[r]);
    SB();
    if constexpr (r < 15) asm volatile("" :: "a"(wt[17 + r]));
    if constexpr (r == 3) asm volatile("" :: "v"(q[7]));
    if constexpr (r == 12) asm volatile("" :: "v"(q[15]));
    if constexpr (r < 8) q[8 + r] = __builtin_bit_cast(f32x4, lds_ld(qa + (8 + r) * 256));
    wstep(IC<40 + r>{});
    if constexpr (r >= 4) twrite(IC<r - 40>{});
    if constexpr (r >= 2 && r < 10) { stepE(2 * r - 4); stepE(2 * r - 3); SB(); }
    if constexpr (r >= 4) tslot(IC<r - 40>{});
  });
  sfor<12, 16>([&](auto ir) { twrite(IC<decltype(ir)::value - 36>{}); tslot(IC<decltype(ir)::value - 36>{}); });
  asm volatile("" : "+v"(fe[1]));
#pragma unroll
  for (int i = 16; i < 32; ++i) stepE(i);
  const __amdgpu_buffer_rsrc_t ob = mkbuf(out + (size_t)blk * 16 * 8 * DCH, 4000 * 4096 + 65536);
  const int vo = h * 4096 + w * 128 + n * 4;
  if (__builtin_expect(__builtin_amdgcn_ballot_w64(mx > EMAX || q[0][0] < 0.f || q[4][0] < 0.f) != 0, 0)) {
    wload(IC<wcount(56)>::value, 64);
#pragma unroll 1
    for (int st = 0; st < 8; ++st) {
      const float* gp = ge + 4096 + (w * 2 + (st & 1)) * 512 + (h * 8 + (n >> 4) * 4 + (n & 3)) * 4;
#pragma unroll
      for (int f = 0; f < 4; ++f) {
        u32x4 t;
#pragma unroll
        for (int c = 0; c < 2; ++c) {
          const f32x4 gv = *(const f32x4*)(gp + (2 * f + c) * 64);
          float y[4];
#pragma unroll
          for (int i = 0; i < 4; ++i)
            y[i] = 0.5f - __builtin_amdgcn_rcpf(__builtin_amdgcn_exp2f(__shfl(fe[f >> 1][8 * (f & 1) + 4 * c + i], (lane & ~3) + (st >> 1)) + gv[i]) + 1.f);
          t[2 * c] = pk2bf(y[0], y[1]);
          t[2 * c + 1] = pk2bf(y[2], y[3]);
        }
        lds_st(wa + J_T + (st & 1) * 16384 + f * 1024, t);
      }
      BAR();
      f32x16 o[2];
      sfor<0, 16>([&](auto ir) {
        constexpr int r = decltype(ir)::value;
        const u32x4 a = lds_ld((r < 4 ? wa : ra[r < 4 ? 0 : (r >> 2) - 1]) + J_T + (st & 1) * 16384 + (r & 3) * 1024);
#pragma unroll
        for (int z = 0; z < 2; ++z) mm<2, r == 0>(o[z], a, wt[32 + 2 * r + z]);
      });
      MWAIT(o[0], o[1]);
#pragma unroll
      for (int i = 0; i < 16; ++i)
        __builtin_amdgcn_raw_buffer_store_b32(pk2bf(o[0][i], o[1][i]), ob, vo + (st & 1) * 2048 + (st >> 1) * 16384 + (i & 3) * 512,
                                              (i >> 3) * (4000 * 4096) + 2 * ((i >> 2) & 1) * 4096, NT);
    }
    return;
  }
  asm volatile("s_nop 1");
  SB();
  sfor<16, 36>([&](auto ir) { constexpr int i = decltype(ir)::value; wstep(IC<40 + i>{}); twrite(IC<i - 36>{}); tslot(IC<i - 36>{}); });
  f32x16 acc[2][2];
  u32x4 a[16];
  uint32_t ov[16];
  auto store = [&](int st, int i) {
    __builtin_amdgcn_raw_buffer_store_b32(ov[i], ob, vo + ((st & 1) * 4 + (i & 3)) * 512,
                                          (i >> 3) * (4000 * 4096) + ((st >> 1) * 4 + 2 * ((i >> 2) & 1)) * 4096, NT);
  };
  sfor<0, 256>([&](auto iG) {
    constexpr int G = decltype(iG)::value, st = G >> 5, g = G & 31, r = g >> 1, z = g & 1;
    mm<2, r == 0>(acc[st & 1][z], r < 4 ? tp[st & 1][r] : a[r], wt[32 + g]);
    SB();
    if constexpr (g == 2) BAR();
    if constexpr (st == 0 && g < 31) asm volatile("" :: "a"(wt[33 + g]));
    if constexpr (z && r >= 3 && r < 15) asm volatile("" :: "v"(a[r + 1]));
    constexpr int rd = g >= 3 && g < 6 ? g + 1 : (g >= 7 && g <= 23 && z) ? (g + 7) / 2 : 0;
    constexpr int sn = g == 1 ? 15 : g == 2 ? 0 : g == 6 ? 1 : g >= 25 ? g - 17 : (g > 8 && !z && (g & 7)) ? (g - 6) / 2 - (g > 16) : -1;
    if constexpr (rd) a[rd] = lds_ld(ra[(rd >> 2) - 1] + J_T + (st & 1) * 16384 + (rd & 3) * 1024);
    if constexpr (sn >= 0 && st > (g == 1)) store(st - 1 - (g == 1), sn);
    if constexpr (st == 0) wstep(IC<76 + g>{});
    twrite(iG);
    tslot(iG);
    if constexpr (st > 0 && z) ov[r] = pk2bf(acc[(st - 1) & 1][0][r], acc[(st - 1) & 1][1][r]);
    SB();
  });
  store(6, 15);
  MWAIT(acc[1][0], acc[1][1]);
#pragma unroll
  for (int i = 0; i < 16; ++i) { ov[i] = pk2bf(acc[1][0][i], acc[1][1][i]); store(7, i); }
}

}

extern "C" void kernel_launch(void* const* d_in, const int* in_sizes, int n_in, void* d_out, int out_size, void* d_ws,
                              size_t ws_size, hipStream_t stream) {
  const uint16_t* x = (const uint16_t*)d_in[0];
  const uint16_t* w1 = (const uint16_t*)d_in[1];
  const uint16_t* w2 = (const uint16_t*)d_in[2];
  const uint16_t* g = (const uint16_t*)d_in[3];
  const uint16_t* wf = (const uint16_t*)d_in[4];
  const uint16_t* wg = (const uint16_t*)d_in[5];
  const uint16_t* wo = (const uint16_t*)d_in[6];
  uint16_t* out = (uint16_t*)d_out;
#define WOFF 0
  char* ws = (char*)d_ws + ((size_t)WOFF << 20);
  char* u1 = ws;
  char* w1h = ws + (24 << 20);
  char* w2h = ws + (40 << 20);
  char* y1 = ws + (48 << 20);
  char* u2 = ws + (60 << 20);
  char* y2 = ws + (77 << 20);
  u32x4* wff = (u32x4*)(ws + (86 << 20));
  u32x4* wof = (u32x4*)(ws + (86 << 20) + 131072);
  float* ge = (float*)(ws + (86 << 20) + 262144);
  constexpr int G1LDS = 7 * 11 * 2048 + 256, G2LDS = 8 * 8 * 2048 + 256;

  (void)hipFuncSetAttribute((const void*)gemm_kernel<1>, hipFuncAttributeMaxDynamicSharedMemorySize, G1LDS);
  (void)hipFuncSetAttribute((const void*)gemm_kernel<2>, hipFuncAttributeMaxDynamicSharedMemorySize, G2LDS);
  (void)hipFuncSetAttribute((const void*)joint_kernel, hipFuncAttributeMaxDynamicSharedMemorySize, JOINT_LDS);

  pre_kernel<<<256, 512, 0, stream>>>(x, w1, w2, wf, wo, g, wg, u1, w1h, w2h, wff, wof, ge);
  gemm_kernel<1><<<256, 256, G1LDS, stream>>>(u1, w1h, y1, (const char*)wff);
  mid_kernel<<<256, 1024, 0, stream>>>(y1, u2);
  gemm_kernel<2><<<256, 256, G2LDS, stream>>>(u2, w2h, y2, (const char*)wff);
  joint_kernel<<<250, 256, JOINT_LDS, stream>>>(y2, (const char*)wff, ge, out);
}
```

```cpp
#define PF1 7
#include <hip/hip_runtime.h>
#include <stdint.h>

typedef __attribute__((ext_vector_type(8))) short bf16x8;
typedef __attribute__((ext_vector_type(16))) float f32x16;
typedef __attribute__((ext_vector_type(4))) float f32x4;
typedef __attribute__((ext_vector_type(2))) float f32x2;
typedef __attribute__((ext_vector_type(4))) unsigned int u32x4;
typedef __attribute__((ext_vector_type(2))) unsigned int u32x2;

#define ORD0 0x0132
#define ORD1 0x2310
#define GLA 6
#define TS0 22
#define TS1 55
#define TU0 22
#define TU1 55
#define THR 7
#define THR2 7
#define GPRO 2
namespace {

constexpr int DCH = 256;

__device__ __forceinline__ float bf2f(uint32_t v) { return __uint_as_float(v << 16); }
__device__ __forceinline__ uint32_t f2bf(float f) {
  uint32_t u = __float_as_uint(f);
  u += 0x7FFF + ((u >> 16) & 1);
  return u >> 16;
}
__device__ __forceinline__ uint32_t pk2bf(float a, float b) {
  typedef __attribute__((ext_vector_type(2))) __bf16 b2;
  f32x2 v = {a, b};
  b2 r = __builtin_convertvector(v, b2);
  return __builtin_bit_cast(uint32_t, r);
}
__device__ __forceinline__ uint32_t gelu2(float a0, float a1) {
  const f32x2 x = {a0, a1};
  const f32x2 c0 = {-2.302208198144325f, -2.302208198144325f}, c1 = {-0.10294323970002349f, -0.10294323970002349f};
  const f32x2 z = x * __builtin_elementwise_fma(x * x, c1, c0);
  const f32x2 e = {__builtin_amdgcn_exp2f(z[0]), __builtin_amdgcn_exp2f(z[1])};
  const f32x2 d = e + f32x2{1.0f, 1.0f};
  const f32x2 y = x * f32x2{__builtin_amdgcn_rcpf(d[0]), __builtin_amdgcn_rcpf(d[1])};
  return pk2bf(y[0], y[1]);
}
__device__ __forceinline__ f32x16 mfma32(const u32x4& a, const u32x4& b, const f32x16& c) {
  return __builtin_amdgcn_mfma_f32_32x32x16_bf16(__builtin_bit_cast(bf16x8, a), __builtin_bit_cast(bf16x8, b), c, 0, 0, 0);
}
__device__ __forceinline__ f32x4 mfma16(const u32x4& a, const u32x4& b, const f32x4& c) {
  return __builtin_amdgcn_mfma_f32_16x16x32_bf16(__builtin_bit_cast(bf16x8, a), __builtin_bit_cast(bf16x8, b), c, 0, 0, 0);
}
__device__ __forceinline__ f32x16 zero16() {
  f32x16 z;
#pragma unroll
  for (int i = 0; i < 16; ++i) z[i] = 0.f;
  return z;
}
__device__ __forceinline__ __amdgpu_buffer_rsrc_t mkbuf(const void* p, int nbytes) {
  return __builtin_amdgcn_make_buffer_rsrc((void*)p, (short)0, nbytes, 0x00020000);
}
template <int V> struct IC { static constexpr int value = V; };
template <int I, int N, class F>
__device__ __forceinline__ void sfor(F&& f) {
  if constexpr (I < N) {
    f(IC<I>{});
    sfor<I + 1, N>(f);
  }
}
#define SB() __builtin_amdgcn_sched_barrier(0)
#define PHASE() do { SB(); asm volatile("s_waitcnt vmcnt(0)\n\ts_barrier" ::: "memory"); SB(); } while (0)

constexpr double PI_ = 3.14159265358979323846;
constexpr double c128(int m) {
  m = ((m % 128) + 128) % 128;
  if (m > 64) m = 128 - m;
  double sg = 1.0;
  if (m > 32) { m = 64 - m; sg = -1.0; }
  if (m == 32) return 0.0;
  const double a = PI_ * m / 64.0;
  double term = 1.0, sum = 1.0;
  for (int i = 1; i < 16; ++i) { term *= -a * a / ((2 * i - 1) * (2 * i)); sum += term; }
  return sg * sum;
}
constexpr double s128(int m) { return c128(m - 32); }
constexpr uint16_t cbf(double v) {
  uint32_t u = __builtin_bit_cast(uint32_t, (float)v);
  u += 0x7FFF + ((u >> 16) & 1);
  return (uint16_t)(u >> 16);
}
constexpr double cbf2d(uint16_t v) { return (double)__builtin_bit_cast(float, (uint32_t)v << 16); }
struct Tabs {
  uint16_t f1[2][3][64][8];
  uint16_t e1[2][2][64][8];
  uint16_t e2[2][64][8];
  uint16_t g1[64][8][8];
  uint16_t f2[64][8];
  uint16_t g2[16][32];
};
constexpr Tabs make_tabs() {
  Tabs t{};
  for (int l = 0; l < 64; ++l)
    for (int e = 0; e < 8; ++e) {
      const int h = l >> 5, f = l & 31, ri = f & 1;
      for (int fb = 0; fb < 2; ++fb) {
        const int k = 16 * fb + (f >> 1);
        for (int ks = 0; ks < 3; ++ks) {
          const int a = (2 * k + 1) * (16 * ks + 8 * h + e);
          t.f1[fb][ks][l][e] = cbf(ri ? -s128(a) : c128(a));
        }
        const int a = (2 * k + 1) * (8 * h + e);
        const double v = ri ? s128(a) : c128(a);
        t.e1[fb][0][l][e] = cbf(v);
        t.e1[fb][1][l][e] = cbf(v - cbf2d(cbf(v)));
      }
      const int a = 2 * (2 * (f >> 1) + 1) * (8 * h + e);
      const double v = ri ? s128(a) : c128(a);
      t.e2[0][l][e] = cbf(v);
      t.e2[1][l][e] = cbf(v - cbf2d(cbf(v)));
      t.f2[l][e] = cbf(ri ? -v : v);
    }
  for (int m = -15; m < 48; ++m)
    for (int f = 0; f < 64; ++f) {
      const int a = (2 * (f >> 1) + 1) * m;
      t.g1[m + 16][(f >> 3) ^ (((m + 16) >> 1) & 7)][f & 7] = cbf(((f & 1) ? -s128(a) : c128(a)) / 32.0);
    }
  for (int m = 0; m < 16; ++m)
    for (int f = 0; f < 32; ++f) {
      const int a = 2 * (2 * (f >> 1) + 1) * m;
      t.g2[m][f] = cbf(((f & 1) ? -s128(a) : c128(a)) / 16.0);
    }
  return t;
}
__device__ __attribute__((aligned(64))) const Tabs tabs = make_tabs();

constexpr int OOB = 0x7FFFFF00;
constexpr float C2 = 2.885390081777927f;
#define EMAXV 120.f
constexpr float EMAX = EMAXV;
constexpr int WT = 16;
constexpr int NT = 2;
constexpr int R1 = 336;
constexpr int R2 = 502;
constexpr int PL1 = R1 * 128;
constexpr int PL2 = R2 * 128;
constexpr int U1BYTES = 32 * 16 * PL1, U2BYTES = 16 * 16 * PL2;
constexpr int WBIN = 16 * 16 * 2 * 1024;
constexpr int YP1 = R1 * 64;
constexpr int YP2 = 500 * 64;
constexpr int Y1BYTES = 32 * 16 * YP1, Y2BYTES = 16 * 16 * YP2;
__device__ __forceinline__ int dmap(int cb, int n) { return 64 * (cb >> 2) + 32 * (cb & 1) + 2 * n + ((cb >> 1) & 1); }

__device__ __forceinline__ void swap16(u32x4& a, u32x4& b) {
#pragma unroll
  for (int i = 0; i < 4; ++i) {
    const auto r = __builtin_amdgcn_permlane16_swap(a[i], b[i], false, false);
    a[i] = r[0];
    b[i] = r[1];
  }
}

__device__ __forceinline__ void st_pad(const u32x4& a, const u32x4& b) { asm volatile("s_nop 1" :: "v"(a), "v"(b) : "memory"); }

typedef __attribute__((address_space(3))) u32x4 lds_u32x4;
__device__ __forceinline__ u32x4 lds_ld(int a) { return *(const lds_u32x4*)(uintptr_t)(uint32_t)a; }
__device__ __forceinline__ uint32_t lds_ld32(int a) { return *(const __attribute__((address_space(3))) uint32_t*)(uintptr_t)(uint32_t)a; }
template <int SZ>
__device__ __forceinline__ void dma(__amdgpu_buffer_rsrc_t rsrc, char* lds, int voff, int soff) {
#if defined(__HIP_DEVICE_COMPILE__)
  typedef __attribute__((address_space(3))) void* ldsp;
  if constexpr (SZ == 16) __builtin_amdgcn_raw_ptr_buffer_load_lds(rsrc, (ldsp)lds, 16, voff, soff, 0, 0);
  else __builtin_amdgcn_raw_ptr_buffer_load_lds(rsrc, (ldsp)lds, 4, voff, soff, 0, 0);
#endif
}

#define LABEL_ADDR(p, name) asm volatile("s_getpc_b64 s[40:41]\n\ts_add_u32 s40, s40, " name "@rel32@lo+4\n\ts_addc_u32 s41, s41, " name "@rel32@hi+12\n\ts_mov_b64 %0, s[40:41]" : "=s"(p) :: "s40", "s41", "scc")
__device__ __forceinline__ void code_pf(const char* p, int n, int part, char* lds) {
  dma<4>(mkbuf(p, n), lds, part * 8192 + (threadIdx.x & 63) * 128, 0);
}

template <int CONV>
__device__ __forceinline__ void wtrans(const uint16_t* __restrict__ w, char* __restrict__ wh, int wid, int lane) {
  const int dq = wid & 3, pc = wid >> 2;
  const int ks = pc >> 2, kq = pc & 3;
  const int n32 = lane & 31, h = lane >> 5;
  const int r = CONV == 1 ? ks >> 3 : kq >> 1;
  const int c0 = CONV == 1 ? (ks & 7) * 32 + kq * 8 : dmap(ks, 4 * (kq & 1));
  const __amdgpu_buffer_rsrc_t wr = mkbuf(w, 31 * 131072);
  const int vb = (16 * h + r + 1) * 131072 + c0 * 512 + dq * 128 + n32 * 4;
  const int vb0 = (h + r == 0) ? OOB : vb - 262144;
  constexpr int NFB = CONV == 1 ? 2 : 1;
  const u32x4* et = CONV == 1 ? (const u32x4*)&tabs.e1[0][0][0][0] : (const u32x4*)&tabs.e2[0][0][0];
  u32x4 E[NFB][2];
#pragma unroll
  for (int fb = 0; fb < NFB; ++fb) { E[fb][0] = et[fb * 128 + lane]; E[fb][1] = et[fb * 128 + 64 + lane]; }
  uint32_t v[8][8];
#pragma unroll
  for (int el = 0; el < 8; ++el)
#pragma unroll
    for (int jj = 0; jj < 8; ++jj) {
      const int co = CONV == 1 ? el * 512 : (el & 3) * 1024;
      const int so = (CONV == 1 ? 0 : (el >> 2) * 8192) + (jj ? (jj - 1) * 262144 : 0);
      v[el][jj] = __builtin_amdgcn_raw_buffer_load_b32(wr, (jj ? vb : vb0) + co, so, NT);
    }
  PHASE();
  const __amdgpu_buffer_rsrc_t dr = mkbuf(wh, (CONV == 1 ? 32 : 16) * WBIN);
  const int vo = 2 * h * WBIN + (4 * dq + (n32 >> 4)) * 32768 + ks * 2048 + kq * 256 + (n32 & 15) * 16;
#pragma unroll
  for (int e = 0; e < 2; ++e) {
    u32x4 b[8];
#pragma unroll
    for (int el = 0; el < 8; ++el)
#pragma unroll
      for (int j = 0; j < 4; ++j) b[el][j] = __builtin_amdgcn_perm(v[el][2 * j + 1], v[el][2 * j], e ? 0x07060302u : 0x05040100u);
#pragma unroll
    for (int fb = 0; fb < NFB; ++fb) {
      u32x4 o[16];
#pragma unroll
      for (int ep = 0; ep < 4; ++ep) {
        f32x16 d[2];
#pragma unroll
        for (int z = 0; z < 2; ++z) d[z] = mfma32(E[fb][1], b[2 * ep + z], zero16());
#pragma unroll
        for (int z = 0; z < 2; ++z) d[z] = mfma32(E[fb][0], b[2 * ep + z], d[z]);
#pragma unroll
        for (int i = 0; i < 16; ++i) o[i][ep] = pk2bf(d[0][i], d[1][i]);
      }
#pragma unroll
      for (int i = 0; i < 16; ++i)
        __builtin_amdgcn_raw_buffer_store_b128(o[i], dr, vo + (i & 1) * 1024,
                                               (16 * fb + 4 * (i >> 2) + ((i >> 1) & 1)) * WBIN + e * 65536, WT);
      st_pad(o[14], o[15]);
      SB();
    }
  }
}

__device__ __forceinline__ void xload(const uint16_t* __restrict__ x, int u, int lane, uint32_t (&v)[3][8]) {
  const int cq = u & 7, b = (u >> 3) & 1, sg = u >> 4;
  const __amdgpu_buffer_rsrc_t xr = mkbuf(x + (size_t)b * 16000 * DCH, 8192000);
#pragma unroll
  for (int ks = 0; ks < 3; ++ks) {
    const int row0 = 48 * sg - 24 + 16 * ks + 8 * (lane >> 5);
    const int vb = row0 >= 0 ? row0 * 1024 + cq * 128 + (lane & 31) * 4 : OOB;
#pragma unroll
    for (int e = 0; e < 8; ++e) v[ks][e] = __builtin_amdgcn_raw_buffer_load_b32(xr, vb + (e & 3) * 1024, (e >> 2) * 4096, NT);
  }
}
__device__ __forceinline__ void xtrans(char* __restrict__ u1, int u, int lane, const uint32_t (&v)[3][8], const u32x4 (&F)[2][3]) {
  const __amdgpu_buffer_rsrc_t ur = mkbuf(u1 + (2 * (u & 7)) * PL1 + (u >> 3) * 128, U1BYTES - (2 * (u & 7)) * PL1 - (u >> 3) * 128);
  const int vo = ((lane & 15) >> 1) * 16 * PL1 + ((lane >> 4) & 1) * 64 + (lane & 1) * 32 + (lane >> 5) * 16;
  u32x4 a[2][3];
#pragma unroll
  for (int ks = 0; ks < 3; ++ks)
#pragma unroll
    for (int j = 0; j < 4; ++j) {
      a[0][ks][j] = __builtin_amdgcn_perm(v[ks][2 * j + 1], v[ks][2 * j], 0x05040100u);
      a[1][ks][j] = __builtin_amdgcn_perm(v[ks][2 * j + 1], v[ks][2 * j], 0x07060302u);
    }
#pragma unroll
  for (int fb = 0; fb < 2; ++fb) {
    f32x16 d[2] = {zero16(), zero16()};
#pragma unroll
    for (int ks = 0; ks < 3; ++ks)
#pragma unroll
      for (int e = 0; e < 2; ++e) d[e] = mfma32(a[e][ks], F[fb][ks], d[e]);
#pragma unroll
    for (int qp = 0; qp < 2; ++qp) {
      u32x4 o[2];
#pragma unroll
      for (int z = 0; z < 2; ++z)
#pragma unroll
        for (int i = 0; i < 4; ++i) o[z][i] = pk2bf(d[0][8 * qp + 4 * z + i], d[1][8 * qp + 4 * z + i]);
      swap16(o[0], o[1]);
#pragma unroll
      for (int z = 0; z < 2; ++z) __builtin_amdgcn_raw_buffer_store_b128(o[z], ur, vo, ((fb * 16 + z * 8) * 16 + qp) * PL1, WT);
      st_pad(o[0], o[1]);
    }
  }
}

__global__ __launch_bounds__(512, 2) void pre_kernel(const uint16_t* __restrict__ x, const uint16_t* __restrict__ w1,
                                                     const uint16_t* __restrict__ w2, const uint16_t* __restrict__ wf,
                                                     const uint16_t* __restrict__ wo, const uint16_t* __restrict__ g,
                                                     const uint16_t* __restrict__ wg, char* __restrict__ u1,
                                                     char* __restrict__ w1h, char* __restrict__ w2h, u32x4* __restrict__ wff,
                                                     u32x4* __restrict__ wof, float* __restrict__ ge) {
  const int blk = blockIdx.x;
  const int tid = threadIdx.x, lane = tid & 63;
  const int wave = __builtin_amdgcn_readfirstlane(tid >> 6);
  const int n = lane & 31, h = lane >> 5;
  __shared__ char pfs[256];
  if (wave == PF1) {
    const char *p0 = (const char*)&tabs - 320, *p1;
    LABEL_ADDR(p1, ".Lce_g2");
    code_pf(p0, (int)(p1 - p0), blk >> 3, pfs);
  }
  if (wave == 0) {
    wtrans<1>(w1, w1h, blk, lane);
  } else if (wave == 1) {
    wtrans<2>(w2, w2h, blk, lane);
  } else {
    const int bq = (blk & 7) * 32 + (blk >> 3);
    const int u0 = ((21 * bq) >> 1) + wave - 2, ue = (21 * (bq + 1)) >> 1;
    u32x4 F[2][3];
#pragma unroll
    for (int fb = 0; fb < 2; ++fb)
#pragma unroll
      for (int ks = 0; ks < 3; ++ks) F[fb][ks] = ((const u32x4*)&tabs.f1[fb][ks][0][0])[lane];
    uint32_t v0[3][8];
    xload(x, u0, lane, v0);
    if (u0 + 6 < ue) {
      uint32_t v1[3][8];
      xload(x, u0 + 6, lane, v1);
      PHASE();
      xtrans(u1, u0, lane, v0, F);
      SB();
      xtrans(u1, u0 + 6, lane, v1, F);
    } else if (wave == 7) {
      const int s = blk & 15, eb = (blk >> 4) & 7, second = blk >> 7;
      const int r = (s - 4 * (eb >> 1)) & 15;
      const uint16_t* src = second ? wo + (16 * s + 4 * h) * DCH + 64 * (eb >> 1) + 2 * n + (eb & 1) : wf + dmap(s, 4 * h) * DCH + 32 * eb + n;
      const int st = second ? DCH : 2 * DCH;
      uint32_t v[8];
#pragma unroll
      for (int j = 0; j < 8; ++j) v[j] = src[(8 * (j >> 2) + (j & 3)) * st];
      PHASE();
      xtrans(u1, u0, lane, v0, F);
      const float sc = second ? 2.f : 1.f;
      u32x4 o;
#pragma unroll
      for (int j = 0; j < 4; ++j) o[j] = pk2bf(bf2f(v[2 * j]) * sc, bf2f(v[2 * j + 1]) * sc);
      (second ? wof + (((eb >> 1) * 16 + r) * 2 + (eb & 1)) * 64 : wff + (eb * 16 + r) * 64)[lane] = o;
    } else if (blk < 8) {
      const int c0 = 32 * blk + 2 * (lane & 15), kq = lane >> 4;
      uint32_t v[8][8];
      u32x4 ga[8];
#pragma unroll
      for (int ks = 0; ks < 8; ++ks) {
        ga[ks] = *(const u32x4*)(g + (lane & 15) * DCH + ks * 32 + 8 * kq);
#pragma unroll
        for (int e = 0; e < 8; ++e) v[ks][e] = *(const uint32_t*)(wg + (ks * 32 + 8 * kq + e) * DCH + c0);
      }
      PHASE();
      xtrans(u1, u0, lane, v0, F);
      f32x4 d[2] = {{0.f, 0.f, 0.f, 0.f}, {0.f, 0.f, 0.f, 0.f}};
#pragma unroll
      for (int ks = 0; ks < 8; ++ks)
#pragma unroll
        for (int e = 0; e < 2; ++e) {
          u32x4 bb;
#pragma unroll
          for (int j = 0; j < 4; ++j) bb[j] = __builtin_amdgcn_perm(v[ks][2 * j + 1], v[ks][2 * j], e ? 0x07060302u : 0x05040100u);
          d[e] = mfma16(ga[ks], bb, d[e]);
        }
      float* dst = ge + ((((blk >> 1) * 2 + (kq & 1)) * 2 + (blk & 1)) * 4 + ((lane >> 2) & 3)) * 64 + (((lane >> 1) & 1) * 8 + (kq >> 1) * 4) * 4 + 2 * (lane & 1);
      f32x2 gs[4];
      bool far = false;
#pragma unroll
      for (int i = 0; i < 4; ++i) {
        gs[i] = f32x2{bf2f(f2bf(d[0][i])) * C2, bf2f(f2bf(d[1][i])) * C2};
        far = far || __builtin_fabsf(gs[i][0]) > EMAX || __builtin_fabsf(gs[i][1]) > EMAX;
      }
      far = __builtin_amdgcn_ballot_w64(far) != 0;
#pragma unroll
      for (int i = 0; i < 4; ++i) {
        *(f32x2*)(dst + 4 * i) = far ? f32x2{-1.f, -1.f} : f32x2{__builtin_amdgcn_exp2f(-gs[i][0]), __builtin_amdgcn_exp2f(-gs[i][1])};
        *(f32x2*)(dst + 4096 + 4 * i) = gs[i];
      }
    } else {
      PHASE();
      xtrans(u1, u0, lane, v0, F);
    }
  }
}

constexpr int issued(int g, int MB, int OPS, int NST, int BAR, int PRO) {
  int o = PRO * OPS;
  for (int t = 0; t < g; ++t)
    if (o < 16 * OPS && (o / OPS < NST || t >= (o / OPS - NST + 1) * MB + BAR) && (MB != 11 || 2 * o * MB <= (2 * t + GLA * MB) * OPS)) ++o;
  return o;
}
template <int CONV>
__global__ __launch_bounds__(256, 1) void gemm_kernel(const char* __restrict__ U, const char* __restrict__ W,
                                                      char* __restrict__ Y, const char* __restrict__ pf) {
  constexpr int MB = CONV == 1 ? 11 : 8;
  constexpr int STG = MB * 2048;
  constexpr int NST = CONV == 1 ? 7 : 8;
  constexpr int PL = CONV == 1 ? PL1 : PL2;
  constexpr int NDMA = CONV == 1 ? 6 : 4;
  constexpr int OPS = NDMA + 2;
  extern __shared__ __attribute__((aligned(16))) char smem[];
  const int bid = blockIdx.x, xcd = bid & 7, slot = bid >> 3;
  const int bin = CONV == 1 ? xcd * 4 + (slot >> 3) : xcd * 2 + (slot >> 4);
  const int mt = CONV == 1 ? (slot >> 2) & 1 : (slot >> 2) & 3;
  const int tid = threadIdx.x, lane = tid & 63;
  const int wave = __builtin_amdgcn_readfirstlane(tid >> 6);
  const int cb = (slot & 3) * 4 + wave;
  const int row0 = CONV == 1 ? mt * 168 : mt * 126;
  const __amdgpu_buffer_rsrc_t ur = mkbuf(U + (size_t)bin * 16 * PL + row0 * 128, 16 * PL - row0 * 128);
  const __amdgpu_buffer_rsrc_t wr = mkbuf(W + (size_t)bin * WBIN + cb * 32768, 32768);
  int vo[2];
  if constexpr (CONV == 1) {
    vo[0] = wave * 1024 + (lane >> 3) * 128 + (((lane & 7) ^ ((4 * wave + (lane >> 4)) & 7)) * 16);
    vo[1] = 20480 + wave * 256 + (lane >> 5) * 128 + ((((lane >> 2) & 7) ^ wave) * 16) + (lane & 3) * 4;
  } else {
#pragma unroll
    for (int z = 0; z < 2; ++z) vo[z] = wave * 4096 + (lane >> 3) * 128 + (((lane & 7) ^ (2 * wave + z)) * 16);
  }
  u32x4 B[NST][2];
  auto issue = [&](auto is, auto ii) {
    constexpr int s = decltype(is)::value, i = decltype(ii)::value;
    char* buf = smem + (s % NST) * STG;
    if constexpr (i < 2) {
      B[s % NST][i] = __builtin_amdgcn_raw_buffer_load_b128(wr, lane * 16 + i * 1024, s * 2048, 0);
    } else if constexpr (CONV == 1) {
      if constexpr (i < 7) dma<16>(ur, buf + wave * 1024 + (i - 2) * 4096, vo[0], s * PL + (i - 2) * 4096);
      else dma<4>(ur, buf + 20480 + wave * 256, vo[1], s * PL);
    } else {
      dma<16>(ur, buf + wave * 4096 + (i - 2) * 1024, vo[(i - 2) >> 1], s * PL + (i - 2) * 1024);
    }
  };
  constexpr int AR = 4, AL = 3;
  constexpr int BAR = MB - AL;
  constexpr int PRO = GPRO;
  sfor<0, PRO>([&](auto is) { sfor<0, OPS>([&](auto ii) { issue(is, ii); SB(); }); });
  const int rho = lane & 15, kq = lane >> 4;
  const int lds0 = (int)(uint32_t)(uintptr_t)(__attribute__((address_space(3))) char*)smem;
  constexpr int WIN = CONV == 1 ? 2 : 4;
  constexpr int NWIN = (NST + WIN - 1) / WIN;
  int ab[NWIN][2];
#pragma unroll
  for (int ri = 0; ri < 2; ++ri) {
    const int pos = ((kq >> 1) * 4 + ri * 2 + (kq & 1)) ^ (rho >> 1);
    const int a = lds0 + (CONV == 1 ? rho * 128 : (rho >> 1) * 2048 + (rho & 1) * 128) + pos * 16;
#pragma unroll
    for (int wn = 0; wn < NWIN; ++wn) { ab[wn][ri] = a + wn * WIN * STG; asm volatile("" : "+v"(ab[wn][ri])); }
  }
  auto aread = [&](int s, int mb, int ri) -> u32x4 {
    const int bf = s % NST;
    return lds_ld(ab[bf / WIN][ri] + (bf % WIN) * STG + mb * (CONV == 1 ? 2048 : 256));
  };
  f32x4 yr[MB], yi[MB];
#pragma unroll
  for (int mb = 0; mb < MB; ++mb) { yr[mb] = f32x4{0.f, 0.f, 0.f, 0.f}; yi[mb] = f32x4{0.f, 0.f, 0.f, 0.f}; }
  u32x4 A[AR][2];
  asm volatile("s_waitcnt vmcnt(%0)" ::"n"((PRO - 1) * OPS) : "memory");
  __builtin_amdgcn_s_barrier();
  asm volatile("" ::: "memory");
#pragma unroll
  for (int z = 0; z < AL; ++z) { A[z][0] = aread(0, z, 0); A[z][1] = aread(0, z, 1); }
  SB();
  u32x4 nwi;
#pragma unroll
  for (int j = 0; j < 4; ++j) nwi[j] = B[0][1][j] ^ 0x80008000u;
  sfor<0, 16>([&](auto is) {
    constexpr int s = decltype(is)::value;
    u32x4 nwn;
    sfor<0, MB>([&](auto im) {
      constexpr int mb = decltype(im)::value;
      constexpr int g = s * MB + mb, gn = g + AL;
      constexpr int o0 = issued(g, MB, OPS, NST, BAR, PRO), o1 = issued(g + 1, MB, OPS, NST, BAR, PRO);
      if constexpr (mb == BAR && s + 1 < 16) {
        static_assert(o0 >= (s + 2) * OPS, "next stage not requested yet");
        asm volatile("s_waitcnt vmcnt(%0)" ::"n"(o0 - (s + 2) * OPS) : "memory");
        __builtin_amdgcn_s_barrier();
        asm volatile("" ::: "memory");
        SB();
      }
      auto mf = [&](int i) {
        if (i < 2) yr[mb] = mfma16(i ? nwi : B[s % NST][0], A[g % AR][i], yr[mb]);
        else yi[mb] = mfma16(B[s % NST][3 - i], A[g % AR][i - 2], yi[mb]);
      };
      constexpr int ord = (mb & 1) ? ORD1 : ORD0;
      mf((ord >> 12) & 3);
      SB();
      mf((ord >> 8) & 3);
      if constexpr (gn < 16 * MB) A[gn % AR][0] = aread(gn / MB, gn % MB, 0);
      SB();
      mf((ord >> 4) & 3);
      if constexpr (o1 > o0) issue(IC<o0 / OPS>{}, IC<o0 % OPS>{});
      SB();
      mf(ord & 3);
      if constexpr (CONV == 1 ? (g >= TS0 && g < TS1) : (g >= TU0 && g < TU1)) asm volatile("s_nop %0" ::"n"(CONV == 1 ? THR : THR2));
      if constexpr (gn < 16 * MB) A[gn % AR][1] = aread(gn / MB, gn % MB, 1);
      if constexpr (mb == MB - 2 && s + 1 < 16) {
#pragma unroll
        for (int j = 0; j < 4; ++j) nwn[j] = B[(s + 1) % NST][1][j] ^ 0x80008000u;
      }
      SB();
    });
    if constexpr (s + 1 < 16) nwi = nwn;
  });
  if constexpr (CONV == 1) {
    const __amdgpu_buffer_rsrc_t yb = mkbuf(Y + (size_t)(bin * 16 + cb) * YP1 + row0 * 64, 168 * 64);
    u32x4 o[MB];
#pragma unroll
    for (int mb = 0; mb < MB; ++mb) {
#pragma unroll
      for (int i = 0; i < 4; ++i) o[mb][i] = pk2bf(yr[mb][i], yi[mb][i]);
      if (mb) asm volatile("" :: "v"(o[mb - 1]), "v"(o[mb]) : "memory");
      __builtin_amdgcn_raw_buffer_store_b128(o[mb], yb, rho * 64 + kq * 16 + (mb & 3) * 1024, (mb >> 2) * 4096, WT);
    }
  } else {
    const __amdgpu_buffer_rsrc_t yb = mkbuf(Y + (size_t)(bin * 16 + cb) * YP2 + row0 * 64, YP2 - row0 * 64);
    const float sg = (bin & 1) ? -1.f : 1.f;
    f32x4 nr, ni;
#pragma unroll
    for (int i = 0; i < 4; ++i) { nr[i] = __shfl_down(yr[0][i], 2, 16); ni[i] = __shfl_down(yi[0][i], 2, 16); }
    const int vy = (rho >> 1) * 1024 + (rho & 1) * 64 + kq * 16;
#pragma unroll
    for (int mb = 0; mb < MB; ++mb) {
      const f32x4 xr = mb < MB - 1 ? yr[mb < MB - 1 ? mb + 1 : 0] : nr, xi = mb < MB - 1 ? yi[mb < MB - 1 ? mb + 1 : 0] : ni;
      u32x4 o;
#pragma unroll
      for (int i = 0; i < 4; ++i) o[i] = pk2bf(__builtin_fmaf(sg, xi[i], yr[mb][i]), __builtin_fmaf(-sg, xr[i], yi[mb][i]));
      __builtin_amdgcn_raw_buffer_store_b128(o, yb, (mb == MB - 1 && rho >= 14) ? OOB : vy + mb * 128, 0, WT);
    }
  }
  if constexpr (CONV == 1) asm volatile(".Lce_g1:"); else asm volatile(".Lce_g2:");
}

template <int Z>
__device__ __forceinline__ void mid_unit(char* __restrict__ U, int ya, int ga, int cb, int s, const u32x4& F) {
  const int lane = threadIdx.x & 63;
  const int n32 = lane & 31, h = lane >> 5;
  constexpr int NSG = Z == 0 ? 1 : 2;
  const int tau = 16 * ((n32 >> 2) & 1) + 8 * ((n32 >> 3) & 1) + 2 * (n32 & 3) + (n32 >> 4);
  const int r[2] = {Z == 0 ? tau + 16 : Z == 1 ? (tau < 16 ? tau + 48 : 0) : tau + 32, Z == 1 ? tau : (tau >= 16 ? tau - 16 : 0)};
  f32x16 d = zero16();
#pragma unroll
  for (int sg = 0; sg < NSG; ++sg)
#pragma unroll
    for (int ks = 0; ks < 4; ++ks) {
      u32x4 yv;
#pragma unroll
      for (int j = 0; j < 4; ++j) yv[j] = lds_ld32(ya + sg * 4096 + ks * 1024 + j * 128);
      d = mfma32(lds_ld(ga + r[sg] * 128 + (((2 * ks + h) ^ ((r[sg] >> 1) & 7)) * 16)), yv, d);
    }
  const __amdgpu_buffer_rsrc_t ub = mkbuf(U, U2BYTES);
  const int vu = (((lane & 15) >> 1) * 16 + cb) * PL2 + s * 256 + ((lane >> 4) & 1) * 64 + (lane & 1) * 32 + h * 16;
  const int t0r = 32 * s - 16 + 16 * h;
  const bool ok = t0r >= 0 && t0r < 8000;
  f32x16 u[2];
#pragma unroll
  for (int q = 0; q < 2; ++q) {
    u32x4 a;
#pragma unroll
    for (int j = 0; j < 4; ++j) {
      const uint32_t gv = gelu2(d[8 * q + 2 * j], d[8 * q + 2 * j + 1]);
      a[j] = ok ? gv : 0u;
    }
    u[q] = mfma32(a, F, zero16());
  }
#pragma unroll
  for (int b = 0; b < 2; ++b) {
    u32x4 o[2];
#pragma unroll
    for (int q = 0; q < 2; ++q)
#pragma unroll
      for (int j = 0; j < 4; ++j) o[q][j] = pk2bf(u[q][8 * b + 2 * j], u[q][8 * b + 2 * j + 1]);
    swap16(o[0], o[1]);
#pragma unroll
    for (int z = 0; z < 2; ++z) __builtin_amdgcn_raw_buffer_store_b128(o[z], ub, vu + b * 128, z * 8 * 16 * PL2, WT);
    st_pad(o[0], o[1]);
  }
}
__global__ __launch_bounds__(1024) void mid_kernel(const char* __restrict__ Y, char* __restrict__ U) {
  __shared__ __attribute__((aligned(16))) char sm[8192 + 49152];
  __shared__ uint32_t landed;
  const int blk = blockIdx.x, lane = threadIdx.x & 63;
  const int wave = __builtin_amdgcn_readfirstlane(threadIdx.x >> 6);
  const int cb = blk & 15, s0 = blk & ~15, s = s0 + wave;
  const int sg0 = (2 * s0) / 3;
  if (wave < 4) {
    const __amdgpu_buffer_rsrc_t tb = mkbuf(&tabs.g1[0][0][0], 8192);
#pragma unroll
    for (int i = 0; i < 2; ++i) dma<16>(tb, sm + wave * 2048 + i * 1024, lane * 16, wave * 2048 + i * 1024);
  }
  {
    const __amdgpu_buffer_rsrc_t yb = mkbuf(Y, Y1BYTES);
    const int vy = ((lane >> 3) * 16 + cb) * YP1 + sg0 * 128 + (lane & 7) * 16;
#pragma unroll
    for (int i = 0; i < 3; ++i) {
      const int pc = 3 * wave + i;
      dma<16>(yb, sm + 8192 + pc * 1024, vy, (pc & 3) * 128 * YP1 + (pc >> 2) * 128);
    }
  }
  const u32x4 F = ((const u32x4*)&tabs.f2[0][0])[lane];
  if (threadIdx.x == 0) landed = 0;
  asm volatile("s_waitcnt lgkmcnt(0)\n\ts_barrier" ::: "memory");
  {
    asm volatile("s_waitcnt vmcnt(0)" ::: "memory");
    if (lane == 0) __hip_atomic_fetch_or(&landed, 1u << wave, __ATOMIC_RELAXED, __HIP_MEMORY_SCOPE_WORKGROUP);
    if (s < 251) {
      const int p = s / 3, z = s - 3 * p;
      const int l0 = 2 * p + (z == 2) - sg0, l1 = l0 + (z != 0);
      const uint32_t need = 15u | (((2u << ((4 * l1 + 3) / 3)) - 1u) & ~((1u << ((4 * l0) / 3)) - 1u));
      while ((__builtin_amdgcn_readfirstlane(__hip_atomic_load(&landed, __ATOMIC_RELAXED, __HIP_MEMORY_SCOPE_WORKGROUP)) & need) != need)
        __builtin_amdgcn_s_sleep(1);
      asm volatile("" ::: "memory");
      const int l = (int)(uint32_t)(uintptr_t)(__attribute__((address_space(3))) char*)sm;
      const int ya = l + 8192 + l0 * 4096 + (lane >> 5) * 512 + (lane & 31) * 4;
      if (z == 0) mid_unit<0>(U, ya, l, cb, s, F);
      else if (z == 1) mid_unit<1>(U, ya, l, cb, s, F);
      else mid_unit<2>(U, ya, l, cb, s, F);
    }
  }
}

constexpr int J_F = 0;
constexpr int J_T = 16384;
constexpr int J_G = 49152;
constexpr int JOINT_LDS = 65536;
__device__ __forceinline__ void lds_st(int a, const u32x4& v) { *(lds_u32x4*)(uintptr_t)(uint32_t)a = v; }
#define BAR() do { SB(); asm volatile("s_waitcnt lgkmcnt(0)\n\ts_barrier" ::: "memory"); SB(); } while (0)
template <int M, bool Z>
__device__ __forceinline__ void mm(f32x16& c, const u32x4& a, const u32x4& b) {
  if constexpr (Z) {
    if constexpr (M == 0) asm volatile("v_mfma_f32_32x32x16_bf16 %0, %1, %2, 0" : "=&v"(c) : "v"(a), "v"(b));
    else if constexpr (M == 1) asm volatile("v_mfma_f32_32x32x16_bf16 %0, %1, %2, 0" : "=&v"(c) : "a"(a), "v"(b));
    else asm volatile("v_mfma_f32_32x32x16_bf16 %0, %1, %2, 0" : "=&v"(c) : "v"(a), "a"(b));
  } else {
    if constexpr (M == 0) asm volatile("v_mfma_f32_32x32x16_bf16 %0, %1, %2, %0" : "+v"(c) : "v"(a), "v"(b));
    else if constexpr (M == 1) asm volatile("v_mfma_f32_32x32x16_bf16 %0, %1, %2, %0" : "+v"(c) : "a"(a), "v"(b));
    else asm volatile("v_mfma_f32_32x32x16_bf16 %0, %1, %2, %0" : "+v"(c) : "v"(a), "a"(b));
  }
}
#define MWAIT(c0, c1) asm volatile("s_nop 7\n\ts_nop 3" : "+v"(c0), "+v"(c1))
template <int K>
__device__ __forceinline__ float add_quad(float a, float b) {
  float r;
  asm volatile("v_add_f32_dpp %0, %1, %2 quad_perm:[%3,%3,%3,%3] row_mask:0xf bank_mask:0xf" : "=v"(r) : "v"(a), "v"(b), "n"(K));
  return r;
}

#define WI 2
#define WA 6
#define WB 4
#define WZ 3
constexpr int wcount(int t) {
  int c = 0;
  for (int i = 0; i < t; ++i) c += i < 4 ? WI : i == 4 ? WA : i < 8 ? WB : i < 24 ? 0 : i < 56 ? (i % WZ != 0) : i < 76 ? (i & 1) : 1;
  return c < 64 ? c : 64;
}
__global__ __launch_bounds__(256, 1) void joint_kernel(const char* __restrict__ Y, const char* __restrict__ wff,
                                                       const float* __restrict__ ge, uint16_t* __restrict__ out) {
  extern __shared__ __attribute__((aligned(16))) char smem[];
  const int blk = blockIdx.x;
  const int tid = threadIdx.x, lane = tid & 63;
  const int w = __builtin_amdgcn_readfirstlane(tid >> 6);
  const int n = lane & 31, h = lane >> 5;
  const int l0 = (int)(uint32_t)(uintptr_t)(__attribute__((address_space(3))) char*)smem;
  u32x4 yv[4][2], gi[2][2];
#pragma unroll
  for (int ks = 0; ks < 2; ++ks) gi[0][ks] = *(const u32x4*)&tabs.g2[4 * (n & 3) + ((n >> 2) & 3)][16 * ks + 8 * h];
  SB();
  const __amdgpu_buffer_rsrc_t yb = mkbuf(Y, Y2BYTES);
  const int vy = (4 * h * 16 + 4 * w) * YP2 + blk * 128 + n * 4;
  const __amdgpu_buffer_rsrc_t wb = mkbuf(wff + w * 32768, 131072 + 32768);
  u32x4 wt[64];
  auto wload = [&](int i0, int i1) {
#pragma unroll
    for (int i = i0; i < i1; ++i) wt[i] = __builtin_amdgcn_raw_buffer_load_b128(wb, lane * 16 + (i & 3) * 1024, (i >> 5) * 131072 + ((i >> 2) & 7) * 4096, 0);
    SB();
  };
  auto wstep = [&](auto it) { constexpr int i0 = wcount(decltype(it)::value), i1 = wcount(decltype(it)::value + 1); wload(i0, i1); };
  sfor<0, 4>([&](auto ic) {
    constexpr int c = decltype(ic)::value;
#pragma unroll
    for (int ks = 0; ks < 2; ++ks)
#pragma unroll
      for (int j = 0; j < 4; ++j) yv[c][ks][j] = __builtin_amdgcn_raw_buffer_load_b32(yb, vy, ((8 * ks + j) * 16 + c) * YP2, 0);
    SB();
    wstep(ic);
  });
  dma<4>(mkbuf(wff, 262144), smem + J_T + w * 256, (lane * 124 + (blk >> 3) * 4 + w) * 128, 0);
  SB();
#pragma unroll
  for (int ks = 0; ks < 2; ++ks) {
    gi[1][ks] = n >= 16 ? gi[0][ks] : u32x4{0u, 0u, 0u, 0u};
    gi[0][ks] = n < 16 ? gi[0][ks] : u32x4{0u, 0u, 0u, 0u};
  }
  int ra[3];
#pragma unroll
  for (int i = 0; i < 3; ++i) ra[i] = l0 + ((w + 1 + i) & 3) * 4096 + lane * 16;
  const int wa = l0 + w * 4096 + lane * 16;
  const int qa = l0 + J_G + w * 4096 + (h * 8 + (n >> 4) * 4 + (n & 3)) * 16;
  asm volatile("s_nop 1" : "+v"(gi[0][0]), "+v"(gi[0][1]), "+v"(gi[1][0]), "+v"(gi[1][1]));
  u32x4 fb[16];
  sfor<0, 2>([&](auto icp) {
    constexpr int cp = decltype(icp)::value;
    f32x16 d;
    sfor<0, 4>([&](auto iI) {
      constexpr int c = decltype(iI)::value >> 1, ks = decltype(iI)::value & 1;
      mm<0, c + ks == 0>(d, yv[2 * cp + c][ks], gi[c][ks]);
    });
    SB();
    if constexpr (cp) wstep(IC<5>{});
    asm volatile("s_nop 7\n\ts_nop 3" : "+v"(d));
#pragma unroll
    for (int j = 0; j < 4; ++j) fb[2 * cp][j] = gelu2(d[2 * j], d[2 * j + 1]);
    SB();
    if constexpr (cp) wstep(IC<6>{});
#pragma unroll
    for (int j = 0; j < 4; ++j) fb[2 * cp + 1][j] = gelu2(d[8 + 2 * j], d[8 + 2 * j + 1]);
    swap16(fb[2 * cp], fb[2 * cp + 1]);
#pragma unroll
    for (int c = 0; c < 2; ++c) lds_st(wa + J_F + (2 * cp + c) * 1024, fb[2 * cp + c]);
    SB();
    if constexpr (cp) {
      const __amdgpu_buffer_rsrc_t gb = mkbuf(ge + w * 1024, 4096);
#pragma unroll
      for (int i = 0; i < 4; ++i) dma<16>(gb, smem + J_G + w * 4096 + i * 1024, lane * 16, i * 1024);
      SB();
    }
    wstep(IC<cp ? 7 : 4>{});
  });
  f32x16 fe[2];
  f32x4 q[16];
  sfor<0, 16>([&](auto ir) {
    constexpr int r = decltype(ir)::value;
    mm<1, r == 0>(fe[0], wt[r], fb[r]);
    if constexpr (r == 0) { BAR(); }
    SB();
    asm volatile("" :: "a"(wt[r + 1]));
    if constexpr (r >= 3 && r < 15) asm volatile("" :: "v"(fb[r + 1]));
    if constexpr (r < 6) {
      fb[2 * r + 4] = lds_ld(ra[r >> 1] + J_F + ((2 * r) & 3) * 1024);
      fb[2 * r + 5] = lds_ld(ra[r >> 1] + J_F + ((2 * r + 1) & 3) * 1024);
    } else if constexpr (r >= 12) {
      if constexpr (r == 12) asm volatile("" :: "a"(wt[wcount(7)]) : "memory");
      q[2 * r - 24] = __builtin_bit_cast(f32x4, lds_ld(qa + (2 * r - 24) * 256));
      q[2 * r - 23] = __builtin_bit_cast(f32x4, lds_ld(qa + (2 * r - 23) * 256));
    }
    SB();
    wstep(IC<24 + r>{});
  });
  float p[32], x[8], mx = 0.f;
  u32x4 tp[2][4];
  auto stepE = [&](int i) {
    fe[i >> 4][i & 15] *= C2;
    mx = __builtin_fmaxf(mx, __builtin_fabsf(fe[i >> 4][i & 15]));
    p[i] = __builtin_amdgcn_exp2f(fe[i >> 4][i & 15]);
  };
  auto twrite = [&](auto iG) {
    constexpr int W = decltype(iG)::value + 31;
    if constexpr (W >= 0 && W < 256 && (W & 7) == 7) lds_st(wa + J_T + ((W >> 5) & 1) * 16384 + ((W >> 3) & 3) * 1024, tp[(W >> 5) & 1][(W >> 3) & 3]);
    SB();
  };
  auto tslot = [&](auto iG) {
    constexpr int G = decltype(iG)::value;
    constexpr int A = G + 36, B = G + 35, C = G + 34, D = G + 33;
    if constexpr (C >= 0 && C < 256) asm volatile("v_fma_f32 %0, -%1, %0, 0.5" : "+v"(x[C & 7]) : "v"(q[((C >> 5) & 1) * 8 + ((C >> 2) & 7)][C & 3]));
    if constexpr (B >= 0 && B < 256) asm volatile("v_rcp_f32 %0, %0" : "+v"(x[B & 7]));
    if constexpr (A >= 0 && A < 256) x[A & 7] = add_quad<(A >> 6)>(p[A & 31], q[((A >> 5) & 1) * 8 + ((A >> 2) & 7)][A & 3]);
    SB();
    if constexpr (D >= 0 && D < 256 && (D & 1)) tp[(D >> 5) & 1][(D >> 3) & 3][(D >> 1) & 3] = pk2bf(x[(D - 1) & 7], x[D & 7]);
    SB();
  };
  sfor<0, 16>([&](auto ir) {
    constexpr int r = decltype(ir)::value;
    mm<1, r == 0>(fe[1], wt[16 + r], fb[r]);
    SB();
    if constexpr (r < 15) asm volatile("" :: "a"(wt[17 + r]));
    if constexpr (r == 3) asm volatile("" :: "v"(q[7]));
    if constexpr (r == 12) asm volatile("" :: "v"(q[15]));
    if constexpr (r < 8) q[8 + r] = __builtin_bit_cast(f32x4, lds_ld(qa + (8 + r) * 256));
    wstep(IC<40 + r>{});
    if constexpr (r >= 4) twrite(IC<r - 40>{});
    if constexpr (r == 2) asm volatile("s_nop 1" : "+v"(fe[0]));
    if constexpr (r >= 2 && r < 10) { stepE(2 * r - 4); stepE(2 * r - 3); SB(); }
    if constexpr (r >= 4) tslot(IC<r - 40>{});
  });
  sfor<12, 16>([&](auto ir) { twrite(IC<decltype(ir)::value - 36>{}); tslot(IC<decltype(ir)::value - 36>{}); });
  asm volatile("" : "+v"(fe[1]));
#pragma unroll
  for (int i = 16; i < 32; ++i) stepE(i);
  const __amdgpu_buffer_rsrc_t ob = mkbuf(out + (size_t)blk * 16 * 8 * DCH, 4000 * 4096 + 65536);
  const int vo = h * 4096 + w * 128 + n * 4;
  if (__builtin_expect(__builtin_amdgcn_ballot_w64(mx > EMAX || q[0][0] < 0.f || q[4][0] < 0.f) != 0, 0)) {
    wload(IC<wcount(56)>::value, 64);
#pragma unroll 1
    for (int st = 0; st < 8; ++st) {
      const float* gp = ge + 4096 + (w * 2 + (st & 1)) * 512 + (h * 8 + (n >> 4) * 4 + (n & 3)) * 4;
#pragma unroll
      for (int f = 0; f < 4; ++f) {
        u32x4 t;
#pragma unroll
        for (int c = 0; c < 2; ++c) {
          const f32x4 gv = *(const f32x4*)(gp + (2 * f + c) * 64);
          float y[4];
#pragma unroll
          for (int i = 0; i < 4; ++i)
            y[i] = 0.5f - __builtin_amdgcn_rcpf(__builtin_amdgcn_exp2f(__shfl(fe[f >> 1][8 * (f & 1) + 4 * c + i], (lane & ~3) + (st >> 1)) + gv[i]) + 1.f);
          t[2 * c] = pk2bf(y[0], y[1]);
          t[2 * c + 1] = pk2bf(y[2], y[3]);
        }
        lds_st(wa + J_T + (st & 1) * 16384 + f * 1024, t);
      }
      BAR();
      f32x16 o[2];
      sfor<0, 16>([&](auto ir) {
        constexpr int r = decltype(ir)::value;
        const u32x4 a = lds_ld((r < 4 ? wa : ra[r < 4 ? 0 : (r >> 2) - 1]) + J_T + (st & 1) * 16384 + (r & 3) * 1024);
#pragma unroll
        for (int z = 0; z < 2; ++z) mm<2, r == 0>(o[z], a, wt[32 + 2 * r + z]);
      });
      MWAIT(o[0], o[1]);
#pragma unroll
      for (int i = 0; i < 16; ++i)
        __builtin_amdgcn_raw_buffer_store_b32(pk2bf(o[0][i], o[1][i]), ob, vo + (st & 1) * 2048 + (st >> 1) * 16384 + (i & 3) * 512,
                                              (i >> 3) * (4000 * 4096) + 2 * ((i >> 2) & 1) * 4096, NT);
    }
    return;
  }
  asm volatile("s_nop 1");
  SB();
  sfor<16, 36>([&](auto ir) { constexpr int i = decltype(ir)::value; wstep(IC<40 + i>{}); twrite(IC<i - 36>{}); tslot(IC<i - 36>{}); });
  f32x16 acc[2][2];
  u32x4 a[16];
  uint32_t ov[16];
  auto store = [&](int st, int i) {
    __builtin_amdgcn_raw_buffer_store_b32(ov[i], ob, vo + ((st & 1) * 4 + (i & 3)) * 512,
                                          (i >> 3) * (4000 * 4096) + ((st >> 1) * 4 + 2 * ((i >> 2) & 1)) * 4096, NT);
  };
  sfor<0, 256>([&](auto iG) {
    constexpr int G = decltype(iG)::value, st = G >> 5, g = G & 31, r = g >> 1, z = g & 1;
    mm<2, r == 0>(acc[st & 1][z], r < 4 ? tp[st & 1][r] : a[r], wt[32 + g]);
    SB();
    if constexpr (g == 2) BAR();
    if constexpr (st == 0 && g < 31) asm volatile("" :: "a"(wt[33 + g]));
    if constexpr (z && r >= 3 && r < 15) asm volatile("" :: "v"(a[r + 1]));
    constexpr int rd = g >= 3 && g < 6 ? g + 1 : (g >= 7 && g <= 23 && z) ? (g + 7) / 2 : 0;
    constexpr int sn = g == 1 ? 15 : g == 2 ? 0 : g == 6 ? 1 : g >= 25 ? g - 17 : (g > 8 && !z && (g & 7)) ? (g - 6) / 2 - (g > 16) : -1;
    if constexpr (rd) a[rd] = lds_ld(ra[(rd >> 2) - 1] + J_T + (st & 1) * 16384 + (rd & 3) * 1024);
    if constexpr (sn >= 0 && st > (g == 1)) store(st - 1 - (g == 1), sn);
    if constexpr (st == 0) wstep(IC<76 + g>{});
    twrite(iG);
    tslot(iG);
    if constexpr (G == 225) asm volatile("s_nop 5" : "+v"(acc[0][0]));
    if constexpr (st > 0 && z) ov[r] = pk2bf(acc[(st - 1) & 1][0][r], acc[(st - 1) & 1][1][r]);
    SB();
  });
  store(6, 15);
  MWAIT(acc[1][0], acc[1][1]);
#pragma unroll
  for (int i = 0; i < 16; ++i) { ov[i] = pk2bf(acc[1][0][i], acc[1][1][i]); store(7, i); }
}

}

extern "C" void kernel_launch(void* const* d_in, const int* in_sizes, int n_in, void* d_out, int out_size, void* d_ws,
                              size_t ws_size, hipStream_t stream) {
  const uint16_t* x = (const uint16_t*)d_in[0];
  const uint16_t* w1 = (const uint16_t*)d_in[1];
  const uint16_t* w2 = (const uint16_t*)d_in[2];
  const uint16_t* g = (const uint16_t*)d_in[3];
  const uint16_t* wf = (const uint16_t*)d_in[4];
  const uint16_t* wg = (const uint16_t*)d_in[5];
  const uint16_t* wo = (const uint16_t*)d_in[6];
  uint16_t* out = (uint16_t*)d_out;
#define WOFF 0
  char* ws = (char*)d_ws + ((size_t)WOFF << 20);
  char* u1 = ws;
  char* w1h = ws + (24 << 20);
  char* w2h = ws + (40 << 20);
  char* y1 = ws + (48 << 20);
  char* u2 = ws + (60 << 20);
  char* y2 = ws + (77 << 20);
  u32x4* wff = (u32x4*)(ws + (86 << 20));
  u32x4* wof = (u32x4*)(ws + (86 << 20) + 131072);
  float* ge = (float*)(ws + (86 << 20) + 262144);
  constexpr int G1LDS = 7 * 11 * 2048 + 256, G2LDS = 8 * 8 * 2048 + 256;

  (void)hipFuncSetAttribute((const void*)gemm_kernel<1>, hipFuncAttributeMaxDynamicSharedMemorySize, G1LDS);
  (void)hipFuncSetAttribute((const void*)gemm_kernel<2>, hipFuncAttributeMaxDynamicSharedMemorySize, G2LDS);
  (void)hipFuncSetAttribute((const void*)joint_kernel, hipFuncAttributeMaxDynamicSharedMemorySize, JOINT_LDS);

  pre_kernel<<<256, 512, 0, stream>>>(x, w1, w2, wf, wo, g, wg, u1, w1h, w2h, wff, wof, ge);
  gemm_kernel<1><<<256, 256, G1LDS, stream>>>(u1, w1h, y1, (const char*)wff);
  mid_kernel<<<256, 1024, 0, stream>>>(y1, u2);
  gemm_kernel<2><<<256, 256, G2LDS, stream>>>(u2, w2h, y2, (const char*)wff);
  joint_kernel<<<250, 256, JOINT_LDS, stream>>>(y2, (const char*)wff, ge, out);
}
```

```cpp
#define PF1 7
#include <hip/hip_runtime.h>
#include <stdint.h>

typedef __attribute__((ext_vector_type(8))) short bf16x8;
typedef __attribute__((ext_vector_type(16))) float f32x16;
typedef __attribute__((ext_vector_type(4))) float f32x4;
typedef __attribute__((ext_vector_type(2))) float f32x2;
typedef __attribute__((ext_vector_type(4))) unsigned int u32x4;
typedef __attribute__((ext_vector_type(2))) unsigned int u32x2;

#define ORD0 0x0132
#define ORD1 0x2310
#define GLA 6
#define TS0 22
#define TS1 55
#define TU0 22
#define TU1 55
#define THR 7
#define THR2 7
#define GPRO 2
constexpr int DCH = 256;

__device__ __forceinline__ float bf2f(uint32_t v) { return __uint_as_float(v << 16); }
__device__ __forceinline__ uint32_t f2bf(float f) {
  uint32_t u = __float_as_uint(f);
  u += 0x7FFF + ((u >> 16) & 1);
  return u >> 16;
}
__device__ __forceinline__ uint32_t pk2bf(float a, float b) {
  typedef __attribute__((ext_vector_type(2))) __bf16 b2;
  f32x2 v = {a, b};
  b2 r = __builtin_convertvector(v, b2);
  return __builtin_bit_cast(uint32_t, r);
}
__device__ __forceinline__ uint32_t gelu2(float a0, float a1) {
  const f32x2 x = {a0, a1};
  const f32x2 c0 = {-2.302208198144325f, -2.302208198144325f}, c1 = {-0.10294323970002349f, -0.10294323970002349f};
  const f32x2 z = x * __builtin_elementwise_fma(x * x, c1, c0);
  const f32x2 e = {__builtin_amdgcn_exp2f(z[0]), __builtin_amdgcn_exp2f(z[1])};
  const f32x2 d = e + f32x2{1.0f, 1.0f};
  const f32x2 y = x * f32x2{__builtin_amdgcn_rcpf(d[0]), __builtin_amdgcn_rcpf(d[1])};
  return pk2bf(y[0], y[1]);
}
__device__ __forceinline__ f32x16 mfma32(const u32x4& a, const u32x4& b, const f32x16& c) {
  return __builtin_amdgcn_mfma_f32_32x32x16_bf16(__builtin_bit_cast(bf16x8, a), __builtin_bit_cast(bf16x8, b), c, 0, 0, 0);
}
__device__ __forceinline__ f32x4 mfma16(const u32x4& a, const u32x4& b, const f32x4& c) {
  return __builtin_amdgcn_mfma_f32_16x16x32_bf16(__builtin_bit_cast(bf16x8, a), __builtin_bit_cast(bf16x8, b), c, 0, 0, 0);
}
__device__ __forceinline__ f32x16 zero16() {
  f32x16 z;
#pragma unroll
  for (int i = 0; i < 16; ++i) z[i] = 0.f;
  return z;
}
__device__ __forceinline__ __amdgpu_buffer_rsrc_t mkbuf(const void* p, int nbytes) {
  return __builtin_amdgcn_make_buffer_rsrc((void*)p, (short)0, nbytes, 0x00020000);
}
template <int V> struct IC { static constexpr int value = V; };
template <int I, int N, class F>
__device__ __forceinline__ void sfor(F&& f) {
  if constexpr (I < N) {
    f(IC<I>{});
    sfor<I + 1, N>(f);
  }
}
#define SB() __builtin_amdgcn_sched_barrier(0)
#define PHASE() do { SB(); asm volatile("s_waitcnt vmcnt(0)\n\ts_barrier" ::: "memory"); SB(); } while (0)

constexpr double PI_ = 3.14159265358979323846;
constexpr double c128(int m) {
  m = ((m % 128) + 128) % 128;
  if (m > 64) m = 128 - m;
  double sg = 1.0;
  if (m > 32) { m = 64 - m; sg = -1.0; }
  if (m == 32) return 0.0;
  const double a = PI_ * m / 64.0;
  double term = 1.0, sum = 1.0;
  for (int i = 1; i < 16; ++i) { term *= -a * a / ((2 * i - 1) * (2 * i)); sum += term; }
  return sg * sum;
}
constexpr double s128(int m) { return c128(m - 32); }
constexpr uint16_t cbf(double v) {
  uint32_t u = __builtin_bit_cast(uint32_t, (float)v);
  u += 0x7FFF + ((u >> 16) & 1);
  return (uint16_t)(u >> 16);
}
constexpr double cbf2d(uint16_t v) { return (double)__builtin_bit_cast(float, (uint32_t)v << 16); }
struct Tabs {
  uint16_t f1[2][3][64][8];
  uint16_t e1[2][2][64][8];
  uint16_t e2[2][64][8];
  uint16_t g1[64][8][8];
  uint16_t f2[64][8];
  uint16_t g2[16][32];
};
constexpr Tabs make_tabs() {
  Tabs t{};
  for (int l = 0; l < 64; ++l)
    for (int e = 0; e < 8; ++e) {
      const int h = l >> 5, f = l & 31, ri = f & 1;
      for (int fb = 0; fb < 2; ++fb) {
        const int k = 16 * fb + (f >> 1);
        for (int ks = 0; ks < 3; ++ks) {
          const int a = (2 * k + 1) * (16 * ks + 8 * h + e);
          t.f1[fb][ks][l][e] = cbf(ri ? -s128(a) : c128(a));
        }
        const int a = (2 * k + 1) * (8 * h + e);
        const double v = ri ? s128(a) : c128(a);
        t.e1[fb][0][l][e] = cbf(v);
        t.e1[fb][1][l][e] = cbf(v - cbf2d(cbf(v)));
      }
      const int a = 2 * (2 * (f >> 1) + 1) * (8 * h + e);
      const double v = ri ? s128(a) : c128(a);
      t.e2[0][l][e] = cbf(v);
      t.e2[1][l][e] = cbf(v - cbf2d(cbf(v)));
      t.f2[l][e] = cbf(ri ? -v : v);
    }
  for (int m = -15; m < 48; ++m)
    for (int f = 0; f < 64; ++f) {
      const int a = (2 * (f >> 1) + 1) * m;
      t.g1[m + 16][(f >> 3) ^ (((m + 16) >> 1) & 7)][f & 7] = cbf(((f & 1) ? -s128(a) : c128(a)) / 32.0);
    }
  for (int m = 0; m < 16; ++m)
    for (int f = 0; f < 32; ++f) {
      const int a = 2 * (2 * (f >> 1) + 1) * m;
      t.g2[m][f] = cbf(((f & 1) ? -s128(a) : c128(a)) / 16.0);
    }
  return t;
}
__device__ __attribute__((aligned(64))) const Tabs tabs = make_tabs();

constexpr int OOB = 0x7FFFFF00;
constexpr float C2 = 2.885390081777927f;
#define EMAXV 120.f
constexpr float EMAX = EMAXV;
constexpr int WT = 16;
constexpr int NT = 2;
constexpr int R1 = 336;
constexpr int R2 = 502;
constexpr int PL1 = R1 * 128;
constexpr int PL2 = R2 * 128;
#define PU1 0
#define PU2 0
#define PY1 0
#define PY2 0
#define PW 0
#define PC 0
constexpr int UB1 = 16 * PL1 + PU1, UB2 = 16 * PL2 + PU2;
constexpr int U1BYTES = 32 * UB1, U2BYTES = 16 * UB2;
constexpr int WCB = 32768 + PC;
constexpr int WBIN = 16 * WCB + PW;
constexpr int YP1 = R1 * 64;
constexpr int YP2 = 512 * 64;
constexpr int YB1 = 16 * YP1 + PY1, YB2 = 16 * YP2 + PY2;
constexpr int Y1BYTES = 32 * YB1, Y2BYTES = 16 * YB2;
#define FMODE 1
#define FSTR 4
constexpr int FGRP = FMODE ? (16 * FSTR < 256 ? 256 : 16 * FSTR) : 256;
constexpr int NFLAG = 32 * FGRP / 4;
__device__ __forceinline__ int dmap(int cb, int n) { return 64 * (cb >> 2) + 32 * (cb & 1) + 2 * n + ((cb >> 1) & 1); }

__device__ __forceinline__ void swap16(u32x4& a, u32x4& b) {
#pragma unroll
  for (int i = 0; i < 4; ++i) {
    const auto r = __builtin_amdgcn_permlane16_swap(a[i], b[i], false, false);
    a[i] = r[0];
    b[i] = r[1];
  }
}

__device__ __forceinline__ void st_pad(const u32x4& a, const u32x4& b) { asm volatile("s_nop 1" :: "v"(a), "v"(b) : "memory"); }

typedef __attribute__((address_space(3))) u32x4 lds_u32x4;
__device__ __forceinline__ u32x4 lds_ld(int a) { return *(const lds_u32x4*)(uintptr_t)(uint32_t)a; }
__device__ __forceinline__ uint32_t lds_ld32(int a) { return *(const __attribute__((address_space(3))) uint32_t*)(uintptr_t)(uint32_t)a; }
template <int SZ>
__device__ __forceinline__ void dma(__amdgpu_buffer_rsrc_t rsrc, char* lds, int voff, int soff) {
#if defined(__HIP_DEVICE_COMPILE__)
  typedef __attribute__((address_space(3))) void* ldsp;
  if constexpr (SZ == 16) __builtin_amdgcn_raw_ptr_buffer_load_lds(rsrc, (ldsp)lds, 16, voff, soff, 0, 0);
  else __builtin_amdgcn_raw_ptr_buffer_load_lds(rsrc, (ldsp)lds, 4, voff, soff, 0, 0);
#endif
}

#define LABEL_ADDR(p, name) asm volatile("s_getpc_b64 s[40:41]\n\ts_add_u32 s40, s40, " name "@rel32@lo+4\n\ts_addc_u32 s41, s41, " name "@rel32@hi+12\n\ts_mov_b64 %0, s[40:41]" : "=s"(p) :: "s40", "s41", "scc")
__device__ __forceinline__ void code_pf(const char* p, int n, int part, char* lds) {
  dma<4>(mkbuf(p, n), lds, part * 8192 + (threadIdx.x & 63) * 128, 0);
}

__device__ __forceinline__ void pad_macros() {
  asm volatile(R"(
.macro PAD1 l
.ifndef .Lw_\l
.Lw_\l:
.endif
\l:
s_cbranch_execz \l\()f
.endm
.macro PADM k, b, m
.ifndef .Lpg_\k
.set .Lpg_\k, -1
.endif
.ifndef .Lcn_\b
.set .Lcn_\b, 0
.endif
.if (((. - \k) & 4095) < 60) && (((. - \k) >> 12) > .Lpg_\k)
.set .Lpg_\k, (. - \k) >> 12
.irp c, 0, 1, 2, 3, 4, 5, 6, 7
.if (.Lcn_\b & \m) == \c
PAD1 \b\c
.endif
.endr
.set .Lcn_\b, .Lcn_\b + 1
.endif
.endm
.macro WALK b, w
s_mov_b64 exec, 0
.irp c, 0, 1, 2, 3, 4, 5, 6, 7
.ifdef .Lw_\b\c
s_cmp_eq_u32 \w, \c
s_cbranch_scc1 .Lw_\b\c
.endif
.endr
.irp c, 0, 1, 2, 3, 4, 5, 6, 7
\b\c:
.endr
s_mov_b64 exec, -1
.endm
)");
}
#define PAD(k, b, m) asm volatile("PADM " #k ", " #b ", " #m)
#define WALK(b, w) asm volatile("WALK " #b ", %0" :: "s"(w) : "scc")
#define KERNEL extern "C" __global__ __attribute__((aligned(4096)))

constexpr int J_T = 0;
constexpr int J_F = 131072;
constexpr int J_G = J_F + 8192;
constexpr int J_R = J_G + 16384;
constexpr int JOINT_LDS = J_R + 1024;
__device__ __forceinline__ void lds_st(int a, const u32x4& v) { *(lds_u32x4*)(uintptr_t)(uint32_t)a = v; }
#define BAR() do { SB(); asm volatile("s_waitcnt lgkmcnt(0)\n\ts_barrier" ::: "memory"); SB(); } while (0)
template <int M, bool Z>
__device__ __forceinline__ void mm(f32x16& c, const u32x4& a, const u32x4& b) {
  if constexpr (Z) asm volatile("v_mfma_f32_32x32x16_bf16 %0, %1, %2, 0" : "=&v"(c) : "v"(a), "a"(b));
  else asm volatile("v_mfma_f32_32x32x16_bf16 %0, %1, %2, %0" : "+v"(c) : "v"(a), "a"(b));
}
template <int M, bool Z>
__device__ __forceinline__ void mm(f32x4& c, const u32x4& a, const u32x4& b) {
  if constexpr (Z) {
    if constexpr (M == 0) asm volatile("v_mfma_f32_16x16x32_bf16 %0, %1, %2, 0" : "=&v"(c) : "v"(a), "v"(b));
    else asm volatile("v_mfma_f32_16x16x32_bf16 %0, %1, %2, 0" : "=&v"(c) : "a"(a), "v"(b));
  } else {
    if constexpr (M == 0) asm volatile("v_mfma_f32_16x16x32_bf16 %0, %1, %2, %0" : "+v"(c) : "v"(a), "v"(b));
    else asm volatile("v_mfma_f32_16x16x32_bf16 %0, %1, %2, %0" : "+v"(c) : "a"(a), "v"(b));
  }
}
#define MWAIT(c0, c1) asm volatile("s_nop 7\n\ts_nop 3" : "+v"(c0), "+v"(c1))
template <int K>
__device__ __forceinline__ float add_quad(float a, float b) {
  float r;
  asm volatile("v_add_f32_dpp %0, %1, %2 quad_perm:[%3,%3,%3,%3] row_mask:0xf bank_mask:0xf" : "=v"(r) : "v"(a), "v"(b), "n"(K));
  return r;
}
__device__ __forceinline__ float next_lane(float v) {
  return __int_as_float(__builtin_amdgcn_update_dpp(0, __float_as_int(v), 0x101, 0xf, 0xf, true));
}
__device__ __forceinline__ void keep_a(const u32x4& v) { asm volatile("" :: "a"(v)); }
__device__ __forceinline__ void keep_v(const u32x4& v) { asm volatile("" :: "v"(v)); }
__device__ __forceinline__ void await(uint32_t v, __amdgpu_buffer_rsrc_t r, int vo, int so) {
  uint32_t cnt;
  asm volatile("v_cmp_gt_u32 vcc, %5, %0\n\ts_cbranch_vccz 2f\n\ts_mov_b32 %1, 0x40000\n"
               "1:\n\ts_sleep 1\n\tbuffer_load_dword %0, %2, %3, %4 offen sc1\n\ts_waitcnt vmcnt(0)\n\t"
               "v_cmp_gt_u32 vcc, %5, %0\n\ts_cbranch_vccz 2f\n\ts_sub_u32 %1, %1, 1\n\ts_cbranch_scc0 1b\n2:"
               : "+v"(v), "=&s"(cnt) : "v"(vo), "s"(r), "s"(so), "n"(FMODE ? 1 : 16) : "vcc", "scc", "memory");
}

#define KRATE 9
#define KD0 93
#define KQW 12
#define KJN 32
#define XST
#define OPOL NT
#define KJR 4
#define KR1 12
#define KPI 108
#define KPC 120
#define KPI1 4
#define KPC1 34
enum { O_NONE, O_W, O_D, O_GI, O_Q, O_J, O_ST, O_SIG, O_POLL, O_Y };
struct Sched {
  int lt[165], li[165];
  int ty[276], ix[276];
  int before[277];
  int last[16];
  int nl;
};
constexpr Sched make_sched() {
  Sched c{};
  int n = 0;
  for (int s = 0; s < 16; ++s)
    for (int k = 0; k < 2; ++k) {
      if (s < 8) for (int i = 0; i < 2; ++i) { c.lt[n] = O_W; c.li[n++] = 4 * s + 2 * k + i; }
      for (int i = 0; i < 2; ++i) { c.lt[n] = O_D; c.li[n++] = 4 * s + 2 * k + i; }
    }
  c.lt[n++] = O_GI;
  for (int i = 0; i < 4; ++i) { c.lt[n] = O_Q; c.li[n++] = i; }
  for (int i = 0; i < 64; ++i) { c.lt[n] = O_J; c.li[n++] = i; }
  int l = 16, o = 16, cr = 16;
  c.last[0] = 8; c.last[1] = 16;
  for (int t = 0; t < 276; ++t) {
    const int g = t >> 1, e = (g & 63) - 1;
    c.before[t] = o;
    cr += g < KD0 ? KRATE : KR1;
    if (cr > 24) cr = 24;
    if (g >= 64 && !(t & 1) && e >= 0 && e < 8 && !(e & 1)) { c.ty[t] = O_ST; c.ix[t] = (g >> 7) * 4 + (e >> 1); }
    else if (t == 2 * KD0) c.ty[t] = O_SIG;
    else if (t == 2 * KPI) c.ty[t] = O_POLL;
    else if (g >= KPC && g < KPC + 8) { c.ty[t] = O_Y; c.ix[t] = t - 2 * KPC; }
    else if (g < 128 && l < 101 + KJN && cr >= 16 && (g < KD0 - KQW || g >= KD0) &&
             (c.lt[l] != O_D || c.li[l] < 32 || g >= (c.li[l] / 4 - 7) * 8 + 5) && (c.lt[l] != O_J || c.li[l] < 48 || g >= 68 + 4 * (c.li[l] - 48))) {
      c.ty[t] = c.lt[l]; c.ix[t] = c.li[l];
      if (c.lt[l] == O_D && (c.li[l] & 3) == 3) c.last[c.li[l] >> 2] = o + 1;
      ++l;
      cr -= 16;
    }
    if (c.ty[t]) ++o;
  }
  c.nl = l;
  return c;
}
constexpr Sched SC = make_sched();
static_assert(SC.nl >= 101 + 32 && SC.nl + 85 * KJR / 8 >= 165 && (KD0 & 7) == 5, "schedule");

#define PADJ() PAD(joint_kernel, 8, 7)
KERNEL __launch_bounds__(256, 1) void joint_kernel(const char* __restrict__ U, const char* __restrict__ W, char* Y,
                                                       const char* __restrict__ wff, const float* __restrict__ ge,
                                                       uint32_t* flags, uint16_t* __restrict__ out) {
  extern __shared__ __attribute__((aligned(16))) char smem[];
  pad_macros();
  PADJ();
  const int bid = blockIdx.x, xcd = bid & 7, slot = bid >> 3;
  const int bin = xcd * 2 + (slot >> 4), mt = (slot >> 2) & 3;
  const int tid = threadIdx.x, lane = tid & 63;
  const int w = __builtin_amdgcn_readfirstlane(tid >> 6);
  const int cb = (slot & 3) * 4 + w;
  const __amdgpu_buffer_rsrc_t ur = mkbuf(U + (size_t)bin * UB2 + mt * (63 * 128), 16 * PL2 - mt * (63 * 128));
  const __amdgpu_buffer_rsrc_t wr = mkbuf(W + (size_t)bin * WBIN + cb * WCB, 32768);
  int vd[2];
#pragma unroll
  for (int z = 0; z < 2; ++z) vd[z] = (32 * z + 4 * (lane >> 3) + w) * 128 + (((lane & 7) ^ (4 * z + (lane >> 4))) * 16);
  const int rho = lane & 15, kq = lane >> 4;
  const int n = lane & 31, h = lane >> 5;
  const int pos = 32 * xcd + slot;
  const int seg = 63 * (pos >> 6) + 4 * (pos & 15) + ((pos >> 4) & 3);
  const __amdgpu_buffer_rsrc_t yb = mkbuf(Y, Y2BYTES);
  const __amdgpu_buffer_rsrc_t wb = mkbuf(wff + w * 32768, 131072 + 32768);
  const __amdgpu_buffer_rsrc_t fr = mkbuf(flags, NFLAG * 4);
  const int vs = bin * YB2 + cb * YP2 + mt * 4096 + rho * 64 + kq * 16;
  const int vy = 4 * kq * YB2 + 4 * w * YP2 + pos * 64 + rho * 4;
  const int vf = tid == 0 ? (slot & 15) * FGRP + (FMODE ? bin * FSTR : 0) : OOB;
  const int vp = ((pos >> 6) * 4 + w) * FGRP + (FMODE ? rho * FSTR : 0);
  u32x4 Wm[32], wt[64], yv[2][4], gi, o[4];
  uint32_t pl;
  auto signal = [&](int b) {
    if constexpr (FMODE) __builtin_amdgcn_raw_buffer_store_b32(1u, fr, vf, b * 16 * FGRP, WT);
    else __builtin_amdgcn_raw_ptr_buffer_atomic_add_i32(1, fr, vf, b * 16 * FGRP, 0);
  };
  auto yload = [&](int b, int i) { yv[b][i >> 2][i & 3] = __builtin_amdgcn_raw_buffer_load_b32(yb, vy, (i & 3) * YB2 + (i >> 2) * YP2 + b * 16384, WT); };
  auto issue = [&](auto it, auto ii) {
    constexpr int t = decltype(it)::value, i = decltype(ii)::value;
    if constexpr (t == O_W) Wm[i] = __builtin_amdgcn_raw_buffer_load_b128(wr, lane * 16 + (i & 3) * 1024, (i >> 2) * 4096, 0);
    else if constexpr (t == O_D) dma<16>(ur, smem + ((i >> 1) & 15) * 8192 + w * 2048 + (i & 1) * 1024, vd[i & 1], ((i >> 1) & 15) * PL2 + (i >> 5) * (251 * 128));
    else if constexpr (t == O_GI) gi = *(const u32x4*)&tabs.g2[4 * (rho & 3) + (rho >> 2)][8 * kq];
    else if constexpr (t == O_Q) dma<16>(mkbuf(ge + w * 1024, 4096), smem + J_G + w * 4096 + i * 1024, lane * 16, i * 1024);
    else if constexpr (t == O_J) wt[i] = __builtin_amdgcn_raw_buffer_load_b128(wb, lane * 16 + (i & 3) * 1024, (i >> 5) * 131072 + ((i >> 2) & 7) * 4096, 0);
    else if constexpr (t == O_ST) __builtin_amdgcn_raw_buffer_store_b128(o[i & 3], yb, vs + (i & 3) * 1024, (i >> 2) * 16384, WT);
    else if constexpr (t == O_SIG) signal(0);
    else if constexpr (t == O_POLL) pl = __builtin_amdgcn_raw_buffer_load_b32(fr, vp, 0, WT);
    else if constexpr (t == O_Y) yload(0, i);
    SB();
  };
  sfor<0, 16>([&](auto il) { issue(IC<SC.lt[decltype(il)::value]>{}, IC<SC.li[decltype(il)::value]>{}); });
  const int l0 = (int)(uint32_t)(uintptr_t)(__attribute__((address_space(3))) char*)smem;
  int ab[2][2];
#pragma unroll
  for (int ri = 0; ri < 2; ++ri)
#pragma unroll
    for (int wn = 0; wn < 2; ++wn) {
      ab[wn][ri] = l0 + rho * 128 + ((((kq >> 1) * 4 + ri * 2 + (kq & 1)) ^ (rho >> 1)) * 16) + wn * 65536;
      asm volatile("" : "+v"(ab[wn][ri]));
    }
  auto aread = [&](int g, int ri) -> u32x4 { return lds_ld(ab[(g >> 5) & 1][ri] + (g & 31) * 2048); };
  const float sg = __int_as_float(0x3f800000 | (bin << 31));
  f32x4 yr[2][5], yi[2][5];
  int ra[4], fa[4], qa, vo;
  u32x4 A[4][2], nw[2];
  asm volatile("s_waitcnt vmcnt(8)\n\ts_barrier" ::: "memory");
#pragma unroll
  for (int z = 0; z < 3; ++z) { A[z][0] = aread(z, 0); A[z][1] = aread(z, 1); }
#pragma unroll
  for (int j = 0; j < 4; ++j) nw[0][j] = Wm[1][j] ^ 0x80008000u;
  SB();
  sfor<0, 136>([&](auto ig) {
    constexpr int g = decltype(ig)::value, b = (g >> 6) & 1, ks = (g >> 2) & 15, mb = g & 3;
    if constexpr ((g & 7) == 5 && g < 120 && (g <= KD0 || SC.last[g < 120 ? (g >> 3) + 1 : 0] > SC.before[2 * KD0])) {
      static_assert(SC.last[(g >> 3) + 1] && SC.before[2 * g] >= SC.last[(g >> 3) + 1], "next stage not requested yet");
      asm volatile("s_waitcnt vmcnt(%0)\n\ts_barrier" ::"n"(g == KD0 ? 0 : SC.before[2 * g] - SC.last[(g >> 3) + 1]) : "memory");
      SB();
    }
    if constexpr (g == KPC) { await(pl, fr, vp, 0); SB(); }
    auto mf = [&](auto ij) {
      constexpr int j = decltype(ij)::value, i = (((mb & 1) ? ORD1 : ORD0) >> (12 - 4 * j)) & 3;
      constexpr bool z = ks == 0 && !(j & 1);
      if constexpr (g < 128) {
        if constexpr (i == 0) mm<1, z>(yr[b][mb], Wm[2 * ks], A[g & 3][0]);
        else if constexpr (i == 1) mm<0, z>(yr[b][mb], nw[ks & 1], A[g & 3][1]);
        else if constexpr (i == 2) mm<0, z>(yi[b][mb], Wm[2 * ks + 1], A[g & 3][0]);
        else mm<1, z>(yi[b][mb], Wm[2 * ks], A[g & 3][1]);
      }
      PADJ();
    };
    auto oadd = [&](auto ij) {
      constexpr int j = decltype(ij)::value, e = ((g + 2) & 63) - 1, c = ((g + 2) >> 6) - 1;
      if constexpr (c >= 0 && e == -1) {
        yr[c][4][j] = next_lane(yr[c][0][j]);
        yi[c][4][j] = next_lane(yi[c][0][j]);
      } else if constexpr (c >= 0 && e < 8) {
        constexpr int m = e >> 1, i = 2 * (e & 1) + (j >> 1);
        if constexpr (j & 1) {
          o[m][i] = pk2bf(yr[c][m][i], yi[c][m][i]);
          if constexpr (m > 0 && i == 0) keep_v(o[m > 0 ? m - 1 : 0]);
        } else {
          yr[c][m][i] = __builtin_fmaf(sg, yi[c][m + 1][i], yr[c][m][i]);
          SB();
          yi[c][m][i] = __builtin_fmaf(-sg, yr[c][m + 1][i], yi[c][m][i]);
        }
      }
      SB();
    };
    mf(IC<0>{});
    issue(IC<SC.ty[2 * g]>{}, IC<SC.ix[2 * g]>{});
    oadd(IC<0>{});
    mf(IC<1>{});
    if constexpr (g + 3 < 128) A[(g + 3) & 3][0] = aread(g + 3, 0);
    SB();
    oadd(IC<1>{});
    mf(IC<2>{});
    issue(IC<SC.ty[2 * g + 1]>{}, IC<SC.ix[2 * g + 1]>{});
    oadd(IC<2>{});
    mf(IC<3>{});
    if constexpr (g >= TU0 && g < TU1) asm volatile("s_nop %0" ::"n"(THR2));
    if constexpr (g + 3 < 128) A[(g + 3) & 3][1] = aread(g + 3, 1);
    if constexpr (mb == 2 && g < 124) {
#pragma unroll
      for (int j = 0; j < 4; ++j) nw[(ks + 1) & 1][j] = Wm[(2 * ks + 3) & 31][j] ^ 0x80008000u;
    }
    SB();
    oadd(IC<3>{});
    if constexpr (g >= 84 && g < 88) { ra[g & 3] = l0 + ((w + 1 + g) & 3) * 4096 + lane * 16; asm volatile("" : "+v"(ra[g & 3])); }
    if constexpr (g >= 88 && g < 92) { fa[g & 3] = l0 + J_F + ((w + 1 + g) & 3) * 2048 + lane * 16; asm volatile("" : "+v"(fa[g & 3])); }
    if constexpr (g == 82) { qa = l0 + J_G + w * 4096 + (h * 8 + (n >> 4) * 4 + (n & 3)) * 16; asm volatile("" : "+v"(qa)); }
    if constexpr (g == 83) { vo = h * 4096 + w * 128 + n * 4 XST; asm volatile("" : "+v"(vo)); }
    SB();
  });
  st_pad(o[3], o[3]);
  float p[2][32], x[8], mx = 0.f;
  f32x4 q[2][8], fe[4];
  u32x4 tp[2][4];
  auto jstep = [&](auto it) {
    constexpr int t = decltype(it)::value, l = SC.nl + t * KJR / 8;
    if constexpr (l < 165 && l < SC.nl + (t + 1) * KJR / 8) issue(IC<O_J>{}, IC<SC.li[l < 165 ? l : 0]>{});
  };
  auto front = [&](auto ib, auto&& after) {
    constexpr int b = decltype(ib)::value & 1;
    f32x4 d[4];
    u32x4 fb[8];
#pragma unroll
    for (int c = 0; c < 4; ++c) {
      mm<0, true>(d[c], yv[b][c], gi);
      if constexpr (decltype(ib)::value < 2) PADJ();
    }
    asm volatile("s_nop 4" : "+v"(d[0]));
#pragma unroll
    for (int j = 0; j < 2; ++j) {
#pragma unroll
      for (int c = 0; c < 4; ++c) {
        fb[j][c] = gelu2(d[2 * j + (c >> 1)][2 * (c & 1)], d[2 * j + (c >> 1)][2 * (c & 1) + 1]);
        if constexpr (decltype(ib)::value < 2) PADJ();
      }
      lds_st(fa[3] + j * 1024, fb[j]);
      SB();
    }
    sfor<0, 32>([&](auto ii) {
      constexpr int i = decltype(ii)::value, eb = i < 8 ? i >> 1 : (i - 8) / 6, r = i < 8 ? i & 1 : 2 + (i - 8) % 6;
      mm<1, r == 0>(fe[eb], wt[i], fb[r]);
      if constexpr (decltype(ib)::value < 2) PADJ();
      SB();
      if constexpr (i == 0) {
        if constexpr (decltype(ib)::value == 0) asm volatile("s_waitcnt vmcnt(0)" ::: "memory");
        BAR();
      }
      if constexpr (i < 3) {
        fb[2 * i + 2] = lds_ld(fa[i]);
        fb[2 * i + 3] = lds_ld(fa[i] + 1024);
      }
      SB();
      after(ii);
    });
  };
  auto twrite = [&](auto iG) {
    constexpr int V = decltype(iG)::value + 31;
    if constexpr (V >= 0 && V < 256 && (V & 7) == 7) lds_st(ra[3] + J_T + ((V >> 5) & 1) * 16384 + ((V >> 3) & 3) * 1024, tp[(V >> 5) & 1][(V >> 3) & 3]);
    SB();
  };
  auto tslot = [&](auto iG) {
    constexpr int G = decltype(iG)::value;
    constexpr int A = G + 36, B = G + 35, C = G + 34, D = G + 33;
    if constexpr (C >= 0 && C < 256) asm volatile("v_fma_f32 %0, -%1, %0, 0.5" : "+v"(x[C & 7]) : "v"(q[C >> 7][(C >> 2) & 7][C & 3]));
    if constexpr (B >= 0 && B < 256) asm volatile("v_rcp_f32 %0, %0" : "+v"(x[B & 7]));
    PADJ();
    if constexpr (A >= 0 && A < 256) x[A & 7] = add_quad<(A >> 5) & 3>(p[A >> 7][A & 31], q[A >> 7][(A >> 2) & 7][A & 3]);
    SB();
    if constexpr (D >= 0 && D < 256 && (D & 1)) tp[(D >> 5) & 1][(D >> 3) & 3][(D >> 1) & 3] = pk2bf(x[(D - 1) & 7], x[D & 7]);
    SB();
  };
  auto stepE = [&](int b, int eb, int i) {
    const float a = fe[eb][i] * C2;
    mx = __builtin_fmaxf(mx, __builtin_fabsf(a));
    const uint32_t e = __builtin_bit_cast(uint32_t, __builtin_amdgcn_exp2f(a));
    const auto r = __builtin_amdgcn_permlane16_swap(e, e, false, false);
    p[b][8 * eb + i] = __builtin_bit_cast(float, (uint32_t)r[0]);
    p[b][8 * eb + 4 + i] = __builtin_bit_cast(float, (uint32_t)r[1]);
  };
  auto head = [&](auto ib) {
    constexpr int b = decltype(ib)::value;
    front(ib, [&](auto ii) {
      constexpr int i = decltype(ii)::value;
      if constexpr (b == 0 && i == 0) {
        signal(1);
        asm volatile("s_cmpk_lt_i32 %0, 250\n\ts_cbranch_scc1 1f\n\ts_endpgm\n1:" :: "s"(seg + (((pos & 63) + 1) & 64) * 4) : "scc");
      }
      if constexpr (i >= 3 && i < 11) q[b][i - 3] = __builtin_bit_cast(f32x4, lds_ld(qa + b * 2048 + (i - 3) * 256));
      if constexpr (b == 0 && i >= 3) jstep(IC<i - 3>{});
      if constexpr (i >= 16 && (i - 16) % 6 < 4) {
        if constexpr ((i - 16) % 6 == 0) asm volatile("s_nop 1" : "+v"(fe[(i - 16) / 6]));
        stepE(b, (i - 16) / 6, (i - 16) % 6);
      }
      SB();
    });
    if constexpr (b == 0) sfor<-36, -32>([&](auto ir) { tslot(ir); });
    else asm volatile("s_nop 7" ::);
    asm volatile("" : "+v"(fe[3]));
#pragma unroll
    for (int i = 0; i < 4; ++i) { stepE(b, 3, i); PADJ(); }
    mx = __builtin_fmaxf(mx, q[b][0][0] < 0.f || q[b][4][0] < 0.f ? 2 * EMAX : 0.f);
    if constexpr (b) *(__attribute__((address_space(3))) float*)(uintptr_t)(uint32_t)(l0 + J_R + tid * 4) = mx;
    asm volatile("s_nop 1");
    SB();
  };
  head(IC<0>{});
  sfor<-32, 0>([&](auto ir) { jstep(IC<decltype(ir)::value + 61>{}); twrite(ir); tslot(ir); });
  const __amdgpu_buffer_rsrc_t ob = mkbuf(out + (size_t)seg * 16 * 8 * DCH, 4000 * 4096 + 65536);
  f32x16 acc[2][2];
  u32x4 a[16];
  uint32_t ov[16];
  auto store = [&](int st, int i) {
    __builtin_amdgcn_raw_buffer_store_b32(ov[i], ob, vo + ((i >> 3) * 4 + (i & 3)) * 512,
                                          (st >> 2) * (4000 * 4096) + (st & 3) * 16384 + ((i >> 2) & 1) * 8192, OPOL);
  };
  sfor<0, 256>([&](auto iG) {
    constexpr int G = decltype(iG)::value, st = G >> 5, g = G & 31, r = g >> 1, z = g & 1;
    if constexpr (G == 92) { head(IC<1>{}); }
    mm<2, r == 0>(acc[st & 1][z], r < 4 ? tp[st & 1][r & 3] : a[r], wt[32 + g]);
    PADJ();
    SB();
    if constexpr (g == 2) BAR();
    if constexpr (st == 0 && g < 31) keep_a(wt[33 + g]);
    if constexpr (z && r >= 3 && r < 15) keep_v(a[r + 1]);
    constexpr int rd = g >= 3 && g < 6 ? g + 1 : (g >= 7 && g <= 23 && z) ? (g + 7) / 2 : 0;
    constexpr int sn = g == 1 ? 15 : g == 2 ? 0 : g == 6 ? 1 : g >= 25 ? g - 17 : (g > 8 && !z && (g & 7)) ? (g - 6) / 2 - (g > 16) : -1;
    if constexpr (rd) a[rd] = lds_ld(ra[(rd >> 2) - 1] + J_T + (st & 1) * 16384 + (rd & 3) * 1024);
    if constexpr (sn >= 0 && st > (g == 1)) store(st - 1 - (g == 1), sn);
    if constexpr (G < 24) jstep(IC<G + 61>{});
    if constexpr (G == KPI1) pl = __builtin_amdgcn_raw_buffer_load_b32(fr, vp, 16 * FGRP, WT);
    if constexpr (G == KPC1) { SB(); await(pl, fr, vp, 16 * FGRP); SB(); }
    if constexpr (G >= KPC1 && G < KPC1 + 16) yload(1, G - KPC1);
    twrite(iG);
    tslot(iG);
    if constexpr (G == 225) asm volatile("s_nop 5" : "+v"(acc[0][0]));
    if constexpr (st > 0 && z) ov[r] = pk2bf(acc[(st - 1) & 1][0][r], acc[(st - 1) & 1][1][r]);
    SB();
  });
  store(6, 15);
  const f32x4 rg = __builtin_bit_cast(f32x4, lds_ld(l0 + J_R + lane * 16));
  MWAIT(acc[1][0], acc[1][1]);
#pragma unroll
  for (int i = 0; i < 16; ++i) { ov[i] = pk2bf(acc[1][0][i], acc[1][1][i]); store(7, i); PADJ(); }
  if (__builtin_expect(__builtin_amdgcn_ballot_w64(rg[0] > EMAX || rg[1] > EMAX || rg[2] > EMAX || rg[3] > EMAX) != 0, 0)) {
    asm volatile("s_waitcnt vmcnt(0)" ::: "memory");
#pragma unroll 1
    for (int b = 0; b < 2; ++b) {
#pragma unroll
      for (int i = 0; i < 16; ++i) yv[0][i >> 2][i & 3] = __builtin_amdgcn_raw_buffer_load_b32(yb, vy + b * 16384, (i & 3) * YB2 + (i >> 2) * YP2, WT);
      front(IC<2>{}, [](auto) {});
      asm volatile("s_nop 7" : "+v"(fe[3]));
#pragma unroll 1
      for (int st = 0; st < 4; ++st) {
        const float* gp = ge + 4096 + (w * 2 + b) * 512 + (h * 8 + (n >> 4) * 4 + (n & 3)) * 4;
#pragma unroll
        for (int f = 0; f < 4; ++f) {
          u32x4 t;
#pragma unroll
          for (int c = 0; c < 2; ++c) {
            const f32x4 gv = *(const f32x4*)(gp + (2 * f + c) * 64);
            float y[4];
#pragma unroll
            for (int i = 0; i < 4; ++i)
              y[i] = 0.5f - __builtin_amdgcn_rcpf(__builtin_amdgcn_exp2f(__shfl(fe[f][i], h * 32 + c * 16 + (lane & 12) + st) * C2 + gv[i]) + 1.f);
            t[2 * c] = pk2bf(y[0], y[1]);
            t[2 * c + 1] = pk2bf(y[2], y[3]);
          }
          lds_st(ra[3] + J_T + (st & 1) * 16384 + f * 1024, t);
        }
        BAR();
        sfor<0, 16>([&](auto ir) {
          constexpr int r = decltype(ir)::value;
          const u32x4 v = lds_ld(ra[(r / 4 + 3) & 3] + J_T + (st & 1) * 16384 + (r & 3) * 1024);
#pragma unroll
          for (int z = 0; z < 2; ++z) mm<2, r == 0>(acc[0][z], v, wt[32 + 2 * r + z]);
        });
        MWAIT(acc[0][0], acc[0][1]);
#pragma unroll
        for (int i = 0; i < 16; ++i)
          __builtin_amdgcn_raw_buffer_store_b32(pk2bf(acc[0][0][i], acc[0][1][i]), ob, vo + st * 16384 + ((i >> 3) * 4 + (i & 3)) * 512,
                                                b * (4000 * 4096) + ((i >> 2) & 1) * 8192, NT);
      }
    }
  }
}

#define PADM() PAD(mid_kernel, 7, 3)
template <int Z>
__device__ __forceinline__ void mid_unit(char* __restrict__ U, int ya, int ga, int cb, int s, const u32x4& F) {
  const int lane = threadIdx.x & 63;
  const int n32 = lane & 31, h = lane >> 5;
  constexpr int NSG = Z == 0 ? 1 : 2;
  const int tau = 16 * ((n32 >> 2) & 1) + 8 * ((n32 >> 3) & 1) + 2 * (n32 & 3) + (n32 >> 4);
  const int r[2] = {Z == 0 ? tau + 16 : Z == 1 ? (tau < 16 ? tau + 48 : 0) : tau + 32, Z == 1 ? tau : (tau >= 16 ? tau - 16 : 0)};
  f32x16 d = zero16();
#pragma unroll
  for (int sg = 0; sg < NSG; ++sg)
#pragma unroll
    for (int ks = 0; ks < 4; ++ks) {
      u32x4 yv;
#pragma unroll
      for (int j = 0; j < 4; ++j) yv[j] = lds_ld32(ya + sg * 4096 + ks * 1024 + j * 128);
      d = mfma32(lds_ld(ga + r[sg] * 128 + (((2 * ks + h) ^ ((r[sg] >> 1) & 7)) * 16)), yv, d);
      PADM();
    }
  const __amdgpu_buffer_rsrc_t ub = mkbuf(U, U2BYTES);
  const int vu = ((lane & 15) >> 1) * UB2 + cb * PL2 + s * 128 + ((lane >> 4) & 1) * 64 + (lane & 1) * 32 + h * 16;
  const int t0r = 32 * s - 16 + 16 * h;
  const bool ok = t0r >= 0 && t0r < 8000;
  f32x16 u[2];
#pragma unroll
  for (int q = 0; q < 2; ++q) {
    u32x4 a;
#pragma unroll
    for (int j = 0; j < 4; ++j) {
      const uint32_t gv = gelu2(d[8 * q + 2 * j], d[8 * q + 2 * j + 1]);
      a[j] = ok ? gv : 0u;
      PADM();
    }
    u[q] = mfma32(a, F, zero16());
  }
#pragma unroll
  for (int b = 0; b < 2; ++b) {
    u32x4 o[2];
#pragma unroll
    for (int q = 0; q < 2; ++q)
#pragma unroll
      for (int j = 0; j < 4; ++j) o[q][j] = pk2bf(u[q][8 * b + 2 * j], u[q][8 * b + 2 * j + 1]);
    PADM();
    swap16(o[0], o[1]);
    PADM();
#pragma unroll
    for (int z = 0; z < 2; ++z) __builtin_amdgcn_raw_buffer_store_b128(o[z], ub, vu, z * 8 * UB2 + b * (251 * 128), WT);
    st_pad(o[0], o[1]);
  }
}
KERNEL __launch_bounds__(1024) void mid_kernel(const char* __restrict__ Y, char* __restrict__ U) {
  __shared__ __attribute__((aligned(16))) char sm[8192 + 49152];
  __shared__ uint32_t landed;
  PADM();
  const int blk = blockIdx.x, lane = threadIdx.x & 63;
  const int wave = __builtin_amdgcn_readfirstlane(threadIdx.x >> 6);
  const int cb = blk & 15, s0 = blk & ~15, s = s0 + wave;
  const int sg0 = (2 * s0) / 3;
  if (wave < 4) {
    const __amdgpu_buffer_rsrc_t tb = mkbuf(&tabs.g1[0][0][0], 8192);
#pragma unroll
    for (int i = 0; i < 2; ++i) dma<16>(tb, sm + wave * 2048 + i * 1024, lane * 16, wave * 2048 + i * 1024);
  }
  {
    const __amdgpu_buffer_rsrc_t yb = mkbuf(Y, Y1BYTES);
    const int vy = (lane >> 3) * YB1 + cb * YP1 + sg0 * 128 + (lane & 7) * 16;
#pragma unroll
    for (int i = 0; i < 3; ++i) {
      const int pc = 3 * wave + i;
      if (sg0 + (pc >> 2) < 168) dma<16>(yb, sm + 8192 + pc * 1024, vy, (pc & 3) * 8 * YB1 + (pc >> 2) * 128);
    }
  }
  const u32x4 F = ((const u32x4*)&tabs.f2[0][0])[lane];
  if (threadIdx.x == 0) landed = 0;
  asm volatile("s_waitcnt lgkmcnt(0)\n\ts_barrier" ::: "memory");
  WALK(8, wave);
  {
    asm volatile("s_waitcnt vmcnt(0)" ::: "memory");
    if (lane == 0) __hip_atomic_fetch_or(&landed, 1u << wave, __ATOMIC_RELAXED, __HIP_MEMORY_SCOPE_WORKGROUP);
    if (s < 251) {
      const int p = s / 3, z = s - 3 * p;
      const int l0 = 2 * p + (z == 2) - sg0, l1 = l0 + (z != 0);
      const uint32_t need = 15u | (((2u << ((4 * l1 + 3) / 3)) - 1u) & ~((1u << ((4 * l0) / 3)) - 1u));
      while ((__builtin_amdgcn_readfirstlane(__hip_atomic_load(&landed, __ATOMIC_RELAXED, __HIP_MEMORY_SCOPE_WORKGROUP)) & need) != need)
        __builtin_amdgcn_s_sleep(1);
      asm volatile("" ::: "memory");
      const int l = (int)(uint32_t)(uintptr_t)(__attribute__((address_space(3))) char*)sm;
      const int ya = l + 8192 + l0 * 4096 + (lane >> 5) * 512 + (lane & 31) * 4;
      if (z == 0) mid_unit<0>(U, ya, l, cb, s, F);
      else if (z == 1) mid_unit<1>(U, ya, l, cb, s, F);
      else mid_unit<2>(U, ya, l, cb, s, F);
    }
  }
}

constexpr int issued(int g, int MB, int OPS, int NST, int BAR, int PRO) {
  int o = PRO * OPS;
  for (int t = 0; t < g; ++t)
    if (o < 16 * OPS && (o / OPS < NST || t >= (o / OPS - NST + 1) * MB + BAR) && (MB != 11 || 2 * o * MB <= (2 * t + GLA * MB) * OPS)) ++o;
  return o;
}
#define PADG() PAD(gemm_kernel, 7, 3)
KERNEL __launch_bounds__(256, 1) void gemm_kernel(const char* __restrict__ U, const char* __restrict__ W,
                                                      char* __restrict__ Y, const char* __restrict__ pf) {
  constexpr int CONV = 1;
  constexpr int MB = CONV == 1 ? 11 : 8;
  constexpr int STG = MB * 2048;
  constexpr int NST = CONV == 1 ? 7 : 8;
  constexpr int PL = CONV == 1 ? PL1 : PL2;
  constexpr int NDMA = CONV == 1 ? 6 : 4;
  constexpr int OPS = NDMA + 2;
  extern __shared__ __attribute__((aligned(16))) char smem[];
  PADG();
  const int bid = blockIdx.x, xcd = bid & 7, slot = bid >> 3;
  const int bin = CONV == 1 ? xcd * 4 + (slot >> 3) : xcd * 2 + (slot >> 4);
  const int mt = CONV == 1 ? (slot >> 2) & 1 : (slot >> 2) & 3;
  const int tid = threadIdx.x, lane = tid & 63;
  const int wave = __builtin_amdgcn_readfirstlane(tid >> 6);
  const int cb = (slot & 3) * 4 + wave;
  const int row0 = CONV == 1 ? mt * 168 : mt * 126;
  const __amdgpu_buffer_rsrc_t ur = mkbuf(U + (size_t)bin * (CONV == 1 ? UB1 : UB2) + row0 * 128, 16 * PL - row0 * 128);
  const __amdgpu_buffer_rsrc_t wr = mkbuf(W + (size_t)bin * WBIN + cb * WCB, 32768);
  int vo[2];
  if constexpr (CONV == 1) {
    vo[0] = wave * 1024 + (lane >> 3) * 128 + (((lane & 7) ^ ((4 * wave + (lane >> 4)) & 7)) * 16);
    vo[1] = 20480 + wave * 256 + (lane >> 5) * 128 + ((((lane >> 2) & 7) ^ wave) * 16) + (lane & 3) * 4;
  } else {
#pragma unroll
    for (int z = 0; z < 2; ++z) vo[z] = wave * 4096 + (lane >> 3) * 128 + (((lane & 7) ^ (2 * wave + z)) * 16);
  }
  u32x4 B[NST][2];
  auto issue = [&](auto is, auto ii) {
    constexpr int s = decltype(is)::value, i = decltype(ii)::value;
    char* buf = smem + (s % NST) * STG;
    if constexpr (i < 2) {
      B[s % NST][i] = __builtin_amdgcn_raw_buffer_load_b128(wr, lane * 16 + i * 1024, s * 2048, 0);
    } else if constexpr (CONV == 1) {
      if constexpr (i < 7) dma<16>(ur, buf + wave * 1024 + (i - 2) * 4096, vo[0], s * PL + (i - 2) * 4096);
      else dma<4>(ur, buf + 20480 + wave * 256, vo[1], s * PL);
    } else {
      dma<16>(ur, buf + wave * 4096 + (i - 2) * 1024, vo[(i - 2) >> 1], s * PL + (i - 2) * 1024);
    }
  };
  constexpr int AR = 4, AL = 3;
  constexpr int BAR = MB - AL;
  constexpr int PRO = GPRO;
  sfor<0, PRO>([&](auto is) { sfor<0, OPS>([&](auto ii) { issue(is, ii); SB(); }); });
  const int rho = lane & 15, kq = lane >> 4;
  const int lds0 = (int)(uint32_t)(uintptr_t)(__attribute__((address_space(3))) char*)smem;
  constexpr int WIN = CONV == 1 ? 2 : 4;
  constexpr int NWIN = (NST + WIN - 1) / WIN;
  int ab[NWIN][2];
#pragma unroll
  for (int ri = 0; ri < 2; ++ri) {
    const int pos = ((kq >> 1) * 4 + ri * 2 + (kq & 1)) ^ (rho >> 1);
    const int a = lds0 + (CONV == 1 ? rho * 128 : (rho >> 1) * 2048 + (rho & 1) * 128) + pos * 16;
#pragma unroll
    for (int wn = 0; wn < NWIN; ++wn) { ab[wn][ri] = a + wn * WIN * STG; asm volatile("" : "+v"(ab[wn][ri])); }
  }
  auto aread = [&](int s, int mb, int ri) -> u32x4 {
    const int bf = s % NST;
    return lds_ld(ab[bf / WIN][ri] + (bf % WIN) * STG + mb * (CONV == 1 ? 2048 : 256));
  };
  f32x4 yr[MB], yi[MB];
#pragma unroll
  for (int mb = 0; mb < MB; ++mb) { yr[mb] = f32x4{0.f, 0.f, 0.f, 0.f}; yi[mb] = f32x4{0.f, 0.f, 0.f, 0.f}; }
  u32x4 A[AR][2];
  asm volatile("s_waitcnt vmcnt(%0)" ::"n"((PRO - 1) * OPS) : "memory");
  __builtin_amdgcn_s_barrier();
  asm volatile("" ::: "memory");
#pragma unroll
  for (int z = 0; z < AL; ++z) { A[z][0] = aread(0, z, 0); A[z][1] = aread(0, z, 1); }
  SB();
  u32x4 nwi;
#pragma unroll
  for (int j = 0; j < 4; ++j) nwi[j] = B[0][1][j] ^ 0x80008000u;
  sfor<0, 16>([&](auto is) {
    constexpr int s = decltype(is)::value;
    u32x4 nwn;
    sfor<0, MB>([&](auto im) {
      constexpr int mb = decltype(im)::value;
      constexpr int g = s * MB + mb, gn = g + AL;
      constexpr int o0 = issued(g, MB, OPS, NST, BAR, PRO), o1 = issued(g + 1, MB, OPS, NST, BAR, PRO);
      if constexpr (mb == BAR && s + 1 < 16) {
        static_assert(o0 >= (s + 2) * OPS, "next stage not requested yet");
        asm volatile("s_waitcnt vmcnt(%0)" ::"n"(o0 - (s + 2) * OPS) : "memory");
        __builtin_amdgcn_s_barrier();
        asm volatile("" ::: "memory");
        SB();
      }
      auto mf = [&](int i) {
        if (i < 2) yr[mb] = mfma16(i ? nwi : B[s % NST][0], A[g % AR][i], yr[mb]);
        else yi[mb] = mfma16(B[s % NST][3 - i], A[g % AR][i - 2], yi[mb]);
        PADG();
      };
      constexpr int ord = (mb & 1) ? ORD1 : ORD0;
      mf((ord >> 12) & 3);
      SB();
      mf((ord >> 8) & 3);
      if constexpr (gn < 16 * MB) A[gn % AR][0] = aread(gn / MB, gn % MB, 0);
      SB();
      mf((ord >> 4) & 3);
      if constexpr (o1 > o0) issue(IC<o0 / OPS>{}, IC<o0 % OPS>{});
      SB();
      mf(ord & 3);
      if constexpr (CONV == 1 ? (g >= TS0 && g < TS1) : (g >= TU0 && g < TU1)) asm volatile("s_nop %0" ::"n"(CONV == 1 ? THR : THR2));
      if constexpr (gn < 16 * MB) A[gn % AR][1] = aread(gn / MB, gn % MB, 1);
      if constexpr (mb == MB - 2 && s + 1 < 16) {
#pragma unroll
        for (int j = 0; j < 4; ++j) nwn[j] = B[(s + 1) % NST][1][j] ^ 0x80008000u;
      }
      SB();
    });
    if constexpr (s + 1 < 16) nwi = nwn;
  });
  if constexpr (CONV == 1) {
    const __amdgpu_buffer_rsrc_t yb = mkbuf(Y + (size_t)bin * YB1 + cb * YP1 + row0 * 64, 168 * 64);
    u32x4 o[MB];
#pragma unroll
    for (int mb = 0; mb < MB; ++mb) {
#pragma unroll
      for (int i = 0; i < 4; ++i) o[mb][i] = pk2bf(yr[mb][i], yi[mb][i]);
      if (mb) asm volatile("" :: "v"(o[mb - 1]), "v"(o[mb]) : "memory");
      __builtin_amdgcn_raw_buffer_store_b128(o[mb], yb, rho * 64 + kq * 16 + (mb & 3) * 1024, (mb >> 2) * 4096, WT);
    }
  } else {
    const __amdgpu_buffer_rsrc_t yb = mkbuf(Y + (size_t)bin * YB2 + cb * YP2 + row0 * 64, YP2 - row0 * 64);
    const float sg = (bin & 1) ? -1.f : 1.f;
    f32x4 nr, ni;
#pragma unroll
    for (int i = 0; i < 4; ++i) { nr[i] = __shfl_down(yr[0][i], 2, 16); ni[i] = __shfl_down(yi[0][i], 2, 16); }
    const int vy = (rho >> 1) * 1024 + (rho & 1) * 64 + kq * 16;
#pragma unroll
    for (int mb = 0; mb < MB; ++mb) {
      const f32x4 xr = mb < MB - 1 ? yr[mb < MB - 1 ? mb + 1 : 0] : nr, xi = mb < MB - 1 ? yi[mb < MB - 1 ? mb + 1 : 0] : ni;
      u32x4 o;
#pragma unroll
      for (int i = 0; i < 4; ++i) o[i] = pk2bf(__builtin_fmaf(sg, xi[i], yr[mb][i]), __builtin_fmaf(-sg, xr[i], yi[mb][i]));
      __builtin_amdgcn_raw_buffer_store_b128(o, yb, (mb == MB - 1 && rho >= 14) ? OOB : vy + mb * 128, 0, WT);
    }
  }
}

template <int CONV>
__device__ __forceinline__ void wtrans(const uint16_t* __restrict__ w, char* __restrict__ wh, int wid, int lane) {
  const int dq = wid & 3, pc = wid >> 2;
  const int ks = pc >> 2, kq = pc & 3;
  const int n32 = lane & 31, h = lane >> 5;
  const int r = CONV == 1 ? ks >> 3 : kq >> 1;
  const int c0 = CONV == 1 ? (ks & 7) * 32 + kq * 8 : dmap(ks, 4 * (kq & 1));
  const __amdgpu_buffer_rsrc_t wr = mkbuf(w, 31 * 131072);
  const int vb = (16 * h + r + 1) * 131072 + c0 * 512 + dq * 128 + n32 * 4;
  const int vb0 = (h + r == 0) ? OOB : vb - 262144;
  constexpr int NFB = CONV == 1 ? 2 : 1;
  const u32x4* et = CONV == 1 ? (const u32x4*)&tabs.e1[0][0][0][0] : (const u32x4*)&tabs.e2[0][0][0];
  u32x4 E[NFB][2];
#pragma unroll
  for (int fb = 0; fb < NFB; ++fb) { E[fb][0] = et[fb * 128 + lane]; E[fb][1] = et[fb * 128 + 64 + lane]; }
  uint32_t v[8][8];
#pragma unroll
  for (int el = 0; el < 8; ++el)
#pragma unroll
    for (int jj = 0; jj < 8; ++jj) {
      const int co = CONV == 1 ? el * 512 : (el & 3) * 1024;
      const int so = (CONV == 1 ? 0 : (el >> 2) * 8192) + (jj ? (jj - 1) * 262144 : 0);
      v[el][jj] = __builtin_amdgcn_raw_buffer_load_b32(wr, (jj ? vb : vb0) + co, so, NT);
    }
  PHASE();
  const __amdgpu_buffer_rsrc_t dr = mkbuf(wh, (CONV == 1 ? 32 : 16) * WBIN);
  const int vo = 2 * h * WBIN + (4 * dq + (n32 >> 4)) * WCB + ks * 2048 + kq * 256 + (n32 & 15) * 16;
#pragma unroll
  for (int e = 0; e < 2; ++e) {
    u32x4 b[8];
#pragma unroll
    for (int el = 0; el < 8; ++el)
#pragma unroll
      for (int j = 0; j < 4; ++j) b[el][j] = __builtin_amdgcn_perm(v[el][2 * j + 1], v[el][2 * j], e ? 0x07060302u : 0x05040100u);
#pragma unroll
    for (int fb = 0; fb < NFB; ++fb) {
      u32x4 o[16];
#pragma unroll
      for (int ep = 0; ep < 4; ++ep) {
        f32x16 d[2];
#pragma unroll
        for (int z = 0; z < 2; ++z) d[z] = mfma32(E[fb][1], b[2 * ep + z], zero16());
#pragma unroll
        for (int z = 0; z < 2; ++z) d[z] = mfma32(E[fb][0], b[2 * ep + z], d[z]);
#pragma unroll
        for (int i = 0; i < 16; ++i) o[i][ep] = pk2bf(d[0][i], d[1][i]);
      }
#pragma unroll
      for (int i = 0; i < 16; ++i)
        __builtin_amdgcn_raw_buffer_store_b128(o[i], dr, vo + (i & 1) * 1024,
                                               (16 * fb + 4 * (i >> 2) + ((i >> 1) & 1)) * WBIN + e * 2 * WCB, WT);
      st_pad(o[14], o[15]);
      SB();
    }
  }
}

__device__ __forceinline__ void xload(const uint16_t* __restrict__ x, int u, int lane, uint32_t (&v)[3][8]) {
  const int cq = u & 7, b = (u >> 3) & 1, sg = u >> 4;
  const __amdgpu_buffer_rsrc_t xr = mkbuf(x + (size_t)b * 16000 * DCH, 8192000);
#pragma unroll
  for (int ks = 0; ks < 3; ++ks) {
    const int row0 = 48 * sg - 24 + 16 * ks + 8 * (lane >> 5);
    const int vb = row0 >= 0 ? row0 * 1024 + cq * 128 + (lane & 31) * 4 : OOB;
#pragma unroll
    for (int e = 0; e < 8; ++e) v[ks][e] = __builtin_amdgcn_raw_buffer_load_b32(xr, vb + (e & 3) * 1024, (e >> 2) * 4096, NT);
  }
}
__device__ __forceinline__ void xtrans(char* __restrict__ u1, int u, int lane, const uint32_t (&v)[3][8], const u32x4 (&F)[2][3]) {
  const __amdgpu_buffer_rsrc_t ur = mkbuf(u1 + (2 * (u & 7)) * PL1 + (u >> 3) * 128, U1BYTES - (2 * (u & 7)) * PL1 - (u >> 3) * 128);
  const int vo = ((lane & 15) >> 1) * UB1 + ((lane >> 4) & 1) * 64 + (lane & 1) * 32 + (lane >> 5) * 16;
  u32x4 a[2][3];
#pragma unroll
  for (int ks = 0; ks < 3; ++ks)
#pragma unroll
    for (int j = 0; j < 4; ++j) {
      a[0][ks][j] = __builtin_amdgcn_perm(v[ks][2 * j + 1], v[ks][2 * j], 0x05040100u);
      a[1][ks][j] = __builtin_amdgcn_perm(v[ks][2 * j + 1], v[ks][2 * j], 0x07060302u);
    }
#pragma unroll
  for (int fb = 0; fb < 2; ++fb) {
    f32x16 d[2] = {zero16(), zero16()};
#pragma unroll
    for (int ks = 0; ks < 3; ++ks)
#pragma unroll
      for (int e = 0; e < 2; ++e) d[e] = mfma32(a[e][ks], F[fb][ks], d[e]);
#pragma unroll
    for (int qp = 0; qp < 2; ++qp) {
      u32x4 o[2];
#pragma unroll
      for (int z = 0; z < 2; ++z)
#pragma unroll
        for (int i = 0; i < 4; ++i) o[z][i] = pk2bf(d[0][8 * qp + 4 * z + i], d[1][8 * qp + 4 * z + i]);
      swap16(o[0], o[1]);
#pragma unroll
      for (int z = 0; z < 2; ++z) __builtin_amdgcn_raw_buffer_store_b128(o[z], ur, vo, (fb * 16 + z * 8) * UB1 + qp * PL1, WT);
      st_pad(o[0], o[1]);
    }
  }
}

KERNEL __launch_bounds__(512, 2) void pre_kernel(const uint16_t* __restrict__ x, const uint16_t* __restrict__ w1,
                                                     const uint16_t* __restrict__ w2, const uint16_t* __restrict__ wf,
                                                     const uint16_t* __restrict__ wo, const uint16_t* __restrict__ g,
                                                     const uint16_t* __restrict__ wg, char* __restrict__ u1,
                                                     char* __restrict__ w1h, char* __restrict__ w2h, u32x4* __restrict__ wff,
                                                     u32x4* __restrict__ wof, float* __restrict__ ge, uint32_t* __restrict__ flags) {
  const int blk = blockIdx.x;
  const int tid = threadIdx.x, lane = tid & 63;
  const int wave = __builtin_amdgcn_readfirstlane(tid >> 6);
  const int n = lane & 31, h = lane >> 5;
  __shared__ char pfs[256];
  if (wave == PF1) {
    const char *p0 = (const char*)&tabs - 320, *p1;
    LABEL_ADDR(p1, ".Lce");
    code_pf(p0, (int)(p1 - p0), blk >> 3, pfs);
  }
  if (wave == 0) {
    wtrans<1>(w1, w1h, blk, lane);
  } else if (wave == 1) {
    wtrans<2>(w2, w2h, blk, lane);
  } else {
    const int bq = (blk & 7) * 32 + (blk >> 3);
    const int u0 = ((21 * bq) >> 1) + wave - 2, ue = (21 * (bq + 1)) >> 1;
    u32x4 F[2][3];
#pragma unroll
    for (int fb = 0; fb < 2; ++fb)
#pragma unroll
      for (int ks = 0; ks < 3; ++ks) F[fb][ks] = ((const u32x4*)&tabs.f1[fb][ks][0][0])[lane];
    uint32_t v0[3][8];
    xload(x, u0, lane, v0);
    if (u0 + 6 < ue) {
      uint32_t v1[3][8];
      xload(x, u0 + 6, lane, v1);
      SB();
      WALK(7, wave - 2);
      PHASE();
      xtrans(u1, u0, lane, v0, F);
      SB();
      xtrans(u1, u0 + 6, lane, v1, F);
    } else if (wave == 7) {
      const int i = blk & 31, w = (blk >> 5) & 3, second = blk >> 7;
      const int eb = i < 8 ? i >> 1 : (i - 8) / 6, r = (2 * w + (i < 8 ? i & 1 : 2 + (i - 8) % 6)) & 7;
      const uint16_t* src = second ? wo + (16 * ((4 * w + (i >> 1)) & 15) + 8 * h) * DCH + 64 * w + 2 * n + (i & 1)
                                   : wf + dmap(2 * r, 4 * (lane >> 4)) * DCH + 64 * w + 16 * eb + (lane & 15);
      uint32_t v[8];
#pragma unroll
      for (int j = 0; j < 8; ++j) v[j] = src[(second ? j : 32 * (j >> 2) + 2 * (j & 3)) * DCH];
      PHASE();
      xtrans(u1, u0, lane, v0, F);
      const float sc = second ? 2.f : 1.f;
      u32x4 o;
#pragma unroll
      for (int j = 0; j < 4; ++j) o[j] = pk2bf(bf2f(v[2 * j]) * sc, bf2f(v[2 * j + 1]) * sc);
      ((second ? wof : wff) + (blk & 127) * 64)[lane] = o;
      if (blk < NFLAG / 64) flags[blk * 64 + lane] = 0;
    } else if (blk < 8) {
      const int c0 = 32 * blk + 2 * (lane & 15), kq = lane >> 4;
      uint32_t v[8][8];
      u32x4 ga[8];
#pragma unroll
      for (int ks = 0; ks < 8; ++ks) {
        ga[ks] = *(const u32x4*)(g + (lane & 15) * DCH + ks * 32 + 8 * kq);
#pragma unroll
        for (int e = 0; e < 8; ++e) v[ks][e] = *(const uint32_t*)(wg + (ks * 32 + 8 * kq + e) * DCH + c0);
      }
      PHASE();
      xtrans(u1, u0, lane, v0, F);
      f32x4 d[2] = {{0.f, 0.f, 0.f, 0.f}, {0.f, 0.f, 0.f, 0.f}};
#pragma unroll
      for (int ks = 0; ks < 8; ++ks)
#pragma unroll
        for (int e = 0; e < 2; ++e) {
          u32x4 bb;
#pragma unroll
          for (int j = 0; j < 4; ++j) bb[j] = __builtin_amdgcn_perm(v[ks][2 * j + 1], v[ks][2 * j], e ? 0x07060302u : 0x05040100u);
          d[e] = mfma16(ga[ks], bb, d[e]);
        }
      float* dst = ge + (((((blk >> 1) * 2 + (kq >> 1)) * 4 + 2 * (blk & 1) + ((lane >> 3) & 1)) * 2 + ((lane >> 1) & 1)) * 2 + ((lane >> 2) & 1)) * 32 + (kq & 1) * 16 + 2 * (lane & 1);
      f32x2 gs[4];
      bool far = false;
#pragma unroll
      for (int i = 0; i < 4; ++i) {
        gs[i] = f32x2{bf2f(f2bf(d[0][i])) * C2, bf2f(f2bf(d[1][i])) * C2};
        far = far || __builtin_fabsf(gs[i][0]) > EMAX || __builtin_fabsf(gs[i][1]) > EMAX;
      }
      far = __builtin_amdgcn_ballot_w64(far) != 0;
#pragma unroll
      for (int i = 0; i < 4; ++i) {
        *(f32x2*)(dst + 4 * i) = far ? f32x2{-1.f, -1.f} : f32x2{__builtin_amdgcn_exp2f(-gs[i][0]), __builtin_amdgcn_exp2f(-gs[i][1])};
        *(f32x2*)(dst + 4096 + 4 * i) = gs[i];
      }
    } else {
      PHASE();
      xtrans(u1, u0, lane, v0, F);
    }
  }
  asm volatile(".Lce:");
}

extern "C" void kernel_launch(void* const* d_in, const int* in_sizes, int n_in, void* d_out, int out_size, void* d_ws,
                              size_t ws_size, hipStream_t stream) {
  const uint16_t* x = (const uint16_t*)d_in[0];
  const uint16_t* w1 = (const uint16_t*)d_in[1];
  const uint16_t* w2 = (const uint16_t*)d_in[2];
  const uint16_t* g = (const uint16_t*)d_in[3];
  const uint16_t* wf = (const uint16_t*)d_in[4];
  const uint16_t* wg = (const uint16_t*)d_in[5];
  const uint16_t* wo = (const uint16_t*)d_in[6];
  uint16_t* out = (uint16_t*)d_out;
#define WOFF 0
  char* ws = (char*)d_ws + ((size_t)WOFF << 20);
  char* u1 = ws;
  char* w1h = ws + (23 << 20);
  char* w2h = ws + (40 << 20);
  char* y1 = ws + (49 << 20);
  char* u2 = ws + (61 << 20);
  char* y2 = ws + (78 << 20);
  u32x4* wff = (u32x4*)(ws + (87 << 20));
  u32x4* wof = (u32x4*)(ws + (87 << 20) + 131072);
  float* ge = (float*)(ws + (87 << 20) + 262144);
  uint32_t* flags = (uint32_t*)(ws + (88 << 20));
  constexpr int G1LDS = 7 * 11 * 2048 + 256;

  (void)hipFuncSetAttribute((const void*)gemm_kernel, hipFuncAttributeMaxDynamicSharedMemorySize, G1LDS);
  (void)hipFuncSetAttribute((const void*)joint_kernel, hipFuncAttributeMaxDynamicSharedMemorySize, JOINT_LDS);

  pre_kernel<<<256, 512, 0, stream>>>(x, w1, w2, wf, wo, g, wg, u1, w1h, w2h, wff, wof, ge, flags);
  gemm_kernel<<<256, 256, G1LDS, stream>>>(u1, w1h, y1, (const char*)wff);
  mid_kernel<<<256, 1024, 0, stream>>>(y1, u2);
  joint_kernel<<<256, 256, JOINT_LDS, stream>>>(u2, w2h, y2, (const char*)wff, ge, flags, out);
}
```

```cpp
#include <hip/hip_runtime.h>
#include <stdint.h>

typedef __attribute__((ext_vector_type(8))) short bf16x8;
typedef __attribute__((ext_vector_type(16))) float f32x16;
typedef __attribute__((ext_vector_type(4))) float f32x4;
typedef __attribute__((ext_vector_type(2))) float f32x2;
typedef __attribute__((ext_vector_type(4))) unsigned int u32x4;
typedef __attribute__((ext_vector_type(2))) unsigned int u32x2;

#define ORD0 0x0132
#define ORD1 0x2310
#define GLA 6
#define TS0 22
#define TS1 55
#define TU0 22
#define TU1 55
#define THR 7
#define THR2 7
#define GPRO 2
constexpr int DCH = 256;

__device__ __forceinline__ float bf2f(uint32_t v) { return __uint_as_float(v << 16); }
__device__ __forceinline__ uint32_t f2bf(float f) {
  uint32_t u = __float_as_uint(f);
  u += 0x7FFF + ((u >> 16) & 1);
  return u >> 16;
}
__device__ __forceinline__ uint32_t pk2bf(float a, float b) {
  typedef __attribute__((ext_vector_type(2))) __bf16 b2;
  f32x2 v = {a, b};
  b2 r = __builtin_convertvector(v, b2);
  return __builtin_bit_cast(uint32_t, r);
}
constexpr double GS = 0.4686686932353139;
__device__ __forceinline__ uint32_t gelu2(float a0, float a1, float one = 1.f) {
  const f32x2 v = {a0, a1};
  constexpr float c = (float)(-2.302208198144325 / GS);
  const f32x2 z = v * __builtin_elementwise_fma(-v, v, f32x2{c, c});
  const f32x2 d = f32x2{__builtin_amdgcn_exp2f(z[0]), __builtin_amdgcn_exp2f(z[1])} + f32x2{one, one};
  const f32x2 y = v * f32x2{__builtin_amdgcn_rcpf(d[0]), __builtin_amdgcn_rcpf(d[1])};
  return pk2bf(y[0], y[1]);
}
__device__ __forceinline__ f32x16 mfma32(const u32x4& a, const u32x4& b, const f32x16& c) {
  return __builtin_amdgcn_mfma_f32_32x32x16_bf16(__builtin_bit_cast(bf16x8, a), __builtin_bit_cast(bf16x8, b), c, 0, 0, 0);
}
__device__ __forceinline__ f32x4 mfma16(const u32x4& a, const u32x4& b, const f32x4& c) {
  return __builtin_amdgcn_mfma_f32_16x16x32_bf16(__builtin_bit_cast(bf16x8, a), __builtin_bit_cast(bf16x8, b), c, 0, 0, 0);
}
__device__ __forceinline__ f32x16 zero16() {
  f32x16 z;
#pragma unroll
  for (int i = 0; i < 16; ++i) z[i] = 0.f;
  return z;
}
__device__ __forceinline__ __amdgpu_buffer_rsrc_t mkbuf(const void* p, int nbytes) {
  return __builtin_amdgcn_make_buffer_rsrc((void*)p, (short)0, nbytes, 0x00020000);
}
template <int V> struct IC { static constexpr int value = V; };
template <int I, int N, class F>
__device__ __forceinline__ void sfor(F&& f) {
  if constexpr (I < N) {
    f(IC<I>{});
    sfor<I + 1, N>(f);
  }
}
#define SB() __builtin_amdgcn_sched_barrier(0)
#define PHASE() do { SB(); asm volatile("s_waitcnt vmcnt(0)\n\ts_barrier" ::: "memory"); SB(); } while (0)

constexpr double PI_ = 3.14159265358979323846;
constexpr double c128(int m) {
  m = ((m % 128) + 128) % 128;
  if (m > 64) m = 128 - m;
  double sg = 1.0;
  if (m > 32) { m = 64 - m; sg = -1.0; }
  if (m == 32) return 0.0;
  const double a = PI_ * m / 64.0;
  double term = 1.0, sum = 1.0;
  for (int i = 1; i < 16; ++i) { term *= -a * a / ((2 * i - 1) * (2 * i)); sum += term; }
  return sg * sum;
}
constexpr double s128(int m) { return c128(m - 32); }
constexpr uint16_t cbf(double v) {
  uint32_t u = __builtin_bit_cast(uint32_t, (float)v);
  u += 0x7FFF + ((u >> 16) & 1);
  return (uint16_t)(u >> 16);
}
constexpr double cbf2d(uint16_t v) { return (double)__builtin_bit_cast(float, (uint32_t)v << 16); }
struct Tabs {
  uint16_t f1[2][3][64][8];
  uint16_t e1[2][2][64][8];
  uint16_t e2[2][64][8];
  uint16_t g1[8][64][8];
  uint16_t f2[64][8];
  uint16_t g2[2][64][8];
};
constexpr Tabs make_tabs() {
  Tabs t{};
  for (int l = 0; l < 64; ++l)
    for (int e = 0; e < 8; ++e) {
      const int h = l >> 5, f = l & 31, ri = f & 1;
      for (int fb = 0; fb < 2; ++fb) {
        const int k = 16 * fb + (f >> 1);
        for (int ks = 0; ks < 3; ++ks) {
          const int a = (2 * k + 1) * (16 * ks + 8 * h + e);
          t.f1[fb][ks][l][e] = cbf(ri ? -s128(a) : c128(a));
        }
        const int a = (2 * k + 1) * (8 * h + e);
        const double v = ri ? s128(a) : c128(a);
        t.e1[fb][0][l][e] = cbf(v);
        t.e1[fb][1][l][e] = cbf(v - cbf2d(cbf(v)));
      }
      const int a = 2 * (2 * (f >> 1) + 1) * (8 * h + e);
      const double v = ri ? s128(a) : c128(a);
      t.e2[0][l][e] = cbf(v);
      t.e2[1][l][e] = cbf(v - cbf2d(cbf(v)));
      t.f2[l][e] = cbf((ri ? -v : v) / GS);
    }
  for (int m = -15; m < 48; ++m)
    for (int f = 0; f < 64; ++f) {
      const int a = (2 * (f >> 1) + 1) * m;
      t.g1[f >> 3][(m + 16) ^ (((m + 16) >> 4) & 1)][f & 7] = cbf(((f & 1) ? -s128(a) : c128(a)) * GS / 32.0);
    }
  for (int c = 0; c < 2; ++c)
    for (int l = 0; l < 64; ++l)
      for (int e = 0; e < 8; ++e) {
        const int m = 4 * (l & 3) + ((l >> 2) & 3) - (l & 16), a = 2 * (2 * (8 * c + 4 * (l >> 5) + (e >> 1)) + 1) * m;
        t.g2[c][l][e] = m > -16 ? cbf(((e & 1) ? -s128(a) : c128(a)) * GS / 16.0) : 0;
      }
  return t;
}
__device__ __attribute__((aligned(64))) const Tabs tabs = make_tabs();

constexpr int OOB = 0x7FFFFF00;
constexpr float C2 = 2.885390081777927f, C3 = (float)(2.885390081777927 / GS);
#define EMAXV 120.f
constexpr float EMAX = EMAXV;
constexpr int WT = 16;
constexpr int NT = 2;
constexpr int PL1 = 336 * 128;
#define PU2 0
#define PY2 0
constexpr int UB1 = 16 * PL1, UB2 = 524288 + PU2;
constexpr int U1BYTES = 32 * UB1, U2BYTES = 32 * UB2;
constexpr int WCB = 32768;
constexpr int WBIN = 16 * WCB;
constexpr int YP1 = 336 * 64;
constexpr int YB1 = 16 * YP1, YB2 = 524288 + PY2;
constexpr int Y1BYTES = 32 * YB1, Y2BYTES = 16 * YB2;
constexpr int F1 = 0, F2 = 2048, F3 = 10240, NFLAG = 18432 / 4;
constexpr int A_U1 = 0, A_W1 = 23 << 20, A_W2 = 40 << 20, A_Y1 = 49 << 20, A_U2 = 61 << 20, A_Y2 = 78 << 20, A_WF = 87 << 20,
              A_GE = A_WF + 262144, A_FL = 88 << 20;
__device__ __forceinline__ int dmap(int cb, int n) { return 64 * (cb >> 2) + 32 * (cb & 1) + 2 * n + ((cb >> 1) & 1); }

__device__ __forceinline__ void swap16(u32x4& a, u32x4& b) {
#pragma unroll
  for (int i = 0; i < 4; ++i) {
    const auto r = __builtin_amdgcn_permlane16_swap(a[i], b[i], false, false);
    a[i] = r[0];
    b[i] = r[1];
  }
}

__device__ __forceinline__ void st_pad(const u32x4& a, const u32x4& b) { asm volatile("s_nop 1" :: "v"(a), "v"(b) : "memory"); }

template <int N>
__device__ __forceinline__ void wait_vm() {
  constexpr int M = N < 63 ? N : 63;
  __builtin_amdgcn_s_waitcnt((M & 15) | ((M >> 4) << 14) | 0x0F70);
}

typedef __attribute__((address_space(3))) u32x4 lds_u32x4;
__device__ __forceinline__ u32x4 lds_ld(int a) { return *(const lds_u32x4*)(uintptr_t)(uint32_t)a; }
__device__ __forceinline__ uint32_t lds_ld32(int a) { return *(const __attribute__((address_space(3))) uint32_t*)(uintptr_t)(uint32_t)a; }
template <int SZ>
__device__ __forceinline__ void dma(__amdgpu_buffer_rsrc_t rsrc, char* lds, int voff, int soff) {
#if defined(__HIP_DEVICE_COMPILE__)
  typedef __attribute__((address_space(3))) void* ldsp;
  if constexpr (SZ == 16) __builtin_amdgcn_raw_ptr_buffer_load_lds(rsrc, (ldsp)lds, 16, voff, soff, 0, 0);
  else __builtin_amdgcn_raw_ptr_buffer_load_lds(rsrc, (ldsp)lds, 4, voff, soff, 0, 0);
#endif
}

#define LABEL_ADDR(p, name) asm volatile("s_getpc_b64 s[40:41]\n\ts_add_u32 s40, s40, " name "@rel32@lo+4\n\ts_addc_u32 s41, s41, " name "@rel32@hi+12\n\ts_mov_b64 %0, s[40:41]" : "=s"(p) :: "s40", "s41", "scc")
__device__ __forceinline__ void code_pf(const char* p, int n, int part, char* lds) {
  dma<4>(mkbuf(p, n), lds, part * 8192 + (threadIdx.x & 63) * 128, 0);
}

__device__ __forceinline__ void pad_macros() {
  asm volatile(R"(
.macro PAD1 l
.ifndef .Lw_\l
.Lw_\l:
.endif
\l:
s_cbranch_execz \l\()f
.endm
.macro PADM k, b, m
.ifndef .Lpg_\k
.set .Lpg_\k, -1
.endif
.ifndef .Lcn_\b
.set .Lcn_\b, 0
.endif
.if (((. - \k) & 4095) < 60) && (((. - \k) >> 12) > .Lpg_\k)
.set .Lpg_\k, (. - \k) >> 12
.irp c, 0, 1, 2, 3, 4, 5, 6, 7
.if (.Lcn_\b & \m) == \c
PAD1 \b\c
.endif
.endr
.set .Lcn_\b, .Lcn_\b + 1
.endif
.endm
.macro WALK b, w
s_mov_b64 exec, 0
.irp c, 0, 1, 2, 3, 4, 5, 6, 7
.ifdef .Lw_\b\c
s_cmp_eq_u32 \w, \c
s_cbranch_scc1 .Lw_\b\c
.endif
.endr
.irp c, 0, 1, 2, 3, 4, 5, 6, 7
\b\c:
.endr
s_mov_b64 exec, -1
.endm
)");
}
#define PAD(k, b, m) asm volatile("PADM " #k ", " #b ", " #m)
#define WALK(b, w) asm volatile("WALK " #b ", %0" :: "s"(w) : "scc")
#define KERNEL extern "C" __global__ __attribute__((aligned(4096)))

constexpr int J_T = 0;
constexpr int J_G = 131072;
constexpr int J_F = J_G + 16384;
constexpr int J_R = J_F + 8192;
constexpr int JOINT_LDS = J_R + 1024;
__device__ __forceinline__ void lds_st(int a, const u32x4& v) { *(lds_u32x4*)(uintptr_t)(uint32_t)a = v; }
#define BAR() do { SB(); asm volatile("s_waitcnt lgkmcnt(0)\n\ts_barrier" ::: "memory"); SB(); } while (0)
#define DRAIN() do { SB(); wait_vm<0>(); asm volatile("s_waitcnt vmcnt(0)\n\ts_barrier" ::: "memory"); SB(); } while (0)
template <int M, bool Z>
__device__ __forceinline__ void mm(f32x16& c, const u32x4& a, const u32x4& b) {
  if constexpr (Z) {
    if constexpr (M == 0) asm volatile("v_mfma_f32_32x32x16_bf16 %0, %1, %2, 0" : "=&v"(c) : "v"(a), "v"(b));
    else asm volatile("v_mfma_f32_32x32x16_bf16 %0, %1, %2, 0" : "=&v"(c) : "v"(a), "a"(b));
  } else {
    if constexpr (M == 0) asm volatile("v_mfma_f32_32x32x16_bf16 %0, %1, %2, %0" : "+v"(c) : "v"(a), "v"(b));
    else asm volatile("v_mfma_f32_32x32x16_bf16 %0, %1, %2, %0" : "+v"(c) : "v"(a), "a"(b));
  }
}
template <int M, bool Z>
__device__ __forceinline__ void mm(f32x4& c, const u32x4& a, const u32x4& b) {
  if constexpr (Z) {
    if constexpr (M == 0) asm volatile("v_mfma_f32_16x16x32_bf16 %0, %1, %2, 0" : "=&v"(c) : "v"(a), "v"(b));
    else asm volatile("v_mfma_f32_16x16x32_bf16 %0, %1, %2, 0" : "=&v"(c) : "a"(a), "v"(b));
  } else {
    if constexpr (M == 0) asm volatile("v_mfma_f32_16x16x32_bf16 %0, %1, %2, %0" : "+v"(c) : "v"(a), "v"(b));
    else asm volatile("v_mfma_f32_16x16x32_bf16 %0, %1, %2, %0" : "+v"(c) : "a"(a), "v"(b));
  }
}
#define MWAIT(c0, c1) asm volatile("s_nop 7\n\ts_nop 3" : "+v"(c0), "+v"(c1))
template <int K>
__device__ __forceinline__ float add_quad(float a, float b) {
  float r;
  asm volatile("v_add_f32_dpp %0, %1, %2 quad_perm:[%3,%3,%3,%3] row_mask:0xf bank_mask:0xf" : "=v"(r) : "v"(a), "v"(b), "n"(K));
  return r;
}
__device__ __forceinline__ uint32_t neg2(uint32_t v) {
  uint32_t r;
  asm volatile("v_xor_b32 %0, 0x80008000, %1" : "=v"(r) : "v"(v));
  return r;
}
__device__ __forceinline__ void keep_a(const u32x4& v) { asm volatile("" :: "a"(v)); }
__device__ __forceinline__ void keep_v(const u32x4& v) { asm volatile("" :: "v"(v)); }
__device__ __forceinline__ int pin(int v) { asm volatile("" : "+v"(v)); return v; }
__device__ __forceinline__ void await(uint32_t v, __amdgpu_buffer_rsrc_t r, int vo, int so) {
  uint32_t cnt;
  SB();
  asm volatile("v_cmp_gt_u32 vcc, 1, %0\n\ts_cbranch_vccz 2f\n\ts_mov_b32 %1, 0x40000\n"
               "1:\n\ts_sleep 1\n\tbuffer_load_dword %0, %2, %3, %4 offen sc1\n\ts_waitcnt vmcnt(0)\n\t"
               "v_cmp_gt_u32 vcc, 1, %0\n\ts_cbranch_vccz 2f\n\ts_sub_u32 %1, %1, 1\n\ts_cbranch_scc0 1b\n2:"
               : "+v"(v), "=&s"(cnt) : "v"(vo), "s"(r), "s"(so) : "vcc", "scc", "memory");
  SB();
}

#define OPOL NT
#define ORD2 0x0132
#define ORD3 0x2310
#define KCY 84
#define KCAP 200
#define KLD 60
#define KLA 40
#define KD1 112
#define KQ1 14
#define KPI1 130
#define KPC1 140
#define KYR 3
#define KT0 160
#define NB0 14
#define NB1 16
#define NX0 16
#define KX0 8
#define KP1 1
#define KXL 40
#define KXU 20
#define KJM 48
#define KJ0 56
#define KSL 0
#define KCY2 72
#define KQJ 99
#define KCJ 68
#define TS2 75
#define THM 4
#define THL -1
#define KS5 31
#define KD2 16
#define KPI2 36
#define KPC2 46
#define KXB 0
#define KD3 82
#define KQ3 16
#define KPI3 104
#define KPC3 114
#define KJR 5
#define KPI4 4
#define KPC4 34
enum { O_NONE, O_W, O_A, O_B, O_T, O_F, O_V, O_GI, O_Q, O_J, O_S1, O_Y1, O_X, O_S2, O_Y2, O_SIG, O_POLL, O_U };
constexpr int fill_t(int l) { return l < 2 ? O_T : l < 3 ? O_F : l < 35 ? O_V : l < 37 ? O_GI : l < 41 ? O_Q : O_J; }
constexpr int fill_j(int l) { return l < 2 ? l : l < 35 ? l - 3 : l < 37 ? l - 35 : l < 41 ? l - 37 : l - 41; }
constexpr int first(int c) { return c < 16 ? 6 * c : c < 32 ? 16 + 5 * c : 999; }
constexpr int chunk(int g) { return g < 96 ? g / 6 : (g - 16) / 5; }
constexpr int cpos(int c) { return c < 16 ? (c + 8) % 12 * 12288 : (c - 16) * 9216; }
struct Sched {
  int ty[3 * 184], ix[3 * 184];
  int before[3 * 184];
  int last[32];
  int tt[16], ti[16];
  int nl, nj;
};
constexpr int main_t(int m) { return m >= 80 ? O_B : m % 5 < 2 ? O_W : O_A; }
constexpr int main_i(int m) { return m >= 80 ? m - 80 : m % 5 < 2 ? m / 5 * 2 + m % 5 : m / 5 * 3 + m % 5 - 2; }
constexpr int main_c(int m) { return m >= 80 ? 16 + (m - 80) / 9 * 4 + ((m - 80) % 9 - 1) / 2 : m / 5; }
constexpr int vfree(int l) { return first(18 + (8 * ((l - 3) >> 1) + 7) / 9) - 3; }
constexpr int hk(int i) { return i < 16 ? i : 4 * i - 48; }
constexpr Sched make_s1() {
  Sched c{};
  int m = 5 * GPRO, l = 0, o = m, y = 0, cr = KCY;
  for (int i = 0; i < GPRO; ++i) c.last[i] = 5 * i + 5;
  for (int t = 0; t < 3 * 184; ++t) {
    const int g = t / 3, j = t % 3;
    const bool thr = g >= TS0 && g < TS1;
    c.before[t] = o;
    if (j == 0) cr += g < TS0 ? 64 : 68 + 4 * (thr ? THR : g < TS2 ? THM - 2 : THL);
    if (cr > KCAP) cr = KCAP;
    if (j == 1 && ((g >= 93 && g < 99) || (g >= 174 && g < 179))) { c.ty[t] = O_S1; c.ix[t] = g < 99 ? g - 93 : g - 168; cr -= KCY; }
    else if (t == 3 * KD1 + 2) { c.ty[t] = O_SIG; c.ix[t] = 0; }
    else if (t == 3 * KPI1 + 2) { c.ty[t] = O_POLL; c.ix[t] = 0; }
    else if (g >= KT0) {
      if (t == 3 * KT0) {
        if (y < 48) return c;
        for (int i = 0; i < NB0; ++i) {
          const bool v = l < 35 && g > vfree(l);
          c.tt[i] = v ? O_V : O_J;
          c.ti[i] = v ? l++ - 3 : c.nj++;
        }
      }
      if (j == 1 && g < KT0 + 4) { c.ty[t] = O_U; c.ix[t] = 4 + g - KT0; }
    } else if (g >= KPC1 && y < 48) {
      if (m < 116) return c;
      if (j >= 3 - KYR) { c.ty[t] = O_Y1; c.ix[t] = y++; }
    }
    else if (g > KD1 - KQ1 && g <= KD1) { if (j == 1 && g <= KD1 - KQJ) { c.ty[t] = O_J; c.ix[t] = c.nj++; } }
    else if ((j == 1 || (j == 2 && thr)) && cr >= KCY) {
      const int k = main_c(m), fr = k < 12 ? 0 : first(k < 16 ? k - 10 : 6 + (9 * (k - ((m - 80) % 9 ? 16 : 13)) + 8) / 12) - 3;
      const bool fo = l < 35 && m >= 80 && (l < 3 || g >= vfree(l));
      if (m < 116 && g >= fr && first(k) - g <= (k < 16 ? KLA : KLD) && (m < 80 || l >= 3)) {
        c.ty[t] = main_t(m); c.ix[t] = main_i(m);
        cr -= g < TS0 ? 64 : KCY;
        c.last[k] = o + 1;
        ++m;
      } else if (fo) {
        c.ty[t] = fill_t(l); c.ix[t] = fill_j(l);
        cr -= KCY;
        ++l;
      } else if (g >= KJ0 && c.nj < KJM) {
        c.ty[t] = O_J; c.ix[t] = c.nj++;
        cr -= KCJ;
      }
    }
    if (c.ty[t]) ++o;
  }
  c.nl = l;
  return c;
}
constexpr Sched S1 = make_s1();
constexpr int NFILL = 3 + 32 + 2 + 4 + 64 - S1.nj;
constexpr int fill_i(int l) { return fill_j(l) + (l < 41 ? 0 : S1.nj); }
constexpr int NL1 = S1.nl + NB1;
static_assert(S1.nl > 2 && NL1 >= 35 && NL1 <= NFILL && KT0 < 170 && first(chunk(KD1 + 4)) == KD1 + 4, "schedule");
constexpr Sched make_s2() {
  Sched c{};
  int l = NL1, x = NX0, y = 0, cr = 0;
  for (int t = 0; t < 3 * 136; ++t) {
    const int g = t / 3, j = t % 3;
    const bool thr = g >= TU0 && g < TU1, fs = j == 1 || (j == 2 && thr);
    if (j == 0) cr += 68 + 4 * (thr ? THR2 : THL);
    if (cr > KCAP) cr = KCAP;
    if (j == 1 && (g & 1) && ((g > 60 && g < 68) || (g > 124 && g < 132))) { c.ty[t] = O_S2; c.ix[t] = g < 68 ? (g - 61) / 2 : (g - 117) / 2; cr -= KCY2; }
    else if (t == 3 * KD2 + 2) { c.ty[t] = O_SIG; c.ix[t] = 3; }
    else if (t == 3 * KPI2 + 2) { c.ty[t] = O_POLL; c.ix[t] = 3; }
    else if (t == 3 * KD3 + 2) { c.ty[t] = O_SIG; c.ix[t] = 4; }
    else if (t == 3 * KPI3 + 2) { c.ty[t] = O_POLL; c.ix[t] = 4; }
    else if (g >= KPC3) { if (y < 32) { c.ty[t] = O_Y2; c.ix[t] = y++; } }
    else if (g <= KD2 || (g > KD3 - KQ3 && g <= KD3)) {
      if (j == 1 && g <= (g > KD2 ? KD3 : KD2) - KQJ && l >= 41 && l < NFILL) { c.ty[t] = O_J; c.ix[t] = fill_i(l++); }
    } else if (x < (g >= KPC2 ? 64 : 32) && ((j && x >= 32 && x < 32 + KXB) || (j == 1 && 2 * x - g <= KXU) || (fs && cr >= KCY2 && 2 * x - g <= KXL))) {
      c.ty[t] = O_X; c.ix[t] = x++;
      cr -= KCY2;
      if (cr < 0) cr = 0;
    } else if (fs && cr >= KCY2 && l < NFILL) {
      c.ty[t] = fill_t(l); c.ix[t] = fill_i(l);
      cr -= KCJ;
      ++l;
    }
    if (t == 3 * KD3 && l < 41) return c;
    if (c.ty[t] == O_X && g >= (2 * c.ix[t] & ~7) - 1) return c;
  }
  c.nl = x == 64 && y == 32 ? l : -1;
  return c;
}
constexpr Sched S2 = make_s2();
static_assert(S2.nl + S1.nj >= 41 + 32 && S2.nl + (87 - KS5) * KJR / 8 >= NFILL, "schedule");

#define PADG() PAD(joint_kernel, 7, 3)
#define PADJ() PADG()
KERNEL __launch_bounds__(256, 1) void joint_kernel(char* ws, uint16_t* __restrict__ out) {
  extern __shared__ __attribute__((aligned(16))) char smem[];
  pad_macros();
  PADG();
  SB();
  const int bid = blockIdx.x, xcd = bid & 7, slot = bid >> 3;
  const int tid = threadIdx.x, lane = tid & 63;
  const int w = __builtin_amdgcn_readfirstlane(tid >> 6);
  const int cb = (slot & 3) * 4 + w;
  const int bin1 = xcd * 4 + (slot >> 3), mt1 = (slot >> 2) & 1;
  const int bin2 = xcd * 2 + (slot >> 4);
  const __amdgpu_buffer_rsrc_t ur = mkbuf(ws + (unsigned)(A_U1 + bin1 * UB1 + mt1 * (168 * 128)), UB1);
  const __amdgpu_buffer_rsrc_t wr = mkbuf(ws + (unsigned)(A_W1 + bin1 * WBIN + cb * WCB), WCB);
  const int vd = w * 1024 + (lane >> 3) * 128 + (((lane & 7) ^ ((4 * w + (lane >> 4)) & 7)) * 16);
  const int rho = lane & 15, kq = lane >> 4;
  const int n = lane & 31, h = lane >> 5;
  const float* ge = (const float*)(ws + A_GE);
  __amdgpu_buffer_rsrc_t vr, ys, yl, ub, yb, wb, fr;
  u32x4 Wm[32], X[64], wt[64], y1[3][4], yv[2][4][2], F, gi[2], o[11], up[8];
  int ve, vt, v1[3], vx, vs, vy, vu, vf[3], vp[3];
  uint32_t pl[2];
  constexpr int FO[6] = {F1, F1 + 1024, F2, F2 + 4096, F3, F3 + 4096};
  auto yload = [&](int b, int i) { yv[b][i & 3][i >> 4][(i >> 2) & 3] = __builtin_amdgcn_raw_buffer_load_b32(yb, vy + (i & 3) * 1024, ((i >> 4) * 8 + ((i >> 2) & 3)) * YB2 + b * 65536, WT); };
  auto issue = [&](auto it, auto ii) {
    constexpr int t = decltype(it)::value, i = decltype(ii)::value;
    if constexpr (t == O_W) Wm[i] = __builtin_amdgcn_raw_buffer_load_b128(wr, lane * 16 + (i & 3) * 1024, (i >> 2) * 4096, 0);
    else if constexpr (t == O_A) dma<16>(ur, smem + cpos(i / 3) + i % 3 * 4096 + w * 1024, vd, i / 3 * PL1 + i % 3 * 4096);
    else if constexpr (t == O_B && i % 9) dma<16>(ur, smem + cpos(main_c(80 + i)) + (i % 9 - 1 & 1) * 4096 + w * 1024, vd, (main_c(80 + i) - 16) * PL1 + (3 + (i % 9 - 1 & 1)) * 4096);
    else if constexpr (t == O_B) dma<16>(ur, smem + cpos(main_c(80 + i)) + 8192 + w * 9216, ve, i / 9 * 4 * PL1);
    else if constexpr (t == O_T) dma<16>(mkbuf(&tabs.g1[2 * w][0][0], 2048), smem + J_F + w * 2048 + i * 1024, lane * 16, i * 1024);
    else if constexpr (t == O_F) F = ((const u32x4*)&tabs.f2[0][0])[lane];
    else if constexpr (t == O_V) dma<16>(vr, smem + (i >> 1) * 8192 + w * 2048 + (i & 1) * 1024, lane * 16, i * 1024);
    else if constexpr (t == O_GI) gi[i] = ((const u32x4*)&tabs.g2[i][0][0])[lane];
    else if constexpr (t == O_Q) dma<16>(mkbuf(ge + w * 1024, 4096), smem + J_G + w * 4096 + i * 1024, lane * 16, i * 1024);
    else if constexpr (t == O_J) wt[i] = __builtin_amdgcn_raw_buffer_load_b128(wb, lane * 16 + (i & 3) * 1024, (i >> 5) * 131072 + ((i >> 2) & 7) * 4096, 0);
    else if constexpr (t == O_S1) __builtin_amdgcn_raw_buffer_store_b128(o[i], ys, vt + (i & 3) * 1024, (i >> 2) * 4096, WT);
    else if constexpr (t == O_Y1) y1[i % 3][i % 48 / 12][i / 3 & 3] = __builtin_amdgcn_raw_buffer_load_b32(yl, v1[i % 3], (i % 48 / 12 * 8 + (i / 3 & 3)) * YB1 + i / 48 * (84 * 64), WT);
    else if constexpr (t == O_X) X[i] = __builtin_amdgcn_raw_buffer_load_b128(ub, vx + (i & 3) * 1024, (i >> 5) * 16 * UB2 + ((i >> 2) & 7) * 4096, WT);
    else if constexpr (t == O_S2) __builtin_amdgcn_raw_buffer_store_b128(o[i & 3], yb, vs + (i & 3) * 1024, (i >> 2) * 65536, WT);
    else if constexpr (t == O_Y2) yload(0, i);
    else if constexpr (t == O_SIG) __builtin_amdgcn_raw_buffer_store_b32(1u, fr, vf[i >> 1], FO[i], WT);
    else if constexpr (t == O_POLL) pl[i & 1] = __builtin_amdgcn_raw_buffer_load_b32(fr, vp[i >> 1], FO[i], WT);
    else if constexpr (t == O_U) __builtin_amdgcn_raw_buffer_store_b128(up[i & 7], ub, vu + ((i >> 1) & 3) * 256, ((i >> 3) * 16 + (i & 1) * 8) * UB2, WT);
    SB();
  };
  auto fills = [&](auto i0, auto i1) {
    sfor<decltype(i0)::value, decltype(i1)::value>([&](auto il) { issue(IC<fill_t(decltype(il)::value)>{}, IC<fill_i(decltype(il)::value)>{}); });
  };
  sfor<0, 5 * GPRO>([&](auto il) { issue(IC<main_t(decltype(il)::value)>{}, IC<main_i(decltype(il)::value)>{}); });
  vr = mkbuf(ws + (unsigned)(A_W2 + bin2 * WBIN + cb * WCB), WCB);
  ys = mkbuf(ws + (unsigned)(A_Y1 + bin1 * YB1 + cb * YP1 + mt1 * (168 * 64)), 168 * 64);
  yl = mkbuf(ws + A_Y1, Y1BYTES);
  ub = mkbuf(ws + A_U2, U2BYTES);
  yb = mkbuf(ws + A_Y2, Y2BYTES);
  wb = mkbuf(ws + (unsigned)(A_WF + w * 32768), 131072 + 32768);
  fr = mkbuf(ws + A_FL, NFLAG * 4);
  const int l0 = (int)(uint32_t)(uintptr_t)(__attribute__((address_space(3))) char*)smem;
  int ab[3][2], ga[4];
  float ok[2];
#pragma unroll
  for (int ri = 0; ri < 2; ++ri)
#pragma unroll
    for (int wn = 0; wn < 3; ++wn) ab[wn][ri] = pin(l0 + rho * 128 + ((((kq >> 1) * 4 + ri * 2 + (kq & 1)) ^ (rho >> 1)) * 16) + wn * 65536);
  ve = pin(20480 + w * PL1 + (lane >> 3) * 128 + (((lane & 7) ^ kq) * 16));
  vt = pin(rho * 64 + kq * 16);
  {
    const int j = 4 * (slot & 15) + w, ty = j % 3, a = j / 3 * 4 + (ty ? ty : -1);
    const int tau = 16 * ((n >> 2) & 1) + 8 * ((n >> 3) & 1) + 2 * (n & 3) + (n >> 4);
#pragma unroll
    for (int i = 0; i < 3; ++i) {
      const int g = a + i + 84 * ((n >> 2) & 1), e = g >= 84, r = g - 84 * e;
      const int v = pin(4 * h * YB1 + bin2 * YP1 + (84 * (r >= 42) + 2 * r + e) * 64 + (n >> 3) * 16 + (n & 3) * 4);
      v1[i] = pin(g < (j < 63 && i != 2 * ty ? 168 : -2) ? v : OOB);
    }
#pragma unroll
    for (int i = 0; i < 4; ++i) {
      const int r = tau + 16 + (ty ? 16 * ty : 48) - 16 * (i >> 1) - 48 * (i & 1);
      ga[i] = pin(l0 + J_F + h * 1024 + ((unsigned)r < 64u ? r ^ ((r >> 4) & 1) : 0) * 16);
    }
#pragma unroll
    for (int i = 0; i < 2; ++i) ok[i] = __builtin_bit_cast(float, pin((unsigned)(32 * (2 * j + i + 126 * ((n >> 2) & 1)) - 16 + 16 * h) < 8000u ? 0x3F800000 : 0x7F800000));
    const int ge_ = a + (h ? 2 - (ty == 1) : ty == 0);
    vp[0] = pin((bin2 >> 2) * 256 + (ge_ >= 42 && ge_ < 84) * 128 + n * 4);
    vf[0] = pin((slot & 3) * 256 + mt1 * 128 + bin1 * 4 + tid * 65536);
    vf[1] = vf[2] = pin((slot & 15) * 256 + bin2 * 4 + tid * 65536);
    vu = pin((rho >> 1) * UB2 + (slot & 15) * 32768 + bin2 * 2048 + (w >> 1) * 1024 + (w & 1) * 128 + (lane & 1) * 64 + kq * 16);
  }
  auto aread = [&](int g, int ri) -> u32x4 {
    const int a = cpos(chunk(g)) + (g - first(chunk(g))) * 2048;
    return lds_ld(ab[a >> 16][ri] + (a & 65535));
  };
  f32x4 yr[11], yi[11];
  u32x4 A[4][2], nw[2], gf[16];
  auto gread = [&](int i) { gf[i] = lds_ld(ga[i >> 2] + (i & 3) * 2048); };
  auto trans = [&](auto ib, auto&& hook) {
    constexpr int b = decltype(ib)::value;
    f32x16 d[2], u[2][2];
    u32x4 a;
    sfor<0, 16>([&](auto ii) {
      constexpr int i = decltype(ii)::value;
      mm<0, (i & 7) == 0>(d[i >> 3], gf[i], y1[(i >> 3) + ((i >> 2) & 1)][i & 3]);
      PADG();
      if constexpr (i + 6 < 16) gread(i + 6);
      SB();
      hook(ii);
    });
    auto pack = [&](int q) {
#pragma unroll
      for (int e = 0; e < 2; ++e) {
#pragma unroll
        for (int z = 0; z < 2; ++z)
#pragma unroll
          for (int j = 0; j < 4; ++j) up[4 * q + 2 * e + z][j] = pk2bf(u[q][z][8 * e + 2 * j], u[q][z][8 * e + 2 * j + 1]);
        PADG();
        swap16(up[4 * q + 2 * e], up[4 * q + 2 * e + 1]);
        PADG();
      }
      SB();
    };
    auto put = [&](auto iq) {
      constexpr int q = decltype(iq)::value;
      sfor<8 * b + 4 * q, 8 * b + 4 * q + 4>([&](auto ii) { issue(IC<O_U>{}, ii); });
      PADG();
      st_pad(up[4 * q + 2], up[4 * q + 3]);
      SB();
    };
    sfor<0, 4>([&](auto ii) {
      constexpr int i = decltype(ii)::value, q = i >> 1, z = i & 1;
      if constexpr (i < 2) asm volatile("s_nop 2" : "+v"(d[z]));
#pragma unroll
      for (int j = 0; j < 2; ++j) { a[j] = gelu2(d[z][8 * q + 2 * j], d[z][8 * q + 2 * j + 1], ok[z]); PADG(); }
      SB();
      hook(IC<16 + 2 * i>{});
#pragma unroll
      for (int j = 2; j < 4; ++j) { a[j] = gelu2(d[z][8 * q + 2 * j], d[z][8 * q + 2 * j + 1], ok[z]); PADG(); }
      asm volatile("s_nop 1" : "+v"(a));
      SB();
      mm<0, true>(u[q][z], a, F);
      SB();
      hook(IC<17 + 2 * i>{});
      if constexpr (i == 2) { pack(0); if constexpr (!b || KP1) put(IC<0>{}); }
    });
    MWAIT(u[1][0], u[1][1]);
    pack(1);
    if constexpr (b && !KP1) put(IC<0>{});
    if constexpr (b) put(IC<1>{});
  };
  SB();
  wait_vm<5 * GPRO - 5>();
  asm volatile("s_barrier" ::: "memory");
#pragma unroll
  for (int z = 0; z < 3; ++z) { A[z][1] = aread(z, 1); SB(); A[z][0] = aread(z, 0); SB(); }
#pragma unroll
  for (int j = 0; j < 4; ++j) nw[0][j] = neg2(Wm[1][j]);
  SB();
  sfor<0, 179>([&](auto ig) {
    constexpr int g = decltype(ig)::value, c = chunk(g), ks = c & 15, mb = g - first(c) + (c < 16 ? 0 : 6), e = first(c + 1) - g;
    if constexpr (g == KPC1) { await(pl[0], fr, vp[0], FO[0]); }
    if constexpr (g == KT0) {
      trans(IC<0>{}, [&](auto ii) {
        constexpr int i = decltype(ii)::value;
        if constexpr (i < 22) sfor<NB0 * hk(i) / hk(22), NB0 * hk(i + 1) / hk(22)>([&](auto il) { issue(IC<S1.tt[decltype(il)::value]>{}, IC<S1.ti[decltype(il)::value]>{}); });
      });
    }
    auto mf = [&](auto ij) {
      constexpr int j = decltype(ij)::value, i = (((g & 1) ? ORD1 : ORD0) >> (12 - 4 * j)) & 3;
      constexpr bool z = ks == 0 && !(j & 1);
      if constexpr (g < 176) {
        if constexpr (i == 0) mm<1, z>(yr[mb], Wm[2 * ks], A[g & 3][0]);
        else if constexpr (i == 1) mm<0, z>(yr[mb], nw[c & 1], A[g & 3][1]);
        else if constexpr (i == 2) mm<0, z>(yi[mb], Wm[2 * ks + 1], A[g & 3][0]);
        else mm<1, z>(yi[mb], Wm[2 * ks], A[g & 3][1]);
      }
      PADG();
    };
    auto cvt = [&](int j) {
      constexpr int m = g >= 92 && g < 98 ? g - 92 : g >= 173 && g < 178 ? g - 167 : -1;
      if constexpr (m >= 0) {
        if constexpr (g == 177) if (!j) asm volatile("s_nop 1" : "+v"(yr[m]), "+v"(yi[m]));
#pragma unroll
        for (int i = 2 * j; i < 2 * j + 2; ++i) o[m][i] = pk2bf(yr[m][i], yi[m][i]);
      }
      if constexpr (S1.ty[3 * g + 1] == O_S1) if (j) keep_v(o[S1.ix[3 * g + 1]]);
      if constexpr (S1.ty[3 * g + 1] == O_U) if (j) keep_v(up[S1.ix[3 * g + 1]]);
      if constexpr (g >= KT0 - 3 && g < KT0) gread(2 * (g - KT0 + 3) + j);
      SB();
    };
    if constexpr (g < 176 && !(g & 1)) keep_v(A[(g + 1) & 3][0]);
    mf(IC<0>{});
    if constexpr (g + 3 < 176) { A[(g + 3) & 3][1] = aread(g + 3, 1); SB(); A[(g + 3) & 3][0] = aread(g + 3, 0); }
    SB();
    mf(IC<1>{});
    issue(IC<S1.ty[3 * g]>{}, IC<S1.ix[3 * g]>{});
    cvt(0);
    mf(IC<2>{});
    issue(IC<S1.ty[3 * g + 1]>{}, IC<S1.ix[3 * g + 1]>{});
    mf(IC<3>{});
    if constexpr (e == 4) {
      static_assert(S1.last[c + 1] && S1.before[3 * g + 2] >= S1.last[c + 1], "next chunk not requested yet");
      SB();
      if constexpr (g == KD1) { DRAIN(); }
      else {
        if constexpr (g < KD1 || (g < KT0 && S1.last[c + 1] > S1.before[3 * KD1 + 2])) wait_vm<S1.before[3 * g + 2] - S1.last[c + 1]>();
        asm volatile("s_barrier" ::: "memory");
      }
      SB();
    }
    issue(IC<S1.ty[3 * g + 2]>{}, IC<S1.ix[3 * g + 2]>{});
    if constexpr (g >= TS0 && g < TS1) asm volatile("s_nop %0" ::"n"(S1.ty[3 * g + 2] ? THR - 3 : THR));
    else if constexpr (g >= TS1 && g < TS2) asm volatile("s_nop %0" ::"n"(THM));
    else if constexpr (THL >= 0 && g >= TS1 && g < 173 && !S1.ty[3 * g + 2]) asm volatile("s_nop %0" ::"n"(THL + 2));
    if constexpr (e <= 3 && e >= 2) {
#pragma unroll
      for (int j = 6 - 2 * e; j < 8 - 2 * e; ++j) nw[(c + 1) & 1][j] = neg2(Wm[(2 * ks + 3) & 31][j]);
    }
    SB();
    cvt(1);
  });
  st_pad(o[10], o[10]);
  const int pos = 32 * xcd + slot, seg = pos < 250 ? pos : 0;
  int wa[2], ra[4], fa[4], qa, vo;
  {
    const int grp = (slot & 12) + w, s = seg + (kq & 1), e = s >= 126, r = s - 126 * e;
        vx = pin(bin2 * UB2 + grp * 32768 + lane * 16);
    vs = pin(bin2 * YB2 + (slot & 3) * 131072 + grp * 4096 + ((rho & 2) * 4 + (rho & 4) + (rho >> 3) * 2 + (rho & 1)) * 64 + kq * 16);
    vy = pin(4 * h * YB2 + w * 131072 + (r >> 3) * 4096 + (8 * e + (r & 7)) * 64 + rho * 4);
    vp[1] = pin(grp * 256 + rho * 4);
    vp[2] = pin(((r >> 5) * 4 + w) * 256 + rho * 4);
#pragma unroll
    for (int i = 0; i < 2; ++i) wa[i] = pin(l0 + lane * 16 + i * 65536);
  }
  DRAIN();
  issue(IC<O_SIG>{}, IC<1>{});
  issue(IC<O_SIG>{}, IC<2>{});
  fills(IC<S1.nl>{}, IC<NL1>{});
  if constexpr (KSL) __builtin_amdgcn_s_sleep(KSL);
  issue(IC<O_POLL>{}, IC<1>{});
  issue(IC<O_POLL>{}, IC<2>{});
#pragma unroll
  for (int i = 0; i < 6; ++i) gread(i);
  await(pl[1], fr, vp[0], FO[1]);
  await(pl[0], fr, vp[1], FO[2]);
  sfor<0, 48>([&](auto ii) { issue(IC<O_Y1>{}, IC<48 + decltype(ii)::value % 16 * 3 + decltype(ii)::value / 16>{}); PADJ(); });
  auto wread = [&](int g, int j) -> u32x4 { return lds_ld(wa[(g >> 5) & 1] + ((g >> 3 & 3) * 16 + (g & 7) + j * 8) * 1024); };
  trans(IC<1>{}, [&](auto ii) {
    constexpr int i = decltype(ii)::value, e = KP1 ? 22 : 24;
    if constexpr (i >= KX0 && i < e) sfor<NX0 * (hk(i) - KX0) / (hk(e) - KX0), NX0 * (hk(i + 1) - KX0) / (hk(e) - KX0)>([&](auto ix) { issue(IC<O_X>{}, ix); PADJ(); });
    if constexpr (i == 19) asm volatile("s_barrier" ::: "memory");
    if constexpr (i == 23) {
#pragma unroll
      for (int z = 0; z < 3; ++z) { A[z][1] = wread(z, 1); SB(); A[z][0] = wread(z, 0); SB(); }
    }
  });
  f32x4 ac[16][2];
  float re, im;
  sfor<0, 132>([&](auto ig) {
    constexpr int g = decltype(ig)::value, k = g >> 3, mb = (g & 7) + (g & 64) / 8;
    if constexpr (g == KPC2) { await(pl[1], fr, vp[1], FO[3]); }
    if constexpr (g == KPC3) { await(pl[0], fr, vp[2], FO[4]); }
    auto mf = [&](auto ij) {
      constexpr int j = decltype(ij)::value, i = (((g & 1) ? ORD3 : ORD2) >> (12 - 4 * j)) & 3;
      if constexpr (g < 128) mm<0, (k & 7) == 0 && !(j & 1)>(ac[mb][i >> 1], A[g & 3][i & 1], X[4 * k + 2 * (i & 1) + (i >> 1)]);
      PADJ();
    };
    auto cvt = [&](int j) {
      constexpr int m = g >= 59 && g < 67 ? g - 59 : g >= 123 && g < 131 ? g - 115 : -1;
      if constexpr (m >= 0) {
        const int i = 2 * (m & 1) + j;
        if constexpr (g == 129) if (!j) asm volatile("s_nop 1" : "+v"(ac[15][0]), "+v"(ac[15][1]));
        asm volatile("v_subrev_f32_dpp %0, %4, %2 row_shl:4 row_mask:0xf bank_mask:0x5\n\t"
                     "v_add_f32_dpp %1, %2, %4 row_shl:4 row_mask:0xf bank_mask:0x5\n\t"
                     "v_sub_f32_dpp %0, %3, %5 row_shr:4 row_mask:0xf bank_mask:0xa\n\t"
                     "v_add_f32_dpp %1, %5, %3 row_shr:4 row_mask:0xf bank_mask:0xa"
                     : "=&v"(re), "=&v"(im) : "v"(ac[m & 14][0][i]), "v"(ac[m & 14][1][i]), "v"(ac[m | 1][0][i]), "v"(ac[m | 1][1][i]));
        o[(m >> 1) & 3][i] = pk2bf(re, im);
      }
      if constexpr (S2.ty[3 * g + 1] == O_S2) if (j) keep_v(o[S2.ix[3 * g + 1] & 3]);
      SB();
    };
    if constexpr (g < 128 && !(g & 1)) keep_v(A[(g + 1) & 3][0]);
    mf(IC<0>{});
    if constexpr (g + 3 < 128) { A[(g + 3) & 3][1] = wread(g + 3, 1); SB(); A[(g + 3) & 3][0] = wread(g + 3, 0); }
    SB();
    mf(IC<1>{});
    issue(IC<S2.ty[3 * g]>{}, IC<S2.ix[3 * g]>{});
    cvt(0);
    mf(IC<2>{});
    issue(IC<S2.ty[3 * g + 1]>{}, IC<S2.ix[3 * g + 1]>{});
    mf(IC<3>{});
    if constexpr (g == KD2) { DRAIN(); }
    if constexpr (g == KD3) { DRAIN(); }
    issue(IC<S2.ty[3 * g + 2]>{}, IC<S2.ix[3 * g + 2]>{});
    if constexpr (g >= TU0 && g < TU1) asm volatile("s_nop %0" ::"n"(S2.ty[3 * g + 2] ? THR2 - 3 : THR2));
    else if constexpr (THL >= 0 && g < 123 && !S2.ty[3 * g + 2]) asm volatile("s_nop %0" ::"n"(THL + 2));
    if constexpr ((g & 7) == 7 && g < 127) { keep_v(X[4 * k + 7]); }
    SB();
    cvt(1);
    if constexpr (g >= 84 && g < 88) ra[g & 3] = pin(l0 + ((w + 1 + g) & 3) * 4096 + lane * 16);
    if constexpr (g >= 88 && g < 92) fa[g & 3] = pin(l0 + J_F + ((w + 1 + g) & 3) * 2048 + lane * 16);
    if constexpr (g == 82) qa = pin(l0 + J_G + w * 4096 + (h * 8 + (n >> 4) * 4 + (n & 3)) * 16);
    if constexpr (g == 83) vo = pin(h * 4096 + w * 128 + n * 4);
    SB();
  });
  st_pad(o[3], o[3]);
  float p[2][32], x[8], mx = 0.f;
  f32x4 q[2][8], fe[4];
  u32x4 tp[2][4];
  auto jstep = [&](auto it) {
    constexpr int t = decltype(it)::value, l = S2.nl + t * KJR / 8;
    if constexpr (l < NFILL && l < S2.nl + (t + 1) * KJR / 8) issue(IC<O_J>{}, IC<fill_i(l)>{});
  };
  auto front = [&](auto ib, auto&& after) {
    constexpr int b = decltype(ib)::value & 1;
    f32x4 d[4];
    u32x4 fb[8];
#pragma unroll
    for (int c = 0; c < 4; ++c) {
      mm<0, true>(d[c], yv[b][c][0], gi[0]);
      if constexpr (decltype(ib)::value < 2) PADJ();
    }
#pragma unroll
    for (int c = 0; c < 4; ++c) {
      mm<0, false>(d[c], yv[b][c][1], gi[1]);
      if constexpr (decltype(ib)::value < 2) PADJ();
    }
    asm volatile("s_nop 4" : "+v"(d[0]));
#pragma unroll
    for (int j = 0; j < 2; ++j) {
#pragma unroll
      for (int c = 0; c < 4; ++c) {
        fb[j][c] = gelu2(d[2 * j + (c >> 1)][2 * (c & 1)], d[2 * j + (c >> 1)][2 * (c & 1) + 1]);
        if constexpr (decltype(ib)::value < 2) PADJ();
      }
      lds_st(fa[3] + j * 1024, fb[j]);
      SB();
    }
    sfor<0, 32>([&](auto ii) {
      constexpr int i = decltype(ii)::value, eb = i < 8 ? i >> 1 : (i - 8) / 6, r = i < 8 ? i & 1 : 2 + (i - 8) % 6;
      mm<1, r == 0>(fe[eb], wt[i], fb[r]);
      if constexpr (decltype(ib)::value < 2) PADJ();
      SB();
      if constexpr (i == 0) {
        BAR();
      }
      if constexpr (i < 3) {
        fb[2 * i + 2] = lds_ld(fa[i]);
        fb[2 * i + 3] = lds_ld(fa[i] + 1024);
      }
      SB();
      after(ii);
    });
  };
  auto twrite = [&](auto iG) {
    constexpr int V = decltype(iG)::value + 31;
    if constexpr (V >= 0 && V < 256 && (V & 7) == 7) lds_st(ra[3] + J_T + ((V >> 5) & 1) * 16384 + ((V >> 3) & 3) * 1024, tp[(V >> 5) & 1][(V >> 3) & 3]);
    SB();
  };
  auto tslot = [&](auto iG) {
    constexpr int G = decltype(iG)::value;
    constexpr int A = G + 36, B = G + 35, C = G + 34, D = G + 33;
    if constexpr (B >= 0 && B < 256) asm volatile("v_rcp_f32 %0, %0" : "+v"(x[B & 7]));
    if constexpr (C >= 0 && C < 256) asm volatile("v_fma_f32 %0, -%1, %0, 0.5" : "+v"(x[C & 7]) : "v"(q[C >> 7][(C >> 2) & 7][C & 3]));
    PADJ();
    if constexpr (A >= 0 && A < 256) x[A & 7] = add_quad<(A >> 5) & 3>(p[A >> 7][A & 31], q[A >> 7][(A >> 2) & 7][A & 3]);
    SB();
    if constexpr (D >= 0 && D < 256 && (D & 1)) tp[(D >> 5) & 1][(D >> 3) & 3][(D >> 1) & 3] = pk2bf(x[(D - 1) & 7], x[D & 7]);
    SB();
  };
  auto stepE = [&](int b, int eb, int i) {
    const float a = fe[eb][i] * C3;
    mx = __builtin_fmaxf(mx, __builtin_fabsf(a));
    const uint32_t e = __builtin_bit_cast(uint32_t, __builtin_amdgcn_exp2f(a));
    const auto r = __builtin_amdgcn_permlane16_swap(e, e, false, false);
    p[b][8 * eb + i] = __builtin_bit_cast(float, (uint32_t)r[0]);
    p[b][8 * eb + 4 + i] = __builtin_bit_cast(float, (uint32_t)r[1]);
  };
  auto head = [&](auto ib) {
    constexpr int b = decltype(ib)::value;
    front(ib, [&](auto ii) {
      constexpr int i = decltype(ii)::value;
      if constexpr (b == 0 && i == KS5) {
        DRAIN();
        issue(IC<O_SIG>{}, IC<5>{});
        asm volatile("s_cmpk_lt_i32 %0, 250\n\ts_cbranch_scc1 1f\n\ts_endpgm\n1:" :: "s"(pos) : "scc");
      }
      if constexpr (i >= 3 && i < 11) q[b][i - 3] = __builtin_bit_cast(f32x4, lds_ld(qa + b * 2048 + (i - 3) * 256));
      if constexpr (b == 0 && i > KS5) jstep(IC<i - KS5 - 1>{});
      if constexpr (i >= 16 && (i - 16) % 6 < 4) {
        if constexpr ((i - 16) % 6 == 0) asm volatile("s_nop 1" : "+v"(fe[(i - 16) / 6]));
        stepE(b, (i - 16) / 6, (i - 16) % 6);
      }
      SB();
    });
    if constexpr (b == 0) sfor<-36, -32>([&](auto ir) { tslot(ir); });
    else asm volatile("s_nop 7" ::);
    asm volatile("" : "+v"(fe[3]));
#pragma unroll
    for (int i = 0; i < 4; ++i) { stepE(b, 3, i); PADJ(); }
    mx = __builtin_fmaxf(mx, q[b][0][0] < 0.f || q[b][4][0] < 0.f ? 2 * EMAX : 0.f);
    if constexpr (b) *(__attribute__((address_space(3))) float*)(uintptr_t)(uint32_t)(l0 + J_R + tid * 4) = mx;
    asm volatile("s_nop 1");
    SB();
  };
  head(IC<0>{});
  sfor<-32, 0>([&](auto ir) { jstep(IC<decltype(ir)::value + 63 - KS5>{}); twrite(ir); tslot(ir); });
  const __amdgpu_buffer_rsrc_t ob = mkbuf(out + (size_t)seg * 16 * 8 * DCH, 4000 * 4096 + 65536);
  f32x16 acc[2][2];
  u32x4 a[16];
  uint32_t ov[16];
  int vq[2] = {vo, vo + 8192};
  auto store = [&](int i) { __builtin_amdgcn_raw_buffer_store_b32(ov[i], ob, vq[(i >> 2) & 1] + ((i >> 3) * 4 + (i & 3)) * 512, 0, OPOL); };
  auto next = [&](int st, int j) {
    vq[j] += (st & 3) ? 16384 : 4000 * 4096 - 3 * 16384;
    asm volatile("" : "+v"(vq[j]));
  };
  sfor<0, 256>([&](auto iG) {
    constexpr int G = decltype(iG)::value, st = G >> 5, g = G & 31, r = g >> 1, z = g & 1;
    if constexpr (G == 92) { head(IC<1>{}); }
    mm<2, r == 0>(acc[st & 1][z], r < 4 ? tp[st & 1][r & 3] : a[r], wt[32 + g]);
    PADJ();
    SB();
    if constexpr (g == 2) BAR();
    if constexpr (st == 0 && g < 31) keep_a(wt[33 + g]);
    if constexpr (z && r >= 3 && r < 15) keep_v(a[r + 1]);
    constexpr int rd = g >= 3 && g < 6 ? g + 1 : (g >= 7 && g <= 23 && z) ? (g + 7) / 2 : 0;
    constexpr int sn = g == 1 ? 15 : g == 2 ? 0 : g == 6 ? 1 : g >= 25 ? g - 17 : (g > 8 && !z && (g & 7)) ? (g - 6) / 2 - (g > 16) : -1;
    if constexpr (rd) a[rd] = lds_ld(ra[(rd >> 2) - 1] + J_T + (st & 1) * 16384 + (rd & 3) * 1024);
    if constexpr (sn >= 0 && st > (g == 1)) store(sn);
    if constexpr (G < 24) jstep(IC<G + 63 - KS5>{});
    if constexpr (G == KPI4) issue(IC<O_POLL>{}, IC<5>{});
    if constexpr (G == KPC4) await(pl[1], fr, vp[2], FO[5]);
    if constexpr (G >= KPC4 && G < KPC4 + 32) yload(1, G - KPC4);
    twrite(iG);
    tslot(iG);
    if constexpr (st > 0 && g == 30) next(st, 0);
    if constexpr (st > 1 && g == 4) next(st - 1, 1);
    if constexpr (G == 225) asm volatile("s_nop 5" : "+v"(acc[0][0]));
    if constexpr (st > 0 && z) ov[r] = pk2bf(acc[(st - 1) & 1][0][r], acc[(st - 1) & 1][1][r]);
    SB();
  });
  store(15);
  next(7, 1);
  const f32x4 rg = __builtin_bit_cast(f32x4, lds_ld(l0 + J_R + lane * 16));
  MWAIT(acc[1][0], acc[1][1]);
#pragma unroll
  for (int i = 0; i < 16; ++i) { ov[i] = pk2bf(acc[1][0][i], acc[1][1][i]); store(i); PADJ(); }
  if (__builtin_expect(__builtin_amdgcn_ballot_w64(rg[0] > EMAX || rg[1] > EMAX || rg[2] > EMAX || rg[3] > EMAX) != 0, 0)) {
    asm volatile("s_waitcnt vmcnt(0)" ::: "memory");
#pragma unroll 1
    for (int b = 0; b < 2; ++b) {
#pragma unroll
      for (int i = 0; i < 32; ++i) yload(0, i);
      vy += 65536;
      front(IC<2>{}, [](auto) {});
      asm volatile("s_nop 7" : "+v"(fe[3]));
#pragma unroll 1
      for (int st = 0; st < 4; ++st) {
        const float* gp = ge + 4096 + (w * 2 + b) * 512 + (h * 8 + (n >> 4) * 4 + (n & 3)) * 4;
#pragma unroll
        for (int f = 0; f < 4; ++f) {
          u32x4 t;
#pragma unroll
          for (int c = 0; c < 2; ++c) {
            const f32x4 gv = *(const f32x4*)(gp + (2 * f + c) * 64);
            float y[4];
#pragma unroll
            for (int i = 0; i < 4; ++i)
              y[i] = 0.5f - __builtin_amdgcn_rcpf(__builtin_amdgcn_exp2f(__shfl(fe[f][i], h * 32 + c * 16 + (lane & 12) + st) * C3 + gv[i]) + 1.f);
            t[2 * c] = pk2bf(y[0], y[1]);
            t[2 * c + 1] = pk2bf(y[2], y[3]);
          }
          lds_st(ra[3] + J_T + (st & 1) * 16384 + f * 1024, t);
        }
        BAR();
        sfor<0, 16>([&](auto ir) {
          constexpr int r = decltype(ir)::value;
          const u32x4 v = lds_ld(ra[(r / 4 + 3) & 3] + J_T + (st & 1) * 16384 + (r & 3) * 1024);
#pragma unroll
          for (int z = 0; z < 2; ++z) mm<2, r == 0>(acc[0][z], v, wt[32 + 2 * r + z]);
        });
        MWAIT(acc[0][0], acc[0][1]);
#pragma unroll
        for (int i = 0; i < 16; ++i)
          __builtin_amdgcn_raw_buffer_store_b32(pk2bf(acc[0][0][i], acc[0][1][i]), ob, vo + st * 16384 + ((i >> 3) * 4 + (i & 3)) * 512,
                                                b * (4000 * 4096) + ((i >> 2) & 1) * 8192, NT);
      }
    }
  }
}

template <int CONV>
__device__ __forceinline__ void wtrans(const uint16_t* __restrict__ w, char* __restrict__ wh, int wid, int lane) {
  const int dq = wid & 3, pc = wid >> 2;
  const int ks = pc >> 2, kq = pc & 3;
  const int n32 = lane & 31, h = lane >> 5;
  const int r = CONV == 1 ? ks >> 3 : kq >> 1;
  const int c0 = CONV == 1 ? (ks & 7) * 32 + kq * 8 : dmap(ks, 8 * (kq & 1));
  const __amdgpu_buffer_rsrc_t wr = mkbuf(w, 31 * 131072);
  const int vb = (16 * h + r + 1) * 131072 + c0 * 512 + dq * 128 + n32 * 4;
  const int vb0 = (h + r == 0) ? OOB : vb - 262144;
  constexpr int NFB = CONV == 1 ? 2 : 1;
  const u32x4* et = CONV == 1 ? (const u32x4*)&tabs.e1[0][0][0][0] : (const u32x4*)&tabs.e2[0][0][0];
  u32x4 E[NFB][2];
#pragma unroll
  for (int fb = 0; fb < NFB; ++fb) { E[fb][0] = et[fb * 128 + lane]; E[fb][1] = et[fb * 128 + 64 + lane]; }
  uint32_t v[8][8];
#pragma unroll
  for (int el = 0; el < 8; ++el)
#pragma unroll
    for (int jj = 0; jj < 8; ++jj) {
      const int co = CONV == 1 ? el * 512 : (el & 3) * 1024;
      const int so = (CONV == 1 ? 0 : (el >> 2) * 4096) + (jj ? (jj - 1) * 262144 : 0);
      v[el][jj] = __builtin_amdgcn_raw_buffer_load_b32(wr, (jj ? vb : vb0) + co, so, NT);
    }
  PHASE();
  const __amdgpu_buffer_rsrc_t dr = mkbuf(wh, (CONV == 1 ? 32 : 16) * WBIN);
  const int vo = 2 * h * WBIN + (4 * dq + (n32 >> 4)) * WCB + ks * 2048 + kq * 256 + (n32 & 15) * 16;
#pragma unroll
  for (int e = 0; e < 2; ++e) {
    u32x4 b[8];
#pragma unroll
    for (int el = 0; el < 8; ++el)
#pragma unroll
      for (int j = 0; j < 4; ++j) b[el][j] = __builtin_amdgcn_perm(v[el][2 * j + 1], v[el][2 * j], e ? 0x07060302u : 0x05040100u);
#pragma unroll
    for (int fb = 0; fb < NFB; ++fb) {
      u32x4 o[16];
#pragma unroll
      for (int ep = 0; ep < 4; ++ep) {
        f32x16 d[2];
#pragma unroll
        for (int z = 0; z < 2; ++z) d[z] = mfma32(E[fb][1], b[2 * ep + z], zero16());
#pragma unroll
        for (int z = 0; z < 2; ++z) d[z] = mfma32(E[fb][0], b[2 * ep + z], d[z]);
#pragma unroll
        for (int i = 0; i < 16; ++i) o[i][ep] = pk2bf(d[0][i], d[1][i]);
      }
#pragma unroll
      for (int i = 0; i < 16; ++i)
        __builtin_amdgcn_raw_buffer_store_b128(o[i], dr, vo + (i & 1) * 1024,
                                               (16 * fb + 4 * (i >> 2) + ((i >> 1) & 1)) * WBIN + e * 2 * WCB, WT);
      st_pad(o[14], o[15]);
      SB();
    }
  }
}

__device__ __forceinline__ void xload(const uint16_t* __restrict__ x, int u, int lane, uint32_t (&v)[3][8]) {
  const int cq = u & 7, m = u >= 8 * 168, b = u - 8 * 168 * m >= 8 * 84, r = (u >> 3) - 168 * m - 84 * b, sg = 84 * (r & 1) + 42 * m + (r >> 1);
  const __amdgpu_buffer_rsrc_t xr = mkbuf(x + (size_t)b * 16000 * DCH, 8192000);
#pragma unroll
  for (int ks = 0; ks < 3; ++ks) {
    const int row0 = 48 * sg - 24 + 16 * ks + 8 * (lane >> 5);
    const int vb = row0 >= 0 ? row0 * 1024 + cq * 128 + (lane & 31) * 4 : OOB;
#pragma unroll
    for (int e = 0; e < 8; ++e) v[ks][e] = __builtin_amdgcn_raw_buffer_load_b32(xr, vb + (e & 3) * 1024, (e >> 2) * 4096, NT);
  }
}
__device__ __forceinline__ void xtrans(char* __restrict__ u1, int u, int lane, const uint32_t (&v)[3][8], const u32x4 (&F)[2][3]) {
  const __amdgpu_buffer_rsrc_t ur = mkbuf(u1 + (2 * (u & 7)) * PL1 + (u >> 3) * 128, U1BYTES - (2 * (u & 7)) * PL1 - (u >> 3) * 128);
  const int vo = ((lane & 15) >> 1) * UB1 + ((lane >> 4) & 1) * 64 + (lane & 1) * 32 + (lane >> 5) * 16;
  u32x4 a[2][3];
#pragma unroll
  for (int ks = 0; ks < 3; ++ks)
#pragma unroll
    for (int j = 0; j < 4; ++j) {
      a[0][ks][j] = __builtin_amdgcn_perm(v[ks][2 * j + 1], v[ks][2 * j], 0x05040100u);
      a[1][ks][j] = __builtin_amdgcn_perm(v[ks][2 * j + 1], v[ks][2 * j], 0x07060302u);
    }
#pragma unroll
  for (int fb = 0; fb < 2; ++fb) {
    f32x16 d[2] = {zero16(), zero16()};
#pragma unroll
    for (int ks = 0; ks < 3; ++ks)
#pragma unroll
      for (int e = 0; e < 2; ++e) d[e] = mfma32(a[e][ks], F[fb][ks], d[e]);
#pragma unroll
    for (int qp = 0; qp < 2; ++qp) {
      u32x4 o[2];
#pragma unroll
      for (int z = 0; z < 2; ++z)
#pragma unroll
        for (int i = 0; i < 4; ++i) o[z][i] = pk2bf(d[0][8 * qp + 4 * z + i], d[1][8 * qp + 4 * z + i]);
      swap16(o[0], o[1]);
#pragma unroll
      for (int z = 0; z < 2; ++z) __builtin_amdgcn_raw_buffer_store_b128(o[z], ur, vo, (fb * 16 + z * 8) * UB1 + qp * PL1, WT);
      st_pad(o[0], o[1]);
    }
  }
}

KERNEL __launch_bounds__(512, 2) void pre_kernel(const uint16_t* __restrict__ x, const uint16_t* __restrict__ w1,
                                                     const uint16_t* __restrict__ w2, const uint16_t* __restrict__ wf,
                                                     const uint16_t* __restrict__ wo, const uint16_t* __restrict__ g,
                                                     const uint16_t* __restrict__ wg, char* __restrict__ u1,
                                                     char* __restrict__ w1h, char* __restrict__ w2h, u32x4* __restrict__ wff,
                                                     u32x4* __restrict__ wof, float* __restrict__ ge, uint32_t* __restrict__ flags) {
  const int blk = blockIdx.x;
  const int tid = threadIdx.x, lane = tid & 63;
  const int wave = __builtin_amdgcn_readfirstlane(tid >> 6);
  const int n = lane & 31, h = lane >> 5;
  __shared__ char pfs[256];
  if (wave == 7) {
    const char *p0 = (const char*)&tabs - 320, *p1;
    LABEL_ADDR(p1, ".Lce");
    code_pf(p0, (int)(p1 - p0), blk >> 3, pfs);
  }
  if (wave == 0) {
    wtrans<1>(w1, w1h, blk, lane);
  } else if (wave == 1) {
    wtrans<2>(w2, w2h, blk, lane);
  } else {
    const int bq = (blk & 7) * 32 + (blk >> 3);
    const int u0 = ((21 * bq) >> 1) + wave - 2, ue = (21 * (bq + 1)) >> 1;
    u32x4 F[2][3];
#pragma unroll
    for (int fb = 0; fb < 2; ++fb)
#pragma unroll
      for (int ks = 0; ks < 3; ++ks) F[fb][ks] = ((const u32x4*)&tabs.f1[fb][ks][0][0])[lane];
    uint32_t v0[3][8];
    xload(x, u0, lane, v0);
    if (u0 + 6 < ue) {
      uint32_t v1[3][8];
      xload(x, u0 + 6, lane, v1);
      SB();
      WALK(7, wave - 2);
      PHASE();
      xtrans(u1, u0, lane, v0, F);
      SB();
      xtrans(u1, u0 + 6, lane, v1, F);
    } else if (wave == 7) {
      const int i = blk & 31, w = (blk >> 5) & 3, second = blk >> 7;
      const int eb = i < 8 ? i >> 1 : (i - 8) / 6, r = (2 * w + (i < 8 ? i & 1 : 2 + (i - 8) % 6)) & 7;
      const uint16_t* src = second ? wo + (16 * ((4 * w + (i >> 1)) & 15) + 8 * h) * DCH + 64 * w + 2 * n + (i & 1)
                                   : wf + dmap(2 * r, 4 * (lane >> 4)) * DCH + 64 * w + 16 * eb + (lane & 15);
      uint32_t v[8];
#pragma unroll
      for (int j = 0; j < 8; ++j) v[j] = src[(second ? j : 32 * (j >> 2) + 2 * (j & 3)) * DCH];
      PHASE();
      xtrans(u1, u0, lane, v0, F);
      const float sc = second ? 2.f : 1.f;
      u32x4 o;
#pragma unroll
      for (int j = 0; j < 4; ++j) o[j] = pk2bf(bf2f(v[2 * j]) * sc, bf2f(v[2 * j + 1]) * sc);
      ((second ? wof : wff) + (blk & 127) * 64)[lane] = o;
      if (blk < NFLAG / 64) flags[blk * 64 + lane] = 0;
    } else if (blk < 8) {
      const int c0 = 32 * blk + 2 * (lane & 15), kq = lane >> 4;
      uint32_t v[8][8];
      u32x4 ga[8];
#pragma unroll
      for (int ks = 0; ks < 8; ++ks) {
        ga[ks] = *(const u32x4*)(g + (lane & 15) * DCH + ks * 32 + 8 * kq);
#pragma unroll
        for (int e = 0; e < 8; ++e) v[ks][e] = *(const uint32_t*)(wg + (ks * 32 + 8 * kq + e) * DCH + c0);
      }
      PHASE();
      xtrans(u1, u0, lane, v0, F);
      f32x4 d[2] = {{0.f, 0.f, 0.f, 0.f}, {0.f, 0.f, 0.f, 0.f}};
#pragma unroll
      for (int ks = 0; ks < 8; ++ks)
#pragma unroll
        for (int e = 0; e < 2; ++e) {
          u32x4 bb;
#pragma unroll
          for (int j = 0; j < 4; ++j) bb[j] = __builtin_amdgcn_perm(v[ks][2 * j + 1], v[ks][2 * j], e ? 0x07060302u : 0x05040100u);
          d[e] = mfma16(ga[ks], bb, d[e]);
        }
      float* dst = ge + (((((blk >> 1) * 2 + (kq >> 1)) * 4 + 2 * (blk & 1) + ((lane >> 3) & 1)) * 2 + ((lane >> 1) & 1)) * 2 + ((lane >> 2) & 1)) * 32 + (kq & 1) * 16 + 2 * (lane & 1);
      f32x2 gs[4];
      bool far = false;
#pragma unroll
      for (int i = 0; i < 4; ++i) {
        gs[i] = f32x2{bf2f(f2bf(d[0][i])) * C2, bf2f(f2bf(d[1][i])) * C2};
        far = far || __builtin_fabsf(gs[i][0]) > EMAX || __builtin_fabsf(gs[i][1]) > EMAX;
      }
      far = __builtin_amdgcn_ballot_w64(far) != 0;
#pragma unroll
      for (int i = 0; i < 4; ++i) {
        *(f32x2*)(dst + 4 * i) = far ? f32x2{-1.f, -1.f} : f32x2{__builtin_amdgcn_exp2f(-gs[i][0]), __builtin_amdgcn_exp2f(-gs[i][1])};
        *(f32x2*)(dst + 4096 + 4 * i) = gs[i];
      }
    } else {
      PHASE();
      xtrans(u1, u0, lane, v0, F);
    }
  }
  asm volatile(".Lce:");
}

extern "C" void kernel_launch(void* const* d_in, const int* in_sizes, int n_in, void* d_out, int out_size, void* d_ws,
                              size_t ws_size, hipStream_t stream) {
  const uint16_t* x = (const uint16_t*)d_in[0];
  const uint16_t* w1 = (const uint16_t*)d_in[1];
  const uint16_t* w2 = (const uint16_t*)d_in[2];
  const uint16_t* g = (const uint16_t*)d_in[3];
  const uint16_t* wf = (const uint16_t*)d_in[4];
  const uint16_t* wg = (const uint16_t*)d_in[5];
  const uint16_t* wo = (const uint16_t*)d_in[6];
  uint16_t* out = (uint16_t*)d_out;
  char* ws = (char*)d_ws;
  (void)hipFuncSetAttribute((const void*)joint_kernel, hipFuncAttributeMaxDynamicSharedMemorySize, JOINT_LDS);

  pre_kernel<<<256, 512, 0, stream>>>(x, w1, w2, wf, wo, g, wg, ws + A_U1, ws + A_W1, ws + A_W2, (u32x4*)(ws + A_WF),
                                      (u32x4*)(ws + A_WF + 131072), (float*)(ws + A_GE), (uint32_t*)(ws + A_FL));
  joint_kernel<<<256, 256, JOINT_LDS, stream>>>(ws, out);
}
```

```cpp
#define PF1 7
#define DBG_DUMP
#include <hip/hip_runtime.h>
#include <stdint.h>

typedef __attribute__((ext_vector_type(8))) short bf16x8;
typedef __attribute__((ext_vector_type(16))) float f32x16;
typedef __attribute__((ext_vector_type(4))) float f32x4;
typedef __attribute__((ext_vector_type(2))) float f32x2;
typedef __attribute__((ext_vector_type(4))) unsigned int u32x4;
typedef __attribute__((ext_vector_type(2))) unsigned int u32x2;

#define ORD0 0x0132
#define ORD1 0x2310
#define GLA 6
#define TS0 22
#define TS1 55
#define TU0 22
#define TU1 55
#define THR 7
#define THR2 7
#define GPRO 2
constexpr int DCH = 256;

__device__ __forceinline__ float bf2f(uint32_t v) { return __uint_as_float(v << 16); }
__device__ __forceinline__ uint32_t f2bf(float f) {
  uint32_t u = __float_as_uint(f);
  u += 0x7FFF + ((u >> 16) & 1);
  return u >> 16;
}
__device__ __forceinline__ uint32_t pk2bf(float a, float b) {
  typedef __attribute__((ext_vector_type(2))) __bf16 b2;
  f32x2 v = {a, b};
  b2 r = __builtin_convertvector(v, b2);
  return __builtin_bit_cast(uint32_t, r);
}
constexpr double GS = 0.4686686932353139;
__device__ __forceinline__ uint32_t gelu2(float a0, float a1, float one = 1.f) {
  const f32x2 v = {a0, a1};
  constexpr float c = (float)(-2.302208198144325 / GS);
  const f32x2 z = v * __builtin_elementwise_fma(-v, v, f32x2{c, c});
  const f32x2 d = f32x2{__builtin_amdgcn_exp2f(z[0]), __builtin_amdgcn_exp2f(z[1])} + f32x2{one, one};
  const f32x2 y = v * f32x2{__builtin_amdgcn_rcpf(d[0]), __builtin_amdgcn_rcpf(d[1])};
  return pk2bf(y[0], y[1]);
}
__device__ __forceinline__ f32x16 mfma32(const u32x4& a, const u32x4& b, const f32x16& c) {
  return __builtin_amdgcn_mfma_f32_32x32x16_bf16(__builtin_bit_cast(bf16x8, a), __builtin_bit_cast(bf16x8, b), c, 0, 0, 0);
}
__device__ __forceinline__ f32x4 mfma16(const u32x4& a, const u32x4& b, const f32x4& c) {
  return __builtin_amdgcn_mfma_f32_16x16x32_bf16(__builtin_bit_cast(bf16x8, a), __builtin_bit_cast(bf16x8, b), c, 0, 0, 0);
}
__device__ __forceinline__ f32x16 zero16() {
  f32x16 z;
#pragma unroll
  for (int i = 0; i < 16; ++i) z[i] = 0.f;
  return z;
}
__device__ __forceinline__ __amdgpu_buffer_rsrc_t mkbuf(const void* p, int nbytes) {
  return __builtin_amdgcn_make_buffer_rsrc((void*)p, (short)0, nbytes, 0x00020000);
}
template <int V> struct IC { static constexpr int value = V; };
template <int I, int N, class F>
__device__ __forceinline__ void sfor(F&& f) {
  if constexpr (I < N) {
    f(IC<I>{});
    sfor<I + 1, N>(f);
  }
}
#define SB() __builtin_amdgcn_sched_barrier(0)
#define PHASE() do { SB(); asm volatile("s_waitcnt vmcnt(0)\n\ts_barrier" ::: "memory"); SB(); } while (0)
__device__ unsigned int g_st[5][4096][24];
#define ST_DECL unsigned int st_[24] = {}
#define STAMP(k) do { SB(); st_[k] = (unsigned int)__builtin_amdgcn_s_memrealtime(); SB(); } while (0)
#define XREAL(k) ((k) % 24 < 22)
#define CSTAMP(k) do { SB(); st_[k] = (unsigned int)__builtin_amdgcn_s_memtime(); SB(); } while (0)
#define ST_OUT(kid, wid) do { asm volatile("s_waitcnt vmcnt(0)" ::: "memory"); STAMP(21); CSTAMP(23); if ((threadIdx.x & 63) == 0) { _Pragma("unroll") for (int i_ = 0; i_ < 24; ++i_) g_st[kid][wid][i_] = st_[i_]; } } while (0)
#define XT_DECL unsigned int stv_ = 0, stp_ = 0; int stk_ = 63
#define XSTAMP(k) do { SB(); asm volatile("v_writelane_b32 %0, %1, %2" : "+v"(stv_) : "s"(stp_), "i"(stk_)); stp_ = XREAL(k) ? (unsigned int)__builtin_amdgcn_s_memrealtime() : (unsigned int)__builtin_amdgcn_s_memtime(); stk_ = k; SB(); } while (0)
#define XT_OUT(wid) do { asm volatile("s_waitcnt vmcnt(0)" ::: "memory"); XSTAMP(45); XSTAMP(47); XSTAMP(63); if ((threadIdx.x & 63) < 48) g_st[(threadIdx.x & 63) < 24 ? 1 : 3][wid][(threadIdx.x & 63) % 24] = stv_; } while (0)

constexpr double PI_ = 3.14159265358979323846;
constexpr double c128(int m) {
  m = ((m % 128) + 128) % 128;
  if (m > 64) m = 128 - m;
  double sg = 1.0;
  if (m > 32) { m = 64 - m; sg = -1.0; }
  if (m == 32) return 0.0;
  const double a = PI_ * m / 64.0;
  double term = 1.0, sum = 1.0;
  for (int i = 1; i < 16; ++i) { term *= -a * a / ((2 * i - 1) * (2 * i)); sum += term; }
  return sg * sum;
}
constexpr double s128(int m) { return c128(m - 32); }
constexpr uint16_t cbf(double v) {
  uint32_t u = __builtin_bit_cast(uint32_t, (float)v);
  u += 0x7FFF + ((u >> 16) & 1);
  return (uint16_t)(u >> 16);
}
constexpr double cbf2d(uint16_t v) { return (double)__builtin_bit_cast(float, (uint32_t)v << 16); }
struct Tabs {
  uint16_t f1[2][3][64][8];
  uint16_t e1[2][2][64][8];
  uint16_t e2[2][64][8];
  uint16_t g1[8][64][8];
  uint16_t f2[64][8];
  uint16_t g2[2][64][8];
};
constexpr Tabs make_tabs() {
  Tabs t{};
  for (int l = 0; l < 64; ++l)
    for (int e = 0; e < 8; ++e) {
      const int h = l >> 5, f = l & 31, ri = f & 1;
      for (int fb = 0; fb < 2; ++fb) {
        const int k = 16 * fb + (f >> 1);
        for (int ks = 0; ks < 3; ++ks) {
          const int a = (2 * k + 1) * (16 * ks + 8 * h + e);
          t.f1[fb][ks][l][e] = cbf(ri ? -s128(a) : c128(a));
        }
        const int a = (2 * k + 1) * (8 * h + e);
        const double v = ri ? s128(a) : c128(a);
        t.e1[fb][0][l][e] = cbf(v);
        t.e1[fb][1][l][e] = cbf(v - cbf2d(cbf(v)));
      }
      const int a = 2 * (2 * (f >> 1) + 1) * (8 * h + e);
      const double v = ri ? s128(a) : c128(a);
      t.e2[0][l][e] = cbf(v);
      t.e2[1][l][e] = cbf(v - cbf2d(cbf(v)));
      t.f2[l][e] = cbf((ri ? -v : v) / GS);
    }
  for (int m = -15; m < 48; ++m)
    for (int f = 0; f < 64; ++f) {
      const int a = (2 * (f >> 1) + 1) * m;
      t.g1[f >> 3][(m + 16) ^ (((m + 16) >> 4) & 1)][f & 7] = cbf(((f & 1) ? -s128(a) : c128(a)) * GS / 32.0);
    }
  for (int c = 0; c < 2; ++c)
    for (int l = 0; l < 64; ++l)
      for (int e = 0; e < 8; ++e) {
        const int m = 4 * (l & 3) + ((l >> 2) & 3) - (l & 16), a = 2 * (2 * (8 * c + 4 * (l >> 5) + (e >> 1)) + 1) * m;
        t.g2[c][l][e] = m > -16 ? cbf(((e & 1) ? -s128(a) : c128(a)) * GS / 16.0) : 0;
      }
  return t;
}
__device__ __attribute__((aligned(64))) const Tabs tabs = make_tabs();

constexpr int OOB = 0x7FFFFF00;
constexpr float C2 = 2.885390081777927f, C3 = (float)(2.885390081777927 / GS);
#define EMAXV 120.f
constexpr float EMAX = EMAXV;
constexpr int WT = 16;
constexpr int NT = 2;
constexpr int PL1 = 336 * 128;
#define PU2 0
#define PY2 0
constexpr int UB1 = 16 * PL1, UB2 = 524288 + PU2;
constexpr int U1BYTES = 32 * UB1, U2BYTES = 32 * UB2;
constexpr int WCB = 32768;
constexpr int WBIN = 16 * WCB;
constexpr int YP1 = 336 * 64;
constexpr int YB1 = 16 * YP1, YB2 = 524288 + PY2;
constexpr int Y1BYTES = 32 * YB1, Y2BYTES = 16 * YB2;
constexpr int F1 = 0, F2 = 2048, F3 = 10240, NFLAG = 18432 / 4;
constexpr int A_U1 = 0, A_W1 = 23 << 20, A_W2 = 40 << 20, A_Y1 = 49 << 20, A_U2 = 61 << 20, A_Y2 = 78 << 20, A_WF = 87 << 20,
              A_GE = A_WF + 262144, A_FL = 88 << 20;
__device__ __forceinline__ int dmap(int cb, int n) { return 64 * (cb >> 2) + 32 * (cb & 1) + 2 * n + ((cb >> 1) & 1); }

__device__ __forceinline__ void swap16(u32x4& a, u32x4& b) {
#pragma unroll
  for (int i = 0; i < 4; ++i) {
    const auto r = __builtin_amdgcn_permlane16_swap(a[i], b[i], false, false);
    a[i] = r[0];
    b[i] = r[1];
  }
}

__device__ __forceinline__ void st_pad(const u32x4& a, const u32x4& b) { asm volatile("s_nop 1" :: "v"(a), "v"(b) : "memory"); }

template <int N>
__device__ __forceinline__ void wait_vm() {
  constexpr int M = N < 63 ? N : 63;
  __builtin_amdgcn_s_waitcnt((M & 15) | ((M >> 4) << 14) | 0x0F70);
}

typedef __attribute__((address_space(3))) u32x4 lds_u32x4;
__device__ __forceinline__ u32x4 lds_ld(int a) { return *(const lds_u32x4*)(uintptr_t)(uint32_t)a; }
__device__ __forceinline__ uint32_t lds_ld32(int a) { return *(const __attribute__((address_space(3))) uint32_t*)(uintptr_t)(uint32_t)a; }
template <int SZ>
__device__ __forceinline__ void dma(__amdgpu_buffer_rsrc_t rsrc, char* lds, int voff, int soff) {
#if defined(__HIP_DEVICE_COMPILE__)
  typedef __attribute__((address_space(3))) void* ldsp;
  if constexpr (SZ == 16) __builtin_amdgcn_raw_ptr_buffer_load_lds(rsrc, (ldsp)lds, 16, voff, soff, 0, 0);
  else __builtin_amdgcn_raw_ptr_buffer_load_lds(rsrc, (ldsp)lds, 4, voff, soff, 0, 0);
#endif
}

#define LABEL_ADDR(p, name) asm volatile("s_getpc_b64 s[40:41]\n\ts_add_u32 s40, s40, " name "@rel32@lo+4\n\ts_addc_u32 s41, s41, " name "@rel32@hi+12\n\ts_mov_b64 %0, s[40:41]" : "=s"(p) :: "s40", "s41", "scc")
__device__ __forceinline__ void code_pf(const char* p, int n, int part, char* lds) {
  dma<4>(mkbuf(p, n), lds, part * 8192 + (threadIdx.x & 63) * 128, 0);
}

__device__ __forceinline__ void pad_macros() {
  asm volatile(R"(
.macro PAD1 l
.ifndef .Lw_\l
.Lw_\l:
.endif
\l:
s_cbranch_execz \l\()f
.endm
.macro PADM k, b, m
.ifndef .Lpg_\k
.set .Lpg_\k, -1
.endif
.ifndef .Lcn_\b
.set .Lcn_\b, 0
.endif
.if (((. - \k) & 4095) < 60) && (((. - \k) >> 12) > .Lpg_\k)
.set .Lpg_\k, (. - \k) >> 12
.irp c, 0, 1, 2, 3, 4, 5, 6, 7
.if (.Lcn_\b & \m) == \c
PAD1 \b\c
.endif
.endr
.set .Lcn_\b, .Lcn_\b + 1
.endif
.endm
.macro WALK b, w
s_mov_b64 exec, 0
.irp c, 0, 1, 2, 3, 4, 5, 6, 7
.ifdef .Lw_\b\c
s_cmp_eq_u32 \w, \c
s_cbranch_scc1 .Lw_\b\c
.endif
.endr
.irp c, 0, 1, 2, 3, 4, 5, 6, 7
\b\c:
.endr
s_mov_b64 exec, -1
.endm
)");
}
#define PAD(k, b, m) asm volatile("PADM " #k ", " #b ", " #m)
#define WALK(b, w) asm volatile("WALK " #b ", %0" :: "s"(w) : "scc")
#define KERNEL extern "C" __global__ __attribute__((aligned(4096)))

constexpr int J_T = 0;
constexpr int J_G = 131072;
constexpr int J_F = J_G + 16384;
constexpr int J_R = J_F + 8192;
constexpr int JOINT_LDS = J_R + 1024;
__device__ __forceinline__ void lds_st(int a, const u32x4& v) { *(lds_u32x4*)(uintptr_t)(uint32_t)a = v; }
#define BAR() do { SB(); asm volatile("s_waitcnt lgkmcnt(0)\n\ts_barrier" ::: "memory"); SB(); } while (0)
#define DRAIN() do { SB(); wait_vm<0>(); asm volatile("s_waitcnt vmcnt(0)\n\ts_barrier" ::: "memory"); SB(); } while (0)
template <int M, bool Z>
__device__ __forceinline__ void mm(f32x16& c, const u32x4& a, const u32x4& b) {
  if constexpr (Z) {
    if constexpr (M == 0) asm volatile("v_mfma_f32_32x32x16_bf16 %0, %1, %2, 0" : "=&v"(c) : "v"(a), "v"(b));
    else asm volatile("v_mfma_f32_32x32x16_bf16 %0, %1, %2, 0" : "=&v"(c) : "v"(a), "a"(b));
  } else {
    if constexpr (M == 0) asm volatile("v_mfma_f32_32x32x16_bf16 %0, %1, %2, %0" : "+v"(c) : "v"(a), "v"(b));
    else asm volatile("v_mfma_f32_32x32x16_bf16 %0, %1, %2, %0" : "+v"(c) : "v"(a), "a"(b));
  }
}
template <int M, bool Z>
__device__ __forceinline__ void mm(f32x4& c, const u32x4& a, const u32x4& b) {
  if constexpr (Z) {
    if constexpr (M == 0) asm volatile("v_mfma_f32_16x16x32_bf16 %0, %1, %2, 0" : "=&v"(c) : "v"(a), "v"(b));
    else asm volatile("v_mfma_f32_16x16x32_bf16 %0, %1, %2, 0" : "=&v"(c) : "a"(a), "v"(b));
  } else {
    if constexpr (M == 0) asm volatile("v_mfma_f32_16x16x32_bf16 %0, %1, %2, %0" : "+v"(c) : "v"(a), "v"(b));
    else asm volatile("v_mfma_f32_16x16x32_bf16 %0, %1, %2, %0" : "+v"(c) : "a"(a), "v"(b));
  }
}
#define MWAIT(c0, c1) asm volatile("s_nop 7\n\ts_nop 3" : "+v"(c0), "+v"(c1))
template <int K>
__device__ __forceinline__ float add_quad(float a, float b) {
  float r;
  asm volatile("v_add_f32_dpp %0, %1, %2 quad_perm:[%3,%3,%3,%3] row_mask:0xf bank_mask:0xf" : "=v"(r) : "v"(a), "v"(b), "n"(K));
  return r;
}
__device__ __forceinline__ uint32_t neg2(uint32_t v) {
  uint32_t r;
  asm volatile("v_xor_b32 %0, 0x80008000, %1" : "=v"(r) : "v"(v));
  return r;
}
__device__ __forceinline__ void keep_a(const u32x4& v) { asm volatile("" :: "a"(v)); }
__device__ __forceinline__ void keep_v(const u32x4& v) { asm volatile("" :: "v"(v)); }
__device__ __forceinline__ int pin(int v) { asm volatile("" : "+v"(v)); return v; }
__device__ __forceinline__ void await(uint32_t v, __amdgpu_buffer_rsrc_t r, int vo, int so) {
  uint32_t cnt;
  SB();
  asm volatile("v_cmp_gt_u32 vcc, 1, %0\n\ts_cbranch_vccz 2f\n\ts_mov_b32 %1, 0x40000\n"
               "1:\n\ts_sleep 1\n\tbuffer_load_dword %0, %2, %3, %4 offen sc1\n\ts_waitcnt vmcnt(0)\n\t"
               "v_cmp_gt_u32 vcc, 1, %0\n\ts_cbranch_vccz 2f\n\ts_sub_u32 %1, %1, 1\n\ts_cbranch_scc0 1b\n2:"
               : "+v"(v), "=&s"(cnt) : "v"(vo), "s"(r), "s"(so) : "vcc", "scc", "memory");
  SB();
}

#define XST
#define OPOL NT
#define ORD2 0x0132
#define ORD3 0x2310
#define KCY 84
#define KCAP 200
#define KLD 60
#define KLA 40
#define KD1 112
#define KQ1 14
#define KPI1 130
#define KPC1 140
#define KYR 3
#define KT0 160
#define NB0 14
#define NB1 16
#define NX0 16
#define KX0 8
#define KP1 1
#define KXL 40
#define KXU 20
#define KJM 48
#define KJ0 56
#define KSL 0
#define KCY2 72
#define KQJ 99
#define KCJ 68
#define TS2 75
#define THM 4
#define THL -1
#define KS5 31
#define KD2 16
#define KPI2 36
#define KPC2 46
#define KXB 0
#define KD3 82
#define KQ3 16
#define KPI3 104
#define KPC3 114
#define KJR 5
#define KPI4 4
#define KPC4 34
enum { O_NONE, O_W, O_A, O_B, O_T, O_F, O_V, O_GI, O_Q, O_J, O_S1, O_Y1, O_X, O_S2, O_Y2, O_SIG, O_POLL, O_U };
constexpr int fill_t(int l) { return l < 2 ? O_T : l < 3 ? O_F : l < 35 ? O_V : l < 37 ? O_GI : l < 41 ? O_Q : O_J; }
constexpr int fill_j(int l) { return l < 2 ? l : l < 35 ? l - 3 : l < 37 ? l - 35 : l < 41 ? l - 37 : l - 41; }
constexpr int first(int c) { return c < 16 ? 6 * c : c < 32 ? 16 + 5 * c : 999; }
constexpr int chunk(int g) { return g < 96 ? g / 6 : (g - 16) / 5; }
constexpr int cpos(int c) { return c < 16 ? (c + 8) % 12 * 12288 : (c - 16) * 9216; }
struct Sched {
  int ty[3 * 184], ix[3 * 184];
  int before[3 * 184];
  int last[32];
  int tt[16], ti[16];
  int nl, nj;
};
constexpr int main_t(int m) { return m >= 80 ? O_B : m % 5 < 2 ? O_W : O_A; }
constexpr int main_i(int m) { return m >= 80 ? m - 80 : m % 5 < 2 ? m / 5 * 2 + m % 5 : m / 5 * 3 + m % 5 - 2; }
constexpr int main_c(int m) { return m >= 80 ? 16 + (m - 80) / 9 * 4 + ((m - 80) % 9 - 1) / 2 : m / 5; }
constexpr int vfree(int l) { return first(18 + (8 * ((l - 3) >> 1) + 7) / 9) - 3; }
constexpr int hk(int i) { return i < 16 ? i : 4 * i - 48; }
constexpr Sched make_s1() {
  Sched c{};
  int m = 5 * GPRO, l = 0, o = m, y = 0, cr = KCY;
  for (int i = 0; i < GPRO; ++i) c.last[i] = 5 * i + 5;
  for (int t = 0; t < 3 * 184; ++t) {
    const int g = t / 3, j = t % 3;
    const bool thr = g >= TS0 && g < TS1;
    c.before[t] = o;
    if (j == 0) cr += g < TS0 ? 64 : 68 + 4 * (thr ? THR : g < TS2 ? THM - 2 : THL);
    if (cr > KCAP) cr = KCAP;
    if (j == 1 && ((g >= 93 && g < 99) || (g >= 174 && g < 179))) { c.ty[t] = O_S1; c.ix[t] = g < 99 ? g - 93 : g - 168; cr -= KCY; }
    else if (t == 3 * KD1 + 2) { c.ty[t] = O_SIG; c.ix[t] = 0; }
    else if (t == 3 * KPI1 + 2) { c.ty[t] = O_POLL; c.ix[t] = 0; }
    else if (g >= KT0) {
      if (t == 3 * KT0) {
        if (y < 48) return c;
        for (int i = 0; i < NB0; ++i) {
          const bool v = l < 35 && g > vfree(l);
          c.tt[i] = v ? O_V : O_J;
          c.ti[i] = v ? l++ - 3 : c.nj++;
        }
      }
      if (j == 1 && g < KT0 + 4) { c.ty[t] = O_U; c.ix[t] = 4 + g - KT0; }
    } else if (g >= KPC1 && y < 48) {
      if (m < 116) return c;
      if (j >= 3 - KYR) { c.ty[t] = O_Y1; c.ix[t] = y++; }
    }
    else if (g > KD1 - KQ1 && g <= KD1) { if (j == 1 && g <= KD1 - KQJ) { c.ty[t] = O_J; c.ix[t] = c.nj++; } }
    else if ((j == 1 || (j == 2 && thr)) && cr >= KCY) {
      const int k = main_c(m), fr = k < 12 ? 0 : first(k < 16 ? k - 10 : 6 + (9 * (k - ((m - 80) % 9 ? 16 : 13)) + 8) / 12) - 3;
      const bool fo = l < 35 && m >= 80 && (l < 3 || g >= vfree(l));
      if (m < 116 && g >= fr && first(k) - g <= (k < 16 ? KLA : KLD) && (m < 80 || l >= 3)) {
        c.ty[t] = main_t(m); c.ix[t] = main_i(m);
        cr -= g < TS0 ? 64 : KCY;
        c.last[k] = o + 1;
        ++m;
      } else if (fo) {
        c.ty[t] = fill_t(l); c.ix[t] = fill_j(l);
        cr -= KCY;
        ++l;
      } else if (g >= KJ0 && c.nj < KJM) {
        c.ty[t] = O_J; c.ix[t] = c.nj++;
        cr -= KCJ;
      }
    }
    if (c.ty[t]) ++o;
  }
  c.nl = l;
  return c;
}
constexpr Sched S1 = make_s1();
constexpr int NFILL = 3 + 32 + 2 + 4 + 64 - S1.nj;
constexpr int fill_i(int l) { return fill_j(l) + (l < 41 ? 0 : S1.nj); }
constexpr int NL1 = S1.nl + NB1;
static_assert(S1.nl > 2 && NL1 >= 35 && NL1 <= NFILL && KT0 < 170 && first(chunk(KD1 + 4)) == KD1 + 4, "schedule");
constexpr Sched make_s2() {
  Sched c{};
  int l = NL1, x = NX0, y = 0, cr = 0;
  for (int t = 0; t < 3 * 136; ++t) {
    const int g = t / 3, j = t % 3;
    const bool thr = g >= TU0 && g < TU1, fs = j == 1 || (j == 2 && thr);
    if (j == 0) cr += 68 + 4 * (thr ? THR2 : THL);
    if (cr > KCAP) cr = KCAP;
    if (j == 1 && (g & 1) && ((g > 60 && g < 68) || (g > 124 && g < 132))) { c.ty[t] = O_S2; c.ix[t] = g < 68 ? (g - 61) / 2 : (g - 117) / 2; cr -= KCY2; }
    else if (t == 3 * KD2 + 2) { c.ty[t] = O_SIG; c.ix[t] = 3; }
    else if (t == 3 * KPI2 + 2) { c.ty[t] = O_POLL; c.ix[t] = 3; }
    else if (t == 3 * KD3 + 2) { c.ty[t] = O_SIG; c.ix[t] = 4; }
    else if (t == 3 * KPI3 + 2) { c.ty[t] = O_POLL; c.ix[t] = 4; }
    else if (g >= KPC3) { if (y < 32) { c.ty[t] = O_Y2; c.ix[t] = y++; } }
    else if (g <= KD2 || (g > KD3 - KQ3 && g <= KD3)) {
      if (j == 1 && g <= (g > KD2 ? KD3 : KD2) - KQJ && l >= 41 && l < NFILL) { c.ty[t] = O_J; c.ix[t] = fill_i(l++); }
    } else if (x < (g >= KPC2 ? 64 : 32) && ((j && x >= 32 && x < 32 + KXB) || (j == 1 && 2 * x - g <= KXU) || (fs && cr >= KCY2 && 2 * x - g <= KXL))) {
      c.ty[t] = O_X; c.ix[t] = x++;
      cr -= KCY2;
      if (cr < 0) cr = 0;
    } else if (fs && cr >= KCY2 && l < NFILL) {
      c.ty[t] = fill_t(l); c.ix[t] = fill_i(l);
      cr -= KCJ;
      ++l;
    }
    if (t == 3 * KD3 && l < 41) return c;
    if (c.ty[t] == O_X && g >= (2 * c.ix[t] & ~7) - 1) return c;
  }
  c.nl = x == 64 && y == 32 ? l : -1;
  return c;
}
constexpr Sched S2 = make_s2();
static_assert(S2.nl + S1.nj >= 41 + 32 && S2.nl + (87 - KS5) * KJR / 8 >= NFILL, "schedule");

#define PADG() PAD(joint_kernel, 7, 3)
#define PADJ() PADG()
KERNEL __launch_bounds__(256, 1) void joint_kernel(char* ws, uint16_t* __restrict__ out) {
  extern __shared__ __attribute__((aligned(16))) char smem[];
  pad_macros();
  PADG();
  SB();
  XT_DECL; XSTAMP(0); XSTAMP(22);
  const int bid = blockIdx.x, xcd = bid & 7, slot = bid >> 3;
  const int tid = threadIdx.x, lane = tid & 63;
  const int w = __builtin_amdgcn_readfirstlane(tid >> 6);
  const int cb = (slot & 3) * 4 + w;
  const int bin1 = xcd * 4 + (slot >> 3), mt1 = (slot >> 2) & 1;
  const int bin2 = xcd * 2 + (slot >> 4);
  const __amdgpu_buffer_rsrc_t ur = mkbuf(ws + (unsigned)(A_U1 + bin1 * UB1 + mt1 * (168 * 128)), UB1);
  const __amdgpu_buffer_rsrc_t wr = mkbuf(ws + (unsigned)(A_W1 + bin1 * WBIN + cb * WCB), WCB);
  const int vd = w * 1024 + (lane >> 3) * 128 + (((lane & 7) ^ ((4 * w + (lane >> 4)) & 7)) * 16);
  const int rho = lane & 15, kq = lane >> 4;
  const int n = lane & 31, h = lane >> 5;
  const float* ge = (const float*)(ws + A_GE);
  __amdgpu_buffer_rsrc_t vr, ys, yl, ub, yb, wb, fr;
  u32x4 Wm[32], X[64], wt[64], y1[3][4], yv[2][4][2], F, gi[2], o[11], up[8];
  int ve, vt, v1[3], vx, vs, vy, vu, vf[3], vp[3];
  uint32_t pl[2];
  constexpr int FO[6] = {F1, F1 + 1024, F2, F2 + 4096, F3, F3 + 4096};
  auto yload = [&](int b, int i) { yv[b][i & 3][i >> 4][(i >> 2) & 3] = __builtin_amdgcn_raw_buffer_load_b32(yb, vy + (i & 3) * 1024, ((i >> 4) * 8 + ((i >> 2) & 3)) * YB2 + b * 65536, WT); };
  auto issue = [&](auto it, auto ii) {
    constexpr int t = decltype(it)::value, i = decltype(ii)::value;
    if constexpr (t == O_W) Wm[i] = __builtin_amdgcn_raw_buffer_load_b128(wr, lane * 16 + (i & 3) * 1024, (i >> 2) * 4096, 0);
    else if constexpr (t == O_A) dma<16>(ur, smem + cpos(i / 3) + i % 3 * 4096 + w * 1024, vd, i / 3 * PL1 + i % 3 * 4096);
    else if constexpr (t == O_B && i % 9) dma<16>(ur, smem + cpos(main_c(80 + i)) + (i % 9 - 1 & 1) * 4096 + w * 1024, vd, (main_c(80 + i) - 16) * PL1 + (3 + (i % 9 - 1 & 1)) * 4096);
    else if constexpr (t == O_B) dma<16>(ur, smem + cpos(main_c(80 + i)) + 8192 + w * 9216, ve, i / 9 * 4 * PL1);
    else if constexpr (t == O_T) dma<16>(mkbuf(&tabs.g1[2 * w][0][0], 2048), smem + J_F + w * 2048 + i * 1024, lane * 16, i * 1024);
    else if constexpr (t == O_F) F = ((const u32x4*)&tabs.f2[0][0])[lane];
    else if constexpr (t == O_V) dma<16>(vr, smem + (i >> 1) * 8192 + w * 2048 + (i & 1) * 1024, lane * 16, i * 1024);
    else if constexpr (t == O_GI) gi[i] = ((const u32x4*)&tabs.g2[i][0][0])[lane];
    else if constexpr (t == O_Q) dma<16>(mkbuf(ge + w * 1024, 4096), smem + J_G + w * 4096 + i * 1024, lane * 16, i * 1024);
    else if constexpr (t == O_J) wt[i] = __builtin_amdgcn_raw_buffer_load_b128(wb, lane * 16 + (i & 3) * 1024, (i >> 5) * 131072 + ((i >> 2) & 7) * 4096, 0);
    else if constexpr (t == O_S1) __builtin_amdgcn_raw_buffer_store_b128(o[i], ys, vt + (i & 3) * 1024, (i >> 2) * 4096, WT);
    else if constexpr (t == O_Y1) y1[i % 3][i % 48 / 12][i / 3 & 3] = __builtin_amdgcn_raw_buffer_load_b32(yl, v1[i % 3], (i % 48 / 12 * 8 + (i / 3 & 3)) * YB1 + i / 48 * (84 * 64), WT);
    else if constexpr (t == O_X) X[i] = __builtin_amdgcn_raw_buffer_load_b128(ub, vx + (i & 3) * 1024, (i >> 5) * 16 * UB2 + ((i >> 2) & 7) * 4096, WT);
    else if constexpr (t == O_S2) __builtin_amdgcn_raw_buffer_store_b128(o[i & 3], yb, vs + (i & 3) * 1024, (i >> 2) * 65536, WT);
    else if constexpr (t == O_Y2) yload(0, i);
    else if constexpr (t == O_SIG) __builtin_amdgcn_raw_buffer_store_b32(1u, fr, vf[i >> 1], FO[i], WT);
    else if constexpr (t == O_POLL) pl[i & 1] = __builtin_amdgcn_raw_buffer_load_b32(fr, vp[i >> 1], FO[i], WT);
    else if constexpr (t == O_U) __builtin_amdgcn_raw_buffer_store_b128(up[i & 7], ub, vu + ((i >> 1) & 3) * 256, ((i >> 3) * 16 + (i & 1) * 8) * UB2, WT);
    SB();
  };
  auto fills = [&](auto i0, auto i1) {
    sfor<decltype(i0)::value, decltype(i1)::value>([&](auto il) { issue(IC<fill_t(decltype(il)::value)>{}, IC<fill_i(decltype(il)::value)>{}); });
  };
  sfor<0, 5 * GPRO>([&](auto il) { issue(IC<main_t(decltype(il)::value)>{}, IC<main_i(decltype(il)::value)>{}); });
  vr = mkbuf(ws + (unsigned)(A_W2 + bin2 * WBIN + cb * WCB), WCB);
  ys = mkbuf(ws + (unsigned)(A_Y1 + bin1 * YB1 + cb * YP1 + mt1 * (168 * 64)), 168 * 64);
  yl = mkbuf(ws + A_Y1, Y1BYTES);
  ub = mkbuf(ws + A_U2, U2BYTES);
  yb = mkbuf(ws + A_Y2, Y2BYTES);
  wb = mkbuf(ws + (unsigned)(A_WF + w * 32768), 131072 + 32768);
  fr = mkbuf(ws + A_FL, NFLAG * 4);
  const int l0 = (int)(uint32_t)(uintptr_t)(__attribute__((address_space(3))) char*)smem;
  int ab[3][2], ga[4];
  float ok[2];
#pragma unroll
  for (int ri = 0; ri < 2; ++ri)
#pragma unroll
    for (int wn = 0; wn < 3; ++wn) ab[wn][ri] = pin(l0 + rho * 128 + ((((kq >> 1) * 4 + ri * 2 + (kq & 1)) ^ (rho >> 1)) * 16) + wn * 65536);
  ve = pin(20480 + w * PL1 + (lane >> 3) * 128 + (((lane & 7) ^ kq) * 16));
  vt = pin(rho * 64 + kq * 16);
  {
    const int j = 4 * (slot & 15) + w, ty = j % 3, a = j / 3 * 4 + (ty ? ty : -1);
    const int tau = 16 * ((n >> 2) & 1) + 8 * ((n >> 3) & 1) + 2 * (n & 3) + (n >> 4);
#pragma unroll
    for (int i = 0; i < 3; ++i) {
      const int g = a + i + 84 * ((n >> 2) & 1), e = g >= 84, r = g - 84 * e;
      const int v = pin(4 * h * YB1 + bin2 * YP1 + (84 * (r >= 42) + 2 * r + e) * 64 + (n >> 3) * 16 + (n & 3) * 4);
      v1[i] = pin(g < (j < 63 && i != 2 * ty ? 168 : -2) ? v : OOB);
    }
#pragma unroll
    for (int i = 0; i < 4; ++i) {
      const int r = tau + 16 + (ty ? 16 * ty : 48) - 16 * (i >> 1) - 48 * (i & 1);
      ga[i] = pin(l0 + J_F + h * 1024 + ((unsigned)r < 64u ? r ^ ((r >> 4) & 1) : 0) * 16);
    }
#pragma unroll
    for (int i = 0; i < 2; ++i) ok[i] = __builtin_bit_cast(float, pin((unsigned)(32 * (2 * j + i + 126 * ((n >> 2) & 1)) - 16 + 16 * h) < 8000u ? 0x3F800000 : 0x7F800000));
    const int ge_ = a + (h ? 2 - (ty == 1) : ty == 0);
    vp[0] = pin((bin2 >> 2) * 256 + (ge_ >= 42 && ge_ < 84) * 128 + n * 4);
    vf[0] = pin((slot & 3) * 256 + mt1 * 128 + bin1 * 4 + tid * 65536);
    vf[1] = vf[2] = pin((slot & 15) * 256 + bin2 * 4 + tid * 65536);
    vu = pin((rho >> 1) * UB2 + (slot & 15) * 32768 + bin2 * 2048 + (w >> 1) * 1024 + (w & 1) * 128 + (lane & 1) * 64 + kq * 16);
  }
  auto aread = [&](int g, int ri) -> u32x4 {
    const int a = cpos(chunk(g)) + (g - first(chunk(g))) * 2048;
    return lds_ld(ab[a >> 16][ri] + (a & 65535));
  };
  f32x4 yr[11], yi[11];
  u32x4 A[4][2], nw[2], gf[16];
  auto gread = [&](int i) { gf[i] = lds_ld(ga[i >> 2] + (i & 3) * 2048); };
  auto trans = [&](auto ib, auto&& hook) {
    constexpr int b = decltype(ib)::value;
    f32x16 d[2], u[2][2];
    u32x4 a;
    sfor<0, 16>([&](auto ii) {
      constexpr int i = decltype(ii)::value;
      mm<0, (i & 7) == 0>(d[i >> 3], gf[i], y1[(i >> 3) + ((i >> 2) & 1)][i & 3]);
      PADG();
      if constexpr (i + 6 < 16) gread(i + 6);
      SB();
      hook(ii);
    });
    auto pack = [&](int q) {
#pragma unroll
      for (int e = 0; e < 2; ++e) {
#pragma unroll
        for (int z = 0; z < 2; ++z)
#pragma unroll
          for (int j = 0; j < 4; ++j) up[4 * q + 2 * e + z][j] = pk2bf(u[q][z][8 * e + 2 * j], u[q][z][8 * e + 2 * j + 1]);
        PADG();
        swap16(up[4 * q + 2 * e], up[4 * q + 2 * e + 1]);
        PADG();
      }
      SB();
    };
    auto put = [&](auto iq) {
      constexpr int q = decltype(iq)::value;
      sfor<8 * b + 4 * q, 8 * b + 4 * q + 4>([&](auto ii) { issue(IC<O_U>{}, ii); });
      PADG();
      st_pad(up[4 * q + 2], up[4 * q + 3]);
      SB();
    };
    sfor<0, 4>([&](auto ii) {
      constexpr int i = decltype(ii)::value, q = i >> 1, z = i & 1;
      if constexpr (i < 2) asm volatile("s_nop 2" : "+v"(d[z]));
#pragma unroll
      for (int j = 0; j < 2; ++j) { a[j] = gelu2(d[z][8 * q + 2 * j], d[z][8 * q + 2 * j + 1], ok[z]); PADG(); }
      SB();
      hook(IC<16 + 2 * i>{});
#pragma unroll
      for (int j = 2; j < 4; ++j) { a[j] = gelu2(d[z][8 * q + 2 * j], d[z][8 * q + 2 * j + 1], ok[z]); PADG(); }
      asm volatile("s_nop 1" : "+v"(a));
      SB();
      mm<0, true>(u[q][z], a, F);
      SB();
      hook(IC<17 + 2 * i>{});
      if constexpr (i == 2) { pack(0); if constexpr (!b || KP1) put(IC<0>{}); }
    });
    MWAIT(u[1][0], u[1][1]);
    pack(1);
    if constexpr (b && !KP1) put(IC<0>{});
    if constexpr (b) put(IC<1>{});
  };
  XSTAMP(1);
  SB();
  wait_vm<5 * GPRO - 5>();
  asm volatile("s_barrier" ::: "memory");
  XSTAMP(2);
#pragma unroll
  for (int z = 0; z < 3; ++z) { A[z][1] = aread(z, 1); SB(); A[z][0] = aread(z, 0); SB(); }
#pragma unroll
  for (int j = 0; j < 4; ++j) nw[0][j] = neg2(Wm[1][j]);
  SB();
  sfor<0, 179>([&](auto ig) {
    constexpr int g = decltype(ig)::value, c = chunk(g), ks = c & 15, mb = g - first(c) + (c < 16 ? 0 : 6), e = first(c + 1) - g;
    if constexpr (g == KPC1) { XSTAMP(8); await(pl[0], fr, vp[0], FO[0]); XSTAMP(9); }
    if constexpr (g == KT0) {
      XSTAMP(12);
      trans(IC<0>{}, [&](auto ii) {
        constexpr int i = decltype(ii)::value;
        if constexpr (i == 15) XSTAMP(13);
        if constexpr (i < 22) sfor<NB0 * hk(i) / hk(22), NB0 * hk(i + 1) / hk(22)>([&](auto il) { issue(IC<S1.tt[decltype(il)::value]>{}, IC<S1.ti[decltype(il)::value]>{}); });
      });
      XSTAMP(14);
    }
    auto mf = [&](auto ij) {
      constexpr int j = decltype(ij)::value, i = (((g & 1) ? ORD1 : ORD0) >> (12 - 4 * j)) & 3;
      constexpr bool z = ks == 0 && !(j & 1);
      if constexpr (g < 176) {
        if constexpr (i == 0) mm<1, z>(yr[mb], Wm[2 * ks], A[g & 3][0]);
        else if constexpr (i == 1) mm<0, z>(yr[mb], nw[c & 1], A[g & 3][1]);
        else if constexpr (i == 2) mm<0, z>(yi[mb], Wm[2 * ks + 1], A[g & 3][0]);
        else mm<1, z>(yi[mb], Wm[2 * ks], A[g & 3][1]);
      }
      PADG();
    };
    auto cvt = [&](int j) {
      constexpr int m = g >= 92 && g < 98 ? g - 92 : g >= 173 && g < 178 ? g - 167 : -1;
      if constexpr (m >= 0) {
        if constexpr (g == 177) if (!j) asm volatile("s_nop 1" : "+v"(yr[m]), "+v"(yi[m]));
#pragma unroll
        for (int i = 2 * j; i < 2 * j + 2; ++i) o[m][i] = pk2bf(yr[m][i], yi[m][i]);
      }
      if constexpr (S1.ty[3 * g + 1] == O_S1) if (j) keep_v(o[S1.ix[3 * g + 1]]);
      if constexpr (S1.ty[3 * g + 1] == O_U) if (j) keep_v(up[S1.ix[3 * g + 1]]);
      if constexpr (g >= KT0 - 3 && g < KT0) gread(2 * (g - KT0 + 3) + j);
      SB();
    };
    if constexpr (g < 176 && !(g & 1)) keep_v(A[(g + 1) & 3][0]);
    mf(IC<0>{});
    if constexpr (g + 3 < 176) { A[(g + 3) & 3][1] = aread(g + 3, 1); SB(); A[(g + 3) & 3][0] = aread(g + 3, 0); }
    SB();
    mf(IC<1>{});
    issue(IC<S1.ty[3 * g]>{}, IC<S1.ix[3 * g]>{});
    cvt(0);
    mf(IC<2>{});
    issue(IC<S1.ty[3 * g + 1]>{}, IC<S1.ix[3 * g + 1]>{});
    mf(IC<3>{});
    if constexpr (e == 4) {
      static_assert(S1.last[c + 1] && S1.before[3 * g + 2] >= S1.last[c + 1], "next chunk not requested yet");
      SB();
      if constexpr (g == KD1) { XSTAMP(6); DRAIN(); XSTAMP(7); }
      else {
        if constexpr (g < KD1 || (g < KT0 && S1.last[c + 1] > S1.before[3 * KD1 + 2])) wait_vm<S1.before[3 * g + 2] - S1.last[c + 1]>();
        asm volatile("s_barrier" ::: "memory");
      }
      SB();
    }
    issue(IC<S1.ty[3 * g + 2]>{}, IC<S1.ix[3 * g + 2]>{});
    if constexpr (g >= TS0 && g < TS1) asm volatile("s_nop %0" ::"n"(S1.ty[3 * g + 2] ? THR - 3 : THR));
    else if constexpr (g >= TS1 && g < TS2) asm volatile("s_nop %0" ::"n"(THM));
    else if constexpr (THL >= 0 && g >= TS1 && g < 173 && !S1.ty[3 * g + 2]) asm volatile("s_nop %0" ::"n"(THL + 2));
    if constexpr (e <= 3 && e >= 2) {
#pragma unroll
      for (int j = 6 - 2 * e; j < 8 - 2 * e; ++j) nw[(c + 1) & 1][j] = neg2(Wm[(2 * ks + 3) & 31][j]);
    }
    SB();
    cvt(1);
    if constexpr (g == 11) XSTAMP(3);
    if constexpr (g == 53) XSTAMP(4);
    if constexpr (g == 95) XSTAMP(5);
    if constexpr (g == 175) XSTAMP(10);
  });
  st_pad(o[10], o[10]);
  XSTAMP(11);
  const int pos = 32 * xcd + slot, seg = pos < 250 ? pos : 0;
  int wa[2], ra[4], fa[4], qa, vo;
  {
    const int grp = (slot & 12) + w, s = seg + (kq & 1), e = s >= 126, r = s - 126 * e;
        vx = pin(bin2 * UB2 + grp * 32768 + lane * 16);
    vs = pin(bin2 * YB2 + (slot & 3) * 131072 + grp * 4096 + ((rho & 2) * 4 + (rho & 4) + (rho >> 3) * 2 + (rho & 1)) * 64 + kq * 16);
    vy = pin(4 * h * YB2 + w * 131072 + (r >> 3) * 4096 + (8 * e + (r & 7)) * 64 + rho * 4);
    vp[1] = pin(grp * 256 + rho * 4);
    vp[2] = pin(((r >> 5) * 4 + w) * 256 + rho * 4);
#pragma unroll
    for (int i = 0; i < 2; ++i) wa[i] = pin(l0 + lane * 16 + i * 65536);
  }
  XSTAMP(17);
  DRAIN();
  issue(IC<O_SIG>{}, IC<1>{});
  issue(IC<O_SIG>{}, IC<2>{});
  XSTAMP(21); XSTAMP(24); XSTAMP(23); XSTAMP(46);
  fills(IC<S1.nl>{}, IC<NL1>{});
  if constexpr (KSL) __builtin_amdgcn_s_sleep(KSL);
  issue(IC<O_POLL>{}, IC<1>{});
  issue(IC<O_POLL>{}, IC<2>{});
#pragma unroll
  for (int i = 0; i < 6; ++i) gread(i);
  XSTAMP(25);
  await(pl[1], fr, vp[0], FO[1]);
  await(pl[0], fr, vp[1], FO[2]);
  XSTAMP(26);
  sfor<0, 48>([&](auto ii) { issue(IC<O_Y1>{}, IC<48 + decltype(ii)::value % 16 * 3 + decltype(ii)::value / 16>{}); PADJ(); });
  auto wread = [&](int g, int j) -> u32x4 { return lds_ld(wa[(g >> 5) & 1] + ((g >> 3 & 3) * 16 + (g & 7) + j * 8) * 1024); };
  trans(IC<1>{}, [&](auto ii) {
    constexpr int i = decltype(ii)::value, e = KP1 ? 22 : 24;
    if constexpr (i == 0) XSTAMP(27);
    if constexpr (i == 15) XSTAMP(28);
    if constexpr (i >= KX0 && i < e) sfor<NX0 * (hk(i) - KX0) / (hk(e) - KX0), NX0 * (hk(i + 1) - KX0) / (hk(e) - KX0)>([&](auto ix) { issue(IC<O_X>{}, ix); PADJ(); });
    if constexpr (i == 19) asm volatile("s_barrier" ::: "memory");
    if constexpr (i == 23) {
#pragma unroll
      for (int z = 0; z < 3; ++z) { A[z][1] = wread(z, 1); SB(); A[z][0] = wread(z, 0); SB(); }
    }
  });
  XSTAMP(29);
  f32x4 ac[16][2];
  float re, im;
  sfor<0, 132>([&](auto ig) {
    constexpr int g = decltype(ig)::value, k = g >> 3, mb = (g & 7) + (g & 64) / 8;
    if constexpr (g == KPC2) { XSTAMP(32); await(pl[1], fr, vp[1], FO[3]); XSTAMP(33); }
    if constexpr (g == KPC3) { XSTAMP(36); await(pl[0], fr, vp[2], FO[4]); XSTAMP(37); }
    auto mf = [&](auto ij) {
      constexpr int j = decltype(ij)::value, i = (((g & 1) ? ORD3 : ORD2) >> (12 - 4 * j)) & 3;
      if constexpr (g < 128) mm<0, (k & 7) == 0 && !(j & 1)>(ac[mb][i >> 1], A[g & 3][i & 1], X[4 * k + 2 * (i & 1) + (i >> 1)]);
      PADJ();
      if constexpr (g == 0 && j == 0) XSTAMP(30);
    };
    auto cvt = [&](int j) {
      constexpr int m = g >= 59 && g < 67 ? g - 59 : g >= 123 && g < 131 ? g - 115 : -1;
      if constexpr (m >= 0) {
        const int i = 2 * (m & 1) + j;
        if constexpr (g == 129) if (!j) asm volatile("s_nop 1" : "+v"(ac[15][0]), "+v"(ac[15][1]));
        asm volatile("v_subrev_f32_dpp %0, %4, %2 row_shl:4 row_mask:0xf bank_mask:0x5\n\t"
                     "v_add_f32_dpp %1, %2, %4 row_shl:4 row_mask:0xf bank_mask:0x5\n\t"
                     "v_sub_f32_dpp %0, %3, %5 row_shr:4 row_mask:0xf bank_mask:0xa\n\t"
                     "v_add_f32_dpp %1, %5, %3 row_shr:4 row_mask:0xf bank_mask:0xa"
                     : "=&v"(re), "=&v"(im) : "v"(ac[m & 14][0][i]), "v"(ac[m & 14][1][i]), "v"(ac[m | 1][0][i]), "v"(ac[m | 1][1][i]));
        o[(m >> 1) & 3][i] = pk2bf(re, im);
      }
      if constexpr (S2.ty[3 * g + 1] == O_S2) if (j) keep_v(o[S2.ix[3 * g + 1] & 3]);
      SB();
    };
    if constexpr (g < 128 && !(g & 1)) keep_v(A[(g + 1) & 3][0]);
    mf(IC<0>{});
    if constexpr (g + 3 < 128) { A[(g + 3) & 3][1] = wread(g + 3, 1); SB(); A[(g + 3) & 3][0] = wread(g + 3, 0); }
    SB();
    mf(IC<1>{});
    issue(IC<S2.ty[3 * g]>{}, IC<S2.ix[3 * g]>{});
    cvt(0);
    mf(IC<2>{});
    issue(IC<S2.ty[3 * g + 1]>{}, IC<S2.ix[3 * g + 1]>{});
    mf(IC<3>{});
    if constexpr (g == KD2) { XSTAMP(18); DRAIN(); XSTAMP(31); }
    if constexpr (g == KD3) { XSTAMP(19); DRAIN(); XSTAMP(35); }
    issue(IC<S2.ty[3 * g + 2]>{}, IC<S2.ix[3 * g + 2]>{});
    if constexpr (g >= TU0 && g < TU1) asm volatile("s_nop %0" ::"n"(S2.ty[3 * g + 2] ? THR2 - 3 : THR2));
    else if constexpr (THL >= 0 && g < 123 && !S2.ty[3 * g + 2]) asm volatile("s_nop %0" ::"n"(THL + 2));
    if constexpr ((g & 7) == 7 && g < 127) { keep_v(X[4 * k + 7]); }
    SB();
    cvt(1);
    if constexpr (g >= 84 && g < 88) ra[g & 3] = pin(l0 + ((w + 1 + g) & 3) * 4096 + lane * 16);
    if constexpr (g >= 88 && g < 92) fa[g & 3] = pin(l0 + J_F + ((w + 1 + g) & 3) * 2048 + lane * 16);
    if constexpr (g == 82) qa = pin(l0 + J_G + w * 4096 + (h * 8 + (n >> 4) * 4 + (n & 3)) * 16);
    if constexpr (g == 83) vo = pin(h * 4096 + w * 128 + n * 4 XST);
    SB();
    if constexpr (g == 63) XSTAMP(34);
    if constexpr (g == 127) XSTAMP(38);
  });
  st_pad(o[3], o[3]);
  float p[2][32], x[8], mx = 0.f;
  f32x4 q[2][8], fe[4];
  u32x4 tp[2][4];
  auto jstep = [&](auto it) {
    constexpr int t = decltype(it)::value, l = S2.nl + t * KJR / 8;
    if constexpr (l < NFILL && l < S2.nl + (t + 1) * KJR / 8) issue(IC<O_J>{}, IC<fill_i(l)>{});
  };
  auto front = [&](auto ib, auto&& after) {
    constexpr int b = decltype(ib)::value & 1;
    f32x4 d[4];
    u32x4 fb[8];
#pragma unroll
    for (int c = 0; c < 4; ++c) {
      mm<0, true>(d[c], yv[b][c][0], gi[0]);
      if constexpr (decltype(ib)::value < 2) PADJ();
    }
#pragma unroll
    for (int c = 0; c < 4; ++c) {
      mm<0, false>(d[c], yv[b][c][1], gi[1]);
      if constexpr (decltype(ib)::value < 2) PADJ();
    }
    asm volatile("s_nop 4" : "+v"(d[0]));
    if constexpr (decltype(ib)::value == 0) XSTAMP(39);
#pragma unroll
    for (int j = 0; j < 2; ++j) {
#pragma unroll
      for (int c = 0; c < 4; ++c) {
        fb[j][c] = gelu2(d[2 * j + (c >> 1)][2 * (c & 1)], d[2 * j + (c >> 1)][2 * (c & 1) + 1]);
        if constexpr (decltype(ib)::value < 2) PADJ();
      }
      lds_st(fa[3] + j * 1024, fb[j]);
      SB();
    }
    sfor<0, 32>([&](auto ii) {
      constexpr int i = decltype(ii)::value, eb = i < 8 ? i >> 1 : (i - 8) / 6, r = i < 8 ? i & 1 : 2 + (i - 8) % 6;
      mm<1, r == 0>(fe[eb], wt[i], fb[r]);
      if constexpr (decltype(ib)::value < 2) PADJ();
      SB();
      if constexpr (i == 0) {
        BAR();
      }
      if constexpr (i < 3) {
        fb[2 * i + 2] = lds_ld(fa[i]);
        fb[2 * i + 3] = lds_ld(fa[i] + 1024);
      }
      SB();
      after(ii);
    });
  };
  auto twrite = [&](auto iG) {
    constexpr int V = decltype(iG)::value + 31;
    if constexpr (V >= 0 && V < 256 && (V & 7) == 7) lds_st(ra[3] + J_T + ((V >> 5) & 1) * 16384 + ((V >> 3) & 3) * 1024, tp[(V >> 5) & 1][(V >> 3) & 3]);
    SB();
  };
  auto tslot = [&](auto iG) {
    constexpr int G = decltype(iG)::value;
    constexpr int A = G + 36, B = G + 35, C = G + 34, D = G + 33;
    if constexpr (B >= 0 && B < 256) asm volatile("v_rcp_f32 %0, %0" : "+v"(x[B & 7]));
    if constexpr (C >= 0 && C < 256) asm volatile("v_fma_f32 %0, -%1, %0, 0.5" : "+v"(x[C & 7]) : "v"(q[C >> 7][(C >> 2) & 7][C & 3]));
    PADJ();
    if constexpr (A >= 0 && A < 256) x[A & 7] = add_quad<(A >> 5) & 3>(p[A >> 7][A & 31], q[A >> 7][(A >> 2) & 7][A & 3]);
    SB();
    if constexpr (D >= 0 && D < 256 && (D & 1)) tp[(D >> 5) & 1][(D >> 3) & 3][(D >> 1) & 3] = pk2bf(x[(D - 1) & 7], x[D & 7]);
    SB();
  };
  auto stepE = [&](int b, int eb, int i) {
    const float a = fe[eb][i] * C3;
    mx = __builtin_fmaxf(mx, __builtin_fabsf(a));
    const uint32_t e = __builtin_bit_cast(uint32_t, __builtin_amdgcn_exp2f(a));
    const auto r = __builtin_amdgcn_permlane16_swap(e, e, false, false);
    p[b][8 * eb + i] = __builtin_bit_cast(float, (uint32_t)r[0]);
    p[b][8 * eb + 4 + i] = __builtin_bit_cast(float, (uint32_t)r[1]);
  };
  auto head = [&](auto ib) {
    constexpr int b = decltype(ib)::value;
    front(ib, [&](auto ii) {
      constexpr int i = decltype(ii)::value;
      if constexpr (b == 0 && i == 0) XSTAMP(40);
      if constexpr (b == 0 && i == KS5) {
        DRAIN();
        issue(IC<O_SIG>{}, IC<5>{});
        asm volatile("s_cmpk_lt_i32 %0, 250\n\ts_cbranch_scc1 1f\n\ts_endpgm\n1:" :: "s"(pos) : "scc");
      }
      if constexpr (i >= 3 && i < 11) q[b][i - 3] = __builtin_bit_cast(f32x4, lds_ld(qa + b * 2048 + (i - 3) * 256));
      if constexpr (b == 0 && i > KS5) jstep(IC<i - KS5 - 1>{});
      if constexpr (i >= 16 && (i - 16) % 6 < 4) {
        if constexpr ((i - 16) % 6 == 0) asm volatile("s_nop 1" : "+v"(fe[(i - 16) / 6]));
        stepE(b, (i - 16) / 6, (i - 16) % 6);
      }
      SB();
    });
    if constexpr (b == 0) sfor<-36, -32>([&](auto ir) { tslot(ir); });
    else asm volatile("s_nop 7" ::);
    asm volatile("" : "+v"(fe[3]));
#pragma unroll
    for (int i = 0; i < 4; ++i) { stepE(b, 3, i); PADJ(); }
    mx = __builtin_fmaxf(mx, q[b][0][0] < 0.f || q[b][4][0] < 0.f ? 2 * EMAX : 0.f);
    if constexpr (b) *(__attribute__((address_space(3))) float*)(uintptr_t)(uint32_t)(l0 + J_R + tid * 4) = mx;
    asm volatile("s_nop 1");
    SB();
  };
  head(IC<0>{});
  sfor<-32, 0>([&](auto ir) { jstep(IC<decltype(ir)::value + 63 - KS5>{}); twrite(ir); tslot(ir); });
  XSTAMP(41);
  const __amdgpu_buffer_rsrc_t ob = mkbuf(out + (size_t)seg * 16 * 8 * DCH, 4000 * 4096 + 65536);
  f32x16 acc[2][2];
  u32x4 a[16];
  uint32_t ov[16];
  int vq[2] = {vo, vo + 8192};
  auto store = [&](int i) { __builtin_amdgcn_raw_buffer_store_b32(ov[i], ob, vq[(i >> 2) & 1] + ((i >> 3) * 4 + (i & 3)) * 512, 0, OPOL); };
  auto next = [&](int st, int j) {
    vq[j] += (st & 3) ? 16384 : 4000 * 4096 - 3 * 16384;
    asm volatile("" : "+v"(vq[j]));
  };
  sfor<0, 256>([&](auto iG) {
    constexpr int G = decltype(iG)::value, st = G >> 5, g = G & 31, r = g >> 1, z = g & 1;
    if constexpr (G == 92) { XSTAMP(42); head(IC<1>{}); XSTAMP(44); }
    mm<2, r == 0>(acc[st & 1][z], r < 4 ? tp[st & 1][r & 3] : a[r], wt[32 + g]);
    PADJ();
    SB();
    if constexpr (g == 2) BAR();
    if constexpr (st == 0 && g < 31) keep_a(wt[33 + g]);
    if constexpr (z && r >= 3 && r < 15) keep_v(a[r + 1]);
    constexpr int rd = g >= 3 && g < 6 ? g + 1 : (g >= 7 && g <= 23 && z) ? (g + 7) / 2 : 0;
    constexpr int sn = g == 1 ? 15 : g == 2 ? 0 : g == 6 ? 1 : g >= 25 ? g - 17 : (g > 8 && !z && (g & 7)) ? (g - 6) / 2 - (g > 16) : -1;
    if constexpr (rd) a[rd] = lds_ld(ra[(rd >> 2) - 1] + J_T + (st & 1) * 16384 + (rd & 3) * 1024);
    if constexpr (sn >= 0 && st > (g == 1)) store(sn);
    if constexpr (G < 24) jstep(IC<G + 63 - KS5>{});
    if constexpr (G == KPI4) issue(IC<O_POLL>{}, IC<5>{});
    if constexpr (G == KPC4) await(pl[1], fr, vp[2], FO[5]);
    if constexpr (G >= KPC4 && G < KPC4 + 32) yload(1, G - KPC4);
    twrite(iG);
    tslot(iG);
    if constexpr (st > 0 && g == 30) next(st, 0);
    if constexpr (st > 1 && g == 4) next(st - 1, 1);
    if constexpr (G == 225) asm volatile("s_nop 5" : "+v"(acc[0][0]));
    if constexpr (st > 0 && z) ov[r] = pk2bf(acc[(st - 1) & 1][0][r], acc[(st - 1) & 1][1][r]);
    SB();
  });
  store(15);
  next(7, 1);
  const f32x4 rg = __builtin_bit_cast(f32x4, lds_ld(l0 + J_R + lane * 16));
  MWAIT(acc[1][0], acc[1][1]);
#pragma unroll
  for (int i = 0; i < 16; ++i) { ov[i] = pk2bf(acc[1][0][i], acc[1][1][i]); store(i); PADJ(); }
  XSTAMP(43);
  if (__builtin_expect(__builtin_amdgcn_ballot_w64(rg[0] > EMAX || rg[1] > EMAX || rg[2] > EMAX || rg[3] > EMAX) != 0, 0)) {
    asm volatile("s_waitcnt vmcnt(0)" ::: "memory");
#pragma unroll 1
    for (int b = 0; b < 2; ++b) {
#pragma unroll
      for (int i = 0; i < 32; ++i) yload(0, i);
      vy += 65536;
      front(IC<2>{}, [](auto) {});
      asm volatile("s_nop 7" : "+v"(fe[3]));
#pragma unroll 1
      for (int st = 0; st < 4; ++st) {
        const float* gp = ge + 4096 + (w * 2 + b) * 512 + (h * 8 + (n >> 4) * 4 + (n & 3)) * 4;
#pragma unroll
        for (int f = 0; f < 4; ++f) {
          u32x4 t;
#pragma unroll
          for (int c = 0; c < 2; ++c) {
            const f32x4 gv = *(const f32x4*)(gp + (2 * f + c) * 64);
            float y[4];
#pragma unroll
            for (int i = 0; i < 4; ++i)
              y[i] = 0.5f - __builtin_amdgcn_rcpf(__builtin_amdgcn_exp2f(__shfl(fe[f][i], h * 32 + c * 16 + (lane & 12) + st) * C3 + gv[i]) + 1.f);
            t[2 * c] = pk2bf(y[0], y[1]);
            t[2 * c + 1] = pk2bf(y[2], y[3]);
          }
          lds_st(ra[3] + J_T + (st & 1) * 16384 + f * 1024, t);
        }
        BAR();
        sfor<0, 16>([&](auto ir) {
          constexpr int r = decltype(ir)::value;
          const u32x4 v = lds_ld(ra[(r / 4 + 3) & 3] + J_T + (st & 1) * 16384 + (r & 3) * 1024);
#pragma unroll
          for (int z = 0; z < 2; ++z) mm<2, r == 0>(acc[0][z], v, wt[32 + 2 * r + z]);
        });
        MWAIT(acc[0][0], acc[0][1]);
#pragma unroll
        for (int i = 0; i < 16; ++i)
          __builtin_amdgcn_raw_buffer_store_b32(pk2bf(acc[0][0][i], acc[0][1][i]), ob, vo + st * 16384 + ((i >> 3) * 4 + (i & 3)) * 512,
                                                b * (4000 * 4096) + ((i >> 2) & 1) * 8192, NT);
      }
    }
  }
  XT_OUT(bid * 4 + w);
}

template <int CONV>
__device__ __forceinline__ void wtrans(const uint16_t* __restrict__ w, char* __restrict__ wh, int wid, int lane) {
  const int dq = wid & 3, pc = wid >> 2;
  const int ks = pc >> 2, kq = pc & 3;
  const int n32 = lane & 31, h = lane >> 5;
  const int r = CONV == 1 ? ks >> 3 : kq >> 1;
  const int c0 = CONV == 1 ? (ks & 7) * 32 + kq * 8 : dmap(ks, 8 * (kq & 1));
  const __amdgpu_buffer_rsrc_t wr = mkbuf(w, 31 * 131072);
  const int vb = (16 * h + r + 1) * 131072 + c0 * 512 + dq * 128 + n32 * 4;
  const int vb0 = (h + r == 0) ? OOB : vb - 262144;
  constexpr int NFB = CONV == 1 ? 2 : 1;
  const u32x4* et = CONV == 1 ? (const u32x4*)&tabs.e1[0][0][0][0] : (const u32x4*)&tabs.e2[0][0][0];
  u32x4 E[NFB][2];
#pragma unroll
  for (int fb = 0; fb < NFB; ++fb) { E[fb][0] = et[fb * 128 + lane]; E[fb][1] = et[fb * 128 + 64 + lane]; }
  uint32_t v[8][8];
#pragma unroll
  for (int el = 0; el < 8; ++el)
#pragma unroll
    for (int jj = 0; jj < 8; ++jj) {
      const int co = CONV == 1 ? el * 512 : (el & 3) * 1024;
      const int so = (CONV == 1 ? 0 : (el >> 2) * 4096) + (jj ? (jj - 1) * 262144 : 0);
      v[el][jj] = __builtin_amdgcn_raw_buffer_load_b32(wr, (jj ? vb : vb0) + co, so, NT);
    }
  PHASE();
  const __amdgpu_buffer_rsrc_t dr = mkbuf(wh, (CONV == 1 ? 32 : 16) * WBIN);
  const int vo = 2 * h * WBIN + (4 * dq + (n32 >> 4)) * WCB + ks * 2048 + kq * 256 + (n32 & 15) * 16;
#pragma unroll
  for (int e = 0; e < 2; ++e) {
    u32x4 b[8];
#pragma unroll
    for (int el = 0; el < 8; ++el)
#pragma unroll
      for (int j = 0; j < 4; ++j) b[el][j] = __builtin_amdgcn_perm(v[el][2 * j + 1], v[el][2 * j], e ? 0x07060302u : 0x05040100u);
#pragma unroll
    for (int fb = 0; fb < NFB; ++fb) {
      u32x4 o[16];
#pragma unroll
      for (int ep = 0; ep < 4; ++ep) {
        f32x16 d[2];
#pragma unroll
        for (int z = 0; z < 2; ++z) d[z] = mfma32(E[fb][1], b[2 * ep + z], zero16());
#pragma unroll
        for (int z = 0; z < 2; ++z) d[z] = mfma32(E[fb][0], b[2 * ep + z], d[z]);
#pragma unroll
        for (int i = 0; i < 16; ++i) o[i][ep] = pk2bf(d[0][i], d[1][i]);
      }
#pragma unroll
      for (int i = 0; i < 16; ++i)
        __builtin_amdgcn_raw_buffer_store_b128(o[i], dr, vo + (i & 1) * 1024,
                                               (16 * fb + 4 * (i >> 2) + ((i >> 1) & 1)) * WBIN + e * 2 * WCB, WT);
      st_pad(o[14], o[15]);
      SB();
    }
  }
}

__device__ __forceinline__ void xload(const uint16_t* __restrict__ x, int u, int lane, uint32_t (&v)[3][8]) {
  const int cq = u & 7, m = u >= 8 * 168, b = u - 8 * 168 * m >= 8 * 84, r = (u >> 3) - 168 * m - 84 * b, sg = 84 * (r & 1) + 42 * m + (r >> 1);
  const __amdgpu_buffer_rsrc_t xr = mkbuf(x + (size_t)b * 16000 * DCH, 8192000);
#pragma unroll
  for (int ks = 0; ks < 3; ++ks) {
    const int row0 = 48 * sg - 24 + 16 * ks + 8 * (lane >> 5);
    const int vb = row0 >= 0 ? row0 * 1024 + cq * 128 + (lane & 31) * 4 : OOB;
#pragma unroll
    for (int e = 0; e < 8; ++e) v[ks][e] = __builtin_amdgcn_raw_buffer_load_b32(xr, vb + (e & 3) * 1024, (e >> 2) * 4096, NT);
  }
}
__device__ __forceinline__ void xtrans(char* __restrict__ u1, int u, int lane, const uint32_t (&v)[3][8], const u32x4 (&F)[2][3]) {
  const __amdgpu_buffer_rsrc_t ur = mkbuf(u1 + (2 * (u & 7)) * PL1 + (u >> 3) * 128, U1BYTES - (2 * (u & 7)) * PL1 - (u >> 3) * 128);
  const int vo = ((lane & 15) >> 1) * UB1 + ((lane >> 4) & 1) * 64 + (lane & 1) * 32 + (lane >> 5) * 16;
  u32x4 a[2][3];
#pragma unroll
  for (int ks = 0; ks < 3; ++ks)
#pragma unroll
    for (int j = 0; j < 4; ++j) {
      a[0][ks][j] = __builtin_amdgcn_perm(v[ks][2 * j + 1], v[ks][2 * j], 0x05040100u);
      a[1][ks][j] = __builtin_amdgcn_perm(v[ks][2 * j + 1], v[ks][2 * j], 0x07060302u);
    }
#pragma unroll
  for (int fb = 0; fb < 2; ++fb) {
    f32x16 d[2] = {zero16(), zero16()};
#pragma unroll
    for (int ks = 0; ks < 3; ++ks)
#pragma unroll
      for (int e = 0; e < 2; ++e) d[e] = mfma32(a[e][ks], F[fb][ks], d[e]);
#pragma unroll
    for (int qp = 0; qp < 2; ++qp) {
      u32x4 o[2];
#pragma unroll
      for (int z = 0; z < 2; ++z)
#pragma unroll
        for (int i = 0; i < 4; ++i) o[z][i] = pk2bf(d[0][8 * qp + 4 * z + i], d[1][8 * qp + 4 * z + i]);
      swap16(o[0], o[1]);
#pragma unroll
      for (int z = 0; z < 2; ++z) __builtin_amdgcn_raw_buffer_store_b128(o[z], ur, vo, (fb * 16 + z * 8) * UB1 + qp * PL1, WT);
      st_pad(o[0], o[1]);
    }
  }
}

KERNEL __launch_bounds__(512, 2) void pre_kernel(const uint16_t* __restrict__ x, const uint16_t* __restrict__ w1,
                                                     const uint16_t* __restrict__ w2, const uint16_t* __restrict__ wf,
                                                     const uint16_t* __restrict__ wo, const uint16_t* __restrict__ g,
                                                     const uint16_t* __restrict__ wg, char* __restrict__ u1,
                                                     char* __restrict__ w1h, char* __restrict__ w2h, u32x4* __restrict__ wff,
                                                     u32x4* __restrict__ wof, float* __restrict__ ge, uint32_t* __restrict__ flags) {
  ST_DECL; STAMP(0); CSTAMP(22);
  const int blk = blockIdx.x;
  const int tid = threadIdx.x, lane = tid & 63;
  const int wave = __builtin_amdgcn_readfirstlane(tid >> 6);
  const int n = lane & 31, h = lane >> 5;
  __shared__ char pfs[256];
  if (wave == PF1) {
    const char *p0 = (const char*)&tabs - 320, *p1;
    LABEL_ADDR(p1, ".Lce");
    code_pf(p0, (int)(p1 - p0), blk >> 3, pfs);
  }
  if (wave == 0) {
    wtrans<1>(w1, w1h, blk, lane);
  } else if (wave == 1) {
    wtrans<2>(w2, w2h, blk, lane);
  } else {
    const int bq = (blk & 7) * 32 + (blk >> 3);
    const int u0 = ((21 * bq) >> 1) + wave - 2, ue = (21 * (bq + 1)) >> 1;
    u32x4 F[2][3];
#pragma unroll
    for (int fb = 0; fb < 2; ++fb)
#pragma unroll
      for (int ks = 0; ks < 3; ++ks) F[fb][ks] = ((const u32x4*)&tabs.f1[fb][ks][0][0])[lane];
    uint32_t v0[3][8];
    xload(x, u0, lane, v0);
    if (u0 + 6 < ue) {
      uint32_t v1[3][8];
      xload(x, u0 + 6, lane, v1);
      SB();
      WALK(7, wave - 2);
      PHASE();
      STAMP(1);
      xtrans(u1, u0, lane, v0, F);
      SB();
      xtrans(u1, u0 + 6, lane, v1, F);
    } else if (wave == 7) {
      const int i = blk & 31, w = (blk >> 5) & 3, second = blk >> 7;
      const int eb = i < 8 ? i >> 1 : (i - 8) / 6, r = (2 * w + (i < 8 ? i & 1 : 2 + (i - 8) % 6)) & 7;
      const uint16_t* src = second ? wo + (16 * ((4 * w + (i >> 1)) & 15) + 8 * h) * DCH + 64 * w + 2 * n + (i & 1)
                                   : wf + dmap(2 * r, 4 * (lane >> 4)) * DCH + 64 * w + 16 * eb + (lane & 15);
      uint32_t v[8];
#pragma unroll
      for (int j = 0; j < 8; ++j) v[j] = src[(second ? j : 32 * (j >> 2) + 2 * (j & 3)) * DCH];
      PHASE();
      xtrans(u1, u0, lane, v0, F);
      const float sc = second ? 2.f : 1.f;
      u32x4 o;
#pragma unroll
      for (int j = 0; j < 4; ++j) o[j] = pk2bf(bf2f(v[2 * j]) * sc, bf2f(v[2 * j + 1]) * sc);
      ((second ? wof : wff) + (blk & 127) * 64)[lane] = o;
      if (blk < NFLAG / 64) flags[blk * 64 + lane] = 0;
    } else if (blk < 8) {
      const int c0 = 32 * blk + 2 * (lane & 15), kq = lane >> 4;
      uint32_t v[8][8];
      u32x4 ga[8];
#pragma unroll
      for (int ks = 0; ks < 8; ++ks) {
        ga[ks] = *(const u32x4*)(g + (lane & 15) * DCH + ks * 32 + 8 * kq);
#pragma unroll
        for (int e = 0; e < 8; ++e) v[ks][e] = *(const uint32_t*)(wg + (ks * 32 + 8 * kq + e) * DCH + c0);
      }
      PHASE();
      xtrans(u1, u0, lane, v0, F);
      f32x4 d[2] = {{0.f, 0.f, 0.f, 0.f}, {0.f, 0.f, 0.f, 0.f}};
#pragma unroll
      for (int ks = 0; ks < 8; ++ks)
#pragma unroll
        for (int e = 0; e < 2; ++e) {
          u32x4 bb;
#pragma unroll
          for (int j = 0; j < 4; ++j) bb[j] = __builtin_amdgcn_perm(v[ks][2 * j + 1], v[ks][2 * j], e ? 0x07060302u : 0x05040100u);
          d[e] = mfma16(ga[ks], bb, d[e]);
        }
      float* dst = ge + (((((blk >> 1) * 2 + (kq >> 1)) * 4 + 2 * (blk & 1) + ((lane >> 3) & 1)) * 2 + ((lane >> 1) & 1)) * 2 + ((lane >> 2) & 1)) * 32 + (kq & 1) * 16 + 2 * (lane & 1);
      f32x2 gs[4];
      bool far = false;
#pragma unroll
      for (int i = 0; i < 4; ++i) {
        gs[i] = f32x2{bf2f(f2bf(d[0][i])) * C2, bf2f(f2bf(d[1][i])) * C2};
        far = far || __builtin_fabsf(gs[i][0]) > EMAX || __builtin_fabsf(gs[i][1]) > EMAX;
      }
      far = __builtin_amdgcn_ballot_w64(far) != 0;
#pragma unroll
      for (int i = 0; i < 4; ++i) {
        *(f32x2*)(dst + 4 * i) = far ? f32x2{-1.f, -1.f} : f32x2{__builtin_amdgcn_exp2f(-gs[i][0]), __builtin_amdgcn_exp2f(-gs[i][1])};
        *(f32x2*)(dst + 4096 + 4 * i) = gs[i];
      }
    } else {
      PHASE();
      xtrans(u1, u0, lane, v0, F);
    }
  }
  STAMP(19);
  ST_OUT(0, blk * 8 + wave);
  asm volatile(".Lce:");
}

__global__ __launch_bounds__(1024) void probe_kernel() {
  __shared__ unsigned int ra[1024], rb[1024];
  unsigned int ref = g_st[0][0][0];
  unsigned int a = 0 ? 0u : ~0u, b = 0 ? 0u : ~0u;
  for (int w = threadIdx.x; w < 4096; w += 1024) {
    const unsigned int va = g_st[0][w][0], vb = g_st[0][w][0];
    if (va) { const unsigned int d = va - ref + 100000u; a = 0 ? max(a, d) : min(a, d); }
    if (vb) { const unsigned int d = vb - ref + 100000u; b = 0 ? max(b, d) : min(b, d); }
  }
  ra[threadIdx.x] = a; rb[threadIdx.x] = b;
  __syncthreads();
  if (threadIdx.x == 0) {
    for (int i = 1; i < 1024; ++i) { a = 0 ? max(a, ra[i]) : min(a, ra[i]); b = 0 ? max(b, rb[i]) : min(b, rb[i]); }
    unsigned int span = b - a;
    if (span > 20000u) span = 20000u;
    const unsigned long long t0 = __builtin_amdgcn_s_memrealtime();
    while (__builtin_amdgcn_s_memrealtime() - t0 < 10ull * span) __builtin_amdgcn_s_sleep(2);
  }
}

extern "C" void kernel_launch(void* const* d_in, const int* in_sizes, int n_in, void* d_out, int out_size, void* d_ws,
                              size_t ws_size, hipStream_t stream) {
  const uint16_t* x = (const uint16_t*)d_in[0];
  const uint16_t* w1 = (const uint16_t*)d_in[1];
  const uint16_t* w2 = (const uint16_t*)d_in[2];
  const uint16_t* g = (const uint16_t*)d_in[3];
  const uint16_t* wf = (const uint16_t*)d_in[4];
  const uint16_t* wg = (const uint16_t*)d_in[5];
  const uint16_t* wo = (const uint16_t*)d_in[6];
  uint16_t* out = (uint16_t*)d_out;
  char* ws = (char*)d_ws;
  (void)hipFuncSetAttribute((const void*)joint_kernel, hipFuncAttributeMaxDynamicSharedMemorySize, JOINT_LDS);

  pre_kernel<<<256, 512, 0, stream>>>(x, w1, w2, wf, wo, g, wg, ws + A_U1, ws + A_W1, ws + A_W2, (u32x4*)(ws + A_WF),
                                      (u32x4*)(ws + A_WF + 131072), (float*)(ws + A_GE), (uint32_t*)(ws + A_FL));
  joint_kernel<<<256, 256, JOINT_LDS, stream>>>(ws, out);
  probe_kernel<<<1, 1024, 0, stream>>>();
}
```

```cpp
#include <hip/hip_runtime.h>
#include <stdint.h>

typedef __attribute__((ext_vector_type(8))) short bf16x8;
typedef __attribute__((ext_vector_type(16))) float f32x16;
typedef __attribute__((ext_vector_type(4))) float f32x4;
typedef __attribute__((ext_vector_type(2))) float f32x2;
typedef __attribute__((ext_vector_type(4))) unsigned int u32x4;
typedef __attribute__((ext_vector_type(2))) unsigned int u32x2;

#define ORD0 0x0132
#define ORD1 0x2310
#define GLA 6
#define TS0 28
#define TS1 58
#define TU0 0
#define TU1 0
#define THR 10
#define THR2 7
#define GPRO 2
constexpr int DCH = 256;

__device__ __forceinline__ float bf2f(uint32_t v) { return __uint_as_float(v << 16); }
__device__ __forceinline__ uint32_t f2bf(float f) {
  uint32_t u = __float_as_uint(f);
  u += 0x7FFF + ((u >> 16) & 1);
  return u >> 16;
}
__device__ __forceinline__ uint32_t pk2bf(float a, float b) {
  typedef __attribute__((ext_vector_type(2))) __bf16 b2;
  f32x2 v = {a, b};
  b2 r = __builtin_convertvector(v, b2);
  return __builtin_bit_cast(uint32_t, r);
}
constexpr double GS = 0.4686686932353139;
__device__ __forceinline__ uint32_t gelu2(float a0, float a1, float one = 1.f) {
  const f32x2 v = {a0, a1};
  constexpr float c = (float)(-2.302208198144325 / GS);
  const f32x2 z = v * __builtin_elementwise_fma(-v, v, f32x2{c, c});
  const f32x2 d = f32x2{__builtin_amdgcn_exp2f(z[0]), __builtin_amdgcn_exp2f(z[1])} + f32x2{one, one};
  const f32x2 y = v * f32x2{__builtin_amdgcn_rcpf(d[0]), __builtin_amdgcn_rcpf(d[1])};
  return pk2bf(y[0], y[1]);
}
__device__ __forceinline__ f32x16 mfma32(const u32x4& a, const u32x4& b, const f32x16& c) {
  return __builtin_amdgcn_mfma_f32_32x32x16_bf16(__builtin_bit_cast(bf16x8, a), __builtin_bit_cast(bf16x8, b), c, 0, 0, 0);
}
__device__ __forceinline__ f32x4 mfma16(const u32x4& a, const u32x4& b, const f32x4& c) {
  return __builtin_amdgcn_mfma_f32_16x16x32_bf16(__builtin_bit_cast(bf16x8, a), __builtin_bit_cast(bf16x8, b), c, 0, 0, 0);
}
__device__ __forceinline__ f32x16 zero16() {
  f32x16 z;
#pragma unroll
  for (int i = 0; i < 16; ++i) z[i] = 0.f;
  return z;
}
__device__ __forceinline__ __amdgpu_buffer_rsrc_t mkbuf(const void* p, int nbytes) {
  return __builtin_amdgcn_make_buffer_rsrc((void*)p, (short)0, nbytes, 0x00020000);
}
template <int V> struct IC { static constexpr int value = V; };
template <int I, int N, class F>
__device__ __forceinline__ void sfor(F&& f) {
  if constexpr (I < N) {
    f(IC<I>{});
    sfor<I + 1, N>(f);
  }
}
#define SB() __builtin_amdgcn_sched_barrier(0)
#define PHASE() do { SB(); asm volatile("s_waitcnt vmcnt(0)\n\ts_barrier" ::: "memory"); SB(); } while (0)

constexpr double PI_ = 3.14159265358979323846;
constexpr double c128(int m) {
  m = ((m % 128) + 128) % 128;
  if (m > 64) m = 128 - m;
  double sg = 1.0;
  if (m > 32) { m = 64 - m; sg = -1.0; }
  if (m == 32) return 0.0;
  const double a = PI_ * m / 64.0;
  double term = 1.0, sum = 1.0;
  for (int i = 1; i < 16; ++i) { term *= -a * a / ((2 * i - 1) * (2 * i)); sum += term; }
  return sg * sum;
}
constexpr double s128(int m) { return c128(m - 32); }
constexpr uint16_t cbf(double v) {
  uint32_t u = __builtin_bit_cast(uint32_t, (float)v);
  u += 0x7FFF + ((u >> 16) & 1);
  return (uint16_t)(u >> 16);
}
constexpr double cbf2d(uint16_t v) { return (double)__builtin_bit_cast(float, (uint32_t)v << 16); }
struct Tabs {
  uint16_t f1[2][3][64][8];
  uint16_t e1[2][2][64][8];
  uint16_t e2[2][64][8];
  uint16_t g1[8][64][8];
  uint16_t f2[64][8];
  uint16_t g2[2][64][8];
};
constexpr Tabs make_tabs() {
  Tabs t{};
  for (int l = 0; l < 64; ++l)
    for (int e = 0; e < 8; ++e) {
      const int h = l >> 5, f = l & 31, ri = f & 1;
      for (int fb = 0; fb < 2; ++fb) {
        const int k = 16 * fb + (f >> 1);
        for (int ks = 0; ks < 3; ++ks) {
          const int a = (2 * k + 1) * (16 * ks + 8 * h + e);
          t.f1[fb][ks][l][e] = cbf(ri ? -s128(a) : c128(a));
        }
        const int a = (2 * k + 1) * (8 * h + e);
        const double v = ri ? s128(a) : c128(a);
        t.e1[fb][0][l][e] = cbf(v);
        t.e1[fb][1][l][e] = cbf(v - cbf2d(cbf(v)));
      }
      const int a = 2 * (2 * (f >> 1) + 1) * (8 * h + e);
      const double v = ri ? s128(a) : c128(a);
      t.e2[0][l][e] = cbf(v);
      t.e2[1][l][e] = cbf(v - cbf2d(cbf(v)));
      t.f2[l][e] = cbf((ri ? -v : v) / GS);
    }
  for (int m = -15; m < 48; ++m)
    for (int f = 0; f < 64; ++f) {
      const int a = (2 * (f >> 1) + 1) * m;
      t.g1[f >> 3][(m + 16) ^ (((m + 16) >> 4) & 1)][f & 7] = cbf(((f & 1) ? -s128(a) : c128(a)) * GS / 32.0);
    }
  for (int c = 0; c < 2; ++c)
    for (int l = 0; l < 64; ++l)
      for (int e = 0; e < 8; ++e) {
        const int m = 4 * (l & 3) + ((l >> 2) & 3) - (l & 16), a = 2 * (2 * (8 * c + 4 * (l >> 5) + (e >> 1)) + 1) * m;
        t.g2[c][l][e] = m > -16 ? cbf(((e & 1) ? -s128(a) : c128(a)) * GS / 16.0) : 0;
      }
  return t;
}
__device__ __attribute__((aligned(64))) const Tabs tabs = make_tabs();

constexpr int OOB = 0x7FFFFF00;
constexpr float C2 = 2.885390081777927f, C3 = (float)(2.885390081777927 / GS);
#define EMAXV 120.f
constexpr float EMAX = EMAXV;
constexpr int WT = 16;
constexpr int NT = 2;
constexpr int PL1 = 336 * 128;
#define PU2 0
#define PY2 0
constexpr int UB1 = 16 * PL1, UB2 = 524288 + PU2;
constexpr int U1BYTES = 32 * UB1, U2BYTES = 32 * UB2;
constexpr int WCB = 32768;
constexpr int WBIN = 16 * WCB;
constexpr int YP1 = 336 * 64;
constexpr int YB1 = 16 * YP1, YB2 = 524288 + PY2;
constexpr int Y1BYTES = 32 * YB1, Y2BYTES = 16 * YB2;
constexpr int F1 = 0, F2 = 2048, F3 = 10240, NFLAG = 18432 / 4;
constexpr int A_U1 = 0, A_W1 = 23 << 20, A_W2 = 40 << 20, A_Y1 = 49 << 20, A_U2 = 61 << 20, A_Y2 = 78 << 20, A_WF = 87 << 20,
              A_GE = A_WF + 262144, A_FL = 88 << 20;
__device__ __forceinline__ int dmap(int cb, int n) { return 64 * (cb >> 2) + 32 * (cb & 1) + 2 * n + ((cb >> 1) & 1); }

__device__ __forceinline__ void swap16(u32x4& a, u32x4& b) {
#pragma unroll
  for (int i = 0; i < 4; ++i) {
    const auto r = __builtin_amdgcn_permlane16_swap(a[i], b[i], false, false);
    a[i] = r[0];
    b[i] = r[1];
  }
}

__device__ __forceinline__ void st_pad(const u32x4& a, const u32x4& b) { asm volatile("s_nop 1" :: "v"(a), "v"(b) : "memory"); }

template <int N>
__device__ __forceinline__ void wait_vm() {
  constexpr int M = N < 63 ? N : 63;
  __builtin_amdgcn_s_waitcnt((M & 15) | ((M >> 4) << 14) | 0x0F70);
}

typedef __attribute__((address_space(3))) u32x4 lds_u32x4;
__device__ __forceinline__ u32x4 lds_ld(int a) { return *(const lds_u32x4*)(uintptr_t)(uint32_t)a; }
__device__ __forceinline__ uint32_t lds_ld32(int a) { return *(const __attribute__((address_space(3))) uint32_t*)(uintptr_t)(uint32_t)a; }
template <int SZ>
__device__ __forceinline__ void dma(__amdgpu_buffer_rsrc_t rsrc, char* lds, int voff, int soff) {
#if defined(__HIP_DEVICE_COMPILE__)
  typedef __attribute__((address_space(3))) void* ldsp;
  if constexpr (SZ == 16) __builtin_amdgcn_raw_ptr_buffer_load_lds(rsrc, (ldsp)lds, 16, voff, soff, 0, 0);
  else __builtin_amdgcn_raw_ptr_buffer_load_lds(rsrc, (ldsp)lds, 4, voff, soff, 0, 0);
#endif
}

#define LABEL_ADDR(p, name) asm volatile("s_getpc_b64 s[40:41]\n\ts_add_u32 s40, s40, " name "@rel32@lo+4\n\ts_addc_u32 s41, s41, " name "@rel32@hi+12\n\ts_mov_b64 %0, s[40:41]" : "=s"(p) :: "s40", "s41", "scc")
__device__ __forceinline__ void code_pf(const char* p, int n, int part, char* lds) {
  dma<4>(mkbuf(p, n), lds, part * 8192 + (threadIdx.x & 63) * 128, 0);
}

__device__ __forceinline__ void pad_macros() {
  asm volatile(R"(
.macro PAD1 l
.ifndef .Lw_\l
.Lw_\l:
.endif
\l:
s_cbranch_execz \l\()f
.endm
.macro PADM k, b, m
.ifndef .Lpg_\k
.set .Lpg_\k, -1
.endif
.ifndef .Lcn_\b
.set .Lcn_\b, 0
.endif
.if (((. - \k) & 4095) < 60) && (((. - \k) >> 12) > .Lpg_\k)
.set .Lpg_\k, (. - \k) >> 12
.irp c, 0, 1, 2, 3, 4, 5, 6, 7
.if (.Lcn_\b & \m) == \c
PAD1 \b\c
.endif
.endr
.set .Lcn_\b, .Lcn_\b + 1
.endif
.endm
.macro WALK b, w
s_mov_b64 exec, 0
.irp c, 0, 1, 2, 3, 4, 5, 6, 7
.ifdef .Lw_\b\c
s_cmp_eq_u32 \w, \c
s_cbranch_scc1 .Lw_\b\c
.endif
.endr
.irp c, 0, 1, 2, 3, 4, 5, 6, 7
\b\c:
.endr
s_mov_b64 exec, -1
.endm
)");
}
#define PAD(k, b, m) asm volatile("PADM " #k ", " #b ", " #m)
#define WALK(b, w) asm volatile("WALK " #b ", %0" :: "s"(w) : "scc")
#define KERNEL extern "C" __global__ __attribute__((aligned(4096)))

constexpr int J_T = 0;
constexpr int J_G = 131072;
constexpr int J_F = J_G + 16384;
constexpr int J_R = J_F + 8192;
constexpr int JOINT_LDS = J_R + 1024;
__device__ __forceinline__ void lds_st(int a, const u32x4& v) { *(lds_u32x4*)(uintptr_t)(uint32_t)a = v; }
#define BAR() do { SB(); asm volatile("s_waitcnt lgkmcnt(0)\n\ts_barrier" ::: "memory"); SB(); } while (0)
#define DRAIN() do { SB(); wait_vm<0>(); asm volatile("s_waitcnt vmcnt(0)\n\ts_barrier" ::: "memory"); SB(); } while (0)
template <int M, bool Z>
__device__ __forceinline__ void mm(f32x16& c, const u32x4& a, const u32x4& b) {
  if constexpr (Z) {
    if constexpr (M == 0) asm volatile("v_mfma_f32_32x32x16_bf16 %0, %1, %2, 0" : "=&v"(c) : "v"(a), "v"(b));
    else asm volatile("v_mfma_f32_32x32x16_bf16 %0, %1, %2, 0" : "=&v"(c) : "v"(a), "a"(b));
  } else {
    if constexpr (M == 0) asm volatile("v_mfma_f32_32x32x16_bf16 %0, %1, %2, %0" : "+v"(c) : "v"(a), "v"(b));
    else asm volatile("v_mfma_f32_32x32x16_bf16 %0, %1, %2, %0" : "+v"(c) : "v"(a), "a"(b));
  }
}
template <int M, bool Z>
__device__ __forceinline__ void mm(f32x4& c, const u32x4& a, const u32x4& b) {
  if constexpr (Z) {
    if constexpr (M == 0) asm volatile("v_mfma_f32_16x16x32_bf16 %0, %1, %2, 0" : "=&v"(c) : "v"(a), "v"(b));
    else asm volatile("v_mfma_f32_16x16x32_bf16 %0, %1, %2, 0" : "=&v"(c) : "a"(a), "v"(b));
  } else {
    if constexpr (M == 0) asm volatile("v_mfma_f32_16x16x32_bf16 %0, %1, %2, %0" : "+v"(c) : "v"(a), "v"(b));
    else asm volatile("v_mfma_f32_16x16x32_bf16 %0, %1, %2, %0" : "+v"(c) : "a"(a), "v"(b));
  }
}
#define MWAIT(c0, c1) asm volatile("s_nop 7\n\ts_nop 3" : "+v"(c0), "+v"(c1))
template <int K>
__device__ __forceinline__ float add_quad(float a, float b) {
  float r;
  asm volatile("v_add_f32_dpp %0, %1, %2 quad_perm:[%3,%3,%3,%3] row_mask:0xf bank_mask:0xf" : "=v"(r) : "v"(a), "v"(b), "n"(K));
  return r;
}
__device__ __forceinline__ uint32_t neg2(uint32_t v) {
  uint32_t r;
  asm volatile("v_xor_b32 %0, 0x80008000, %1" : "=v"(r) : "v"(v));
  return r;
}
__device__ __forceinline__ void keep_a(const u32x4& v) { asm volatile("" :: "a"(v)); }
__device__ __forceinline__ void keep_v(const u32x4& v) { asm volatile("" :: "v"(v)); }
__device__ __forceinline__ int pin(int v) { asm volatile("" : "+v"(v)); return v; }
__device__ __forceinline__ void await(uint32_t v, __amdgpu_buffer_rsrc_t r, int vo, int so) {
  uint32_t cnt;
  SB();
  asm volatile("v_cmp_gt_u32 vcc, 1, %0\n\ts_cbranch_vccz 2f\n\ts_mov_b32 %1, 0x40000\n"
               "1:\n\ts_sleep 1\n\tbuffer_load_dword %0, %2, %3, %4 offen sc1\n\ts_waitcnt vmcnt(0)\n\t"
               "v_cmp_gt_u32 vcc, 1, %0\n\ts_cbranch_vccz 2f\n\ts_sub_u32 %1, %1, 1\n\ts_cbranch_scc0 1b\n2:"
               : "+v"(v), "=&s"(cnt) : "v"(vo), "s"(r), "s"(so) : "vcc", "scc", "memory");
  SB();
}

#define OPOL NT
#define ORD2 0x0132
#define ORD3 0x2310
#define KCY 84
#define KCAP 200
#define KLD 60
#define KLA 40
#define KD1 112
#define KQ1 14
#define KPI1 130
#define KPC1 140
#define KYR 2
#define KVE 173
#define KS1 19
#define KAW 0
#define KSU 6
#define KU0 0
#define KU2 4
#define NX1 16
#define KXE 25
#define NB2 8
#define KXP 16
#define KXA 19
#define NXA 4
#define NB0 0
#define NB1 0
#define NX0 10
#define KP1 1
#define KXL 40
#define KXU 30
#define KJM 48
#define KJ0 60
#define KSL 8
#define KCY2 72
#define KQJ 99
#define KCJ 68
#define TS2 80
#define THM 5
#define THL -1
#define KS5 31
#define KD2 20
#define KPI2 40
#define KPC2 50
#define KXB 12
#define KD3 84
#define KQ3 12
#define KPI3 104
#define KPC3 114
#define KJR 5
#define KPI4 4
#define KPC4 34
enum { O_NONE, O_W, O_A, O_B, O_T, O_F, O_V, O_GI, O_Q, O_J, O_S1, O_Y1, O_X, O_S2, O_Y2, O_SIG, O_POLL, O_U };
constexpr int fill_t(int l) { return l < 2 ? O_T : l < 3 ? O_F : l < 35 ? O_V : l < 37 ? O_GI : l < 41 ? O_Q : O_J; }
constexpr int fill_j(int l) { return l < 2 ? l : l < 35 ? l - 3 : l < 37 ? l - 35 : l < 41 ? l - 37 : l - 41; }
constexpr int first(int c) { return c < 16 ? 6 * c : c < 32 ? 16 + 5 * c : 999; }
constexpr int chunk(int g) { return g < 96 ? g / 6 : (g - 16) / 5; }
constexpr int cpos(int c) { return c < 16 ? (c + 8) % 12 * 12288 : (c - 16) * 9216; }
struct Sched {
  int ty[3 * 184], ix[3 * 184];
  int before[3 * 184];
  int last[32];
  int nl, nj;
};
constexpr int main_t(int m) { return m >= 80 ? O_B : m % 5 < 2 ? O_W : O_A; }
constexpr int main_i(int m) { return m >= 80 ? m - 80 : m % 5 < 2 ? m / 5 * 2 + m % 5 : m / 5 * 3 + m % 5 - 2; }
constexpr int main_c(int m) { return m >= 80 ? 16 + (m - 80) / 9 * 4 + ((m - 80) % 9 - 1) / 2 : m / 5; }
constexpr int vfree(int l) { return first(18 + (8 * ((l - 3) >> 1) + 7) / 9) - 3; }
constexpr int hk(int i) { return i < 16 ? i : 4 * i - 45 + (i > 21) * 5 + (i > 23) * 3; }
constexpr Sched make_s1() {
  Sched c{};
  int m = 5 * GPRO, l = 0, o = m, y = 0, cr = KCY;
  for (int i = 0; i < GPRO; ++i) c.last[i] = 5 * i + 5;
  for (int t = 0; t < 3 * 184; ++t) {
    const int g = t / 3, j = t % 3;
    const bool thr = g >= TS0 && g < TS1;
    c.before[t] = o;
    if (j == 0) cr += g < TS0 ? 64 : 68 + 4 * (thr ? THR : g < TS2 ? THM - 2 : THL);
    if (cr > KCAP) cr = KCAP;
    if (j == 1 && ((g >= 93 && g < 99) || (g >= 174 && g < 179))) { c.ty[t] = O_S1; c.ix[t] = g < 99 ? g - 93 : g - 168; cr -= KCY; }
    else if (t == 3 * KD1 + 2) { c.ty[t] = O_SIG; c.ix[t] = 0; }
    else if (t == 3 * KPI1 + 2) { c.ty[t] = O_POLL; c.ix[t] = 0; }
    else if (g >= KVE) { if (y < 48 || m < 116) return c; }
    else if (g >= KPC1 && y < 48 && (j != 1 || KYR == 3)) { c.ty[t] = O_Y1; c.ix[t] = y++; }
    else if (g > KD1 - KQ1 && g <= KD1) { if (j == 1 && g <= KD1 - KQJ) { c.ty[t] = O_J; c.ix[t] = c.nj++; } }
    else if ((j == 1 || (j == 2 && thr)) && cr >= KCY) {
      const int k = main_c(m), fr = k < 12 ? 0 : first(k < 16 ? k - 10 : 6 + (9 * (k - ((m - 80) % 9 ? 16 : 13)) + 8) / 12) - 3;
      const bool fo = l < 35 && m >= 80 && (l < 3 || g >= vfree(l));
      if (m < 116 && g >= fr && first(k) - g <= (k < 16 ? KLA : KLD) && (m < 80 || l >= 3)) {
        c.ty[t] = main_t(m); c.ix[t] = main_i(m);
        cr -= g < TS0 ? 64 : KCY;
        c.last[k] = o + 1;
        ++m;
      } else if (fo) {
        c.ty[t] = fill_t(l); c.ix[t] = fill_j(l);
        cr -= KCY;
        ++l;
      } else if (g >= KJ0 && c.nj < KJM) {
        c.ty[t] = O_J; c.ix[t] = c.nj++;
        cr -= KCJ;
      }
    }
    if (c.ty[t]) ++o;
  }
  c.nl = l;
  return c;
}
constexpr Sched S1 = make_s1();
constexpr int NFILL = 3 + 32 + 2 + 4 + 64 - S1.nj;
constexpr int fill_i(int l) { return fill_j(l) + (l < 41 ? 0 : S1.nj); }
constexpr int NL0 = S1.nl + NB0, NL1 = NL0 + NB1, NL2 = NL1 + NB2;
static_assert(S1.nl > 2 && NL0 <= 33 && NL2 >= 35 && NL2 <= NFILL && first(chunk(KD1 + 4)) == KD1 + 4, "schedule");
constexpr Sched make_s2() {
  Sched c{};
  int l = NL2, x = NX0, y = 0, cr = 0;
  for (int t = 0; t < 3 * 136; ++t) {
    const int g = t / 3, j = t % 3;
    const bool thr = g >= TU0 && g < TU1, fs = j == 1 || (j == 2 && thr);
    if (j == 0) cr += 68 + 4 * (thr ? THR2 : THL);
    if (cr > KCAP) cr = KCAP;
    if (j == 1 && (g & 1) && ((g > 60 && g < 68) || (g > 124 && g < 132))) { c.ty[t] = O_S2; c.ix[t] = g < 68 ? (g - 61) / 2 : (g - 117) / 2; cr -= KCY2; }
    else if (t == 3 * KD2 + 2) { c.ty[t] = O_SIG; c.ix[t] = 3; }
    else if (t == 3 * KPI2 + 2) { c.ty[t] = O_POLL; c.ix[t] = 3; }
    else if (t == 3 * KD3 + 2) { c.ty[t] = O_SIG; c.ix[t] = 4; }
    else if (t == 3 * KPI3 + 2) { c.ty[t] = O_POLL; c.ix[t] = 4; }
    else if (g >= KPC3) { if (y < 32) { c.ty[t] = O_Y2; c.ix[t] = y++; } }
    else if (g <= KD2) {
      if (j == 1 && g >= KU0 && g < KU0 + KU2) { c.ty[t] = O_U; c.ix[t] = 16 - KU2 + g - KU0; }
      else if (j == 1 && x < NX1) { c.ty[t] = O_X; c.ix[t] = x++; }
    } else if (g > KD3 - KQ3 && g <= KD3) {
    } else if (x < (g >= KPC2 ? 64 : 32) && ((j && x >= 32 && x < 32 + KXB) || (j == 1 && 2 * x - g <= KXU) || (fs && cr >= KCY2 && 2 * x - g <= KXL))) {
      c.ty[t] = O_X; c.ix[t] = x++;
      cr -= KCY2;
      if (cr < 0) cr = 0;
    } else if (fs && cr >= KCY2 && l < NFILL) {
      c.ty[t] = fill_t(l); c.ix[t] = fill_i(l);
      cr -= KCJ;
      ++l;
    }
    if (t == 3 * KD3 && l < 41) return c;
    if (c.ty[t] == O_X && g >= (2 * c.ix[t] & ~7) - 1) return c;
  }
  c.nl = x == 64 && y == 32 ? l : -1;
  return c;
}
constexpr Sched S2 = make_s2();
static_assert((KU2 == 0 || KU2 == 4 || (KU2 == 8 && !KP1)) && KXA > KXP && KXA < 22 && KS1 < 22 && S2.nl + S1.nj >= 41 + 32 && S2.nl + (87 - KS5) * KJR / 8 >= NFILL, "schedule");

#define PADG() PAD(joint_kernel, 7, 3)
#define PADJ() PADG()
KERNEL __launch_bounds__(256, 1) void joint_kernel(char* ws, uint16_t* __restrict__ out) {
  extern __shared__ __attribute__((aligned(16))) char smem[];
  pad_macros();
  PADG();
  SB();
  const int bid = blockIdx.x, xcd = bid & 7, slot = bid >> 3;
  const int tid = threadIdx.x, lane = tid & 63;
  const int w = __builtin_amdgcn_readfirstlane(tid >> 6);
  const int cb = (slot & 3) * 4 + w;
  const int bin1 = xcd * 4 + (slot >> 3), mt1 = (slot >> 2) & 1;
  const int bin2 = xcd * 2 + (slot >> 4);
  const __amdgpu_buffer_rsrc_t ur = mkbuf(ws + (unsigned)(A_U1 + bin1 * UB1 + mt1 * (168 * 128)), UB1);
  const __amdgpu_buffer_rsrc_t wr = mkbuf(ws + (unsigned)(A_W1 + bin1 * WBIN + cb * WCB), WCB);
  const int vd = w * 1024 + (lane >> 3) * 128 + (((lane & 7) ^ ((4 * w + (lane >> 4)) & 7)) * 16);
  const int rho = lane & 15, kq = lane >> 4;
  const int n = lane & 31, h = lane >> 5;
  const float* ge = (const float*)(ws + A_GE);
  __amdgpu_buffer_rsrc_t vr, ys, yl, ub, yb, wb, fr;
  u32x4 Wm[32], X[64], wt[64], y1[3][4], yv[2][4][2], F, gi[2], o[11], up[8];
  int ve, vt, v1[3], vx, vs, vy, vu, vf[3], vp[3];
  uint32_t pl[2];
  constexpr int FO[6] = {F1, F1 + 1024, F2, F2 + 4096, F3, F3 + 4096};
  auto yload = [&](int b, int i) { yv[b][i & 3][i >> 4][(i >> 2) & 3] = __builtin_amdgcn_raw_buffer_load_b32(yb, vy + (i & 3) * 1024, ((i >> 4) * 8 + ((i >> 2) & 3)) * YB2 + b * 65536, WT); };
  auto issue = [&](auto it, auto ii) {
    constexpr int t = decltype(it)::value, i = decltype(ii)::value;
    if constexpr (t == O_W) Wm[i] = __builtin_amdgcn_raw_buffer_load_b128(wr, lane * 16 + (i & 3) * 1024, (i >> 2) * 4096, 0);
    else if constexpr (t == O_A) dma<16>(ur, smem + cpos(i / 3) + i % 3 * 4096 + w * 1024, vd, i / 3 * PL1 + i % 3 * 4096);
    else if constexpr (t == O_B && i % 9) dma<16>(ur, smem + cpos(main_c(80 + i)) + (i % 9 - 1 & 1) * 4096 + w * 1024, vd, (main_c(80 + i) - 16) * PL1 + (3 + (i % 9 - 1 & 1)) * 4096);
    else if constexpr (t == O_B) dma<16>(ur, smem + cpos(main_c(80 + i)) + 8192 + w * 9216, ve, i / 9 * 4 * PL1);
    else if constexpr (t == O_T) dma<16>(mkbuf(&tabs.g1[2 * w][0][0], 2048), smem + J_F + w * 2048 + i * 1024, lane * 16, i * 1024);
    else if constexpr (t == O_F) F = ((const u32x4*)&tabs.f2[0][0])[lane];
    else if constexpr (t == O_V) dma<16>(vr, smem + (i >> 1) * 8192 + w * 2048 + (i & 1) * 1024, lane * 16, i * 1024);
    else if constexpr (t == O_GI) gi[i] = ((const u32x4*)&tabs.g2[i][0][0])[lane];
    else if constexpr (t == O_Q) dma<16>(mkbuf(ge + w * 1024, 4096), smem + J_G + w * 4096 + i * 1024, lane * 16, i * 1024);
    else if constexpr (t == O_J) wt[i] = __builtin_amdgcn_raw_buffer_load_b128(wb, lane * 16 + (i & 3) * 1024, (i >> 5) * 131072 + ((i >> 2) & 7) * 4096, 0);
    else if constexpr (t == O_S1) __builtin_amdgcn_raw_buffer_store_b128(o[i], ys, vt + (i & 3) * 1024, (i >> 2) * 4096, WT);
    else if constexpr (t == O_Y1) y1[i % 3][i % 48 / 12][i / 3 & 3] = __builtin_amdgcn_raw_buffer_load_b32(yl, v1[i % 3], (i % 48 / 12 * 8 + (i / 3 & 3)) * YB1 + i / 48 * (84 * 64), WT);
    else if constexpr (t == O_X) X[i] = __builtin_amdgcn_raw_buffer_load_b128(ub, vx + (i & 3) * 1024, (i >> 5) * 16 * UB2 + ((i >> 2) & 7) * 4096, WT);
    else if constexpr (t == O_S2) __builtin_amdgcn_raw_buffer_store_b128(o[i & 3], yb, vs + (i & 3) * 1024, (i >> 2) * 65536, WT);
    else if constexpr (t == O_Y2) yload(0, i);
    else if constexpr (t == O_SIG) __builtin_amdgcn_raw_buffer_store_b32(1u, fr, vf[i >> 1], FO[i], WT);
    else if constexpr (t == O_POLL) pl[i & 1] = __builtin_amdgcn_raw_buffer_load_b32(fr, vp[i >> 1], FO[i], WT);
    else if constexpr (t == O_U) __builtin_amdgcn_raw_buffer_store_b128(up[i & 7], ub, vu + ((i >> 1) & 3) * 256, ((i >> 3) * 16 + (i & 1) * 8) * UB2, WT);
    SB();
  };
  auto fills = [&](auto i0, auto i1) {
    sfor<decltype(i0)::value, decltype(i1)::value>([&](auto il) { issue(IC<fill_t(decltype(il)::value)>{}, IC<fill_i(decltype(il)::value)>{}); });
  };
  sfor<0, 5 * GPRO>([&](auto il) { issue(IC<main_t(decltype(il)::value)>{}, IC<main_i(decltype(il)::value)>{}); });
  const int l0 = (int)(uint32_t)(uintptr_t)(__attribute__((address_space(3))) char*)smem;
  int ab[3][2], ga[4], tj, ty, ta, tau;
  float ok[2];
#pragma unroll
  for (int ri = 0; ri < 2; ++ri)
#pragma unroll
    for (int wn = 0; wn < 3; ++wn) ab[wn][ri] = pin(l0 + rho * 128 + ((((kq >> 1) * 4 + ri * 2 + (kq & 1)) ^ (rho >> 1)) * 16) + wn * 65536);
  auto setup = [&](int k) {
    asm volatile("" : "+s"(ws));
    if (k == 0) {
      vr = mkbuf(ws + (unsigned)(A_W2 + bin2 * WBIN + cb * WCB), WCB);
      ys = mkbuf(ws + (unsigned)(A_Y1 + bin1 * YB1 + cb * YP1 + mt1 * (168 * 64)), 168 * 64);
    } else if (k == 1) {
      yl = mkbuf(ws + A_Y1, Y1BYTES);
      ub = mkbuf(ws + A_U2, U2BYTES);
      yb = mkbuf(ws + A_Y2, Y2BYTES);
      wb = mkbuf(ws + (unsigned)(A_WF + w * 32768), 131072 + 32768);
      fr = mkbuf(ws + A_FL, NFLAG * 4);
    } else if (k == 2) {
      ve = pin(20480 + w * PL1 + (lane >> 3) * 128 + (((lane & 7) ^ kq) * 16));
      vt = pin(rho * 64 + kq * 16);
      tj = 4 * (slot & 15) + w;
      asm volatile("" : "+s"(tj));
      ty = tj % 3;
      ta = tj / 3 * 4 + (ty ? ty : -1);
      asm volatile("" : "+s"(ty), "+s"(ta));
    } else if (k < 6) {
      const int i = k - 3, g = ta + i + 84 * ((n >> 2) & 1), e = g >= 84, r = g - 84 * e;
      const int v = pin(4 * h * YB1 + bin2 * YP1 + (84 * (r >= 42) + 2 * r + e) * 64 + (n >> 3) * 16 + (n & 3) * 4);
      v1[i] = pin(g < (tj < 63 && i != 2 * ty ? 168 : -2) ? v : OOB);
      if (i) ok[i - 1] = __builtin_bit_cast(float, pin((unsigned)(32 * (2 * tj + i - 1 + 126 * ((n >> 2) & 1)) - 16 + 16 * h) < 8000u ? 0x3F800000 : 0x7F800000));
    } else if (k < 8) {
      if (k == 6) tau = pin(16 * ((n >> 2) & 1) + 8 * ((n >> 3) & 1) + 2 * (n & 3) + (n >> 4) + 16 + (ty ? 16 * ty : 48));
#pragma unroll
      for (int i = (3 * k - 18) >> 1; i < 3 * k - 17; ++i) {
        const int r = tau - 16 * (i >> 1) - 48 * (i & 1);
        ga[i] = pin(l0 + J_F + h * 1024 + ((unsigned)r < 64u ? r ^ ((r >> 4) & 1) : 0) * 16);
      }
    } else if (k == 8) {
      const int ge_ = ta + (h ? 2 - (ty == 1) : ty == 0);
      vp[0] = pin((bin2 >> 2) * 256 + (ge_ >= 42 && ge_ < 84) * 128 + n * 4);
      vf[0] = pin((slot & 3) * 256 + mt1 * 128 + bin1 * 4 + tid * 65536);
    } else {
      vf[1] = vf[2] = pin((slot & 15) * 256 + bin2 * 4 + tid * 65536);
      vu = pin((rho >> 1) * UB2 + (slot & 15) * 32768 + bin2 * 2048 + (w >> 1) * 1024 + (w & 1) * 128 + (lane & 1) * 64 + kq * 16);
    }
    SB();
  };
  auto aread = [&](int g, int ri) -> u32x4 {
    const int a = cpos(chunk(g)) + (g - first(chunk(g))) * 2048;
    return lds_ld(ab[a >> 16][ri] + (a & 65535));
  };
  f32x4 yr[11], yi[11];
  u32x4 A[4][2], nw[2], gf[16];
  auto gread = [&](int i) { gf[i] = lds_ld(ga[i >> 2] + (i & 3) * 2048); };
  auto trans = [&](auto ib, auto&& hook) {
    constexpr int b = decltype(ib)::value;
    f32x16 d[2], u[2][2];
    u32x4 a;
    sfor<0, 16>([&](auto ii) {
      constexpr int i = decltype(ii)::value;
      mm<0, (i & 7) == 0>(d[i >> 3], gf[i], y1[(i >> 3) + ((i >> 2) & 1)][i & 3]);
      PADG();
      if constexpr (i + 6 < 16) gread(i + 6);
      SB();
      hook(ii);
    });
    auto pack = [&](int q) {
#pragma unroll
      for (int e = 0; e < 2; ++e) {
#pragma unroll
        for (int z = 0; z < 2; ++z)
#pragma unroll
          for (int j = 0; j < 4; ++j) up[4 * q + 2 * e + z][j] = pk2bf(u[q][z][8 * e + 2 * j], u[q][z][8 * e + 2 * j + 1]);
        PADG();
        swap16(up[4 * q + 2 * e], up[4 * q + 2 * e + 1]);
        PADG();
      }
      SB();
    };
    auto put = [&](auto iq) {
      constexpr int q = decltype(iq)::value;
      sfor<8 * b + 4 * q, 8 * b + 4 * q + 4>([&](auto ii) { issue(IC<O_U>{}, ii); });
      PADG();
      st_pad(up[4 * q + 2], up[4 * q + 3]);
      SB();
    };
    sfor<0, 4>([&](auto ii) {
      constexpr int i = decltype(ii)::value, q = i >> 1, z = i & 1;
      if constexpr (i < 2) asm volatile("s_nop 2" : "+v"(d[z]));
#pragma unroll
      for (int j = 0; j < 2; ++j) { a[j] = gelu2(d[z][8 * q + 2 * j], d[z][8 * q + 2 * j + 1], ok[z]); PADG(); }
      SB();
      hook(IC<16 + 2 * i>{});
#pragma unroll
      for (int j = 2; j < 4; ++j) { a[j] = gelu2(d[z][8 * q + 2 * j], d[z][8 * q + 2 * j + 1], ok[z]); PADG(); }
      asm volatile("s_nop 1" : "+v"(a));
      SB();
      mm<0, true>(u[q][z], a, F);
      SB();
      hook(IC<17 + 2 * i>{});
      if constexpr (i == 2) { pack(0); if constexpr (!b || KP1) put(IC<0>{}); }
    });
    MWAIT(u[1][0], u[1][1]);
    pack(1);
    hook(IC<24>{});
    if constexpr (b && !KP1 && KU2 < 8) put(IC<0>{});
    if constexpr (!b || !KU2) put(IC<1>{});
  };
#pragma unroll
  for (int k = 0; k < KSU; ++k) setup(k);
  SB();
  wait_vm<5 * GPRO - 5>();
  asm volatile("s_barrier" ::: "memory");
#pragma unroll
  for (int z = 0; z < 3; ++z) { A[z][1] = aread(z, 1); SB(); A[z][0] = aread(z, 0); SB(); }
#pragma unroll
  for (int j = 0; j < 4; ++j) nw[0][j] = neg2(Wm[1][j]);
  SB();
  sfor<0, 179>([&](auto ig) {
    constexpr int g = decltype(ig)::value, c = chunk(g), ks = c & 15, mb = g - first(c) + (c < 16 ? 0 : 6), e = first(c + 1) - g;
    if constexpr (g == KPC1) { await(pl[0], fr, vp[0], FO[0]); }
    auto mf = [&](auto ij) {
      constexpr int j = decltype(ij)::value, i = (((g & 1) ? ORD1 : ORD0) >> (12 - 4 * j)) & 3;
      constexpr bool z = ks == 0 && !(j & 1);
      if constexpr (g < 176) {
        if constexpr (i == 0) mm<1, z>(yr[mb], Wm[2 * ks], A[g & 3][0]);
        else if constexpr (i == 1) mm<0, z>(yr[mb], nw[c & 1], A[g & 3][1]);
        else if constexpr (i == 2) mm<0, z>(yi[mb], Wm[2 * ks + 1], A[g & 3][0]);
        else mm<1, z>(yi[mb], Wm[2 * ks], A[g & 3][1]);
      }
      PADG();
    };
    auto cvt = [&](int j) {
      constexpr int m = g >= 92 && g < 98 ? g - 92 : g >= 173 && g < 178 ? g - 167 : -1;
      if constexpr (m >= 0) {
        if constexpr (g == 177) if (!j) asm volatile("s_nop 1" : "+v"(yr[m]), "+v"(yi[m]));
#pragma unroll
        for (int i = 2 * j; i < 2 * j + 2; ++i) o[m][i] = pk2bf(yr[m][i], yi[m][i]);
      }
      if constexpr (S1.ty[3 * g + 1] == O_S1) if (j) keep_v(o[S1.ix[3 * g + 1]]);
      if constexpr (g >= 173 && g < 176) gread(2 * (g - 173) + j);
      SB();
    };
    if constexpr (g < 176 && !(g & 1)) keep_v(A[(g + 1) & 3][0]);
    mf(IC<0>{});
    if constexpr (g + 3 < 176) { A[(g + 3) & 3][1] = aread(g + 3, 1); SB(); A[(g + 3) & 3][0] = aread(g + 3, 0); }
    SB();
    mf(IC<1>{});
    issue(IC<S1.ty[3 * g]>{}, IC<S1.ix[3 * g]>{});
    cvt(0);
    mf(IC<2>{});
    issue(IC<S1.ty[3 * g + 1]>{}, IC<S1.ix[3 * g + 1]>{});
    mf(IC<3>{});
    if constexpr (e == 4) {
      static_assert(S1.last[c + 1] && S1.before[3 * g + 2] >= S1.last[c + 1], "next chunk not requested yet");
      SB();
      if constexpr (g == KD1) { DRAIN(); }
      else {
        if constexpr (g < KD1 || S1.last[c + 1] > S1.before[3 * KD1 + 2]) wait_vm<S1.before[3 * g + 2] - S1.last[c + 1]>();
        asm volatile("s_barrier" ::: "memory");
      }
      SB();
    }
    issue(IC<S1.ty[3 * g + 2]>{}, IC<S1.ix[3 * g + 2]>{});
    if constexpr (g >= TS0 && g < TS0 + 30 - 3 * KSU) { if constexpr ((g - TS0) % 3 == 1) setup(KSU + (g - TS0) / 3); }
    else if constexpr (g >= TS0 && g < TS1) asm volatile("s_nop %0" ::"n"(S1.ty[3 * g + 2] ? THR - 3 : THR));
    else if constexpr (g >= TS1 && g < TS2) asm volatile("s_nop %0" ::"n"(THM));
    else if constexpr (THL >= 0 && g >= TS1 && g < 173 && !S1.ty[3 * g + 2]) asm volatile("s_nop %0" ::"n"(THL + 2));
    if constexpr (e <= 3 && e >= 2) {
#pragma unroll
      for (int j = 6 - 2 * e; j < 8 - 2 * e; ++j) nw[(c + 1) & 1][j] = neg2(Wm[(2 * ks + 3) & 31][j]);
    }
    SB();
    cvt(1);
  });
  st_pad(o[10], o[10]);
  trans(IC<0>{}, [&](auto ii) {
    constexpr int i = decltype(ii)::value;
    if constexpr (i < 16) fills(IC<S1.nl + NB0 * i / 16>{}, IC<S1.nl + NB0 * (i + 1) / 16>{});
    if constexpr (i == KS1) { DRAIN(); issue(IC<O_SIG>{}, IC<1>{}); }
  });
  const int pos = 32 * xcd + slot, seg = pos < 250 ? pos : 0;
  int wa[2], ra[4], fa[4], qa, vo;
  {
    const int grp = (slot & 12) + w, s = seg + (kq & 1), e = s >= 126, r = s - 126 * e;
        vx = pin(bin2 * UB2 + grp * 32768 + lane * 16);
    vs = pin(bin2 * YB2 + (slot & 3) * 131072 + grp * 4096 + ((rho & 2) * 4 + (rho & 4) + (rho >> 3) * 2 + (rho & 1)) * 64 + kq * 16);
    vy = pin(4 * h * YB2 + w * 131072 + (r >> 3) * 4096 + (8 * e + (r & 7)) * 64 + rho * 4);
    vp[1] = pin(grp * 256 + rho * 4);
    vp[2] = pin(((r >> 5) * 4 + w) * 256 + rho * 4);
#pragma unroll
    for (int i = 0; i < 2; ++i) wa[i] = pin(l0 + lane * 16 + i * 65536);
  }
#pragma unroll
  for (int i = 0; i < 6; ++i) gread(i);
  if constexpr (KSL) __builtin_amdgcn_s_sleep(KSL);
  issue(IC<O_POLL>{}, IC<1>{});
  if constexpr (!KAW) { DRAIN(); issue(IC<O_SIG>{}, IC<2>{}); }
  await(pl[1], fr, vp[0], FO[1]);
  if constexpr (KAW == 1) { DRAIN(); issue(IC<O_SIG>{}, IC<2>{}); }
  sfor<0, 48>([&](auto ii) { issue(IC<O_Y1>{}, IC<48 + decltype(ii)::value % 16 * 3 + decltype(ii)::value / 16>{}); PADJ(); });
  if constexpr (KAW == 2) { DRAIN(); issue(IC<O_SIG>{}, IC<2>{}); }
  fills(IC<NL0>{}, IC<NL1>{});
  auto wread = [&](int g, int j) -> u32x4 { return lds_ld(wa[(g >> 5) & 1] + ((g >> 3 & 3) * 16 + (g & 7) + j * 8) * 1024); };
  trans(IC<1>{}, [&](auto ii) {
    constexpr int i = decltype(ii)::value, e = KXE;
    if constexpr (i < KXP) fills(IC<NL1 + NB2 * i / KXP>{}, IC<NL1 + NB2 * (i + 1) / KXP>{});
    if constexpr (i == KXP) issue(IC<O_POLL>{}, IC<2>{});
    if constexpr (i == KXA) {
      await(pl[0], fr, vp[1], FO[2]);
      sfor<0, NXA>([&](auto ix) { issue(IC<O_X>{}, ix); PADJ(); });
    }
    if constexpr (i > KXA && i < e) sfor<NXA + (NX0 - NXA) * (hk(i - 1) - hk(KXA)) / (hk(e - 1) - hk(KXA)), NXA + (NX0 - NXA) * (hk(i) - hk(KXA)) / (hk(e - 1) - hk(KXA))>([&](auto ix) { issue(IC<O_X>{}, ix); PADJ(); });
    if constexpr (i == 22) asm volatile("s_barrier" ::: "memory");
    if constexpr (i == 23) {
#pragma unroll
      for (int z = 0; z < 3; ++z) { A[z][1] = wread(z, 1); SB(); A[z][0] = wread(z, 0); SB(); }
    }
  });
  f32x4 ac[16][2];
  float re, im;
  sfor<0, 132>([&](auto ig) {
    constexpr int g = decltype(ig)::value, k = g >> 3, mb = (g & 7) + (g & 64) / 8;
    if constexpr (g == KPC2) { await(pl[1], fr, vp[1], FO[3]); }
    if constexpr (g == KPC3) { await(pl[0], fr, vp[2], FO[4]); }
    auto mf = [&](auto ij) {
      constexpr int j = decltype(ij)::value, i = (((g & 1) ? ORD3 : ORD2) >> (12 - 4 * j)) & 3;
      if constexpr (g < 128) mm<0, (k & 7) == 0 && !(j & 1)>(ac[mb][i >> 1], A[g & 3][i & 1], X[4 * k + 2 * (i & 1) + (i >> 1)]);
      PADJ();
    };
    auto cvt = [&](int j) {
      constexpr int m = g >= 59 && g < 67 ? g - 59 : g >= 123 && g < 131 ? g - 115 : -1;
      if constexpr (m >= 0) {
        const int i = 2 * (m & 1) + j;
        if constexpr (g == 129) if (!j) asm volatile("s_nop 1" : "+v"(ac[15][0]), "+v"(ac[15][1]));
        asm volatile("v_subrev_f32_dpp %0, %4, %2 row_shl:4 row_mask:0xf bank_mask:0x5\n\t"
                     "v_add_f32_dpp %1, %2, %4 row_shl:4 row_mask:0xf bank_mask:0x5\n\t"
                     "v_sub_f32_dpp %0, %3, %5 row_shr:4 row_mask:0xf bank_mask:0xa\n\t"
                     "v_add_f32_dpp %1, %5, %3 row_shr:4 row_mask:0xf bank_mask:0xa"
                     : "=&v"(re), "=&v"(im) : "v"(ac[m & 14][0][i]), "v"(ac[m & 14][1][i]), "v"(ac[m | 1][0][i]), "v"(ac[m | 1][1][i]));
        o[(m >> 1) & 3][i] = pk2bf(re, im);
      }
      if constexpr (S2.ty[3 * g + 1] == O_S2) if (j) keep_v(o[S2.ix[3 * g + 1] & 3]);
      if constexpr (S2.ty[3 * g + 1] == O_U) if (j) keep_v(up[S2.ix[3 * g + 1] & 7]);
      SB();
    };
    if constexpr (g < 128 && !(g & 1)) keep_v(A[(g + 1) & 3][0]);
    mf(IC<0>{});
    if constexpr (g + 3 < 128) { A[(g + 3) & 3][1] = wread(g + 3, 1); SB(); A[(g + 3) & 3][0] = wread(g + 3, 0); }
    SB();
    mf(IC<1>{});
    issue(IC<S2.ty[3 * g]>{}, IC<S2.ix[3 * g]>{});
    cvt(0);
    mf(IC<2>{});
    issue(IC<S2.ty[3 * g + 1]>{}, IC<S2.ix[3 * g + 1]>{});
    mf(IC<3>{});
    if constexpr (g == KD2) { DRAIN(); }
    if constexpr (g == KD3) { DRAIN(); }
    issue(IC<S2.ty[3 * g + 2]>{}, IC<S2.ix[3 * g + 2]>{});
    if constexpr (g >= TU0 && g < TU1) asm volatile("s_nop %0" ::"n"(S2.ty[3 * g + 2] ? THR2 - 3 : THR2));
    else if constexpr (THL >= 0 && g < 123 && !S2.ty[3 * g + 2]) asm volatile("s_nop %0" ::"n"(THL + 2));
    if constexpr ((g & 7) == 7 && g < 127) { keep_v(X[4 * k + 7]); }
    SB();
    cvt(1);
    if constexpr (g >= 84 && g < 88) ra[g & 3] = pin(l0 + ((w + 1 + g) & 3) * 4096 + lane * 16);
    if constexpr (g >= 88 && g < 92) fa[g & 3] = pin(l0 + J_F + ((w + 1 + g) & 3) * 2048 + lane * 16);
    if constexpr (g == 82) qa = pin(l0 + J_G + w * 4096 + (h * 8 + (n >> 4) * 4 + (n & 3)) * 16);
    if constexpr (g == 83) vo = pin(h * 4096 + w * 128 + n * 4);
    SB();
  });
  st_pad(o[3], o[3]);
  float p[2][32], x[8], mx = 0.f;
  f32x4 q[2][8], fe[4];
  u32x4 tp[2][4];
  auto jstep = [&](auto it) {
    constexpr int t = decltype(it)::value, l = S2.nl + t * KJR / 8;
    if constexpr (l < NFILL && l < S2.nl + (t + 1) * KJR / 8) issue(IC<O_J>{}, IC<fill_i(l)>{});
  };
  auto front = [&](auto ib, auto&& after) {
    constexpr int b = decltype(ib)::value & 1;
    f32x4 d[4];
    u32x4 fb[8];
#pragma unroll
    for (int c = 0; c < 4; ++c) {
      mm<0, true>(d[c], yv[b][c][0], gi[0]);
      if constexpr (decltype(ib)::value < 2) PADJ();
    }
#pragma unroll
    for (int c = 0; c < 4; ++c) {
      mm<0, false>(d[c], yv[b][c][1], gi[1]);
      if constexpr (decltype(ib)::value < 2) PADJ();
    }
    asm volatile("s_nop 4" : "+v"(d[0]));
#pragma unroll
    for (int j = 0; j < 2; ++j) {
#pragma unroll
      for (int c = 0; c < 4; ++c) {
        fb[j][c] = gelu2(d[2 * j + (c >> 1)][2 * (c & 1)], d[2 * j + (c >> 1)][2 * (c & 1) + 1]);
        if constexpr (decltype(ib)::value < 2) PADJ();
      }
      lds_st(fa[3] + j * 1024, fb[j]);
      SB();
    }
    sfor<0, 32>([&](auto ii) {
      constexpr int i = decltype(ii)::value, eb = i < 8 ? i >> 1 : (i - 8) / 6, r = i < 8 ? i & 1 : 2 + (i - 8) % 6;
      mm<1, r == 0>(fe[eb], wt[i], fb[r]);
      if constexpr (decltype(ib)::value < 2) PADJ();
      SB();
      if constexpr (i == 0) {
        BAR();
      }
      if constexpr (i < 3) {
        fb[2 * i + 2] = lds_ld(fa[i]);
        fb[2 * i + 3] = lds_ld(fa[i] + 1024);
      }
      SB();
      after(ii);
    });
  };
  auto twrite = [&](auto iG) {
    constexpr int V = decltype(iG)::value + 31;
    if constexpr (V >= 0 && V < 256 && (V & 7) == 7) lds_st(ra[3] + J_T + ((V >> 5) & 1) * 16384 + ((V >> 3) & 3) * 1024, tp[(V >> 5) & 1][(V >> 3) & 3]);
    SB();
  };
  auto tslot = [&](auto iG) {
    constexpr int G = decltype(iG)::value;
    constexpr int A = G + 36, B = G + 35, C = G + 34, D = G + 33;
    if constexpr (B >= 0 && B < 256) asm volatile("v_rcp_f32 %0, %0" : "+v"(x[B & 7]));
    if constexpr (C >= 0 && C < 256) asm volatile("v_fma_f32 %0, -%1, %0, 0.5" : "+v"(x[C & 7]) : "v"(q[C >> 7][(C >> 2) & 7][C & 3]));
    PADJ();
    if constexpr (A >= 0 && A < 256) x[A & 7] = add_quad<(A >> 5) & 3>(p[A >> 7][A & 31], q[A >> 7][(A >> 2) & 7][A & 3]);
    SB();
    if constexpr (D >= 0 && D < 256 && (D & 1)) tp[(D >> 5) & 1][(D >> 3) & 3][(D >> 1) & 3] = pk2bf(x[(D - 1) & 7], x[D & 7]);
    SB();
  };
  auto stepE = [&](int b, int eb, int i) {
    const float a = fe[eb][i] * C3;
    mx = __builtin_fmaxf(mx, __builtin_fabsf(a));
    const uint32_t e = __builtin_bit_cast(uint32_t, __builtin_amdgcn_exp2f(a));
    const auto r = __builtin_amdgcn_permlane16_swap(e, e, false, false);
    p[b][8 * eb + i] = __builtin_bit_cast(float, (uint32_t)r[0]);
    p[b][8 * eb + 4 + i] = __builtin_bit_cast(float, (uint32_t)r[1]);
  };
  auto head = [&](auto ib) {
    constexpr int b = decltype(ib)::value;
    front(ib, [&](auto ii) {
      constexpr int i = decltype(ii)::value;
      if constexpr (b == 0 && i == KS5) {
        DRAIN();
        issue(IC<O_SIG>{}, IC<5>{});
        asm volatile("s_cmpk_lt_i32 %0, 250\n\ts_cbranch_scc1 1f\n\ts_endpgm\n1:" :: "s"(pos) : "scc");
      }
      if constexpr (i >= 3 && i < 11) q[b][i - 3] = __builtin_bit_cast(f32x4, lds_ld(qa + b * 2048 + (i - 3) * 256));
      if constexpr (b == 0 && i > KS5) jstep(IC<i - KS5 - 1>{});
      if constexpr (i >= 16 && (i - 16) % 6 < 4) {
        if constexpr ((i - 16) % 6 == 0) asm volatile("s_nop 1" : "+v"(fe[(i - 16) / 6]));
        stepE(b, (i - 16) / 6, (i - 16) % 6);
      }
      SB();
    });
    if constexpr (b == 0) sfor<-36, -32>([&](auto ir) { tslot(ir); });
    else asm volatile("s_nop 7" ::);
    asm volatile("" : "+v"(fe[3]));
#pragma unroll
    for (int i = 0; i < 4; ++i) { stepE(b, 3, i); PADJ(); }
    mx = __builtin_fmaxf(mx, q[b][0][0] < 0.f || q[b][4][0] < 0.f ? 2 * EMAX : 0.f);
    if constexpr (b) *(__attribute__((address_space(3))) float*)(uintptr_t)(uint32_t)(l0 + J_R + tid * 4) = mx;
    asm volatile("s_nop 1");
    SB();
  };
  head(IC<0>{});
  sfor<-32, 0>([&](auto ir) { jstep(IC<decltype(ir)::value + 63 - KS5>{}); twrite(ir); tslot(ir); });
  const __amdgpu_buffer_rsrc_t ob = mkbuf(out + (size_t)seg * 16 * 8 * DCH, 4000 * 4096 + 65536);
  f32x16 acc[2][2];
  u32x4 a[16];
  uint32_t ov[16];
  int vq[2] = {vo, vo + 8192};
  auto store = [&](int i) { __builtin_amdgcn_raw_buffer_store_b32(ov[i], ob, vq[(i >> 2) & 1] + ((i >> 3) * 4 + (i & 3)) * 512, 0, OPOL); };
  auto next = [&](int st, int j) {
    vq[j] += (st & 3) ? 16384 : 4000 * 4096 - 3 * 16384;
    asm volatile("" : "+v"(vq[j]));
  };
  sfor<0, 256>([&](auto iG) {
    constexpr int G = decltype(iG)::value, st = G >> 5, g = G & 31, r = g >> 1, z = g & 1;
    if constexpr (G == 92) { head(IC<1>{}); }
    mm<2, r == 0>(acc[st & 1][z], r < 4 ? tp[st & 1][r & 3] : a[r], wt[32 + g]);
    PADJ();
    SB();
    if constexpr (g == 2) BAR();
    if constexpr (st == 0 && g < 31) keep_a(wt[33 + g]);
    if constexpr (z && r >= 3 && r < 15) keep_v(a[r + 1]);
    constexpr int rd = g >= 3 && g < 6 ? g + 1 : (g >= 7 && g <= 23 && z) ? (g + 7) / 2 : 0;
    constexpr int sn = g == 1 ? 15 : g == 2 ? 0 : g == 6 ? 1 : g >= 25 ? g - 17 : (g > 8 && !z && (g & 7)) ? (g - 6) / 2 - (g > 16) : -1;
    if constexpr (rd) a[rd] = lds_ld(ra[(rd >> 2) - 1] + J_T + (st & 1) * 16384 + (rd & 3) * 1024);
    if constexpr (sn >= 0 && st > (g == 1)) store(sn);
    if constexpr (G < 24) jstep(IC<G + 63 - KS5>{});
    if constexpr (G == KPI4) issue(IC<O_POLL>{}, IC<5>{});
    if constexpr (G == KPC4) await(pl[1], fr, vp[2], FO[5]);
    if constexpr (G >= KPC4 && G < KPC4 + 32) yload(1, G - KPC4);
    twrite(iG);
    tslot(iG);
    if constexpr (st > 0 && g == 30) next(st, 0);
    if constexpr (st > 1 && g == 4) next(st - 1, 1);
    if constexpr (G == 225) asm volatile("s_nop 5" : "+v"(acc[0][0]));
    if constexpr (st > 0 && z) ov[r] = pk2bf(acc[(st - 1) & 1][0][r], acc[(st - 1) & 1][1][r]);
    SB();
  });
  store(15);
  next(7, 1);
  const f32x4 rg = __builtin_bit_cast(f32x4, lds_ld(l0 + J_R + lane * 16));
  MWAIT(acc[1][0], acc[1][1]);
#pragma unroll
  for (int i = 0; i < 16; ++i) { ov[i] = pk2bf(acc[1][0][i], acc[1][1][i]); store(i); PADJ(); }
  if (__builtin_expect(__builtin_amdgcn_ballot_w64(rg[0] > EMAX || rg[1] > EMAX || rg[2] > EMAX || rg[3] > EMAX) != 0, 0)) {
    asm volatile("s_waitcnt vmcnt(0)" ::: "memory");
#pragma unroll 1
    for (int b = 0; b < 2; ++b) {
#pragma unroll
      for (int i = 0; i < 32; ++i) yload(0, i);
      vy += 65536;
      front(IC<2>{}, [](auto) {});
      asm volatile("s_nop 7" : "+v"(fe[3]));
#pragma unroll 1
      for (int st = 0; st < 4; ++st) {
        const float* gp = ge + 4096 + (w * 2 + b) * 512 + (h * 8 + (n >> 4) * 4 + (n & 3)) * 4;
#pragma unroll
        for (int f = 0; f < 4; ++f) {
          u32x4 t;
#pragma unroll
          for (int c = 0; c < 2; ++c) {
            const f32x4 gv = *(const f32x4*)(gp + (2 * f + c) * 64);
            float y[4];
#pragma unroll
            for (int i = 0; i < 4; ++i)
              y[i] = 0.5f - __builtin_amdgcn_rcpf(__builtin_amdgcn_exp2f(__shfl(fe[f][i], h * 32 + c * 16 + (lane & 12) + st) * C3 + gv[i]) + 1.f);
            t[2 * c] = pk2bf(y[0], y[1]);
            t[2 * c + 1] = pk2bf(y[2], y[3]);
          }
          lds_st(ra[3] + J_T + (st & 1) * 16384 + f * 1024, t);
        }
        BAR();
        sfor<0, 16>([&](auto ir) {
          constexpr int r = decltype(ir)::value;
          const u32x4 v = lds_ld(ra[(r / 4 + 3) & 3] + J_T + (st & 1) * 16384 + (r & 3) * 1024);
#pragma unroll
          for (int z = 0; z < 2; ++z) mm<2, r == 0>(acc[0][z], v, wt[32 + 2 * r + z]);
        });
        MWAIT(acc[0][0], acc[0][1]);
#pragma unroll
        for (int i = 0; i < 16; ++i)
          __builtin_amdgcn_raw_buffer_store_b32(pk2bf(acc[0][0][i], acc[0][1][i]), ob, vo + st * 16384 + ((i >> 3) * 4 + (i & 3)) * 512,
                                                b * (4000 * 4096) + ((i >> 2) & 1) * 8192, NT);
      }
    }
  }
}

template <int CONV>
__device__ __forceinline__ void wtrans(const uint16_t* __restrict__ w, char* __restrict__ wh, int wid, int lane) {
  const int dq = wid & 3, pc = wid >> 2;
  const int ks = pc >> 2, kq = pc & 3;
  const int n32 = lane & 31, h = lane >> 5;
  const int r = CONV == 1 ? ks >> 3 : kq >> 1;
  const int c0 = CONV == 1 ? (ks & 7) * 32 + kq * 8 : dmap(ks, 8 * (kq & 1));
  const __amdgpu_buffer_rsrc_t wr = mkbuf(w, 31 * 131072);
  const int vb = (16 * h + r + 1) * 131072 + c0 * 512 + dq * 128 + n32 * 4;
  const int vb0 = (h + r == 0) ? OOB : vb - 262144;
  constexpr int NFB = CONV == 1 ? 2 : 1;
  const u32x4* et = CONV == 1 ? (const u32x4*)&tabs.e1[0][0][0][0] : (const u32x4*)&tabs.e2[0][0][0];
  u32x4 E[NFB][2];
#pragma unroll
  for (int fb = 0; fb < NFB; ++fb) { E[fb][0] = et[fb * 128 + lane]; E[fb][1] = et[fb * 128 + 64 + lane]; }
  uint32_t v[8][8];
#pragma unroll
  for (int el = 0; el < 8; ++el)
#pragma unroll
    for (int jj = 0; jj < 8; ++jj) {
      const int co = CONV == 1 ? el * 512 : (el & 3) * 1024;
      const int so = (CONV == 1 ? 0 : (el >> 2) * 4096) + (jj ? (jj - 1) * 262144 : 0);
      v[el][jj] = __builtin_amdgcn_raw_buffer_load_b32(wr, (jj ? vb : vb0) + co, so, NT);
    }
  PHASE();
  const __amdgpu_buffer_rsrc_t dr = mkbuf(wh, (CONV == 1 ? 32 : 16) * WBIN);
  const int vo = 2 * h * WBIN + (4 * dq + (n32 >> 4)) * WCB + ks * 2048 + kq * 256 + (n32 & 15) * 16;
#pragma unroll
  for (int e = 0; e < 2; ++e) {
    u32x4 b[8];
#pragma unroll
    for (int el = 0; el < 8; ++el)
#pragma unroll
      for (int j = 0; j < 4; ++j) b[el][j] = __builtin_amdgcn_perm(v[el][2 * j + 1], v[el][2 * j], e ? 0x07060302u : 0x05040100u);
#pragma unroll
    for (int fb = 0; fb < NFB; ++fb) {
      u32x4 o[16];
#pragma unroll
      for (int ep = 0; ep < 4; ++ep) {
        f32x16 d[2];
#pragma unroll
        for (int z = 0; z < 2; ++z) d[z] = mfma32(E[fb][1], b[2 * ep + z], zero16());
#pragma unroll
        for (int z = 0; z < 2; ++z) d[z] = mfma32(E[fb][0], b[2 * ep + z], d[z]);
#pragma unroll
        for (int i = 0; i < 16; ++i) o[i][ep] = pk2bf(d[0][i], d[1][i]);
      }
#pragma unroll
      for (int i = 0; i < 16; ++i)
        __builtin_amdgcn_raw_buffer_store_b128(o[i], dr, vo + (i & 1) * 1024,
                                               (16 * fb + 4 * (i >> 2) + ((i >> 1) & 1)) * WBIN + e * 2 * WCB, WT);
      st_pad(o[14], o[15]);
      SB();
    }
  }
}

__device__ __forceinline__ void xload(const uint16_t* __restrict__ x, int u, int lane, uint32_t (&v)[3][8]) {
  const int cq = u & 7, m = u >= 8 * 168, b = u - 8 * 168 * m >= 8 * 84, r = (u >> 3) - 168 * m - 84 * b, sg = 84 * (r & 1) + 42 * m + (r >> 1);
  const __amdgpu_buffer_rsrc_t xr = mkbuf(x + (size_t)b * 16000 * DCH, 8192000);
#pragma unroll
  for (int ks = 0; ks < 3; ++ks) {
    const int row0 = 48 * sg - 24 + 16 * ks + 8 * (lane >> 5);
    const int vb = row0 >= 0 ? row0 * 1024 + cq * 128 + (lane & 31) * 4 : OOB;
#pragma unroll
    for (int e = 0; e < 8; ++e) v[ks][e] = __builtin_amdgcn_raw_buffer_load_b32(xr, vb + (e & 3) * 1024, (e >> 2) * 4096, NT);
  }
}
__device__ __forceinline__ void xtrans(char* __restrict__ u1, int u, int lane, const uint32_t (&v)[3][8], const u32x4 (&F)[2][3]) {
  const __amdgpu_buffer_rsrc_t ur = mkbuf(u1 + (2 * (u & 7)) * PL1 + (u >> 3) * 128, U1BYTES - (2 * (u & 7)) * PL1 - (u >> 3) * 128);
  const int vo = ((lane & 15) >> 1) * UB1 + ((lane >> 4) & 1) * 64 + (lane & 1) * 32 + (lane >> 5) * 16;
  u32x4 a[2][3];
#pragma unroll
  for (int ks = 0; ks < 3; ++ks)
#pragma unroll
    for (int j = 0; j < 4; ++j) {
      a[0][ks][j] = __builtin_amdgcn_perm(v[ks][2 * j + 1], v[ks][2 * j], 0x05040100u);
      a[1][ks][j] = __builtin_amdgcn_perm(v[ks][2 * j + 1], v[ks][2 * j], 0x07060302u);
    }
#pragma unroll
  for (int fb = 0; fb < 2; ++fb) {
    f32x16 d[2] = {zero16(), zero16()};
#pragma unroll
    for (int ks = 0; ks < 3; ++ks)
#pragma unroll
      for (int e = 0; e < 2; ++e) d[e] = mfma32(a[e][ks], F[fb][ks], d[e]);
#pragma unroll
    for (int qp = 0; qp < 2; ++qp) {
      u32x4 o[2];
#pragma unroll
      for (int z = 0; z < 2; ++z)
#pragma unroll
        for (int i = 0; i < 4; ++i) o[z][i] = pk2bf(d[0][8 * qp + 4 * z + i], d[1][8 * qp + 4 * z + i]);
      swap16(o[0], o[1]);
#pragma unroll
      for (int z = 0; z < 2; ++z) __builtin_amdgcn_raw_buffer_store_b128(o[z], ur, vo, (fb * 16 + z * 8) * UB1 + qp * PL1, WT);
      st_pad(o[0], o[1]);
    }
  }
}

KERNEL __launch_bounds__(512, 2) void pre_kernel(const uint16_t* __restrict__ x, const uint16_t* __restrict__ w1,
                                                     const uint16_t* __restrict__ w2, const uint16_t* __restrict__ wf,
                                                     const uint16_t* __restrict__ wo, const uint16_t* __restrict__ g,
                                                     const uint16_t* __restrict__ wg, char* __restrict__ u1,
                                                     char* __restrict__ w1h, char* __restrict__ w2h, u32x4* __restrict__ wff,
                                                     u32x4* __restrict__ wof, float* __restrict__ ge, uint32_t* __restrict__ flags) {
  const int blk = blockIdx.x;
  const int tid = threadIdx.x, lane = tid & 63;
  const int wave = __builtin_amdgcn_readfirstlane(tid >> 6);
  const int n = lane & 31, h = lane >> 5;
  __shared__ char pfs[256];
  if (wave == 7) {
    const char *p0 = (const char*)&tabs - 320, *p1;
    LABEL_ADDR(p1, ".Lce");
    code_pf(p0, (int)(p1 - p0), blk >> 3, pfs);
  }
  if (wave == 0) {
    wtrans<1>(w1, w1h, blk, lane);
  } else if (wave == 1) {
    wtrans<2>(w2, w2h, blk, lane);
  } else {
    const int bq = (blk & 7) * 32 + (blk >> 3);
    const int u0 = ((21 * bq) >> 1) + wave - 2, ue = (21 * (bq + 1)) >> 1;
    u32x4 F[2][3];
#pragma unroll
    for (int fb = 0; fb < 2; ++fb)
#pragma unroll
      for (int ks = 0; ks < 3; ++ks) F[fb][ks] = ((const u32x4*)&tabs.f1[fb][ks][0][0])[lane];
    uint32_t v0[3][8];
    xload(x, u0, lane, v0);
    if (u0 + 6 < ue) {
      uint32_t v1[3][8];
      xload(x, u0 + 6, lane, v1);
      SB();
      WALK(7, wave - 2);
      PHASE();
      xtrans(u1, u0, lane, v0, F);
      SB();
      xtrans(u1, u0 + 6, lane, v1, F);
    } else if (wave == 7) {
      const int i = blk & 31, w = (blk >> 5) & 3, second = blk >> 7;
      const int eb = i < 8 ? i >> 1 : (i - 8) / 6, r = (2 * w + (i < 8 ? i & 1 : 2 + (i - 8) % 6)) & 7;
      const uint16_t* src = second ? wo + (16 * ((4 * w + (i >> 1)) & 15) + 8 * h) * DCH + 64 * w + 2 * n + (i & 1)
                                   : wf + dmap(2 * r, 4 * (lane >> 4)) * DCH + 64 * w + 16 * eb + (lane & 15);
      uint32_t v[8];
#pragma unroll
      for (int j = 0; j < 8; ++j) v[j] = src[(second ? j : 32 * (j >> 2) + 2 * (j & 3)) * DCH];
      PHASE();
      xtrans(u1, u0, lane, v0, F);
      const float sc = second ? 2.f : 1.f;
      u32x4 o;
#pragma unroll
      for (int j = 0; j < 4; ++j) o[j] = pk2bf(bf2f(v[2 * j]) * sc, bf2f(v[2 * j + 1]) * sc);
      ((second ? wof : wff) + (blk & 127) * 64)[lane] = o;
      if (blk < NFLAG / 64) flags[blk * 64 + lane] = 0;
    } else if (blk < 8) {
      const int c0 = 32 * blk + 2 * (lane & 15), kq = lane >> 4;
      uint32_t v[8][8];
      u32x4 ga[8];
#pragma unroll
      for (int ks = 0; ks < 8; ++ks) {
        ga[ks] = *(const u32x4*)(g + (lane & 15) * DCH + ks * 32 + 8 * kq);
#pragma unroll
        for (int e = 0; e < 8; ++e) v[ks][e] = *(const uint32_t*)(wg + (ks * 32 + 8 * kq + e) * DCH + c0);
      }
      SB();
      asm volatile("s_barrier" ::: "memory");
      SB();
      xtrans(u1, u0, lane, v0, F);
      SB();
      f32x4 d[2] = {{0.f, 0.f, 0.f, 0.f}, {0.f, 0.f, 0.f, 0.f}};
#pragma unroll
      for (int ks = 0; ks < 8; ++ks)
#pragma unroll
        for (int e = 0; e < 2; ++e) {
          u32x4 bb;
#pragma unroll
          for (int j = 0; j < 4; ++j) bb[j] = __builtin_amdgcn_perm(v[ks][2 * j + 1], v[ks][2 * j], e ? 0x07060302u : 0x05040100u);
          d[e] = mfma16(ga[ks], bb, d[e]);
        }
      float* dst = ge + (((((blk >> 1) * 2 + (kq >> 1)) * 4 + 2 * (blk & 1) + ((lane >> 3) & 1)) * 2 + ((lane >> 1) & 1)) * 2 + ((lane >> 2) & 1)) * 32 + (kq & 1) * 16 + 2 * (lane & 1);
      f32x2 gs[4];
      bool far = false;
#pragma unroll
      for (int i = 0; i < 4; ++i) {
        gs[i] = f32x2{bf2f(f2bf(d[0][i])) * C2, bf2f(f2bf(d[1][i])) * C2};
        far = far || __builtin_fabsf(gs[i][0]) > EMAX || __builtin_fabsf(gs[i][1]) > EMAX;
      }
      far = __builtin_amdgcn_ballot_w64(far) != 0;
#pragma unroll
      for (int i = 0; i < 4; ++i) {
        *(f32x2*)(dst + 4 * i) = far ? f32x2{-1.f, -1.f} : f32x2{__builtin_amdgcn_exp2f(-gs[i][0]), __builtin_amdgcn_exp2f(-gs[i][1])};
        *(f32x2*)(dst + 4096 + 4 * i) = gs[i];
      }
    } else {
      PHASE();
      xtrans(u1, u0, lane, v0, F);
    }
  }
  asm volatile(".Lce:");
}

extern "C" void kernel_launch(void* const* d_in, const int* in_sizes, int n_in, void* d_out, int out_size, void* d_ws,
                              size_t ws_size, hipStream_t stream) {
  const uint16_t* x = (const uint16_t*)d_in[0];
  const uint16_t* w1 = (const uint16_t*)d_in[1];
  const uint16_t* w2 = (const uint16_t*)d_in[2];
  const uint16_t* g = (const uint16_t*)d_in[3];
  const uint16_t* wf = (const uint16_t*)d_in[4];
  const uint16_t* wg = (const uint16_t*)d_in[5];
  const uint16_t* wo = (const uint16_t*)d_in[6];
  uint16_t* out = (uint16_t*)d_out;
  char* ws = (char*)d_ws;
  (void)hipFuncSetAttribute((const void*)joint_kernel, hipFuncAttributeMaxDynamicSharedMemorySize, JOINT_LDS);

  pre_kernel<<<256, 512, 0, stream>>>(x, w1, w2, wf, wo, g, wg, ws + A_U1, ws + A_W1, ws + A_W2, (u32x4*)(ws + A_WF),
                                      (u32x4*)(ws + A_WF + 131072), (float*)(ws + A_GE), (uint32_t*)(ws + A_FL));
  joint_kernel<<<256, 256, JOINT_LDS, stream>>>(ws, out);
}
```

```cpp
#include <hip/hip_runtime.h>
#include <stdint.h>

typedef __attribute__((ext_vector_type(8))) short bf16x8;
typedef __attribute__((ext_vector_type(16))) float f32x16;
typedef __attribute__((ext_vector_type(4))) float f32x4;
typedef __attribute__((ext_vector_type(2))) float f32x2;
typedef __attribute__((ext_vector_type(4))) unsigned int u32x4;
typedef __attribute__((ext_vector_type(2))) unsigned int u32x2;

#define ORD0 0x0132
#define ORD1 0x2310
#define GLA 6
#define TS0 28
#define TS1 58
#define TU0 0
#define TU1 0
#define THR 10
#define THR2 7
#define GPRO 2
constexpr int DCH = 256;

__device__ __forceinline__ float bf2f(uint32_t v) { return __uint_as_float(v << 16); }
__device__ __forceinline__ uint32_t f2bf(float f) {
  uint32_t u = __float_as_uint(f);
  u += 0x7FFF + ((u >> 16) & 1);
  return u >> 16;
}
__device__ __forceinline__ uint32_t pk2bf(float a, float b) {
  typedef __attribute__((ext_vector_type(2))) __bf16 b2;
  f32x2 v = {a, b};
  b2 r = __builtin_convertvector(v, b2);
  return __builtin_bit_cast(uint32_t, r);
}
constexpr double GS = 0.4686686932353139;
__device__ __forceinline__ uint32_t gelu2(float a0, float a1, float one = 1.f) {
  const f32x2 v = {a0, a1};
  constexpr float c = (float)(-2.302208198144325 / GS);
  const f32x2 z = v * __builtin_elementwise_fma(-v, v, f32x2{c, c});
  const f32x2 d = f32x2{__builtin_amdgcn_exp2f(z[0]), __builtin_amdgcn_exp2f(z[1])} + f32x2{one, one};
  const f32x2 y = v * f32x2{__builtin_amdgcn_rcpf(d[0]), __builtin_amdgcn_rcpf(d[1])};
  return pk2bf(y[0], y[1]);
}
__device__ __forceinline__ f32x16 mfma32(const u32x4& a, const u32x4& b, const f32x16& c) {
  return __builtin_amdgcn_mfma_f32_32x32x16_bf16(__builtin_bit_cast(bf16x8, a), __builtin_bit_cast(bf16x8, b), c, 0, 0, 0);
}
__device__ __forceinline__ f32x4 mfma16(const u32x4& a, const u32x4& b, const f32x4& c) {
  return __builtin_amdgcn_mfma_f32_16x16x32_bf16(__builtin_bit_cast(bf16x8, a), __builtin_bit_cast(bf16x8, b), c, 0, 0, 0);
}
__device__ __forceinline__ f32x16 zero16() {
  f32x16 z;
#pragma unroll
  for (int i = 0; i < 16; ++i) z[i] = 0.f;
  return z;
}
__device__ __forceinline__ __amdgpu_buffer_rsrc_t mkbuf(const void* p, int nbytes) {
  return __builtin_amdgcn_make_buffer_rsrc((void*)p, (short)0, nbytes, 0x00020000);
}
template <int V> struct IC { static constexpr int value = V; };
template <int I, int N, class F>
__device__ __forceinline__ void sfor(F&& f) {
  if constexpr (I < N) {
    f(IC<I>{});
    sfor<I + 1, N>(f);
  }
}
#define SB() __builtin_amdgcn_sched_barrier(0)
#define PHASE() do { SB(); asm volatile("s_waitcnt vmcnt(0)\n\ts_barrier" ::: "memory"); SB(); } while (0)

constexpr double PI_ = 3.14159265358979323846;
constexpr double c128(int m) {
  m = ((m % 128) + 128) % 128;
  if (m > 64) m = 128 - m;
  double sg = 1.0;
  if (m > 32) { m = 64 - m; sg = -1.0; }
  if (m == 32) return 0.0;
  const double a = PI_ * m / 64.0;
  double term = 1.0, sum = 1.0;
  for (int i = 1; i < 16; ++i) { term *= -a * a / ((2 * i - 1) * (2 * i)); sum += term; }
  return sg * sum;
}
constexpr double s128(int m) { return c128(m - 32); }
constexpr uint16_t cbf(double v) {
  uint32_t u = __builtin_bit_cast(uint32_t, (float)v);
  u += 0x7FFF + ((u >> 16) & 1);
  return (uint16_t)(u >> 16);
}
constexpr double cbf2d(uint16_t v) { return (double)__builtin_bit_cast(float, (uint32_t)v << 16); }
struct Tabs {
  uint16_t f1[2][3][64][8];
  uint16_t e1[2][2][64][8];
  uint16_t e2[2][64][8];
  uint16_t g1[8][64][8];
  uint16_t f2[64][8];
  uint16_t g2[2][64][8];
};
constexpr Tabs make_tabs() {
  Tabs t{};
  for (int l = 0; l < 64; ++l)
    for (int e = 0; e < 8; ++e) {
      const int h = l >> 5, f = l & 31, ri = f & 1;
      for (int fb = 0; fb < 2; ++fb) {
        const int k = 16 * fb + (f >> 1);
        for (int ks = 0; ks < 3; ++ks) {
          const int a = (2 * k + 1) * (16 * ks + 8 * h + e);
          t.f1[fb][ks][l][e] = cbf(ri ? -s128(a) : c128(a));
        }
        const int a = (2 * k + 1) * (8 * h + e);
        const double v = ri ? s128(a) : c128(a);
        t.e1[fb][0][l][e] = cbf(v);
        t.e1[fb][1][l][e] = cbf(v - cbf2d(cbf(v)));
      }
      const int a = 2 * (2 * (f >> 1) + 1) * (8 * h + e);
      const double v = ri ? s128(a) : c128(a);
      t.e2[0][l][e] = cbf(v);
      t.e2[1][l][e] = cbf(v - cbf2d(cbf(v)));
      t.f2[l][e] = cbf((ri ? -v : v) / GS);
    }
  for (int m = -15; m < 48; ++m)
    for (int f = 0; f < 64; ++f) {
      const int a = (2 * (f >> 1) + 1) * m;
      t.g1[f >> 3][(m + 16) ^ (((m + 16) >> 4) & 1)][f & 7] = cbf(((f & 1) ? -s128(a) : c128(a)) * GS / 32.0);
    }
  for (int c = 0; c < 2; ++c)
    for (int l = 0; l < 64; ++l)
      for (int e = 0; e < 8; ++e) {
        const int m = 4 * (l & 3) + ((l >> 2) & 3) - (l & 16), a = 2 * (2 * (8 * c + 4 * (l >> 5) + (e >> 1)) + 1) * m;
        t.g2[c][l][e] = m > -16 ? cbf(((e & 1) ? -s128(a) : c128(a)) * GS / 16.0) : 0;
      }
  return t;
}
__device__ __attribute__((aligned(64))) const Tabs tabs = make_tabs();

constexpr int OOB = 0x7FFFFF00;
constexpr float C2 = 2.885390081777927f, C3 = (float)(2.885390081777927 / GS);
#define EMAXV 120.f
constexpr float EMAX = EMAXV;
constexpr int WT = 16;
constexpr int NT = 2;
constexpr int PL1 = 336 * 128;
#define PU2 0
#define PY2 0
constexpr int UB1 = 16 * PL1, UB2 = 524288 + PU2;
constexpr int U1BYTES = 32 * UB1, U2BYTES = 32 * UB2;
constexpr int WCB = 32768;
constexpr int WBIN = 16 * WCB;
constexpr int YP1 = 336 * 64;
constexpr int YB1 = 16 * YP1, YB2 = 524288 + PY2;
constexpr int Y1BYTES = 32 * YB1, Y2BYTES = 16 * YB2;
constexpr int F1 = 0, F2 = 2048, F3 = 10240, NFLAG = 18432 / 4;
constexpr int A_U1 = 0, A_W1 = 23 << 20, A_W2 = 40 << 20, A_Y1 = 49 << 20, A_U2 = 61 << 20, A_Y2 = 78 << 20, A_WF = 87 << 20,
              A_GE = A_WF + 262144, A_FL = 88 << 20;
__device__ __forceinline__ int dmap(int cb, int n) { return 64 * (cb >> 2) + 32 * (cb & 1) + 2 * n + ((cb >> 1) & 1); }

__device__ __forceinline__ void swap16(u32x4& a, u32x4& b) {
#pragma unroll
  for (int i = 0; i < 4; ++i) {
    const auto r = __builtin_amdgcn_permlane16_swap(a[i], b[i], false, false);
    a[i] = r[0];
    b[i] = r[1];
  }
}

__device__ __forceinline__ void st_pad(const u32x4& a, const u32x4& b) { asm volatile("s_nop 1" :: "v"(a), "v"(b) : "memory"); }

template <int N>
__device__ __forceinline__ void wait_vm() {
  constexpr int M = N < 63 ? N : 63;
  __builtin_amdgcn_s_waitcnt((M & 15) | ((M >> 4) << 14) | 0x0F70);
}

typedef __attribute__((address_space(3))) u32x4 lds_u32x4;
__device__ __forceinline__ u32x4 lds_ld(int a) { return *(const lds_u32x4*)(uintptr_t)(uint32_t)a; }
__device__ __forceinline__ uint32_t lds_ld32(int a) { return *(const __attribute__((address_space(3))) uint32_t*)(uintptr_t)(uint32_t)a; }
template <int SZ>
__device__ __forceinline__ void dma(__amdgpu_buffer_rsrc_t rsrc, char* lds, int voff, int soff) {
#if defined(__HIP_DEVICE_COMPILE__)
  typedef __attribute__((address_space(3))) void* ldsp;
  if constexpr (SZ == 16) __builtin_amdgcn_raw_ptr_buffer_load_lds(rsrc, (ldsp)lds, 16, voff, soff, 0, 0);
  else __builtin_amdgcn_raw_ptr_buffer_load_lds(rsrc, (ldsp)lds, 4, voff, soff, 0, 0);
#endif
}

#define LABEL_ADDR(p, name) asm volatile("s_getpc_b64 s[40:41]\n\ts_add_u32 s40, s40, " name "@rel32@lo+4\n\ts_addc_u32 s41, s41, " name "@rel32@hi+12\n\ts_mov_b64 %0, s[40:41]" : "=s"(p) :: "s40", "s41", "scc")
__device__ __forceinline__ void code_pf(const char* p, int n, int part, char* lds) {
  dma<4>(mkbuf(p, n), lds, part * 8192 + (threadIdx.x & 63) * 128, 0);
}

__device__ __forceinline__ void pad_macros() {
  asm volatile(R"(
.macro PAD1 l
.ifndef .Lw_\l
.Lw_\l:
.endif
\l:
s_cbranch_execz \l\()f
.endm
.macro PADM k, b, m
.ifndef .Lpg_\k
.set .Lpg_\k, -1
.endif
.ifndef .Lcn_\b
.set .Lcn_\b, 0
.endif
.if (((. - \k) & 4095) < 60) && (((. - \k) >> 12) > .Lpg_\k)
.set .Lpg_\k, (. - \k) >> 12
.irp c, 0, 1, 2, 3, 4, 5, 6, 7
.if (.Lcn_\b & \m) == \c
PAD1 \b\c
.endif
.endr
.set .Lcn_\b, .Lcn_\b + 1
.endif
.endm
.macro WALK b, w
s_mov_b64 exec, 0
.irp c, 0, 1, 2, 3, 4, 5, 6, 7
.ifdef .Lw_\b\c
s_cmp_eq_u32 \w, \c
s_cbranch_scc1 .Lw_\b\c
.endif
.endr
.irp c, 0, 1, 2, 3, 4, 5, 6, 7
\b\c:
.endr
s_mov_b64 exec, -1
.endm
)");
}
#define PAD(k, b, m) asm volatile("PADM " #k ", " #b ", " #m)
#define WALK(b, w) asm volatile("WALK " #b ", %0" :: "s"(w) : "scc")
#define KERNEL extern "C" __global__ __attribute__((aligned(4096)))

constexpr int J_T = 0;
constexpr int J_G = 131072;
constexpr int J_F = J_G + 16384;
constexpr int J_R = J_F + 8192;
constexpr int JOINT_LDS = J_R + 1024;
__device__ __forceinline__ void lds_st(int a, const u32x4& v) { *(lds_u32x4*)(uintptr_t)(uint32_t)a = v; }
#define BAR() do { SB(); asm volatile("s_waitcnt lgkmcnt(0)\n\ts_barrier" ::: "memory"); SB(); } while (0)
#define DRAIN() do { SB(); wait_vm<0>(); asm volatile("s_waitcnt vmcnt(0)\n\ts_barrier" ::: "memory"); SB(); } while (0)
template <int M, bool Z>
__device__ __forceinline__ void mm(f32x16& c, const u32x4& a, const u32x4& b) {
  if constexpr (Z) {
    if constexpr (M == 0) asm volatile("v_mfma_f32_32x32x16_bf16 %0, %1, %2, 0" : "=&v"(c) : "v"(a), "v"(b));
    else asm volatile("v_mfma_f32_32x32x16_bf16 %0, %1, %2, 0" : "=&v"(c) : "v"(a), "a"(b));
  } else {
    if constexpr (M == 0) asm volatile("v_mfma_f32_32x32x16_bf16 %0, %1, %2, %0" : "+v"(c) : "v"(a), "v"(b));
    else asm volatile("v_mfma_f32_32x32x16_bf16 %0, %1, %2, %0" : "+v"(c) : "v"(a), "a"(b));
  }
}
template <int M, bool Z>
__device__ __forceinline__ void mm(f32x4& c, const u32x4& a, const u32x4& b) {
  if constexpr (Z) {
    if constexpr (M == 0) asm volatile("v_mfma_f32_16x16x32_bf16 %0, %1, %2, 0" : "=&v"(c) : "v"(a), "v"(b));
    else asm volatile("v_mfma_f32_16x16x32_bf16 %0, %1, %2, 0" : "=&v"(c) : "a"(a), "v"(b));
  } else {
    if constexpr (M == 0) asm volatile("v_mfma_f32_16x16x32_bf16 %0, %1, %2, %0" : "+v"(c) : "v"(a), "v"(b));
    else asm volatile("v_mfma_f32_16x16x32_bf16 %0, %1, %2, %0" : "+v"(c) : "a"(a), "v"(b));
  }
}
#define MWAIT(c0, c1) asm volatile("s_nop 7\n\ts_nop 3" : "+v"(c0), "+v"(c1))
template <int K>
__device__ __forceinline__ float add_quad(float a, float b) {
  float r;
  asm volatile("v_add_f32_dpp %0, %1, %2 quad_perm:[%3,%3,%3,%3] row_mask:0xf bank_mask:0xf" : "=v"(r) : "v"(a), "v"(b), "n"(K));
  return r;
}
__device__ __forceinline__ uint32_t neg2(uint32_t v) {
  uint32_t r;
  asm volatile("v_xor_b32 %0, 0x80008000, %1" : "=v"(r) : "v"(v));
  return r;
}
template <int C>
__device__ __forceinline__ u32x4 column(const u32x4& a, const u32x4& b, const u32x4& c, const u32x4& d) {
  u32x2 l, h;
  asm volatile("v_pk_mov_b32 %0, %1, %2 op_sel:[%3,%3]" : "=v"(l) : "v"(C < 2 ? a.xy : a.zw), "v"(C < 2 ? b.xy : b.zw), "n"(C & 1));
  asm volatile("v_pk_mov_b32 %0, %1, %2 op_sel:[%3,%3]" : "=v"(h) : "v"(C < 2 ? c.xy : c.zw), "v"(C < 2 ? d.xy : d.zw), "n"(C & 1));
  return u32x4{l[0], l[1], h[0], h[1]};
}
__device__ __forceinline__ void keep_a(const u32x4& v) { asm volatile("" :: "a"(v)); }
__device__ __forceinline__ void keep_v(const u32x4& v) { asm volatile("" :: "v"(v)); }
__device__ __forceinline__ int pin(int v) { asm volatile("" : "+v"(v)); return v; }
__device__ __forceinline__ void await(uint32_t v, __amdgpu_buffer_rsrc_t r, int vo, int so) {
  uint32_t cnt;
  SB();
  asm volatile("v_cmp_gt_u32 vcc, 1, %0\n\ts_cbranch_vccz 2f\n\ts_mov_b32 %1, 0x40000\n"
               "1:\n\ts_sleep 1\n\tbuffer_load_dword %0, %2, %3, %4 offen sc1\n\ts_waitcnt vmcnt(0)\n\t"
               "v_cmp_gt_u32 vcc, 1, %0\n\ts_cbranch_vccz 2f\n\ts_sub_u32 %1, %1, 1\n\ts_cbranch_scc0 1b\n2:"
               : "+v"(v), "=&s"(cnt) : "v"(vo), "s"(r), "s"(so) : "vcc", "scc", "memory");
  SB();
}

#define OPOL NT
#define ORD2 0x0132
#define ORD3 0x2310
#define KCY 84
#define KCAP 200
#define KLD 60
#define KLA 40
#define KD1 112
#define KQ1 14
#define KPI1 130
#define KPC1 140
#define KYR 2
#define KVE 173
#define KS1 19
#define KAW 0
#define KSU 6
#define KU0 0
#define KU2 4
#define NX1 16
#define KXE 25
#define NB2 8
#define KXP 16
#define KXA 19
#define NXA 4
#define NB0 0
#define NB1 0
#define NX0 10
#define KP1 1
#define KXL 40
#define KXU 30
#define KJM 48
#define KJ0 60
#define KSL 8
#define KCY2 72
#define KQJ 99
#define KCJ 68
#define KYS 5
#define THS 10
#define TS2 80
#define THM 5
#define THL -1
#define KS5 31
#define KD2 20
#define KPI2 40
#define KPC2 50
#define KXB 12
#define KD3 84
#define KQ3 12
#define KPI3 104
#define KPC3 114
#define KJR 5
#define KPI4 4
#define KPC4 28
enum { O_NONE, O_W, O_A, O_B, O_T, O_F, O_V, O_GI, O_Q, O_J, O_S1, O_Y1, O_X, O_S2, O_Y2, O_SIG, O_POLL, O_U };
constexpr int fill_t(int l) { return l < 2 ? O_T : l < 3 ? O_F : l < 35 ? O_V : l < 37 ? O_GI : l < 41 ? O_Q : O_J; }
constexpr int fill_j(int l) { return l < 2 ? l : l < 35 ? l - 3 : l < 37 ? l - 35 : l < 41 ? l - 37 : l - 41; }
constexpr int first(int c) { return c < 16 ? 6 * c : c < 32 ? 16 + 5 * c : 999; }
constexpr int chunk(int g) { return g < 96 ? g / 6 : (g - 16) / 5; }
constexpr int cpos(int c) { return c < 16 ? (c + 8) % 12 * 12288 : (c - 16) * 9216; }
struct Sched {
  int ty[3 * 184], ix[3 * 184];
  int before[3 * 184];
  int last[32];
  int nl, nj;
};
constexpr int main_t(int m) { return m >= 80 ? O_B : m % 5 < 2 ? O_W : O_A; }
constexpr int main_i(int m) { return m >= 80 ? m - 80 : m % 5 < 2 ? m / 5 * 2 + m % 5 : m / 5 * 3 + m % 5 - 2; }
constexpr int main_c(int m) { return m >= 80 ? 16 + (m - 80) / 9 * 4 + ((m - 80) % 9 - 1) / 2 : m / 5; }
constexpr int vfree(int l) { return first(18 + (8 * ((l - 3) >> 1) + 7) / 9) - 3; }
constexpr int hk(int i) { return i < 16 ? i : 4 * i - 45 + (i > 21) * 5 + (i > 23) * 3; }
constexpr Sched make_s1() {
  Sched c{};
  int m = 5 * GPRO, l = 0, o = m, y = 0, cr = KCY;
  for (int i = 0; i < GPRO; ++i) c.last[i] = 5 * i + 5;
  for (int t = 0; t < 3 * 184; ++t) {
    const int g = t / 3, j = t % 3;
    const bool thr = g >= TS0 && g < TS1;
    c.before[t] = o;
    if (j == 0) cr += g < TS0 ? 64 : 68 + 4 * (thr ? THR : g < TS2 ? THM - 2 : THL);
    if (cr > KCAP) cr = KCAP;
    if (j == 1 && ((g >= 93 && g < 99) || (g >= 174 && g < 179))) { c.ty[t] = O_S1; c.ix[t] = g < 99 ? g - 93 : g - 168; cr -= KCY; }
    else if (t == 3 * KD1 + 2) { c.ty[t] = O_SIG; c.ix[t] = 0; }
    else if (t == 3 * KPI1 + 2) { c.ty[t] = O_POLL; c.ix[t] = 0; }
    else if (g >= KVE) { if (y < 48 || m < 116) return c; }
    else if (g >= KPC1 && y < 48 && (j != 1 || KYR == 3)) { c.ty[t] = O_Y1; c.ix[t] = y++; }
    else if (g > KD1 - KQ1 && g <= KD1) { if (j == 1 && g <= KD1 - KQJ) { c.ty[t] = O_J; c.ix[t] = c.nj++; } }
    else if ((j == 1 || (j == 2 && thr)) && cr >= KCY) {
      const int k = main_c(m), fr = k < 12 ? 0 : first(k < 16 ? k - 10 : 6 + (9 * (k - ((m - 80) % 9 ? 16 : 13)) + 8) / 12) - 3;
      const bool fo = l < 35 && m >= 80 && (l < 3 || g >= vfree(l));
      if (m < 116 && g >= fr && first(k) - g <= (k < 16 ? KLA : KLD) && (m < 80 || l >= 3)) {
        c.ty[t] = main_t(m); c.ix[t] = main_i(m);
        cr -= g < TS0 ? 64 : KCY;
        c.last[k] = o + 1;
        ++m;
      } else if (fo) {
        c.ty[t] = fill_t(l); c.ix[t] = fill_j(l);
        cr -= KCY;
        ++l;
      } else if (g >= KJ0 && c.nj < KJM) {
        c.ty[t] = O_J; c.ix[t] = c.nj++;
        cr -= KCJ;
      }
    }
    if (c.ty[t]) ++o;
  }
  c.nl = l;
  return c;
}
constexpr Sched S1 = make_s1();
constexpr int NFILL = 3 + 32 + 2 + 4 + 64 - S1.nj;
constexpr int fill_i(int l) { return fill_j(l) + (l < 41 ? 0 : S1.nj); }
constexpr int NL0 = S1.nl + NB0, NL1 = NL0 + NB1, NL2 = NL1 + NB2;
static_assert(S1.nl > 2 && NL0 <= 33 && NL2 >= 35 && NL2 <= NFILL && first(chunk(KD1 + 4)) == KD1 + 4, "schedule");
constexpr Sched make_s2() {
  Sched c{};
  int l = NL2, x = NX0, y = 0, cr = 0;
  for (int t = 0; t < 3 * 136; ++t) {
    const int g = t / 3, j = t % 3;
    const bool thr = g >= TU0 && g < TU1, fs = j == 1 || (j == 2 && thr);
    if (j == 0) cr += 68 + 4 * (thr ? THR2 : THL);
    if (cr > KCAP) cr = KCAP;
    if (j == 1 && (g & 1) && ((g > 60 && g < 68) || (g > 124 && g < 132))) { c.ty[t] = O_S2; c.ix[t] = g < 68 ? (g - 61) / 2 : (g - 117) / 2; cr -= KCY2; }
    else if (t == 3 * KD2 + 2) { c.ty[t] = O_SIG; c.ix[t] = 3; }
    else if (t == 3 * KPI2 + 2) { c.ty[t] = O_POLL; c.ix[t] = 3; }
    else if (t == 3 * KD3 + 2) { c.ty[t] = O_SIG; c.ix[t] = 4; }
    else if (t == 3 * KPI3 + 2) { c.ty[t] = O_POLL; c.ix[t] = 4; }
    else if (g >= KPC3) { if (y < 8) { c.ty[t] = O_Y2; c.ix[t] = y++; } }
    else if (g <= KD2) {
      if (j == 1 && g >= KU0 && g < KU0 + KU2) { c.ty[t] = O_U; c.ix[t] = 16 - KU2 + g - KU0; }
      else if (j == 1 && x < NX1) { c.ty[t] = O_X; c.ix[t] = x++; }
    } else if (g > KD3 - KQ3 && g <= KD3) {
    } else if (x < (g >= KPC2 ? 64 : 32) && ((j && x >= 32 && x < 32 + KXB) || (j == 1 && 2 * x - g <= KXU) || (fs && cr >= KCY2 && 2 * x - g <= KXL))) {
      c.ty[t] = O_X; c.ix[t] = x++;
      cr -= KCY2;
      if (cr < 0) cr = 0;
    } else if (fs && cr >= KCY2 && l < NFILL) {
      c.ty[t] = fill_t(l); c.ix[t] = fill_i(l);
      cr -= KCJ;
      ++l;
    }
    if (t == 3 * KD3 && l < 41) return c;
    if (c.ty[t] == O_X && g >= (2 * c.ix[t] & ~7) - 1) return c;
  }
  c.nl = x == 64 && y == 8 ? l : -1;
  return c;
}
constexpr Sched S2 = make_s2();
static_assert((KU2 == 0 || KU2 == 4 || (KU2 == 8 && !KP1)) && KXA > KXP && KXA < 22 && KS1 < 22 && S2.nl + S1.nj >= 41 + 32 && S2.nl + (87 - KS5) * KJR / 8 >= NFILL, "schedule");

#define PADG() PAD(joint_kernel, 7, 3)
#define PADJ() PADG()
KERNEL __launch_bounds__(256, 1) void joint_kernel(char* ws, uint16_t* __restrict__ out) {
  extern __shared__ __attribute__((aligned(16))) char smem[];
  pad_macros();
  PADG();
  SB();
  const int bid = blockIdx.x, xcd = bid & 7, slot = bid >> 3;
  const int tid = threadIdx.x, lane = tid & 63;
  const int w = __builtin_amdgcn_readfirstlane(tid >> 6);
  const int cb = (slot & 3) * 4 + w;
  const int bin1 = xcd * 4 + (slot >> 3), mt1 = (slot >> 2) & 1;
  const int bin2 = xcd * 2 + (slot >> 4);
  const __amdgpu_buffer_rsrc_t ur = mkbuf(ws + (unsigned)(A_U1 + bin1 * UB1 + mt1 * (168 * 128)), UB1);
  const __amdgpu_buffer_rsrc_t wr = mkbuf(ws + (unsigned)(A_W1 + bin1 * WBIN + cb * WCB), WCB);
  const int vd = w * 1024 + (lane >> 3) * 128 + (((lane & 7) ^ ((4 * w + (lane >> 4)) & 7)) * 16);
  const int rho = lane & 15, kq = lane >> 4;
  const int n = lane & 31, h = lane >> 5;
  const float* ge = (const float*)(ws + A_GE);
  __amdgpu_buffer_rsrc_t vr, ys, yl, ub, yb, wb, fr;
  u32x4 Wm[32], X[64], wt[64], y1[3][4], yv[2][4][2], F, gi[2], o[11], up[8];
  int ve, vt, v1[3], vx, vs, vy, vu, vf[3], vp[3];
  uint32_t pl[2];
  constexpr int FO[6] = {F1, F1 + 1024, F2, F2 + 4096, F3, F3 + 4096};
  auto yload = [&](int b, int i) { yv[b][i & 3][i >> 2] = __builtin_amdgcn_raw_buffer_load_b128(yb, vy, ((i >> 2) * 8 + (i & 3)) * YB2 + b * 65536, WT); };
  auto issue = [&](auto it, auto ii) {
    constexpr int t = decltype(it)::value, i = decltype(ii)::value;
    if constexpr (t == O_W) Wm[i] = __builtin_amdgcn_raw_buffer_load_b128(wr, lane * 16 + (i & 3) * 1024, (i >> 2) * 4096, 0);
    else if constexpr (t == O_A) dma<16>(ur, smem + cpos(i / 3) + i % 3 * 4096 + w * 1024, vd, i / 3 * PL1 + i % 3 * 4096);
    else if constexpr (t == O_B && i % 9) dma<16>(ur, smem + cpos(main_c(80 + i)) + (i % 9 - 1 & 1) * 4096 + w * 1024, vd, (main_c(80 + i) - 16) * PL1 + (3 + (i % 9 - 1 & 1)) * 4096);
    else if constexpr (t == O_B) dma<16>(ur, smem + cpos(main_c(80 + i)) + 8192 + w * 9216, ve, i / 9 * 4 * PL1);
    else if constexpr (t == O_T) dma<16>(mkbuf(&tabs.g1[2 * w][0][0], 2048), smem + J_F + w * 2048 + i * 1024, lane * 16, i * 1024);
    else if constexpr (t == O_F) F = ((const u32x4*)&tabs.f2[0][0])[lane];
    else if constexpr (t == O_V) dma<16>(vr, smem + (i >> 1) * 8192 + w * 2048 + (i & 1) * 1024, lane * 16, i * 1024);
    else if constexpr (t == O_GI) gi[i] = ((const u32x4*)&tabs.g2[i][0][0])[lane];
    else if constexpr (t == O_Q) dma<16>(mkbuf(ge + w * 1024, 4096), smem + J_G + w * 4096 + i * 1024, lane * 16, i * 1024);
    else if constexpr (t == O_J) wt[i] = __builtin_amdgcn_raw_buffer_load_b128(wb, lane * 16 + (i & 3) * 1024, (i >> 5) * 131072 + ((i >> 2) & 7) * 4096, 0);
    else if constexpr (t == O_S1) __builtin_amdgcn_raw_buffer_store_b128(o[i], ys, vt + (i & 3) * 1024, (i >> 2) * 4096, WT);
    else if constexpr (t == O_Y1) y1[i % 3][i % 48 / 12][i / 3 & 3] = __builtin_amdgcn_raw_buffer_load_b32(yl, v1[i % 3], (i % 48 / 12 * 8 + (i / 3 & 3)) * YB1 + i / 48 * (84 * 64), WT);
    else if constexpr (t == O_X) X[i] = __builtin_amdgcn_raw_buffer_load_b128(ub, vx + (i & 3) * 1024, (i >> 5) * 16 * UB2 + ((i >> 2) & 7) * 4096, WT);
    else if constexpr (t == O_S2) __builtin_amdgcn_raw_buffer_store_b128(o[i & 3], yb, vs + (i & 3) * 1024, (i >> 2) * 65536, WT);
    else if constexpr (t == O_Y2) yload(0, i);
    else if constexpr (t == O_SIG) __builtin_amdgcn_raw_buffer_store_b32(1u, fr, vf[i >> 1], FO[i], WT);
    else if constexpr (t == O_POLL) pl[i & 1] = __builtin_amdgcn_raw_buffer_load_b32(fr, vp[i >> 1], FO[i], WT);
    else if constexpr (t == O_U) __builtin_amdgcn_raw_buffer_store_b128(up[i & 7], ub, vu + ((i >> 1) & 3) * 256, ((i >> 3) * 16 + (i & 1) * 8) * UB2, WT);
    SB();
  };
  auto fills = [&](auto i0, auto i1) {
    sfor<decltype(i0)::value, decltype(i1)::value>([&](auto il) { issue(IC<fill_t(decltype(il)::value)>{}, IC<fill_i(decltype(il)::value)>{}); });
  };
  sfor<0, 5 * GPRO>([&](auto il) { issue(IC<main_t(decltype(il)::value)>{}, IC<main_i(decltype(il)::value)>{}); });
  const int l0 = (int)(uint32_t)(uintptr_t)(__attribute__((address_space(3))) char*)smem;
  int ab[3][2], ga[4], tj, ty, ta, tau;
  float ok[2];
#pragma unroll
  for (int ri = 0; ri < 2; ++ri)
#pragma unroll
    for (int wn = 0; wn < 3; ++wn) ab[wn][ri] = pin(l0 + rho * 128 + ((((kq >> 1) * 4 + ri * 2 + (kq & 1)) ^ (rho >> 1)) * 16) + wn * 65536);
  auto setup = [&](int k) {
    asm volatile("" : "+s"(ws));
    if (k == 0) {
      vr = mkbuf(ws + (unsigned)(A_W2 + bin2 * WBIN + cb * WCB), WCB);
      ys = mkbuf(ws + (unsigned)(A_Y1 + bin1 * YB1 + cb * YP1 + mt1 * (168 * 64)), 168 * 64);
    } else if (k == 1) {
      yl = mkbuf(ws + A_Y1, Y1BYTES);
      ub = mkbuf(ws + A_U2, U2BYTES);
      yb = mkbuf(ws + A_Y2, Y2BYTES);
      wb = mkbuf(ws + (unsigned)(A_WF + w * 32768), 131072 + 32768);
      fr = mkbuf(ws + A_FL, NFLAG * 4);
    } else if (k == 2) {
      ve = pin(20480 + w * PL1 + (lane >> 3) * 128 + (((lane & 7) ^ kq) * 16));
      vt = pin(rho * 64 + kq * 16);
      tj = 4 * (slot & 15) + w;
      asm volatile("" : "+s"(tj));
      ty = tj % 3;
      ta = tj / 3 * 4 + (ty ? ty : -1);
      asm volatile("" : "+s"(ty), "+s"(ta));
    } else if (k < 6) {
      const int i = k - 3, g = ta + i + 84 * ((n >> 2) & 1), e = g >= 84, r = g - 84 * e;
      const int v = pin(4 * h * YB1 + bin2 * YP1 + (84 * (r >= 42) + 2 * r + e) * 64 + (n >> 3) * 16 + (n & 3) * 4);
      v1[i] = pin(g < (tj < 63 && i != 2 * ty ? 168 : -2) ? v : OOB);
      if (i) ok[i - 1] = __builtin_bit_cast(float, pin((unsigned)(32 * (2 * tj + i - 1 + 126 * ((n >> 2) & 1)) - 16 + 16 * h) < 8000u ? 0x3F800000 : 0x7F800000));
    } else if (k < 8) {
      if (k == 6) tau = pin(16 * ((n >> 2) & 1) + 8 * ((n >> 3) & 1) + 2 * (n & 3) + (n >> 4) + 16 + (ty ? 16 * ty : 48));
#pragma unroll
      for (int i = (3 * k - 18) >> 1; i < 3 * k - 17; ++i) {
        const int r = tau - 16 * (i >> 1) - 48 * (i & 1);
        ga[i] = pin(l0 + J_F + h * 1024 + ((unsigned)r < 64u ? r ^ ((r >> 4) & 1) : 0) * 16);
      }
    } else if (k == 8) {
      const int ge_ = ta + (h ? 2 - (ty == 1) : ty == 0);
      vp[0] = pin((bin2 >> 2) * 256 + (ge_ >= 42 && ge_ < 84) * 128 + n * 4);
      vf[0] = pin((slot & 3) * 256 + mt1 * 128 + bin1 * 4 + tid * 65536);
    } else {
      vf[1] = vf[2] = pin((slot & 15) * 256 + bin2 * 4 + tid * 65536);
      vu = pin((rho >> 1) * UB2 + (slot & 15) * 32768 + bin2 * 2048 + (w >> 1) * 1024 + (w & 1) * 128 + (lane & 1) * 64 + kq * 16);
    }
    SB();
  };
  auto aread = [&](int g, int ri) -> u32x4 {
    const int a = cpos(chunk(g)) + (g - first(chunk(g))) * 2048;
    return lds_ld(ab[a >> 16][ri] + (a & 65535));
  };
  f32x4 yr[11], yi[11];
  u32x4 A[4][2], nw[2], gf[16];
  auto gread = [&](int i) { gf[i] = lds_ld(ga[i >> 2] + (i & 3) * 2048); };
  auto trans = [&](auto ib, auto&& hook) {
    constexpr int b = decltype(ib)::value;
    f32x16 d[2], u[2][2];
    u32x4 a;
    sfor<0, 16>([&](auto ii) {
      constexpr int i = decltype(ii)::value;
      mm<0, (i & 7) == 0>(d[i >> 3], gf[i], y1[(i >> 3) + ((i >> 2) & 1)][i & 3]);
      PADG();
      if constexpr (i + 6 < 16) gread(i + 6);
      SB();
      hook(ii);
    });
    auto pack = [&](int q) {
#pragma unroll
      for (int e = 0; e < 2; ++e) {
#pragma unroll
        for (int z = 0; z < 2; ++z)
#pragma unroll
          for (int j = 0; j < 4; ++j) up[4 * q + 2 * e + z][j] = pk2bf(u[q][z][8 * e + 2 * j], u[q][z][8 * e + 2 * j + 1]);
        PADG();
        swap16(up[4 * q + 2 * e], up[4 * q + 2 * e + 1]);
        PADG();
      }
      SB();
    };
    auto put = [&](auto iq) {
      constexpr int q = decltype(iq)::value;
      sfor<8 * b + 4 * q, 8 * b + 4 * q + 4>([&](auto ii) { issue(IC<O_U>{}, ii); });
      PADG();
      st_pad(up[4 * q + 2], up[4 * q + 3]);
      SB();
    };
    sfor<0, 4>([&](auto ii) {
      constexpr int i = decltype(ii)::value, q = i >> 1, z = i & 1;
      if constexpr (i < 2) asm volatile("s_nop 2" : "+v"(d[z]));
#pragma unroll
      for (int j = 0; j < 2; ++j) { a[j] = gelu2(d[z][8 * q + 2 * j], d[z][8 * q + 2 * j + 1], ok[z]); PADG(); }
      SB();
      hook(IC<16 + 2 * i>{});
#pragma unroll
      for (int j = 2; j < 4; ++j) { a[j] = gelu2(d[z][8 * q + 2 * j], d[z][8 * q + 2 * j + 1], ok[z]); PADG(); }
      asm volatile("s_nop 1" : "+v"(a));
      SB();
      mm<0, true>(u[q][z], a, F);
      SB();
      hook(IC<17 + 2 * i>{});
      if constexpr (i == 2) { pack(0); if constexpr (!b || KP1) put(IC<0>{}); }
    });
    MWAIT(u[1][0], u[1][1]);
    pack(1);
    hook(IC<24>{});
    if constexpr (b && !KP1 && KU2 < 8) put(IC<0>{});
    if constexpr (!b || !KU2) put(IC<1>{});
  };
#pragma unroll
  for (int k = 0; k < KSU; ++k) setup(k);
  SB();
  wait_vm<5 * GPRO - 5>();
  asm volatile("s_barrier" ::: "memory");
#pragma unroll
  for (int z = 0; z < 3; ++z) { A[z][1] = aread(z, 1); SB(); A[z][0] = aread(z, 0); SB(); }
#pragma unroll
  for (int j = 0; j < 4; ++j) nw[0][j] = neg2(Wm[1][j]);
  SB();
  sfor<0, 179>([&](auto ig) {
    constexpr int g = decltype(ig)::value, c = chunk(g), ks = c & 15, mb = g - first(c) + (c < 16 ? 0 : 6), e = first(c + 1) - g;
    if constexpr (g == KPC1) { await(pl[0], fr, vp[0], FO[0]); }
    auto mf = [&](auto ij) {
      constexpr int j = decltype(ij)::value, i = (((g & 1) ? ORD1 : ORD0) >> (12 - 4 * j)) & 3;
      constexpr bool z = ks == 0 && !(j & 1);
      if constexpr (g < 176) {
        if constexpr (i == 0) mm<1, z>(yr[mb], Wm[2 * ks], A[g & 3][0]);
        else if constexpr (i == 1) mm<0, z>(yr[mb], nw[c & 1], A[g & 3][1]);
        else if constexpr (i == 2) mm<0, z>(yi[mb], Wm[2 * ks + 1], A[g & 3][0]);
        else mm<1, z>(yi[mb], Wm[2 * ks], A[g & 3][1]);
      }
      PADG();
    };
    auto cvt = [&](int j) {
      constexpr int m = g >= 92 && g < 98 ? g - 92 : g >= 173 && g < 178 ? g - 167 : -1;
      if constexpr (m >= 0) {
        if constexpr (g == 177) if (!j) asm volatile("s_nop 1" : "+v"(yr[m]), "+v"(yi[m]));
#pragma unroll
        for (int i = 2 * j; i < 2 * j + 2; ++i) o[m][i] = pk2bf(yr[m][i], yi[m][i]);
      }
      if constexpr (S1.ty[3 * g + 1] == O_S1) if (j) keep_v(o[S1.ix[3 * g + 1]]);
      if constexpr (g >= 173 && g < 176) gread(2 * (g - 173) + j);
      SB();
    };
    if constexpr (g < 176 && !(g & 1)) keep_v(A[(g + 1) & 3][0]);
    mf(IC<0>{});
    if constexpr (g + 3 < 176) { A[(g + 3) & 3][1] = aread(g + 3, 1); SB(); A[(g + 3) & 3][0] = aread(g + 3, 0); }
    SB();
    mf(IC<1>{});
    issue(IC<S1.ty[3 * g]>{}, IC<S1.ix[3 * g]>{});
    cvt(0);
    mf(IC<2>{});
    issue(IC<S1.ty[3 * g + 1]>{}, IC<S1.ix[3 * g + 1]>{});
    mf(IC<3>{});
    if constexpr (e == 4) {
      static_assert(S1.last[c + 1] && S1.before[3 * g + 2] >= S1.last[c + 1], "next chunk not requested yet");
      SB();
      if constexpr (g == KD1) { DRAIN(); }
      else {
        if constexpr (g < KD1 || S1.last[c + 1] > S1.before[3 * KD1 + 2]) wait_vm<S1.before[3 * g + 2] - S1.last[c + 1]>();
        asm volatile("s_barrier" ::: "memory");
      }
      SB();
    }
    issue(IC<S1.ty[3 * g + 2]>{}, IC<S1.ix[3 * g + 2]>{});
    if constexpr (g >= TS0 && g < TS0 + 30 - 3 * KSU) {
      if constexpr ((g - TS0) % 3 == 1) setup(KSU + (g - TS0) / 3);
      else if constexpr (THS >= 0) asm volatile("s_nop %0" ::"n"(THS));
    }
    else if constexpr (g >= TS0 && g < TS1) asm volatile("s_nop %0" ::"n"(S1.ty[3 * g + 2] ? THR - 3 : THR));
    else if constexpr (g >= TS1 && g < TS2) asm volatile("s_nop %0" ::"n"(THM));
    else if constexpr (THL >= 0 && g >= TS1 && g < 173 && !S1.ty[3 * g + 2]) asm volatile("s_nop %0" ::"n"(THL + 2));
    if constexpr (e <= 3 && e >= 2) {
#pragma unroll
      for (int j = 6 - 2 * e; j < 8 - 2 * e; ++j) nw[(c + 1) & 1][j] = neg2(Wm[(2 * ks + 3) & 31][j]);
    }
    SB();
    cvt(1);
  });
  st_pad(o[10], o[10]);
  trans(IC<0>{}, [&](auto ii) {
    constexpr int i = decltype(ii)::value;
    if constexpr (i < 16) fills(IC<S1.nl + NB0 * i / 16>{}, IC<S1.nl + NB0 * (i + 1) / 16>{});
    if constexpr (i == KS1) { DRAIN(); issue(IC<O_SIG>{}, IC<1>{}); }
  });
  const int pos = 32 * xcd + slot, seg = pos < 250 ? pos : 0;
  int wa[2], ra[4], fa[4], qa, vo;
  {
    const int grp = (slot & 12) + w, s = seg + (kq & 1), e = s >= 126, r = s - 126 * e;
        vx = pin(bin2 * UB2 + grp * 32768 + lane * 16);
    vs = pin(bin2 * YB2 + (slot & 3) * 131072 + grp * 4096 + ((rho & 2) * 4 + (rho & 4) + (rho >> 3) * 2 + (rho & 1)) * 64 + kq * 16);
    vy = pin(4 * h * YB2 + w * 131072 + (r >> 3) * 4096 + (8 * e + (r & 7)) * 64 + (rho >> 2) * 1024 + (rho & 3) * 16);
    vp[1] = pin(grp * 256 + rho * 4);
    vp[2] = pin(((r >> 5) * 4 + w) * 256 + rho * 4);
#pragma unroll
    for (int i = 0; i < 2; ++i) wa[i] = pin(l0 + lane * 16 + i * 65536);
  }
#pragma unroll
  for (int i = 0; i < 6; ++i) gread(i);
  if constexpr (KSL) __builtin_amdgcn_s_sleep(KSL);
  issue(IC<O_POLL>{}, IC<1>{});
  if constexpr (!KAW) { DRAIN(); issue(IC<O_SIG>{}, IC<2>{}); }
  await(pl[1], fr, vp[0], FO[1]);
  if constexpr (KAW == 1) { DRAIN(); issue(IC<O_SIG>{}, IC<2>{}); }
  sfor<0, 48>([&](auto ii) { issue(IC<O_Y1>{}, IC<48 + decltype(ii)::value % 16 * 3 + decltype(ii)::value / 16>{}); PADJ(); });
  if constexpr (KAW == 2) { DRAIN(); issue(IC<O_SIG>{}, IC<2>{}); }
  fills(IC<NL0>{}, IC<NL1>{});
  auto wread = [&](int g, int j) -> u32x4 { return lds_ld(wa[(g >> 5) & 1] + ((g >> 3 & 3) * 16 + (g & 7) + j * 8) * 1024); };
  trans(IC<1>{}, [&](auto ii) {
    constexpr int i = decltype(ii)::value, e = KXE;
    if constexpr (i < KXP) fills(IC<NL1 + NB2 * i / KXP>{}, IC<NL1 + NB2 * (i + 1) / KXP>{});
    if constexpr (i == KXP) issue(IC<O_POLL>{}, IC<2>{});
    if constexpr (i == KXA) {
      await(pl[0], fr, vp[1], FO[2]);
      sfor<0, NXA>([&](auto ix) { issue(IC<O_X>{}, ix); PADJ(); });
    }
    if constexpr (i > KXA && i < e) sfor<NXA + (NX0 - NXA) * (hk(i - 1) - hk(KXA)) / (hk(e - 1) - hk(KXA)), NXA + (NX0 - NXA) * (hk(i) - hk(KXA)) / (hk(e - 1) - hk(KXA))>([&](auto ix) { issue(IC<O_X>{}, ix); PADJ(); });
    if constexpr (i == 22) asm volatile("s_barrier" ::: "memory");
    if constexpr (i == 23) {
#pragma unroll
      for (int z = 0; z < 3; ++z) { A[z][1] = wread(z, 1); SB(); A[z][0] = wread(z, 0); SB(); }
    }
  });
  f32x4 ac[16][2];
  float re, im;
  sfor<0, 132>([&](auto ig) {
    constexpr int g = decltype(ig)::value, k = g >> 3, mb = (g & 7) + (g & 64) / 8;
    if constexpr (g == KPC2) { await(pl[1], fr, vp[1], FO[3]); }
    if constexpr (g == KPC3) { await(pl[0], fr, vp[2], FO[4]); }
    auto mf = [&](auto ij) {
      constexpr int j = decltype(ij)::value, i = (((g & 1) ? ORD3 : ORD2) >> (12 - 4 * j)) & 3;
      if constexpr (g < 128) mm<0, (k & 7) == 0 && !(j & 1)>(ac[mb][i >> 1], A[g & 3][i & 1], X[4 * k + 2 * (i & 1) + (i >> 1)]);
      PADJ();
    };
    auto cvt = [&](int j) {
      constexpr int m = g >= 59 && g < 67 ? g - 59 : g >= 123 && g < 131 ? g - 115 : -1;
      if constexpr (m >= 0) {
        const int i = 2 * (m & 1) + j;
        if constexpr (g == 129) if (!j) asm volatile("s_nop 1" : "+v"(ac[15][0]), "+v"(ac[15][1]));
        asm volatile("v_subrev_f32_dpp %0, %4, %2 row_shl:4 row_mask:0xf bank_mask:0x5\n\t"
                     "v_add_f32_dpp %1, %2, %4 row_shl:4 row_mask:0xf bank_mask:0x5\n\t"
                     "v_sub_f32_dpp %0, %3, %5 row_shr:4 row_mask:0xf bank_mask:0xa\n\t"
                     "v_add_f32_dpp %1, %5, %3 row_shr:4 row_mask:0xf bank_mask:0xa"
                     : "=&v"(re), "=&v"(im) : "v"(ac[m & 14][0][i]), "v"(ac[m & 14][1][i]), "v"(ac[m | 1][0][i]), "v"(ac[m | 1][1][i]));
        o[(m >> 1) & 3][i] = pk2bf(re, im);
      }
      if constexpr (S2.ty[3 * g + 1] == O_S2) if (j) keep_v(o[S2.ix[3 * g + 1] & 3]);
      if constexpr (S2.ty[3 * g + 1] == O_U) if (j) keep_v(up[S2.ix[3 * g + 1] & 7]);
      SB();
    };
    if constexpr (g < 128 && !(g & 1)) keep_v(A[(g + 1) & 3][0]);
    mf(IC<0>{});
    if constexpr (g + 3 < 128) { A[(g + 3) & 3][1] = wread(g + 3, 1); SB(); A[(g + 3) & 3][0] = wread(g + 3, 0); }
    SB();
    mf(IC<1>{});
    issue(IC<S2.ty[3 * g]>{}, IC<S2.ix[3 * g]>{});
    cvt(0);
    mf(IC<2>{});
    issue(IC<S2.ty[3 * g + 1]>{}, IC<S2.ix[3 * g + 1]>{});
    mf(IC<3>{});
    if constexpr (g == KD2) { DRAIN(); }
    if constexpr (g == KD3) { DRAIN(); }
    issue(IC<S2.ty[3 * g + 2]>{}, IC<S2.ix[3 * g + 2]>{});
    if constexpr (g >= TU0 && g < TU1) asm volatile("s_nop %0" ::"n"(S2.ty[3 * g + 2] ? THR2 - 3 : THR2));
    else if constexpr (THL >= 0 && g < 123 && !S2.ty[3 * g + 2]) asm volatile("s_nop %0" ::"n"(THL + 2));
    if constexpr ((g & 7) == 7 && g < 127) { keep_v(X[4 * k + 7]); }
    SB();
    cvt(1);
    if constexpr (g >= 84 && g < 88) ra[g & 3] = pin(l0 + ((w + 1 + g) & 3) * 4096 + lane * 16);
    if constexpr (g >= 88 && g < 92) fa[g & 3] = pin(l0 + J_F + ((w + 1 + g) & 3) * 2048 + lane * 16);
    if constexpr (g == 82) qa = pin(l0 + J_G + w * 4096 + (h * 8 + (n >> 4) * 4 + (n & 3)) * 16);
    if constexpr (g == 83) vo = pin(h * 4096 + w * 128 + n * 4);
    SB();
  });
  st_pad(o[3], o[3]);
  float p[2][32], x[8], mx = 0.f;
  f32x4 q[2][8], fe[4];
  u32x4 tp[2][4];
  auto jstep = [&](auto it) {
    constexpr int t = decltype(it)::value, l = S2.nl + t * KJR / 8;
    if constexpr (l < NFILL && l < S2.nl + (t + 1) * KJR / 8) issue(IC<O_J>{}, IC<fill_i(l)>{});
  };
  auto front = [&](auto ib, auto&& after) {
    constexpr int b = decltype(ib)::value & 1;
    f32x4 d[4];
    u32x4 fb[8];
    u32x4 yt[2][4];
    sfor<0, 4>([&](auto ic) { constexpr int c = decltype(ic)::value; yt[0][c] = column<c>(yv[b][0][0], yv[b][1][0], yv[b][2][0], yv[b][3][0]); });
    if constexpr (decltype(ib)::value < 2) PADJ();
    SB();
    sfor<0, 4>([&](auto ic) {
      constexpr int c = decltype(ic)::value;
      mm<0, true>(d[c], yt[0][c], gi[0]);
      yt[1][c] = column<c>(yv[b][0][1], yv[b][1][1], yv[b][2][1], yv[b][3][1]);
      if constexpr (decltype(ib)::value < 2) PADJ();
      SB();
    });
#pragma unroll
    for (int c = 0; c < 4; ++c) {
      mm<0, false>(d[c], yt[1][c], gi[1]);
      if constexpr (decltype(ib)::value < 2) PADJ();
    }
    asm volatile("s_nop 4" : "+v"(d[0]));
#pragma unroll
    for (int j = 0; j < 2; ++j) {
#pragma unroll
      for (int c = 0; c < 4; ++c) {
        fb[j][c] = gelu2(d[2 * j + (c >> 1)][2 * (c & 1)], d[2 * j + (c >> 1)][2 * (c & 1) + 1]);
        if constexpr (decltype(ib)::value < 2) PADJ();
      }
      lds_st(fa[3] + j * 1024, fb[j]);
      SB();
    }
    sfor<0, 32>([&](auto ii) {
      constexpr int i = decltype(ii)::value, eb = i < 8 ? i >> 1 : (i - 8) / 6, r = i < 8 ? i & 1 : 2 + (i - 8) % 6;
      mm<1, r == 0>(fe[eb], wt[i], fb[r]);
      if constexpr (decltype(ib)::value < 2) PADJ();
      SB();
      if constexpr (i == 0) {
        BAR();
      }
      if constexpr (i < 3) {
        fb[2 * i + 2] = lds_ld(fa[i]);
        fb[2 * i + 3] = lds_ld(fa[i] + 1024);
      }
      SB();
      after(ii);
    });
  };
  auto twrite = [&](auto iG) {
    constexpr int V = decltype(iG)::value + 31;
    if constexpr (V >= 0 && V < 256 && (V & 7) == 7) lds_st(ra[3] + J_T + ((V >> 5) & 1) * 16384 + ((V >> 3) & 3) * 1024, tp[(V >> 5) & 1][(V >> 3) & 3]);
    SB();
  };
  auto tslot = [&](auto iG) {
    constexpr int G = decltype(iG)::value;
    constexpr int A = G + 36, B = G + 35, C = G + 34, D = G + 33;
    if constexpr (B >= 0 && B < 256) asm volatile("v_rcp_f32 %0, %0" : "+v"(x[B & 7]));
    if constexpr (C >= 0 && C < 256) asm volatile("v_fma_f32 %0, -%1, %0, 0.5" : "+v"(x[C & 7]) : "v"(q[C >> 7][(C >> 2) & 7][C & 3]));
    PADJ();
    if constexpr (A >= 0 && A < 256) x[A & 7] = add_quad<(A >> 5) & 3>(p[A >> 7][A & 31], q[A >> 7][(A >> 2) & 7][A & 3]);
    SB();
    if constexpr (D >= 0 && D < 256 && (D & 1)) tp[(D >> 5) & 1][(D >> 3) & 3][(D >> 1) & 3] = pk2bf(x[(D - 1) & 7], x[D & 7]);
    SB();
  };
  auto stepE = [&](int b, int eb, int i) {
    const float a = fe[eb][i] * C3;
    mx = __builtin_fmaxf(mx, __builtin_fabsf(a));
    const uint32_t e = __builtin_bit_cast(uint32_t, __builtin_amdgcn_exp2f(a));
    const auto r = __builtin_amdgcn_permlane16_swap(e, e, false, false);
    p[b][8 * eb + i] = __builtin_bit_cast(float, (uint32_t)r[0]);
    p[b][8 * eb + 4 + i] = __builtin_bit_cast(float, (uint32_t)r[1]);
  };
  auto head = [&](auto ib) {
    constexpr int b = decltype(ib)::value;
    front(ib, [&](auto ii) {
      constexpr int i = decltype(ii)::value;
      if constexpr (b == 0 && i == KS5) {
        DRAIN();
        issue(IC<O_SIG>{}, IC<5>{});
        asm volatile("s_cmpk_lt_i32 %0, 250\n\ts_cbranch_scc1 1f\n\ts_endpgm\n1:" :: "s"(pos) : "scc");
      }
      if constexpr (i >= 3 && i < 11) q[b][i - 3] = __builtin_bit_cast(f32x4, lds_ld(qa + b * 2048 + (i - 3) * 256));
      if constexpr (b == 0 && i > KS5) jstep(IC<i - KS5 - 1>{});
      if constexpr (i >= 16 && (i - 16) % 6 < 4) {
        if constexpr ((i - 16) % 6 == 0) asm volatile("s_nop 1" : "+v"(fe[(i - 16) / 6]));
        stepE(b, (i - 16) / 6, (i - 16) % 6);
      }
      SB();
    });
    if constexpr (b == 0) sfor<-36, -32>([&](auto ir) { tslot(ir); });
    else asm volatile("s_nop 7" ::);
    asm volatile("" : "+v"(fe[3]));
#pragma unroll
    for (int i = 0; i < 4; ++i) { stepE(b, 3, i); PADJ(); }
    mx = __builtin_fmaxf(mx, q[b][0][0] < 0.f || q[b][4][0] < 0.f ? 2 * EMAX : 0.f);
    if constexpr (b) *(__attribute__((address_space(3))) float*)(uintptr_t)(uint32_t)(l0 + J_R + tid * 4) = mx;
    asm volatile("s_nop 1");
    SB();
  };
  head(IC<0>{});
  sfor<-32, 0>([&](auto ir) { jstep(IC<decltype(ir)::value + 63 - KS5>{}); twrite(ir); tslot(ir); });
  const __amdgpu_buffer_rsrc_t ob = mkbuf(out + (size_t)seg * 16 * 8 * DCH, 4000 * 4096 + 65536);
  f32x16 acc[2][2];
  u32x4 a[16];
  uint32_t ov[16];
  int vq[2] = {vo, vo + 8192};
  auto store = [&](int i) { __builtin_amdgcn_raw_buffer_store_b32(ov[i], ob, vq[(i >> 2) & 1] + ((i >> 3) * 4 + (i & 3)) * 512, 0, OPOL); };
  auto next = [&](int st, int j) {
    vq[j] += (st & 3) ? 16384 : 4000 * 4096 - 3 * 16384;
    asm volatile("" : "+v"(vq[j]));
  };
  sfor<0, 256>([&](auto iG) {
    constexpr int G = decltype(iG)::value, st = G >> 5, g = G & 31, r = g >> 1, z = g & 1;
    if constexpr (G == 92) { head(IC<1>{}); }
    mm<2, r == 0>(acc[st & 1][z], r < 4 ? tp[st & 1][r & 3] : a[r], wt[32 + g]);
    PADJ();
    SB();
    if constexpr (g == 2) BAR();
    if constexpr (st == 0 && g < 31) keep_a(wt[33 + g]);
    if constexpr (z && r >= 3 && r < 15) keep_v(a[r + 1]);
    constexpr int rd = g >= 3 && g < 6 ? g + 1 : (g >= 7 && g <= 23 && z) ? (g + 7) / 2 : 0;
    constexpr int sn = g == 1 ? 15 : g == 2 ? 0 : g == 6 ? 1 : g >= 25 ? g - 17 : (g > 8 && !z && (g & 7)) ? (g - 6) / 2 - (g > 16) : -1;
    if constexpr (rd) a[rd] = lds_ld(ra[(rd >> 2) - 1] + J_T + (st & 1) * 16384 + (rd & 3) * 1024);
    if constexpr (sn >= 0 && st > (g == 1)) store(sn);
    if constexpr (G < 24) jstep(IC<G + 63 - KS5>{});
    if constexpr (G == KPI4) issue(IC<O_POLL>{}, IC<5>{});
    if constexpr (G == KPC4) await(pl[1], fr, vp[2], FO[5]);
    if constexpr (G >= KPC4 && G < KPC4 + 8 * KYS && (G - KPC4) % KYS == 0) yload(1, (G - KPC4) / KYS);
    twrite(iG);
    tslot(iG);
    if constexpr (st > 0 && g == 30) next(st, 0);
    if constexpr (st > 1 && g == 4) next(st - 1, 1);
    if constexpr (G == 225) asm volatile("s_nop 5" : "+v"(acc[0][0]));
    if constexpr (st > 0 && z) ov[r] = pk2bf(acc[(st - 1) & 1][0][r], acc[(st - 1) & 1][1][r]);
    SB();
  });
  store(15);
  next(7, 1);
  const f32x4 rg = __builtin_bit_cast(f32x4, lds_ld(l0 + J_R + lane * 16));
  MWAIT(acc[1][0], acc[1][1]);
#pragma unroll
  for (int i = 0; i < 16; ++i) { ov[i] = pk2bf(acc[1][0][i], acc[1][1][i]); store(i); PADJ(); }
  if (__builtin_expect(__builtin_amdgcn_ballot_w64(rg[0] > EMAX || rg[1] > EMAX || rg[2] > EMAX || rg[3] > EMAX) != 0, 0)) {
    asm volatile("s_waitcnt vmcnt(0)" ::: "memory");
#pragma unroll 1
    for (int b = 0; b < 2; ++b) {
#pragma unroll
      for (int i = 0; i < 8; ++i) yload(0, i);
      vy += 65536;
      front(IC<2>{}, [](auto) {});
      asm volatile("s_nop 7" : "+v"(fe[3]));
#pragma unroll 1
      for (int st = 0; st < 4; ++st) {
        const float* gp = ge + 4096 + (w * 2 + b) * 512 + (h * 8 + (n >> 4) * 4 + (n & 3)) * 4;
#pragma unroll
        for (int f = 0; f < 4; ++f) {
          u32x4 t;
#pragma unroll
          for (int c = 0; c < 2; ++c) {
            const f32x4 gv = *(const f32x4*)(gp + (2 * f + c) * 64);
            float y[4];
#pragma unroll
            for (int i = 0; i < 4; ++i)
              y[i] = 0.5f - __builtin_amdgcn_rcpf(__builtin_amdgcn_exp2f(__shfl(fe[f][i], h * 32 + c * 16 + (lane & 12) + st) * C3 + gv[i]) + 1.f);
            t[2 * c] = pk2bf(y[0], y[1]);
            t[2 * c + 1] = pk2bf(y[2], y[3]);
          }
          lds_st(ra[3] + J_T + (st & 1) * 16384 + f * 1024, t);
        }
        BAR();
        sfor<0, 16>([&](auto ir) {
          constexpr int r = decltype(ir)::value;
          const u32x4 v = lds_ld(ra[(r / 4 + 3) & 3] + J_T + (st & 1) * 16384 + (r & 3) * 1024);
#pragma unroll
          for (int z = 0; z < 2; ++z) mm<2, r == 0>(acc[0][z], v, wt[32 + 2 * r + z]);
        });
        MWAIT(acc[0][0], acc[0][1]);
#pragma unroll
        for (int i = 0; i < 16; ++i)
          __builtin_amdgcn_raw_buffer_store_b32(pk2bf(acc[0][0][i], acc[0][1][i]), ob, vo + st * 16384 + ((i >> 3) * 4 + (i & 3)) * 512,
                                                b * (4000 * 4096) + ((i >> 2) & 1) * 8192, NT);
      }
    }
  }
}

template <int CONV>
__device__ __forceinline__ void wtrans(const uint16_t* __restrict__ w, char* __restrict__ wh, int wid, int lane) {
  const int dq = wid & 3, pc = wid >> 2;
  const int ks = pc >> 2, kq = pc & 3;
  const int n32 = lane & 31, h = lane >> 5;
  const int r = CONV == 1 ? ks >> 3 : kq >> 1;
  const int c0 = CONV == 1 ? (ks & 7) * 32 + kq * 8 : dmap(ks, 8 * (kq & 1));
  const __amdgpu_buffer_rsrc_t wr = mkbuf(w, 31 * 131072);
  const int vb = (16 * h + r + 1) * 131072 + c0 * 512 + dq * 128 + n32 * 4;
  const int vb0 = (h + r == 0) ? OOB : vb - 262144;
  constexpr int NFB = CONV == 1 ? 2 : 1;
  const u32x4* et = CONV == 1 ? (const u32x4*)&tabs.e1[0][0][0][0] : (const u32x4*)&tabs.e2[0][0][0];
  u32x4 E[NFB][2];
#pragma unroll
  for (int fb = 0; fb < NFB; ++fb) { E[fb][0] = et[fb * 128 + lane]; E[fb][1] = et[fb * 128 + 64 + lane]; }
  uint32_t v[8][8];
#pragma unroll
  for (int el = 0; el < 8; ++el)
#pragma unroll
    for (int jj = 0; jj < 8; ++jj) {
      const int co = CONV == 1 ? el * 512 : (el & 3) * 1024;
      const int so = (CONV == 1 ? 0 : (el >> 2) * 4096) + (jj ? (jj - 1) * 262144 : 0);
      v[el][jj] = __builtin_amdgcn_raw_buffer_load_b32(wr, (jj ? vb : vb0) + co, so, NT);
    }
  PHASE();
  const __amdgpu_buffer_rsrc_t dr = mkbuf(wh, (CONV == 1 ? 32 : 16) * WBIN);
  const int vo = 2 * h * WBIN + (4 * dq + (n32 >> 4)) * WCB + ks * 2048 + kq * 256 + (n32 & 15) * 16;
#pragma unroll
  for (int e = 0; e < 2; ++e) {
    u32x4 b[8];
#pragma unroll
    for (int el = 0; el < 8; ++el)
#pragma unroll
      for (int j = 0; j < 4; ++j) b[el][j] = __builtin_amdgcn_perm(v[el][2 * j + 1], v[el][2 * j], e ? 0x07060302u : 0x05040100u);
#pragma unroll
    for (int fb = 0; fb < NFB; ++fb) {
      u32x4 o[16];
#pragma unroll
      for (int ep = 0; ep < 4; ++ep) {
        f32x16 d[2];
#pragma unroll
        for (int z = 0; z < 2; ++z) d[z] = mfma32(E[fb][1], b[2 * ep + z], zero16());
#pragma unroll
        for (int z = 0; z < 2; ++z) d[z] = mfma32(E[fb][0], b[2 * ep + z], d[z]);
#pragma unroll
        for (int i = 0; i < 16; ++i) o[i][ep] = pk2bf(d[0][i], d[1][i]);
      }
#pragma unroll
      for (int i = 0; i < 16; ++i)
        __builtin_amdgcn_raw_buffer_store_b128(o[i], dr, vo + (i & 1) * 1024,
                                               (16 * fb + 4 * (i >> 2) + ((i >> 1) & 1)) * WBIN + e * 2 * WCB, WT);
      st_pad(o[14], o[15]);
      SB();
    }
  }
}

__device__ __forceinline__ void xload(const uint16_t* __restrict__ x, int u, int lane, uint32_t (&v)[3][8]) {
  const int cq = u & 7, m = u >= 8 * 168, b = u - 8 * 168 * m >= 8 * 84, r = (u >> 3) - 168 * m - 84 * b, sg = 84 * (r & 1) + 42 * m + (r >> 1);
  const __amdgpu_buffer_rsrc_t xr = mkbuf(x + (size_t)b * 16000 * DCH, 8192000);
#pragma unroll
  for (int ks = 0; ks < 3; ++ks) {
    const int row0 = 48 * sg - 24 + 16 * ks + 8 * (lane >> 5);
    const int vb = row0 >= 0 ? row0 * 1024 + cq * 128 + (lane & 31) * 4 : OOB;
#pragma unroll
    for (int e = 0; e < 8; ++e) v[ks][e] = __builtin_amdgcn_raw_buffer_load_b32(xr, vb + (e & 3) * 1024, (e >> 2) * 4096, NT);
  }
}
__device__ __forceinline__ void xtrans(char* __restrict__ u1, int u, int lane, const uint32_t (&v)[3][8], const u32x4 (&F)[2][3]) {
  const __amdgpu_buffer_rsrc_t ur = mkbuf(u1 + (2 * (u & 7)) * PL1 + (u >> 3) * 128, U1BYTES - (2 * (u & 7)) * PL1 - (u >> 3) * 128);
  const int vo = ((lane & 15) >> 1) * UB1 + ((lane >> 4) & 1) * 64 + (lane & 1) * 32 + (lane >> 5) * 16;
  u32x4 a[2][3];
#pragma unroll
  for (int ks = 0; ks < 3; ++ks)
#pragma unroll
    for (int j = 0; j < 4; ++j) {
      a[0][ks][j] = __builtin_amdgcn_perm(v[ks][2 * j + 1], v[ks][2 * j], 0x05040100u);
      a[1][ks][j] = __builtin_amdgcn_perm(v[ks][2 * j + 1], v[ks][2 * j], 0x07060302u);
    }
#pragma unroll
  for (int fb = 0; fb < 2; ++fb) {
    f32x16 d[2] = {zero16(), zero16()};
#pragma unroll
    for (int ks = 0; ks < 3; ++ks)
#pragma unroll
      for (int e = 0; e < 2; ++e) d[e] = mfma32(a[e][ks], F[fb][ks], d[e]);
#pragma unroll
    for (int qp = 0; qp < 2; ++qp) {
      u32x4 o[2];
#pragma unroll
      for (int z = 0; z < 2; ++z)
#pragma unroll
        for (int i = 0; i < 4; ++i) o[z][i] = pk2bf(d[0][8 * qp + 4 * z + i], d[1][8 * qp + 4 * z + i]);
      swap16(o[0], o[1]);
#pragma unroll
      for (int z = 0; z < 2; ++z) __builtin_amdgcn_raw_buffer_store_b128(o[z], ur, vo, (fb * 16 + z * 8) * UB1 + qp * PL1, WT);
      st_pad(o[0], o[1]);
    }
  }
}

KERNEL __launch_bounds__(512, 2) void pre_kernel(const uint16_t* __restrict__ x, const uint16_t* __restrict__ w1,
                                                     const uint16_t* __restrict__ w2, const uint16_t* __restrict__ wf,
                                                     const uint16_t* __restrict__ wo, const uint16_t* __restrict__ g,
                                                     const uint16_t* __restrict__ wg, char* __restrict__ u1,
                                                     char* __restrict__ w1h, char* __restrict__ w2h, u32x4* __restrict__ wff,
                                                     u32x4* __restrict__ wof, float* __restrict__ ge, uint32_t* __restrict__ flags) {
  const int blk = blockIdx.x;
  const int tid = threadIdx.x, lane = tid & 63;
  const int wave = __builtin_amdgcn_readfirstlane(tid >> 6);
  const int n = lane & 31, h = lane >> 5;
  __shared__ char pfs[256];
  if (wave == 7) {
    const char *p0 = (const char*)&tabs - 320, *p1;
    LABEL_ADDR(p1, ".Lce");
    code_pf(p0, (int)(p1 - p0), blk >> 3, pfs);
  }
  if (wave == 0) {
    wtrans<1>(w1, w1h, blk, lane);
  } else if (wave == 1) {
    wtrans<2>(w2, w2h, blk, lane);
  } else {
    const int bq = (blk & 7) * 32 + (blk >> 3);
    const int u0 = ((21 * bq) >> 1) + wave - 2, ue = (21 * (bq + 1)) >> 1;
    u32x4 F[2][3];
#pragma unroll
    for (int fb = 0; fb < 2; ++fb)
#pragma unroll
      for (int ks = 0; ks < 3; ++ks) F[fb][ks] = ((const u32x4*)&tabs.f1[fb][ks][0][0])[lane];
    uint32_t v0[3][8];
    xload(x, u0, lane, v0);
    if (u0 + 6 < ue) {
      uint32_t v1[3][8];
      xload(x, u0 + 6, lane, v1);
      SB();
      WALK(7, wave - 2);
      PHASE();
      xtrans(u1, u0, lane, v0, F);
      SB();
      xtrans(u1, u0 + 6, lane, v1, F);
    } else if (wave == 7) {
      const int i = blk & 31, w = (blk >> 5) & 3, second = blk >> 7;
      const int eb = i < 8 ? i >> 1 : (i - 8) / 6, r = (2 * w + (i < 8 ? i & 1 : 2 + (i - 8) % 6)) & 7;
      const uint16_t* src = second ? wo + (16 * ((4 * w + (i >> 1)) & 15) + 8 * h) * DCH + 64 * w + 2 * n + (i & 1)
                                   : wf + dmap(4 * (r >> 1) + (lane >> 4), 2 * (r & 1)) * DCH + 64 * w + 16 * eb + (lane & 15);
      uint32_t v[8];
#pragma unroll
      for (int j = 0; j < 8; ++j) v[j] = src[(second ? j : 8 * (j & 3) + 2 * (j >> 2)) * DCH];
      PHASE();
      xtrans(u1, u0, lane, v0, F);
      const float sc = second ? 2.f : 1.f;
      u32x4 o;
#pragma unroll
      for (int j = 0; j < 4; ++j) o[j] = pk2bf(bf2f(v[2 * j]) * sc, bf2f(v[2 * j + 1]) * sc);
      ((second ? wof : wff) + (blk & 127) * 64)[lane] = o;
      if (blk < NFLAG / 64) flags[blk * 64 + lane] = 0;
    } else if (!(blk & ~0x16)) {
      const int gb = ((blk >> 1) & 3) | ((blk >> 2) & 4);
      const int c0 = 32 * gb + 2 * (lane & 15), kq = lane >> 4;
      uint32_t v[8][8];
      u32x4 ga[8];
#pragma unroll
      for (int ks = 0; ks < 8; ++ks) {
        ga[ks] = *(const u32x4*)(g + (lane & 15) * DCH + ks * 32 + 8 * kq);
#pragma unroll
        for (int e = 0; e < 8; ++e) v[ks][e] = *(const uint32_t*)(wg + (ks * 32 + 8 * kq + e) * DCH + c0);
      }
      SB();
      asm volatile("s_barrier" ::: "memory");
      SB();
      xtrans(u1, u0, lane, v0, F);
      SB();
      f32x4 d[2] = {{0.f, 0.f, 0.f, 0.f}, {0.f, 0.f, 0.f, 0.f}};
#pragma unroll
      for (int ks = 0; ks < 8; ++ks)
#pragma unroll
        for (int e = 0; e < 2; ++e) {
          u32x4 bb;
#pragma unroll
          for (int j = 0; j < 4; ++j) bb[j] = __builtin_amdgcn_perm(v[ks][2 * j + 1], v[ks][2 * j], e ? 0x07060302u : 0x05040100u);
          d[e] = mfma16(ga[ks], bb, d[e]);
        }
      float* dst = ge + (((((gb >> 1) * 2 + (kq >> 1)) * 4 + 2 * (gb & 1) + ((lane >> 3) & 1)) * 2 + ((lane >> 1) & 1)) * 2 + ((lane >> 2) & 1)) * 32 + (kq & 1) * 16 + 2 * (lane & 1);
      f32x2 gs[4];
      bool far = false;
#pragma unroll
      for (int i = 0; i < 4; ++i) {
        gs[i] = f32x2{bf2f(f2bf(d[0][i])) * C2, bf2f(f2bf(d[1][i])) * C2};
        far = far || __builtin_fabsf(gs[i][0]) > EMAX || __builtin_fabsf(gs[i][1]) > EMAX;
      }
      far = __builtin_amdgcn_ballot_w64(far) != 0;
#pragma unroll
      for (int i = 0; i < 4; ++i) {
        *(f32x2*)(dst + 4 * i) = far ? f32x2{-1.f, -1.f} : f32x2{__builtin_amdgcn_exp2f(-gs[i][0]), __builtin_amdgcn_exp2f(-gs[i][1])};
        *(f32x2*)(dst + 4096 + 4 * i) = gs[i];
      }
    } else {
      PHASE();
      xtrans(u1, u0, lane, v0, F);
    }
  }
  asm volatile(".Lce:");
}

extern "C" void kernel_launch(void* const* d_in, const int* in_sizes, int n_in, void* d_out, int out_size, void* d_ws,
                              size_t ws_size, hipStream_t stream) {
  const uint16_t* x = (const uint16_t*)d_in[0];
  const uint16_t* w1 = (const uint16_t*)d_in[1];
  const uint16_t* w2 = (const uint16_t*)d_in[2];
  const uint16_t* g = (const uint16_t*)d_in[3];
  const uint16_t* wf = (const uint16_t*)d_in[4];
  const uint16_t* wg = (const uint16_t*)d_in[5];
  const uint16_t* wo = (const uint16_t*)d_in[6];
  uint16_t* out = (uint16_t*)d_out;
  char* ws = (char*)d_ws;
  (void)hipFuncSetAttribute((const void*)joint_kernel, hipFuncAttributeMaxDynamicSharedMemorySize, JOINT_LDS);

  pre_kernel<<<256, 512, 0, stream>>>(x, w1, w2, wf, wo, g, wg, ws + A_U1, ws + A_W1, ws + A_W2, (u32x4*)(ws + A_WF),
                                      (u32x4*)(ws + A_WF + 131072), (float*)(ws + A_GE), (uint32_t*)(ws + A_FL));
  joint_kernel<<<256, 256, JOINT_LDS, stream>>>(ws, out);
}
```

```cpp
#include <hip/hip_runtime.h>
#include <stdint.h>

typedef __attribute__((ext_vector_type(8))) short bf16x8;
typedef __attribute__((ext_vector_type(16))) float f32x16;
typedef __attribute__((ext_vector_type(4))) float f32x4;
typedef __attribute__((ext_vector_type(2))) float f32x2;
typedef __attribute__((ext_vector_type(4))) unsigned int u32x4;
typedef __attribute__((ext_vector_type(2))) unsigned int u32x2;

#define ORD0 0x0132
#define ORD1 0x2310
#define GLA 6
#define TS0 18
#define TS1 50
#define TU0 0
#define TU1 0
#define THR 10
#define THR2 7
#define GPRO 2
constexpr int DCH = 256;

__device__ __forceinline__ float bf2f(uint32_t v) { return __uint_as_float(v << 16); }
__device__ __forceinline__ uint32_t f2bf(float f) {
  uint32_t u = __float_as_uint(f);
  u += 0x7FFF + ((u >> 16) & 1);
  return u >> 16;
}
__device__ __forceinline__ uint32_t pk2bf(float a, float b) {
  typedef __attribute__((ext_vector_type(2))) __bf16 b2;
  f32x2 v = {a, b};
  b2 r = __builtin_convertvector(v, b2);
  return __builtin_bit_cast(uint32_t, r);
}
constexpr double GS = 0.4686686932353139;
__device__ __forceinline__ uint32_t gelu2(float a0, float a1, float one = 1.f) {
  const f32x2 v = {a0, a1};
  constexpr float c = (float)(-2.302208198144325 / GS);
  const f32x2 z = v * __builtin_elementwise_fma(-v, v, f32x2{c, c});
  const f32x2 d = f32x2{__builtin_amdgcn_exp2f(z[0]), __builtin_amdgcn_exp2f(z[1])} + f32x2{one, one};
  const f32x2 y = v * f32x2{__builtin_amdgcn_rcpf(d[0]), __builtin_amdgcn_rcpf(d[1])};
  return pk2bf(y[0], y[1]);
}
__device__ __forceinline__ f32x16 mfma32(const u32x4& a, const u32x4& b, const f32x16& c) {
  return __builtin_amdgcn_mfma_f32_32x32x16_bf16(__builtin_bit_cast(bf16x8, a), __builtin_bit_cast(bf16x8, b), c, 0, 0, 0);
}
__device__ __forceinline__ f32x4 mfma16(const u32x4& a, const u32x4& b, const f32x4& c) {
  return __builtin_amdgcn_mfma_f32_16x16x32_bf16(__builtin_bit_cast(bf16x8, a), __builtin_bit_cast(bf16x8, b), c, 0, 0, 0);
}
__device__ __forceinline__ f32x16 zero16() {
  f32x16 z;
#pragma unroll
  for (int i = 0; i < 16; ++i) z[i] = 0.f;
  return z;
}
__device__ __forceinline__ __amdgpu_buffer_rsrc_t mkbuf(const void* p, int nbytes) {
  return __builtin_amdgcn_make_buffer_rsrc((void*)p, (short)0, nbytes, 0x00020000);
}
template <int V> struct IC { static constexpr int value = V; };
template <int I, int N, class F>
__device__ __forceinline__ void sfor(F&& f) {
  if constexpr (I < N) {
    f(IC<I>{});
    sfor<I + 1, N>(f);
  }
}
#define SB() __builtin_amdgcn_sched_barrier(0)
#define PHASE() do { SB(); asm volatile("s_waitcnt vmcnt(0)\n\ts_barrier" ::: "memory"); SB(); } while (0)

constexpr double PI_ = 3.14159265358979323846;
constexpr double c128(int m) {
  m = ((m % 128) + 128) % 128;
  if (m > 64) m = 128 - m;
  double sg = 1.0;
  if (m > 32) { m = 64 - m; sg = -1.0; }
  if (m == 32) return 0.0;
  const double a = PI_ * m / 64.0;
  double term = 1.0, sum = 1.0;
  for (int i = 1; i < 16; ++i) { term *= -a * a / ((2 * i - 1) * (2 * i)); sum += term; }
  return sg * sum;
}
constexpr double s128(int m) { return c128(m - 32); }
constexpr uint16_t cbf(double v) {
  uint32_t u = __builtin_bit_cast(uint32_t, (float)v);
  u += 0x7FFF + ((u >> 16) & 1);
  return (uint16_t)(u >> 16);
}
constexpr double cbf2d(uint16_t v) { return (double)__builtin_bit_cast(float, (uint32_t)v << 16); }
struct Tabs {
  uint16_t f1[2][3][64][8];
  uint16_t e1[2][2][64][8];
  uint16_t e2[2][64][8];
  uint16_t g1[8][64][8];
  uint16_t f2[64][8];
  uint16_t g2[2][64][8];
};
constexpr Tabs make_tabs() {
  Tabs t{};
  for (int l = 0; l < 64; ++l)
    for (int e = 0; e < 8; ++e) {
      const int h = l >> 5, f = l & 31, ri = f & 1;
      for (int fb = 0; fb < 2; ++fb) {
        const int k = 16 * fb + (f >> 1);
        for (int ks = 0; ks < 3; ++ks) {
          const int a = (2 * k + 1) * (16 * ks + 8 * h + e);
          t.f1[fb][ks][l][e] = cbf(ri ? -s128(a) : c128(a));
        }
        const int a = (2 * k + 1) * (8 * h + e);
        const double v = ri ? s128(a) : c128(a);
        t.e1[fb][0][l][e] = cbf(v);
        t.e1[fb][1][l][e] = cbf(v - cbf2d(cbf(v)));
      }
      const int a = 2 * (2 * (f >> 1) + 1) * (8 * h + e);
      const double v = ri ? s128(a) : c128(a);
      t.e2[0][l][e] = cbf(v);
      t.e2[1][l][e] = cbf(v - cbf2d(cbf(v)));
      t.f2[l][e] = cbf((ri ? -v : v) / GS);
    }
  for (int m = -15; m < 48; ++m)
    for (int f = 0; f < 64; ++f) {
      const int a = (2 * (f >> 1) + 1) * m;
      t.g1[f >> 3][(m + 16) ^ (((m + 16) >> 4) & 1)][f & 7] = cbf(((f & 1) ? -s128(a) : c128(a)) * GS / 32.0);
    }
  for (int c = 0; c < 2; ++c)
    for (int l = 0; l < 64; ++l)
      for (int e = 0; e < 8; ++e) {
        const int m = 4 * (l & 3) + ((l >> 2) & 3) - (l & 16), a = 2 * (2 * (8 * c + 4 * (l >> 5) + (e >> 1)) + 1) * m;
        t.g2[c][l][e] = m > -16 ? cbf(((e & 1) ? -s128(a) : c128(a)) * GS / 16.0) : 0;
      }
  return t;
}
__device__ __attribute__((aligned(64))) const Tabs tabs = make_tabs();

constexpr int OOB = 0x7FFFFF00;
constexpr float C2 = 2.885390081777927f, C3 = (float)(2.885390081777927 / GS);
#define EMAXV 120.f
constexpr float EMAX = EMAXV;
constexpr int WT = 16;
constexpr int NT = 2;
constexpr int PL1 = 336 * 128;
#define PU2 0
#define PY2 0
constexpr int UB1 = 16 * PL1, UB2 = 524288 + PU2;
constexpr int U1BYTES = 32 * UB1, U2BYTES = 32 * UB2;
constexpr int WCB = 32768;
constexpr int WBIN = 16 * WCB;
constexpr int YP1 = 336 * 64;
constexpr int YB1 = 16 * YP1, YB2 = 524288 + PY2;
constexpr int Y1BYTES = 32 * YB1, Y2BYTES = 16 * YB2;
constexpr int F1 = 0, F2 = 2048, F3 = 10240, NFLAG = 18432 / 4;
constexpr int A_U1 = 0, A_W1 = 23 << 20, A_W2 = 40 << 20, A_Y1 = 49 << 20, A_U2 = 61 << 20, A_Y2 = 78 << 20, A_WF = 87 << 20,
              A_GE = A_WF + 262144, A_FL = 88 << 20;
__device__ __forceinline__ int dmap(int cb, int n) { return 64 * (cb >> 2) + 32 * (cb & 1) + 2 * n + ((cb >> 1) & 1); }

__device__ __forceinline__ void swap16(u32x4& a, u32x4& b) {
#pragma unroll
  for (int i = 0; i < 4; ++i) {
    const auto r = __builtin_amdgcn_permlane16_swap(a[i], b[i], false, false);
    a[i] = r[0];
    b[i] = r[1];
  }
}

__device__ __forceinline__ void st_pad(const u32x4& a, const u32x4& b) { asm volatile("s_nop 1" :: "v"(a), "v"(b) : "memory"); }

template <int N>
__device__ __forceinline__ void wait_vm() {
  constexpr int M = N < 63 ? N : 63;
  __builtin_amdgcn_s_waitcnt((M & 15) | ((M >> 4) << 14) | 0x0F70);
}

typedef __attribute__((address_space(3))) u32x4 lds_u32x4;
__device__ __forceinline__ u32x4 lds_ld(int a) { return *(const lds_u32x4*)(uintptr_t)(uint32_t)a; }
__device__ __forceinline__ uint32_t lds_ld32(int a) { return *(const __attribute__((address_space(3))) uint32_t*)(uintptr_t)(uint32_t)a; }
template <int SZ>
__device__ __forceinline__ void dma(__amdgpu_buffer_rsrc_t rsrc, char* lds, int voff, int soff) {
#if defined(__HIP_DEVICE_COMPILE__)
  typedef __attribute__((address_space(3))) void* ldsp;
  if constexpr (SZ == 16) __builtin_amdgcn_raw_ptr_buffer_load_lds(rsrc, (ldsp)lds, 16, voff, soff, 0, 0);
  else __builtin_amdgcn_raw_ptr_buffer_load_lds(rsrc, (ldsp)lds, 4, voff, soff, 0, 0);
#endif
}

#define LABEL_ADDR(p, name) asm volatile("s_getpc_b64 s[40:41]\n\ts_add_u32 s40, s40, " name "@rel32@lo+4\n\ts_addc_u32 s41, s41, " name "@rel32@hi+12\n\ts_mov_b64 %0, s[40:41]" : "=s"(p) :: "s40", "s41", "scc")
__device__ __forceinline__ void code_pf(const char* p, int n, int part, char* lds) {
  dma<4>(mkbuf(p, n), lds, part * 8192 + (threadIdx.x & 63) * 128, 0);
}

__device__ __forceinline__ void pad_macros() {
  asm volatile(R"(
.macro PAD1 l
.ifndef .Lw_\l
.Lw_\l:
.endif
\l:
s_cbranch_execz \l\()f
.endm
.macro PADM k, b, m
.ifndef .Lpg_\k
.set .Lpg_\k, -1
.endif
.ifndef .Lcn_\b
.set .Lcn_\b, 0
.endif
.if (((. - \k) & 4095) < 60) && (((. - \k) >> 12) > .Lpg_\k)
.set .Lpg_\k, (. - \k) >> 12
.irp c, 0, 1, 2, 3, 4, 5, 6, 7
.if (.Lcn_\b & \m) == \c
PAD1 \b\c
.endif
.endr
.set .Lcn_\b, .Lcn_\b + 1
.endif
.endm
.macro WALK b, w
s_mov_b64 exec, 0
.irp c, 0, 1, 2, 3, 4, 5, 6, 7
.ifdef .Lw_\b\c
s_cmp_eq_u32 \w, \c
s_cbranch_scc1 .Lw_\b\c
.endif
.endr
.irp c, 0, 1, 2, 3, 4, 5, 6, 7
\b\c:
.endr
s_mov_b64 exec, -1
.endm
)");
}
#define PAD(k, b, m) asm volatile("PADM " #k ", " #b ", " #m)
#define WALK(b, w) asm volatile("WALK " #b ", %0" :: "s"(w) : "scc")
#define KERNEL extern "C" __global__ __attribute__((aligned(4096)))

constexpr int J_T = 0;
constexpr int J_G = 131072;
constexpr int J_F = J_G + 16384;
constexpr int J_R = J_F + 8192;
constexpr int JOINT_LDS = J_R + 1024;
__device__ __forceinline__ void lds_st(int a, const u32x4& v) { *(lds_u32x4*)(uintptr_t)(uint32_t)a = v; }
#define BAR() do { SB(); asm volatile("s_waitcnt lgkmcnt(0)\n\ts_barrier" ::: "memory"); SB(); } while (0)
#define DRAIN() do { SB(); wait_vm<0>(); asm volatile("s_waitcnt vmcnt(0)\n\ts_barrier" ::: "memory"); SB(); } while (0)
template <int M, bool Z>
__device__ __forceinline__ void mm(f32x16& c, const u32x4& a, const u32x4& b) {
  if constexpr (Z) {
    if constexpr (M == 0) asm volatile("v_mfma_f32_32x32x16_bf16 %0, %1, %2, 0" : "=&v"(c) : "v"(a), "v"(b));
    else asm volatile("v_mfma_f32_32x32x16_bf16 %0, %1, %2, 0" : "=&v"(c) : "v"(a), "a"(b));
  } else {
    if constexpr (M == 0) asm volatile("v_mfma_f32_32x32x16_bf16 %0, %1, %2, %0" : "+v"(c) : "v"(a), "v"(b));
    else asm volatile("v_mfma_f32_32x32x16_bf16 %0, %1, %2, %0" : "+v"(c) : "v"(a), "a"(b));
  }
}
template <int M, bool Z>
__device__ __forceinline__ void mm(f32x4& c, const u32x4& a, const u32x4& b) {
  if constexpr (Z) {
    if constexpr (M == 0) asm volatile("v_mfma_f32_16x16x32_bf16 %0, %1, %2, 0" : "=&v"(c) : "v"(a), "v"(b));
    else asm volatile("v_mfma_f32_16x16x32_bf16 %0, %1, %2, 0" : "=&v"(c) : "a"(a), "v"(b));
  } else {
    if constexpr (M == 0) asm volatile("v_mfma_f32_16x16x32_bf16 %0, %1, %2, %0" : "+v"(c) : "v"(a), "v"(b));
    else asm volatile("v_mfma_f32_16x16x32_bf16 %0, %1, %2, %0" : "+v"(c) : "a"(a), "v"(b));
  }
}
#define MWAIT(c0, c1) asm volatile("s_nop 7\n\ts_nop 3" : "+v"(c0), "+v"(c1))
template <int K>
__device__ __forceinline__ float add_quad(float a, float b) {
  float r;
  asm volatile("v_add_f32_dpp %0, %1, %2 quad_perm:[%3,%3,%3,%3] row_mask:0xf bank_mask:0xf" : "=v"(r) : "v"(a), "v"(b), "n"(K));
  return r;
}
__device__ __forceinline__ uint32_t neg2(uint32_t v) {
  uint32_t r;
  asm volatile("v_xor_b32 %0, 0x80008000, %1" : "=v"(r) : "v"(v));
  return r;
}
template <int C>
__device__ __forceinline__ u32x4 column(const u32x4& a, const u32x4& b, const u32x4& c, const u32x4& d) {
  u32x2 l, h;
  asm volatile("v_pk_mov_b32 %0, %1, %2 op_sel:[%3,%3]" : "=v"(l) : "v"(C < 2 ? a.xy : a.zw), "v"(C < 2 ? b.xy : b.zw), "n"(C & 1));
  asm volatile("v_pk_mov_b32 %0, %1, %2 op_sel:[%3,%3]" : "=v"(h) : "v"(C < 2 ? c.xy : c.zw), "v"(C < 2 ? d.xy : d.zw), "n"(C & 1));
  return u32x4{l[0], l[1], h[0], h[1]};
}
__device__ __forceinline__ void keep_a(const u32x4& v) { asm volatile("" :: "a"(v)); }
__device__ __forceinline__ void keep_v(const u32x4& v) { asm volatile("" :: "v"(v)); }
__device__ __forceinline__ int pin(int v) { asm volatile("" : "+v"(v)); return v; }
__device__ __forceinline__ void await(uint32_t v, __amdgpu_buffer_rsrc_t r, int vo, int so) {
  uint32_t cnt;
  SB();
  asm volatile("v_cmp_gt_u32 vcc, 1, %0\n\ts_cbranch_vccz 2f\n\ts_mov_b32 %1, 0x40000\n"
               "1:\n\ts_sleep 1\n\tbuffer_load_dword %0, %2, %3, %4 offen sc1\n\ts_waitcnt vmcnt(0)\n\t"
               "v_cmp_gt_u32 vcc, 1, %0\n\ts_cbranch_vccz 2f\n\ts_sub_u32 %1, %1, 1\n\ts_cbranch_scc0 1b\n2:"
               : "+v"(v), "=&s"(cnt) : "v"(vo), "s"(r), "s"(so) : "vcc", "scc", "memory");
  SB();
}

#define OPOL NT
#define ORD2 0x0132
#define ORD3 0x2310
#define KCY 84
#define KCAP 200
#define KLD 60
#define KLA 40
#define KD1 112
#define KQ1 14
#define KPI1 130
#define KPC1 140
#define KYR 2
#define KVE 173
#define KS1 19
#define KAW 0
#define KSU 6
#define KU0 0
#define KU2 4
#define NX1 16
#define KXE 25
#define NB2 8
#define KXP 16
#define KXA 19
#define NXA 4
#define NB0 0
#define NB1 0
#define NX0 10
#define KP1 1
#define KXL 40
#define KXU 30
#define KJM 48
#define KJ0 60
#define KSL 8
#define KCY2 72
#define KQJ 99
#define KCJ 68
#define KYS 5
#define THS 10
#define TS2 76
#define THM 5
#define THL -1
#define KS5 10
#define KD2 20
#define KPI2 40
#define KPC2 50
#define KXB 12
#define KD3 80
#define KQ3 14
#define KPI3 101
#define KPC3 112
#define KJR 4
#define KPI4 4
#define KPC4 28
enum { O_NONE, O_W, O_A, O_B, O_T, O_F, O_V, O_GI, O_Q, O_J, O_S1, O_Y1, O_X, O_S2, O_Y2, O_SIG, O_POLL, O_U };
constexpr int fill_t(int l) { return l < 2 ? O_T : l < 3 ? O_F : l < 35 ? O_V : l < 37 ? O_GI : l < 41 ? O_Q : O_J; }
constexpr int fill_j(int l) { return l < 2 ? l : l < 35 ? l - 3 : l < 37 ? l - 35 : l < 41 ? l - 37 : l - 41; }
constexpr int first(int c) { return c < 16 ? 6 * c : c < 32 ? 16 + 5 * c : 999; }
constexpr int chunk(int g) { return g < 96 ? g / 6 : (g - 16) / 5; }
constexpr int cpos(int c) { return c < 16 ? (c + 8) % 12 * 12288 : (c - 16) * 9216; }
struct Sched {
  int ty[3 * 184], ix[3 * 184];
  int before[3 * 184];
  int last[32];
  int nl, nj;
};
constexpr int main_t(int m) { return m >= 80 ? O_B : m % 5 < 2 ? O_W : O_A; }
constexpr int main_i(int m) { return m >= 80 ? m - 80 : m % 5 < 2 ? m / 5 * 2 + m % 5 : m / 5 * 3 + m % 5 - 2; }
constexpr int main_c(int m) { return m >= 80 ? 16 + (m - 80) / 9 * 4 + ((m - 80) % 9 - 1) / 2 : m / 5; }
constexpr int vfree(int l) { return first(18 + (8 * ((l - 3) >> 1) + 7) / 9) - 3; }
constexpr int hk(int i) { return i < 16 ? i : 4 * i - 45 + (i > 21) * 5 + (i > 23) * 3; }
constexpr Sched make_s1() {
  Sched c{};
  int m = 5 * GPRO, l = 0, o = m, y = 0, cr = KCY;
  for (int i = 0; i < GPRO; ++i) c.last[i] = 5 * i + 5;
  for (int t = 0; t < 3 * 184; ++t) {
    const int g = t / 3, j = t % 3;
    const bool thr = g >= TS0 && g < TS1;
    c.before[t] = o;
    if (j == 0) cr += g < TS0 ? 64 : 68 + 4 * (thr ? THR : g < TS2 ? THM - 2 : THL);
    if (cr > KCAP) cr = KCAP;
    if (j == 1 && ((g >= 93 && g < 99) || (g >= 174 && g < 179))) { c.ty[t] = O_S1; c.ix[t] = g < 99 ? g - 93 : g - 168; cr -= KCY; }
    else if (t == 3 * KD1 + 2) { c.ty[t] = O_SIG; c.ix[t] = 0; }
    else if (t == 3 * KPI1 + 2) { c.ty[t] = O_POLL; c.ix[t] = 0; }
    else if (g >= KVE) { if (y < 48 || m < 116) return c; }
    else if (g >= KPC1 && y < 48 && (j != 1 || KYR == 3)) { c.ty[t] = O_Y1; c.ix[t] = y++; }
    else if (g > KD1 - KQ1 && g <= KD1) { if (j == 1 && g <= KD1 - KQJ) { c.ty[t] = O_J; c.ix[t] = c.nj++; } }
    else if ((j == 1 || (j == 2 && thr)) && cr >= KCY) {
      const int k = main_c(m), fr = k < 12 ? 0 : first(k < 16 ? k - 10 : 6 + (9 * (k - ((m - 80) % 9 ? 16 : 13)) + 8) / 12) - 3;
      const bool fo = l < 35 && m >= 80 && (l < 3 || g >= vfree(l));
      if (m < 116 && g >= fr && first(k) - g <= (k < 16 ? KLA : KLD) && (m < 80 || l >= 3)) {
        c.ty[t] = main_t(m); c.ix[t] = main_i(m);
        cr -= g < TS0 ? 64 : KCY;
        c.last[k] = o + 1;
        ++m;
      } else if (fo) {
        c.ty[t] = fill_t(l); c.ix[t] = fill_j(l);
        cr -= KCY;
        ++l;
      } else if (g >= KJ0 && c.nj < KJM) {
        c.ty[t] = O_J; c.ix[t] = c.nj++;
        cr -= KCJ;
      }
    }
    if (c.ty[t]) ++o;
  }
  c.nl = l;
  return c;
}
constexpr Sched S1 = make_s1();
constexpr int NFILL = 3 + 32 + 2 + 4 + 64 - S1.nj;
constexpr int fill_i(int l) { return fill_j(l) + (l < 41 ? 0 : S1.nj); }
constexpr int NL0 = S1.nl + NB0, NL1 = NL0 + NB1, NL2 = NL1 + NB2;
static_assert(S1.nl > 2 && NL0 <= 33 && NL2 >= 35 && NL2 <= NFILL && first(chunk(KD1 + 4)) == KD1 + 4, "schedule");
constexpr Sched make_s2() {
  Sched c{};
  int l = NL2, x = NX0, y = 0, cr = 0;
  for (int t = 0; t < 3 * 136; ++t) {
    const int g = t / 3, j = t % 3;
    const bool thr = g >= TU0 && g < TU1, fs = j == 1 || (j == 2 && thr);
    if (j == 0) cr += 68 + 4 * (thr ? THR2 : THL);
    if (cr > KCAP) cr = KCAP;
    if (j == 1 && (g & 1) && ((g > 60 && g < 68) || (g > 124 && g < 132))) { c.ty[t] = O_S2; c.ix[t] = g < 68 ? (g - 61) / 2 : (g - 117) / 2; cr -= KCY2; }
    else if (t == 3 * KD2 + 2) { c.ty[t] = O_SIG; c.ix[t] = 3; }
    else if (t == 3 * KPI2 + 2) { c.ty[t] = O_POLL; c.ix[t] = 3; }
    else if (t == 3 * KD3 + 2) { c.ty[t] = O_SIG; c.ix[t] = 4; }
    else if (t == 3 * KPI3 + 2) { c.ty[t] = O_POLL; c.ix[t] = 4; }
    else if (g >= KPC3) { if (y < 8) { c.ty[t] = O_Y2; c.ix[t] = y++; } }
    else if (g <= KD2) {
      if (j == 1 && g >= KU0 && g < KU0 + KU2) { c.ty[t] = O_U; c.ix[t] = 16 - KU2 + g - KU0; }
      else if (j == 1 && x < NX1) { c.ty[t] = O_X; c.ix[t] = x++; }
    } else if (g > KD3 - KQ3 && g <= KD3) {
    } else if (x < (g >= KPC2 ? 64 : 32) && ((j && x >= 32 && x < 32 + KXB) || (j == 1 && 2 * x - g <= KXU) || (fs && cr >= KCY2 && 2 * x - g <= KXL))) {
      c.ty[t] = O_X; c.ix[t] = x++;
      cr -= KCY2;
      if (cr < 0) cr = 0;
    } else if (fs && cr >= KCY2 && l < NFILL) {
      c.ty[t] = fill_t(l); c.ix[t] = fill_i(l);
      cr -= KCJ;
      ++l;
    }
    if (t == 3 * KD3 && l < 41) return c;
    if (c.ty[t] == O_X && g >= (2 * c.ix[t] & ~7) - 1) return c;
  }
  c.nl = x == 64 && y == 8 ? l : -1;
  return c;
}
constexpr Sched S2 = make_s2();
static_assert((KU2 == 0 || KU2 == 4 || (KU2 == 8 && !KP1)) && KXA > KXP && KXA < 22 && KS1 < 22 && S2.nl + S1.nj >= 41 + 32 && S2.nl + (87 - KS5) * KJR / 8 >= NFILL, "schedule");

#define PADG() PAD(joint_kernel, 7, 3)
#define PADJ() PADG()
KERNEL __launch_bounds__(256, 1) void joint_kernel(char* ws, uint16_t* __restrict__ out) {
  extern __shared__ __attribute__((aligned(16))) char smem[];
  pad_macros();
  PADG();
  SB();
  const int bid = blockIdx.x, xcd = bid & 7, slot = bid >> 3;
  const int tid = threadIdx.x, lane = tid & 63;
  const int w = __builtin_amdgcn_readfirstlane(tid >> 6);
  const int cb = (slot & 3) * 4 + w;
  const int bin1 = xcd * 4 + (slot >> 3), mt1 = (slot >> 2) & 1;
  const int bin2 = xcd * 2 + (slot >> 4);
  const __amdgpu_buffer_rsrc_t ur = mkbuf(ws + (unsigned)(A_U1 + bin1 * UB1 + mt1 * (168 * 128)), UB1);
  const __amdgpu_buffer_rsrc_t wr = mkbuf(ws + (unsigned)(A_W1 + bin1 * WBIN + cb * WCB), WCB);
  const int vd = w * 1024 + (lane >> 3) * 128 + (((lane & 7) ^ ((4 * w + (lane >> 4)) & 7)) * 16);
  const int rho = lane & 15, kq = lane >> 4;
  const int n = lane & 31, h = lane >> 5;
  const float* ge = (const float*)(ws + A_GE);
  __amdgpu_buffer_rsrc_t vr, ys, yl, ub, yb, wb, fr;
  u32x4 Wm[32], X[64], wt[64], y1[3][4], yv[2][4][2], F, gi[2], o[11], up[8];
  int ve, vt, v1[3], vx, vs, vy, vu, vf[3], vp[3];
  uint32_t pl[2];
  constexpr int FO[6] = {F1, F1 + 1024, F2, F2 + 4096, F3, F3 + 4096};
  auto yload = [&](int b, int i) { yv[b][i & 3][i >> 2] = __builtin_amdgcn_raw_buffer_load_b128(yb, vy, ((i >> 2) * 8 + (i & 3)) * YB2 + b * 65536, WT); };
  auto issue = [&](auto it, auto ii) {
    constexpr int t = decltype(it)::value, i = decltype(ii)::value;
    if constexpr (t == O_W) Wm[i] = __builtin_amdgcn_raw_buffer_load_b128(wr, lane * 16 + (i & 3) * 1024, (i >> 2) * 4096, 0);
    else if constexpr (t == O_A) dma<16>(ur, smem + cpos(i / 3) + i % 3 * 4096 + w * 1024, vd, i / 3 * PL1 + i % 3 * 4096);
    else if constexpr (t == O_B && i % 9) dma<16>(ur, smem + cpos(main_c(80 + i)) + (i % 9 - 1 & 1) * 4096 + w * 1024, vd, (main_c(80 + i) - 16) * PL1 + (3 + (i % 9 - 1 & 1)) * 4096);
    else if constexpr (t == O_B) dma<16>(ur, smem + cpos(main_c(80 + i)) + 8192 + w * 9216, ve, i / 9 * 4 * PL1);
    else if constexpr (t == O_T) dma<16>(mkbuf(&tabs.g1[2 * w][0][0], 2048), smem + J_F + w * 2048 + i * 1024, lane * 16, i * 1024);
    else if constexpr (t == O_F) F = ((const u32x4*)&tabs.f2[0][0])[lane];
    else if constexpr (t == O_V) dma<16>(vr, smem + (i >> 1) * 8192 + w * 2048 + (i & 1) * 1024, lane * 16, i * 1024);
    else if constexpr (t == O_GI) gi[i] = ((const u32x4*)&tabs.g2[i][0][0])[lane];
    else if constexpr (t == O_Q) dma<16>(mkbuf(ge + w * 1024, 4096), smem + J_G + w * 4096 + i * 1024, lane * 16, i * 1024);
    else if constexpr (t == O_J) wt[i] = __builtin_amdgcn_raw_buffer_load_b128(wb, lane * 16 + (i & 3) * 1024, (i >> 5) * 131072 + ((i >> 2) & 7) * 4096, 0);
    else if constexpr (t == O_S1) __builtin_amdgcn_raw_buffer_store_b128(o[i], ys, vt + (i & 3) * 1024, (i >> 2) * 4096, WT);
    else if constexpr (t == O_Y1) y1[i % 3][i % 48 / 12][i / 3 & 3] = __builtin_amdgcn_raw_buffer_load_b32(yl, v1[i % 3], (i % 48 / 12 * 8 + (i / 3 & 3)) * YB1 + i / 48 * (84 * 64), WT);
    else if constexpr (t == O_X) X[i] = __builtin_amdgcn_raw_buffer_load_b128(ub, vx + (i & 3) * 1024, (i >> 5) * 16 * UB2 + ((i >> 2) & 7) * 4096, WT);
    else if constexpr (t == O_S2) __builtin_amdgcn_raw_buffer_store_b128(o[i & 3], yb, vs + (i & 3) * 1024, (i >> 2) * 65536, WT);
    else if constexpr (t == O_Y2) yload(0, i);
    else if constexpr (t == O_SIG) __builtin_amdgcn_raw_buffer_store_b32(1u, fr, vf[i >> 1], FO[i], WT);
    else if constexpr (t == O_POLL) pl[i & 1] = __builtin_amdgcn_raw_buffer_load_b32(fr, vp[i >> 1], FO[i], WT);
    else if constexpr (t == O_U) __builtin_amdgcn_raw_buffer_store_b128(up[i & 7], ub, vu + ((i >> 1) & 3) * 256, ((i >> 3) * 16 + (i & 1) * 8) * UB2, WT);
    SB();
  };
  auto fills = [&](auto i0, auto i1) {
    sfor<decltype(i0)::value, decltype(i1)::value>([&](auto il) { issue(IC<fill_t(decltype(il)::value)>{}, IC<fill_i(decltype(il)::value)>{}); });
  };
  sfor<0, 5 * GPRO>([&](auto il) { issue(IC<main_t(decltype(il)::value)>{}, IC<main_i(decltype(il)::value)>{}); });
  const int l0 = (int)(uint32_t)(uintptr_t)(__attribute__((address_space(3))) char*)smem;
  int ab[3][2], ga[4], tj, ty, ta, tau;
  float ok[2];
#pragma unroll
  for (int ri = 0; ri < 2; ++ri)
#pragma unroll
    for (int wn = 0; wn < 3; ++wn) ab[wn][ri] = pin(l0 + rho * 128 + ((((kq >> 1) * 4 + ri * 2 + (kq & 1)) ^ (rho >> 1)) * 16) + wn * 65536);
  auto setup = [&](int k) {
    asm volatile("" : "+s"(ws));
    if (k == 0) {
      vr = mkbuf(ws + (unsigned)(A_W2 + bin2 * WBIN + cb * WCB), WCB);
      ys = mkbuf(ws + (unsigned)(A_Y1 + bin1 * YB1 + cb * YP1 + mt1 * (168 * 64)), 168 * 64);
    } else if (k == 1) {
      yl = mkbuf(ws + A_Y1, Y1BYTES);
      ub = mkbuf(ws + A_U2, U2BYTES);
      yb = mkbuf(ws + A_Y2, Y2BYTES);
      wb = mkbuf(ws + (unsigned)(A_WF + w * 32768), 131072 + 32768);
      fr = mkbuf(ws + A_FL, NFLAG * 4);
    } else if (k == 2) {
      ve = pin(20480 + w * PL1 + (lane >> 3) * 128 + (((lane & 7) ^ kq) * 16));
      vt = pin(rho * 64 + kq * 16);
      tj = 4 * (slot & 15) + w;
      asm volatile("" : "+s"(tj));
      ty = tj % 3;
      ta = tj / 3 * 4 + (ty ? ty : -1);
      asm volatile("" : "+s"(ty), "+s"(ta));
    } else if (k < 6) {
      const int i = k - 3, g = ta + i + 84 * ((n >> 2) & 1), e = g >= 84, r = g - 84 * e;
      const int v = pin(4 * h * YB1 + bin2 * YP1 + (84 * (r >= 42) + 2 * r + e) * 64 + (n >> 3) * 16 + (n & 3) * 4);
      v1[i] = pin(g < (tj < 63 && i != 2 * ty ? 168 : -2) ? v : OOB);
      if (i) ok[i - 1] = __builtin_bit_cast(float, pin((unsigned)(32 * (2 * tj + i - 1 + 126 * ((n >> 2) & 1)) - 16 + 16 * h) < 8000u ? 0x3F800000 : 0x7F800000));
    } else if (k < 8) {
      if (k == 6) tau = pin(16 * ((n >> 2) & 1) + 8 * ((n >> 3) & 1) + 2 * (n & 3) + (n >> 4) + 16 + (ty ? 16 * ty : 48));
#pragma unroll
      for (int i = (3 * k - 18) >> 1; i < 3 * k - 17; ++i) {
        const int r = tau - 16 * (i >> 1) - 48 * (i & 1);
        ga[i] = pin(l0 + J_F + h * 1024 + ((unsigned)r < 64u ? r ^ ((r >> 4) & 1) : 0) * 16);
      }
    } else if (k == 8) {
      const int ge_ = ta + (h ? 2 - (ty == 1) : ty == 0);
      vp[0] = pin((bin2 >> 2) * 256 + (ge_ >= 42 && ge_ < 84) * 128 + n * 4);
      vf[0] = pin((slot & 3) * 256 + mt1 * 128 + bin1 * 4 + tid * 65536);
    } else {
      vf[1] = vf[2] = pin((slot & 15) * 256 + bin2 * 4 + tid * 65536);
      vu = pin((rho >> 1) * UB2 + (slot & 15) * 32768 + bin2 * 2048 + (w >> 1) * 1024 + (w & 1) * 128 + (lane & 1) * 64 + kq * 16);
    }
    SB();
  };
  auto aread = [&](int g, int ri) -> u32x4 {
    const int a = cpos(chunk(g)) + (g - first(chunk(g))) * 2048;
    return lds_ld(ab[a >> 16][ri] + (a & 65535));
  };
  f32x4 yr[11], yi[11];
  u32x4 A[4][2], nw[2], gf[16];
  auto gread = [&](int i) { gf[i] = lds_ld(ga[i >> 2] + (i & 3) * 2048); };
  auto trans = [&](auto ib, auto&& hook) {
    constexpr int b = decltype(ib)::value;
    f32x16 d[2], u[2][2];
    u32x4 a;
    sfor<0, 16>([&](auto ii) {
      constexpr int i = decltype(ii)::value;
      mm<0, (i & 7) == 0>(d[i >> 3], gf[i], y1[(i >> 3) + ((i >> 2) & 1)][i & 3]);
      PADG();
      if constexpr (i + 6 < 16) gread(i + 6);
      SB();
      hook(ii);
    });
    auto pack = [&](int q) {
#pragma unroll
      for (int e = 0; e < 2; ++e) {
#pragma unroll
        for (int z = 0; z < 2; ++z)
#pragma unroll
          for (int j = 0; j < 4; ++j) up[4 * q + 2 * e + z][j] = pk2bf(u[q][z][8 * e + 2 * j], u[q][z][8 * e + 2 * j + 1]);
        PADG();
        swap16(up[4 * q + 2 * e], up[4 * q + 2 * e + 1]);
        PADG();
      }
      SB();
    };
    auto put = [&](auto iq) {
      constexpr int q = decltype(iq)::value;
      sfor<8 * b + 4 * q, 8 * b + 4 * q + 4>([&](auto ii) { issue(IC<O_U>{}, ii); });
      PADG();
      st_pad(up[4 * q + 2], up[4 * q + 3]);
      SB();
    };
    sfor<0, 4>([&](auto ii) {
      constexpr int i = decltype(ii)::value, q = i >> 1, z = i & 1;
      if constexpr (i < 2) asm volatile("s_nop 2" : "+v"(d[z]));
#pragma unroll
      for (int j = 0; j < 2; ++j) { a[j] = gelu2(d[z][8 * q + 2 * j], d[z][8 * q + 2 * j + 1], ok[z]); PADG(); }
      SB();
      hook(IC<16 + 2 * i>{});
#pragma unroll
      for (int j = 2; j < 4; ++j) { a[j] = gelu2(d[z][8 * q + 2 * j], d[z][8 * q + 2 * j + 1], ok[z]); PADG(); }
      asm volatile("s_nop 1" : "+v"(a));
      SB();
      mm<0, true>(u[q][z], a, F);
      SB();
      hook(IC<17 + 2 * i>{});
      if constexpr (i == 2) { pack(0); if constexpr (!b || KP1) put(IC<0>{}); }
    });
    MWAIT(u[1][0], u[1][1]);
    pack(1);
    hook(IC<24>{});
    if constexpr (b && !KP1 && KU2 < 8) put(IC<0>{});
    if constexpr (!b || !KU2) put(IC<1>{});
  };
#pragma unroll
  for (int k = 0; k < KSU; ++k) setup(k);
  SB();
  wait_vm<5 * GPRO - 5>();
  asm volatile("s_barrier" ::: "memory");
#pragma unroll
  for (int z = 0; z < 3; ++z) { A[z][1] = aread(z, 1); SB(); A[z][0] = aread(z, 0); SB(); }
#pragma unroll
  for (int j = 0; j < 4; ++j) nw[0][j] = neg2(Wm[1][j]);
  SB();
  sfor<0, 179>([&](auto ig) {
    constexpr int g = decltype(ig)::value, c = chunk(g), ks = c & 15, mb = g - first(c) + (c < 16 ? 0 : 6), e = first(c + 1) - g;
    if constexpr (g == KPC1) { await(pl[0], fr, vp[0], FO[0]); }
    auto mf = [&](auto ij) {
      constexpr int j = decltype(ij)::value, i = (((g & 1) ? ORD1 : ORD0) >> (12 - 4 * j)) & 3;
      constexpr bool z = ks == 0 && !(j & 1);
      if constexpr (g < 176) {
        if constexpr (i == 0) mm<1, z>(yr[mb], Wm[2 * ks], A[g & 3][0]);
        else if constexpr (i == 1) mm<0, z>(yr[mb], nw[c & 1], A[g & 3][1]);
        else if constexpr (i == 2) mm<0, z>(yi[mb], Wm[2 * ks + 1], A[g & 3][0]);
        else mm<1, z>(yi[mb], Wm[2 * ks], A[g & 3][1]);
      }
      PADG();
    };
    auto cvt = [&](int j) {
      constexpr int m = g >= 92 && g < 98 ? g - 92 : g >= 173 && g < 178 ? g - 167 : -1;
      if constexpr (m >= 0) {
        if constexpr (g == 177) if (!j) asm volatile("s_nop 1" : "+v"(yr[m]), "+v"(yi[m]));
#pragma unroll
        for (int i = 2 * j; i < 2 * j + 2; ++i) o[m][i] = pk2bf(yr[m][i], yi[m][i]);
      }
      if constexpr (S1.ty[3 * g + 1] == O_S1) if (j) keep_v(o[S1.ix[3 * g + 1]]);
      if constexpr (g >= 173 && g < 176) gread(2 * (g - 173) + j);
      SB();
    };
    if constexpr (g < 176 && !(g & 1)) keep_v(A[(g + 1) & 3][0]);
    mf(IC<0>{});
    if constexpr (g + 3 < 176) { A[(g + 3) & 3][1] = aread(g + 3, 1); SB(); A[(g + 3) & 3][0] = aread(g + 3, 0); }
    SB();
    mf(IC<1>{});
    issue(IC<S1.ty[3 * g]>{}, IC<S1.ix[3 * g]>{});
    cvt(0);
    mf(IC<2>{});
    issue(IC<S1.ty[3 * g + 1]>{}, IC<S1.ix[3 * g + 1]>{});
    mf(IC<3>{});
    if constexpr (e == 4) {
      static_assert(S1.last[c + 1] && S1.before[3 * g + 2] >= S1.last[c + 1], "next chunk not requested yet");
      SB();
      if constexpr (g == KD1) { DRAIN(); }
      else {
        if constexpr (g < KD1 || S1.last[c + 1] > S1.before[3 * KD1 + 2]) wait_vm<S1.before[3 * g + 2] - S1.last[c + 1]>();
        asm volatile("s_barrier" ::: "memory");
      }
      SB();
    }
    issue(IC<S1.ty[3 * g + 2]>{}, IC<S1.ix[3 * g + 2]>{});
    if constexpr (g >= TS0 && g < TS0 + 30 - 3 * KSU) {
      if constexpr ((g - TS0) % 3 == 1) setup(KSU + (g - TS0) / 3);
      else if constexpr (THS >= 0) asm volatile("s_nop %0" ::"n"(THS));
    }
    else if constexpr (g >= TS0 && g < TS1) asm volatile("s_nop %0" ::"n"(S1.ty[3 * g + 2] ? THR - 3 : THR));
    else if constexpr (g >= TS1 && g < TS2) asm volatile("s_nop %0" ::"n"(THM));
    else if constexpr (THL >= 0 && g >= TS1 && g < 173 && !S1.ty[3 * g + 2]) asm volatile("s_nop %0" ::"n"(THL + 2));
    if constexpr (e <= 3 && e >= 2) {
#pragma unroll
      for (int j = 6 - 2 * e; j < 8 - 2 * e; ++j) nw[(c + 1) & 1][j] = neg2(Wm[(2 * ks + 3) & 31][j]);
    }
    SB();
    cvt(1);
  });
  st_pad(o[10], o[10]);
  trans(IC<0>{}, [&](auto ii) {
    constexpr int i = decltype(ii)::value;
    if constexpr (i < 16) fills(IC<S1.nl + NB0 * i / 16>{}, IC<S1.nl + NB0 * (i + 1) / 16>{});
    if constexpr (i == KS1) { DRAIN(); issue(IC<O_SIG>{}, IC<1>{}); }
  });
  const int pos = 32 * xcd + slot, seg = pos < 250 ? pos : 0;
  int wa[2], ra[4], fa[4], qa, vo;
  {
    const int grp = (slot & 12) + w, s = seg + (kq & 1), e = s >= 126, r = s - 126 * e;
        vx = pin(bin2 * UB2 + grp * 32768 + lane * 16);
    vs = pin(bin2 * YB2 + (slot & 3) * 131072 + grp * 4096 + ((rho & 2) * 4 + (rho & 4) + (rho >> 3) * 2 + (rho & 1)) * 64 + kq * 16);
    vy = pin(4 * h * YB2 + w * 131072 + (r >> 3) * 4096 + (8 * e + (r & 7)) * 64 + (rho >> 2) * 1024 + (rho & 3) * 16);
    vp[1] = pin(grp * 256 + rho * 4);
    vp[2] = pin(((r >> 5) * 4 + w) * 256 + rho * 4);
#pragma unroll
    for (int i = 0; i < 2; ++i) wa[i] = pin(l0 + lane * 16 + i * 65536);
  }
#pragma unroll
  for (int i = 0; i < 6; ++i) gread(i);
  if constexpr (KSL) __builtin_amdgcn_s_sleep(KSL);
  issue(IC<O_POLL>{}, IC<1>{});
  if constexpr (!KAW) { DRAIN(); issue(IC<O_SIG>{}, IC<2>{}); }
  await(pl[1], fr, vp[0], FO[1]);
  if constexpr (KAW == 1) { DRAIN(); issue(IC<O_SIG>{}, IC<2>{}); }
  sfor<0, 48>([&](auto ii) { issue(IC<O_Y1>{}, IC<48 + decltype(ii)::value % 16 * 3 + decltype(ii)::value / 16>{}); PADJ(); });
  if constexpr (KAW == 2) { DRAIN(); issue(IC<O_SIG>{}, IC<2>{}); }
  fills(IC<NL0>{}, IC<NL1>{});
  auto wread = [&](int g, int j) -> u32x4 { return lds_ld(wa[(g >> 5) & 1] + ((g >> 3 & 3) * 16 + (g & 7) + j * 8) * 1024); };
  trans(IC<1>{}, [&](auto ii) {
    constexpr int i = decltype(ii)::value, e = KXE;
    if constexpr (i < KXP) fills(IC<NL1 + NB2 * i / KXP>{}, IC<NL1 + NB2 * (i + 1) / KXP>{});
    if constexpr (i == KXP) issue(IC<O_POLL>{}, IC<2>{});
    if constexpr (i == KXA) {
      await(pl[0], fr, vp[1], FO[2]);
      sfor<0, NXA>([&](auto ix) { issue(IC<O_X>{}, ix); PADJ(); });
    }
    if constexpr (i > KXA && i < e) sfor<NXA + (NX0 - NXA) * (hk(i - 1) - hk(KXA)) / (hk(e - 1) - hk(KXA)), NXA + (NX0 - NXA) * (hk(i) - hk(KXA)) / (hk(e - 1) - hk(KXA))>([&](auto ix) { issue(IC<O_X>{}, ix); PADJ(); });
    if constexpr (i == 22) asm volatile("s_barrier" ::: "memory");
    if constexpr (i == 23) {
#pragma unroll
      for (int z = 0; z < 3; ++z) { A[z][1] = wread(z, 1); SB(); A[z][0] = wread(z, 0); SB(); }
    }
  });
  f32x4 ac[16][2];
  float re, im;
  sfor<0, 132>([&](auto ig) {
    constexpr int g = decltype(ig)::value, k = g >> 3, mb = (g & 7) + (g & 64) / 8;
    if constexpr (g == KPC2) { await(pl[1], fr, vp[1], FO[3]); }
    if constexpr (g == KPC3) { await(pl[0], fr, vp[2], FO[4]); }
    auto mf = [&](auto ij) {
      constexpr int j = decltype(ij)::value, i = (((g & 1) ? ORD3 : ORD2) >> (12 - 4 * j)) & 3;
      if constexpr (g < 128) mm<0, (k & 7) == 0 && !(j & 1)>(ac[mb][i >> 1], A[g & 3][i & 1], X[4 * k + 2 * (i & 1) + (i >> 1)]);
      PADJ();
    };
    auto cvt = [&](int j) {
      constexpr int m = g >= 59 && g < 67 ? g - 59 : g >= 123 && g < 131 ? g - 115 : -1;
      if constexpr (m >= 0) {
        const int i = 2 * (m & 1) + j;
        if constexpr (g == 129) if (!j) asm volatile("s_nop 1" : "+v"(ac[15][0]), "+v"(ac[15][1]));
        asm volatile("v_subrev_f32_dpp %0, %4, %2 row_shl:4 row_mask:0xf bank_mask:0x5\n\t"
                     "v_add_f32_dpp %1, %2, %4 row_shl:4 row_mask:0xf bank_mask:0x5\n\t"
                     "v_sub_f32_dpp %0, %3, %5 row_shr:4 row_mask:0xf bank_mask:0xa\n\t"
                     "v_add_f32_dpp %1, %5, %3 row_shr:4 row_mask:0xf bank_mask:0xa"
                     : "=&v"(re), "=&v"(im) : "v"(ac[m & 14][0][i]), "v"(ac[m & 14][1][i]), "v"(ac[m | 1][0][i]), "v"(ac[m | 1][1][i]));
        o[(m >> 1) & 3][i] = pk2bf(re, im);
      }
      if constexpr (S2.ty[3 * g + 1] == O_S2) if (j) keep_v(o[S2.ix[3 * g + 1] & 3]);
      if constexpr (S2.ty[3 * g + 1] == O_U) if (j) keep_v(up[S2.ix[3 * g + 1] & 7]);
      SB();
    };
    if constexpr (g < 128 && !(g & 1)) keep_v(A[(g + 1) & 3][0]);
    mf(IC<0>{});
    if constexpr (g + 3 < 128) { A[(g + 3) & 3][1] = wread(g + 3, 1); SB(); A[(g + 3) & 3][0] = wread(g + 3, 0); }
    SB();
    mf(IC<1>{});
    issue(IC<S2.ty[3 * g]>{}, IC<S2.ix[3 * g]>{});
    cvt(0);
    mf(IC<2>{});
    issue(IC<S2.ty[3 * g + 1]>{}, IC<S2.ix[3 * g + 1]>{});
    mf(IC<3>{});
    if constexpr (g == KD2) { DRAIN(); }
    if constexpr (g == KD3) { DRAIN(); }
    issue(IC<S2.ty[3 * g + 2]>{}, IC<S2.ix[3 * g + 2]>{});
    if constexpr (g >= TU0 && g < TU1) asm volatile("s_nop %0" ::"n"(S2.ty[3 * g + 2] ? THR2 - 3 : THR2));
    else if constexpr (THL >= 0 && g < 123 && !S2.ty[3 * g + 2]) asm volatile("s_nop %0" ::"n"(THL + 2));
    if constexpr ((g & 7) == 7 && g < 127) { keep_v(X[4 * k + 7]); }
    SB();
    cvt(1);
    if constexpr (g >= 84 && g < 88) ra[g & 3] = pin(l0 + ((w + 1 + g) & 3) * 4096 + lane * 16);
    if constexpr (g >= 88 && g < 92) fa[g & 3] = pin(l0 + J_F + ((w + 1 + g) & 3) * 2048 + lane * 16);
    if constexpr (g == 82) qa = pin(l0 + J_G + w * 4096 + (h * 8 + (n >> 4) * 4 + (n & 3)) * 16);
    if constexpr (g == 83) vo = pin(h * 4096 + w * 128 + n * 4);
    SB();
  });
  st_pad(o[3], o[3]);
  float p[2][32], x[8], mx = 0.f;
  f32x4 q[2][8], fe[4];
  u32x4 tp[2][4];
  auto jstep = [&](auto it) {
    constexpr int t = decltype(it)::value, l = S2.nl + t * KJR / 8;
    if constexpr (l < NFILL && l < S2.nl + (t + 1) * KJR / 8) issue(IC<O_J>{}, IC<fill_i(l)>{});
  };
  auto front = [&](auto ib, auto&& after) {
    constexpr int b = decltype(ib)::value & 1;
    f32x4 d[4];
    u32x4 fb[8];
    u32x4 yt[2][4];
    sfor<0, 4>([&](auto ic) { constexpr int c = decltype(ic)::value; yt[0][c] = column<c>(yv[b][0][0], yv[b][1][0], yv[b][2][0], yv[b][3][0]); });
    if constexpr (decltype(ib)::value < 2) PADJ();
    SB();
    sfor<0, 4>([&](auto ic) {
      constexpr int c = decltype(ic)::value;
      mm<0, true>(d[c], yt[0][c], gi[0]);
      yt[1][c] = column<c>(yv[b][0][1], yv[b][1][1], yv[b][2][1], yv[b][3][1]);
      if constexpr (decltype(ib)::value < 2) PADJ();
      SB();
    });
#pragma unroll
    for (int c = 0; c < 4; ++c) {
      mm<0, false>(d[c], yt[1][c], gi[1]);
      if constexpr (decltype(ib)::value < 2) PADJ();
    }
    asm volatile("s_nop 4" : "+v"(d[0]));
#pragma unroll
    for (int j = 0; j < 2; ++j) {
#pragma unroll
      for (int c = 0; c < 4; ++c) {
        fb[j][c] = gelu2(d[2 * j + (c >> 1)][2 * (c & 1)], d[2 * j + (c >> 1)][2 * (c & 1) + 1]);
        if constexpr (decltype(ib)::value < 2) PADJ();
      }
      lds_st(fa[3] + j * 1024, fb[j]);
      SB();
    }
    sfor<0, 32>([&](auto ii) {
      constexpr int i = decltype(ii)::value, eb = i < 8 ? i >> 1 : (i - 8) / 6, r = i < 8 ? i & 1 : 2 + (i - 8) % 6;
      mm<1, r == 0>(fe[eb], wt[i], fb[r]);
      if constexpr (decltype(ib)::value < 2) PADJ();
      SB();
      if constexpr (i == 0) {
        BAR();
      }
      if constexpr (i < 3) {
        fb[2 * i + 2] = lds_ld(fa[i]);
        fb[2 * i + 3] = lds_ld(fa[i] + 1024);
      }
      SB();
      after(ii);
    });
  };
  auto twrite = [&](auto iG) {
    constexpr int V = decltype(iG)::value + 31;
    if constexpr (V >= 0 && V < 256 && (V & 7) == 7) lds_st(ra[3] + J_T + ((V >> 5) & 1) * 16384 + ((V >> 3) & 3) * 1024, tp[(V >> 5) & 1][(V >> 3) & 3]);
    SB();
  };
  auto tslot = [&](auto iG) {
    constexpr int G = decltype(iG)::value;
    constexpr int A = G + 36, B = G + 35, C = G + 34, D = G + 33;
    if constexpr (B >= 0 && B < 256) asm volatile("v_rcp_f32 %0, %0" : "+v"(x[B & 7]));
    if constexpr (C >= 0 && C < 256) asm volatile("v_fma_f32 %0, -%1, %0, 0.5" : "+v"(x[C & 7]) : "v"(q[C >> 7][(C >> 2) & 7][C & 3]));
    PADJ();
    if constexpr (A >= 0 && A < 256) x[A & 7] = add_quad<(A >> 5) & 3>(p[A >> 7][A & 31], q[A >> 7][(A >> 2) & 7][A & 3]);
    SB();
    if constexpr (D >= 0 && D < 256 && (D & 1)) tp[(D >> 5) & 1][(D >> 3) & 3][(D >> 1) & 3] = pk2bf(x[(D - 1) & 7], x[D & 7]);
    SB();
  };
  auto stepE = [&](int b, int eb, int i) {
    const float a = fe[eb][i] * C3;
    mx = __builtin_fmaxf(mx, __builtin_fabsf(a));
    const uint32_t e = __builtin_bit_cast(uint32_t, __builtin_amdgcn_exp2f(a));
    const auto r = __builtin_amdgcn_permlane16_swap(e, e, false, false);
    p[b][8 * eb + i] = __builtin_bit_cast(float, (uint32_t)r[0]);
    p[b][8 * eb + 4 + i] = __builtin_bit_cast(float, (uint32_t)r[1]);
  };
  auto head = [&](auto ib) {
    constexpr int b = decltype(ib)::value;
    front(ib, [&](auto ii) {
      constexpr int i = decltype(ii)::value;
      if constexpr (b == 0 && i == KS5) {
        DRAIN();
        issue(IC<O_SIG>{}, IC<5>{});
        asm volatile("s_cmpk_lt_i32 %0, 250\n\ts_cbranch_scc1 1f\n\ts_endpgm\n1:" :: "s"(pos) : "scc");
      }
      if constexpr (i >= 3 && i < 11) q[b][i - 3] = __builtin_bit_cast(f32x4, lds_ld(qa + b * 2048 + (i - 3) * 256));
      if constexpr (b == 0 && i > KS5) jstep(IC<i - KS5 - 1>{});
      if constexpr (i >= 16 && (i - 16) % 6 < 4) {
        if constexpr ((i - 16) % 6 == 0) asm volatile("s_nop 1" : "+v"(fe[(i - 16) / 6]));
        stepE(b, (i - 16) / 6, (i - 16) % 6);
      }
      SB();
    });
    if constexpr (b == 0) sfor<-36, -32>([&](auto ir) { tslot(ir); });
    else asm volatile("s_nop 7" ::);
    asm volatile("" : "+v"(fe[3]));
#pragma unroll
    for (int i = 0; i < 4; ++i) { stepE(b, 3, i); PADJ(); }
    mx = __builtin_fmaxf(mx, q[b][0][0] < 0.f || q[b][4][0] < 0.f ? 2 * EMAX : 0.f);
    if constexpr (b) *(__attribute__((address_space(3))) float*)(uintptr_t)(uint32_t)(l0 + J_R + tid * 4) = mx;
    asm volatile("s_nop 1");
    SB();
  };
  head(IC<0>{});
  sfor<-32, 0>([&](auto ir) { jstep(IC<decltype(ir)::value + 63 - KS5>{}); twrite(ir); tslot(ir); });
  const __amdgpu_buffer_rsrc_t ob = mkbuf(out + (size_t)seg * 16 * 8 * DCH, 4000 * 4096 + 65536);
  f32x16 acc[2][2];
  u32x4 a[16];
  uint32_t ov[16];
  int vq[2] = {vo, vo + 8192};
  auto store = [&](int i) { __builtin_amdgcn_raw_buffer_store_b32(ov[i], ob, vq[(i >> 2) & 1] + ((i >> 3) * 4 + (i & 3)) * 512, 0, OPOL); };
  auto next = [&](int st, int j) {
    vq[j] += (st & 3) ? 16384 : 4000 * 4096 - 3 * 16384;
    asm volatile("" : "+v"(vq[j]));
  };
  sfor<0, 256>([&](auto iG) {
    constexpr int G = decltype(iG)::value, st = G >> 5, g = G & 31, r = g >> 1, z = g & 1;
    if constexpr (G == 92) { head(IC<1>{}); }
    mm<2, r == 0>(acc[st & 1][z], r < 4 ? tp[st & 1][r & 3] : a[r], wt[32 + g]);
    PADJ();
    SB();
    if constexpr (g == 2) BAR();
    if constexpr (st == 0 && g < 31) keep_a(wt[33 + g]);
    if constexpr (z && r >= 3 && r < 15) keep_v(a[r + 1]);
    constexpr int rd = g >= 3 && g < 6 ? g + 1 : (g >= 7 && g <= 23 && z) ? (g + 7) / 2 : 0;
    constexpr int sn = g == 1 ? 15 : g == 2 ? 0 : g == 6 ? 1 : g >= 25 ? g - 17 : (g > 8 && !z && (g & 7)) ? (g - 6) / 2 - (g > 16) : -1;
    if constexpr (rd) a[rd] = lds_ld(ra[(rd >> 2) - 1] + J_T + (st & 1) * 16384 + (rd & 3) * 1024);
    if constexpr (sn >= 0 && st > (g == 1)) store(sn);
    if constexpr (G < 24) jstep(IC<G + 63 - KS5>{});
    if constexpr (G == KPI4) issue(IC<O_POLL>{}, IC<5>{});
    if constexpr (G == KPC4) await(pl[1], fr, vp[2], FO[5]);
    if constexpr (G >= KPC4 && G < KPC4 + 8 * KYS && (G - KPC4) % KYS == 0) yload(1, (G - KPC4) / KYS);
    twrite(iG);
    tslot(iG);
    if constexpr (st > 0 && g == 30) next(st, 0);
    if constexpr (st > 1 && g == 4) next(st - 1, 1);
    if constexpr (G == 225) asm volatile("s_nop 5" : "+v"(acc[0][0]));
    if constexpr (st > 0 && z) ov[r] = pk2bf(acc[(st - 1) & 1][0][r], acc[(st - 1) & 1][1][r]);
    SB();
  });
  store(15);
  next(7, 1);
  const f32x4 rg = __builtin_bit_cast(f32x4, lds_ld(l0 + J_R + lane * 16));
  MWAIT(acc[1][0], acc[1][1]);
#pragma unroll
  for (int i = 0; i < 16; ++i) { ov[i] = pk2bf(acc[1][0][i], acc[1][1][i]); store(i); PADJ(); }
  if (__builtin_expect(__builtin_amdgcn_ballot_w64(rg[0] > EMAX || rg[1] > EMAX || rg[2] > EMAX || rg[3] > EMAX) != 0, 0)) {
    asm volatile("s_waitcnt vmcnt(0)" ::: "memory");
#pragma unroll 1
    for (int b = 0; b < 2; ++b) {
#pragma unroll
      for (int i = 0; i < 8; ++i) yload(0, i);
      vy += 65536;
      front(IC<2>{}, [](auto) {});
      asm volatile("s_nop 7" : "+v"(fe[3]));
#pragma unroll 1
      for (int st = 0; st < 4; ++st) {
        const float* gp = ge + 4096 + (w * 2 + b) * 512 + (h * 8 + (n >> 4) * 4 + (n & 3)) * 4;
#pragma unroll
        for (int f = 0; f < 4; ++f) {
          u32x4 t;
#pragma unroll
          for (int c = 0; c < 2; ++c) {
            const f32x4 gv = *(const f32x4*)(gp + (2 * f + c) * 64);
            float y[4];
#pragma unroll
            for (int i = 0; i < 4; ++i)
              y[i] = 0.5f - __builtin_amdgcn_rcpf(__builtin_amdgcn_exp2f(__shfl(fe[f][i], h * 32 + c * 16 + (lane & 12) + st) * C3 + gv[i]) + 1.f);
            t[2 * c] = pk2bf(y[0], y[1]);
            t[2 * c + 1] = pk2bf(y[2], y[3]);
          }
          lds_st(ra[3] + J_T + (st & 1) * 16384 + f * 1024, t);
        }
        BAR();
        sfor<0, 16>([&](auto ir) {
          constexpr int r = decltype(ir)::value;
          const u32x4 v = lds_ld(ra[(r / 4 + 3) & 3] + J_T + (st & 1) * 16384 + (r & 3) * 1024);
#pragma unroll
          for (int z = 0; z < 2; ++z) mm<2, r == 0>(acc[0][z], v, wt[32 + 2 * r + z]);
        });
        MWAIT(acc[0][0], acc[0][1]);
#pragma unroll
        for (int i = 0; i < 16; ++i)
          __builtin_amdgcn_raw_buffer_store_b32(pk2bf(acc[0][0][i], acc[0][1][i]), ob, vo + st * 16384 + ((i >> 3) * 4 + (i & 3)) * 512,
                                                b * (4000 * 4096) + ((i >> 2) & 1) * 8192, NT);
      }
    }
  }
}

template <int CONV>
__device__ __forceinline__ void wtrans(const uint16_t* __restrict__ w, char* __restrict__ wh, int wid, int lane) {
  const int dq = wid & 3, pc = wid >> 2;
  const int ks = pc >> 2, kq = pc & 3;
  const int n32 = lane & 31, h = lane >> 5;
  const int r = CONV == 1 ? ks >> 3 : kq >> 1;
  const int c0 = CONV == 1 ? (ks & 7) * 32 + kq * 8 : dmap(ks, 8 * (kq & 1));
  const __amdgpu_buffer_rsrc_t wr = mkbuf(w, 31 * 131072);
  const int vb = (16 * h + r + 1) * 131072 + c0 * 512 + dq * 128 + n32 * 4;
  const int vb0 = (h + r == 0) ? OOB : vb - 262144;
  constexpr int NFB = CONV == 1 ? 2 : 1;
  const u32x4* et = CONV == 1 ? (const u32x4*)&tabs.e1[0][0][0][0] : (const u32x4*)&tabs.e2[0][0][0];
  u32x4 E[NFB][2];
#pragma unroll
  for (int fb = 0; fb < NFB; ++fb) { E[fb][0] = et[fb * 128 + lane]; E[fb][1] = et[fb * 128 + 64 + lane]; }
  uint32_t v[8][8];
#pragma unroll
  for (int el = 0; el < 8; ++el)
#pragma unroll
    for (int jj = 0; jj < 8; ++jj) {
      const int co = CONV == 1 ? el * 512 : (el & 3) * 1024;
      const int so = (CONV == 1 ? 0 : (el >> 2) * 4096) + (jj ? (jj - 1) * 262144 : 0);
      v[el][jj] = __builtin_amdgcn_raw_buffer_load_b32(wr, (jj ? vb : vb0) + co, so, NT);
    }
  PHASE();
  const __amdgpu_buffer_rsrc_t dr = mkbuf(wh, (CONV == 1 ? 32 : 16) * WBIN);
  const int vo = 2 * h * WBIN + (4 * dq + (n32 >> 4)) * WCB + ks * 2048 + kq * 256 + (n32 & 15) * 16;
#pragma unroll
  for (int e = 0; e < 2; ++e) {
    u32x4 b[8];
#pragma unroll
    for (int el = 0; el < 8; ++el)
#pragma unroll
      for (int j = 0; j < 4; ++j) b[el][j] = __builtin_amdgcn_perm(v[el][2 * j + 1], v[el][2 * j], e ? 0x07060302u : 0x05040100u);
#pragma unroll
    for (int fb = 0; fb < NFB; ++fb) {
      u32x4 o[16];
#pragma unroll
      for (int ep = 0; ep < 4; ++ep) {
        f32x16 d[2];
#pragma unroll
        for (int z = 0; z < 2; ++z) d[z] = mfma32(E[fb][1], b[2 * ep + z], zero16());
#pragma unroll
        for (int z = 0; z < 2; ++z) d[z] = mfma32(E[fb][0], b[2 * ep + z], d[z]);
#pragma unroll
        for (int i = 0; i < 16; ++i) o[i][ep] = pk2bf(d[0][i], d[1][i]);
      }
#pragma unroll
      for (int i = 0; i < 16; ++i)
        __builtin_amdgcn_raw_buffer_store_b128(o[i], dr, vo + (i & 1) * 1024,
                                               (16 * fb + 4 * (i >> 2) + ((i >> 1) & 1)) * WBIN + e * 2 * WCB, WT);
      st_pad(o[14], o[15]);
      SB();
    }
  }
}

__device__ __forceinline__ void xload(const uint16_t* __restrict__ x, int u, int lane, uint32_t (&v)[3][8]) {
  const int cq = u & 7, m = u >= 8 * 168, b = u - 8 * 168 * m >= 8 * 84, r = (u >> 3) - 168 * m - 84 * b, sg = 84 * (r & 1) + 42 * m + (r >> 1);
  const __amdgpu_buffer_rsrc_t xr = mkbuf(x + (size_t)b * 16000 * DCH, 8192000);
#pragma unroll
  for (int ks = 0; ks < 3; ++ks) {
    const int row0 = 48 * sg - 24 + 16 * ks + 8 * (lane >> 5);
    const int vb = row0 >= 0 ? row0 * 1024 + cq * 128 + (lane & 31) * 4 : OOB;
#pragma unroll
    for (int e = 0; e < 8; ++e) v[ks][e] = __builtin_amdgcn_raw_buffer_load_b32(xr, vb + (e & 3) * 1024, (e >> 2) * 4096, NT);
  }
}
__device__ __forceinline__ void xtrans(char* __restrict__ u1, int u, int lane, const uint32_t (&v)[3][8], const u32x4 (&F)[2][3]) {
  const __amdgpu_buffer_rsrc_t ur = mkbuf(u1 + (2 * (u & 7)) * PL1 + (u >> 3) * 128, U1BYTES - (2 * (u & 7)) * PL1 - (u >> 3) * 128);
  const int vo = ((lane & 15) >> 1) * UB1 + ((lane >> 4) & 1) * 64 + (lane & 1) * 32 + (lane >> 5) * 16;
  u32x4 a[2][3];
#pragma unroll
  for (int ks = 0; ks < 3; ++ks)
#pragma unroll
    for (int j = 0; j < 4; ++j) {
      a[0][ks][j] = __builtin_amdgcn_perm(v[ks][2 * j + 1], v[ks][2 * j], 0x05040100u);
      a[1][ks][j] = __builtin_amdgcn_perm(v[ks][2 * j + 1], v[ks][2 * j], 0x07060302u);
    }
#pragma unroll
  for (int fb = 0; fb < 2; ++fb) {
    f32x16 d[2] = {zero16(), zero16()};
#pragma unroll
    for (int ks = 0; ks < 3; ++ks)
#pragma unroll
      for (int e = 0; e < 2; ++e) d[e] = mfma32(a[e][ks], F[fb][ks], d[e]);
#pragma unroll
    for (int qp = 0; qp < 2; ++qp) {
      u32x4 o[2];
#pragma unroll
      for (int z = 0; z < 2; ++z)
#pragma unroll
        for (int i = 0; i < 4; ++i) o[z][i] = pk2bf(d[0][8 * qp + 4 * z + i], d[1][8 * qp + 4 * z + i]);
      swap16(o[0], o[1]);
#pragma unroll
      for (int z = 0; z < 2; ++z) __builtin_amdgcn_raw_buffer_store_b128(o[z], ur, vo, (fb * 16 + z * 8) * UB1 + qp * PL1, WT);
      st_pad(o[0], o[1]);
    }
  }
}

KERNEL __launch_bounds__(512, 2) void pre_kernel(const uint16_t* __restrict__ x, const uint16_t* __restrict__ w1,
                                                     const uint16_t* __restrict__ w2, const uint16_t* __restrict__ wf,
                                                     const uint16_t* __restrict__ wo, const uint16_t* __restrict__ g,
                                                     const uint16_t* __restrict__ wg, char* __restrict__ u1,
                                                     char* __restrict__ w1h, char* __restrict__ w2h, u32x4* __restrict__ wff,
                                                     u32x4* __restrict__ wof, float* __restrict__ ge, uint32_t* __restrict__ flags) {
  const int blk = blockIdx.x;
  const int tid = threadIdx.x, lane = tid & 63;
  const int wave = __builtin_amdgcn_readfirstlane(tid >> 6);
  const int n = lane & 31, h = lane >> 5;
  __shared__ char pfs[256];
  if (wave == 7) {
    const char *p0 = (const char*)&tabs - 320, *p1;
    LABEL_ADDR(p1, ".Lce");
    code_pf(p0, (int)(p1 - p0), blk >> 3, pfs);
  }
  if (wave == 0) {
    wtrans<1>(w1, w1h, blk, lane);
  } else if (wave == 1) {
    wtrans<2>(w2, w2h, blk, lane);
  } else {
    const int bq = (blk & 7) * 32 + (blk >> 3);
    const int u0 = ((21 * bq) >> 1) + wave - 2, ue = (21 * (bq + 1)) >> 1;
    u32x4 F[2][3];
#pragma unroll
    for (int fb = 0; fb < 2; ++fb)
#pragma unroll
      for (int ks = 0; ks < 3; ++ks) F[fb][ks] = ((const u32x4*)&tabs.f1[fb][ks][0][0])[lane];
    uint32_t v0[3][8];
    xload(x, u0, lane, v0);
    if (u0 + 6 < ue) {
      uint32_t v1[3][8];
      xload(x, u0 + 6, lane, v1);
      SB();
      WALK(7, wave - 2);
      PHASE();
      xtrans(u1, u0, lane, v0, F);
      SB();
      xtrans(u1, u0 + 6, lane, v1, F);
    } else if (wave == 7) {
      const int i = blk & 31, w = (blk >> 5) & 3, second = blk >> 7;
      const int eb = i < 8 ? i >> 1 : (i - 8) / 6, r = (2 * w + (i < 8 ? i & 1 : 2 + (i - 8) % 6)) & 7;
      const uint16_t* src = second ? wo + (16 * ((4 * w + (i >> 1)) & 15) + 8 * h) * DCH + 64 * w + 2 * n + (i & 1)
                                   : wf + dmap(4 * (r >> 1) + (lane >> 4), 2 * (r & 1)) * DCH + 64 * w + 16 * eb + (lane & 15);
      uint32_t v[8];
#pragma unroll
      for (int j = 0; j < 8; ++j) v[j] = src[(second ? j : 8 * (j & 3) + 2 * (j >> 2)) * DCH];
      PHASE();
      xtrans(u1, u0, lane, v0, F);
      const float sc = second ? 2.f : 1.f;
      u32x4 o;
#pragma unroll
      for (int j = 0; j < 4; ++j) o[j] = pk2bf(bf2f(v[2 * j]) * sc, bf2f(v[2 * j + 1]) * sc);
      ((second ? wof : wff) + (blk & 127) * 64)[lane] = o;
      if (blk < NFLAG / 64) flags[blk * 64 + lane] = 0;
    } else if (!(blk & ~0x16)) {
      const int gb = ((blk >> 1) & 3) | ((blk >> 2) & 4);
      const int c0 = 32 * gb + 2 * (lane & 15), kq = lane >> 4;
      uint32_t v[8][8];
      u32x4 ga[8];
#pragma unroll
      for (int ks = 0; ks < 8; ++ks) {
        ga[ks] = *(const u32x4*)(g + (lane & 15) * DCH + ks * 32 + 8 * kq);
#pragma unroll
        for (int e = 0; e < 8; ++e) v[ks][e] = *(const uint32_t*)(wg + (ks * 32 + 8 * kq + e) * DCH + c0);
      }
      SB();
      asm volatile("s_barrier" ::: "memory");
      SB();
      xtrans(u1, u0, lane, v0, F);
      SB();
      f32x4 d[2] = {{0.f, 0.f, 0.f, 0.f}, {0.f, 0.f, 0.f, 0.f}};
#pragma unroll
      for (int ks = 0; ks < 8; ++ks)
#pragma unroll
        for (int e = 0; e < 2; ++e) {
          u32x4 bb;
#pragma unroll
          for (int j = 0; j < 4; ++j) bb[j] = __builtin_amdgcn_perm(v[ks][2 * j + 1], v[ks][2 * j], e ? 0x07060302u : 0x05040100u);
          d[e] = mfma16(ga[ks], bb, d[e]);
        }
      float* dst = ge + (((((gb >> 1) * 2 + (kq >> 1)) * 4 + 2 * (gb & 1) + ((lane >> 3) & 1)) * 2 + ((lane >> 1) & 1)) * 2 + ((lane >> 2) & 1)) * 32 + (kq & 1) * 16 + 2 * (lane & 1);
      f32x2 gs[4];
      bool far = false;
#pragma unroll
      for (int i = 0; i < 4; ++i) {
        gs[i] = f32x2{bf2f(f2bf(d[0][i])) * C2, bf2f(f2bf(d[1][i])) * C2};
        far = far || __builtin_fabsf(gs[i][0]) > EMAX || __builtin_fabsf(gs[i][1]) > EMAX;
      }
      far = __builtin_amdgcn_ballot_w64(far) != 0;
#pragma unroll
      for (int i = 0; i < 4; ++i) {
        *(f32x2*)(dst + 4 * i) = far ? f32x2{-1.f, -1.f} : f32x2{__builtin_amdgcn_exp2f(-gs[i][0]), __builtin_amdgcn_exp2f(-gs[i][1])};
        *(f32x2*)(dst + 4096 + 4 * i) = gs[i];
      }
    } else {
      PHASE();
      xtrans(u1, u0, lane, v0, F);
    }
  }
  asm volatile(".Lce:");
}

extern "C" void kernel_launch(void* const* d_in, const int* in_sizes, int n_in, void* d_out, int out_size, void* d_ws,
                              size_t ws_size, hipStream_t stream) {
  const uint16_t* x = (const uint16_t*)d_in[0];
  const uint16_t* w1 = (const uint16_t*)d_in[1];
  const uint16_t* w2 = (const uint16_t*)d_in[2];
  const uint16_t* g = (const uint16_t*)d_in[3];
  const uint16_t* wf = (const uint16_t*)d_in[4];
  const uint16_t* wg = (const uint16_t*)d_in[5];
  const uint16_t* wo = (const uint16_t*)d_in[6];
  uint16_t* out = (uint16_t*)d_out;
  char* ws = (char*)d_ws;
  (void)hipFuncSetAttribute((const void*)joint_kernel, hipFuncAttributeMaxDynamicSharedMemorySize, JOINT_LDS);

  pre_kernel<<<256, 512, 0, stream>>>(x, w1, w2, wf, wo, g, wg, ws + A_U1, ws + A_W1, ws + A_W2, (u32x4*)(ws + A_WF),
                                      (u32x4*)(ws + A_WF + 131072), (float*)(ws + A_GE), (uint32_t*)(ws + A_FL));
  joint_kernel<<<256, 256, JOINT_LDS, stream>>>(ws, out);
}
```
